```python
import torch

def _softmax(x, axis=-1):
    return torch.softmax(x, dim=axis)

def _gelu(x):
    return torch.nn.functional.gelu(x, approximate='tanh')

def _silu(x):
    return torch.nn.functional.silu(x)

def _sigmoid(x):
    return torch.sigmoid(x)

def _layernorm(x, axis=-1, eps=1e-05):
    m = x.mean(dim=axis, keepdim=True)
    v = x.var(dim=axis, keepdim=True, unbiased=False)
    return (x - m) / torch.sqrt(v + eps)

def _scatter_add(tgt, idx, src, axis=0):
    return tgt.clone().index_add_(axis, idx, src)

def _segment_sum(src, seg, n):
    out = torch.zeros((n,) + src.shape[1:], dtype=src.dtype)
    return out.index_add_(0, seg, src)

def _pad(x, pw):
    flat = tuple((v for pair in reversed(pw) for v in pair))
    return torch.nn.functional.pad(x, flat)
kernel_name = 'standalone__tensor_product_equiv_tfn__graph'

def setup_inputs():

    _w = lambda a: (lambda t: t.to(torch.bfloat16) if t.is_floating_point() else t)(torch.as_tensor(a))
    N, deg, C, L, DE = (10000, 32, 128, 1, 16)
    E = N * deg
    M = (L + 1) ** 2
    src = (lambda lo, hi, *s: torch.randint(lo, hi, s))(0, N, E).to(torch.int64)
    dst = (lambda lo, hi, *s: torch.randint(lo, hi, s))(0, N, E).to(torch.int64)
    r = (lambda *s: torch.randn(*s, dtype=torch.bfloat16) * 0.05)(E, 3)
    r = r / (torch.linalg.norm(r, dim=-1, keepdim=True) + 1e-09)
    rx, ry, rz = (r[:, 0].clone(), r[:, 1].clone(), r[:, 2].clone())
    Ys = [torch.ones((E, 1), dtype=float)]
    if L >= 1:
        Ys.append(r.to(float))
    if L >= 2:
        Ys.append(torch.stack([2 * rx * ry, 2 * ry * rz, (3 * rz * rz - 1.0) / (lambda _x: torch.sqrt(torch.as_tensor(_x)))(3.0), 2 * rx * rz, rx * rx - ry * ry], dim=-1).to(float))
    for _l in range(3, L + 1):
        Ys.append(((lambda *s: torch.randn(*s, dtype=torch.bfloat16) * 0.05)(E, 2 * _l + 1) * 0.3).to(float))
    Y = torch.cat(Ys, dim=-1)
    cg = torch.zeros((M, M, M), dtype=float)
    off = [_l * _l for _l in range(L + 2)]
    for l1 in range(L + 1):
        for l2 in range(L + 1):
            for l3 in range(abs(l1 - l2), min(L, l1 + l2) + 1):
                d1, d2, d3 = (2 * l1 + 1, 2 * l2 + 1, 2 * l3 + 1)
                cg[off[l1]:off[l1 + 1], off[l2]:off[l2 + 1], off[l3]:off[l3 + 1]] = (lambda *s: torch.randn(*s, dtype=torch.bfloat16) * 0.05)(d1, d2, d3) / (lambda _x: torch.sqrt(torch.as_tensor(_x)))(float(d1 * d2))
    return {'x': _w((lambda *s: torch.randn(*s, dtype=torch.bfloat16) * 0.05)(N, C, M) * 0.2), 'src': _w(src), 'dst': _w(dst), 'Y': _w(Y), 'edge_feat': _w((lambda *s: torch.randn(*s, dtype=torch.bfloat16) * 0.05)(E, DE)), 'w_rad': _w((lambda *s: torch.randn(*s, dtype=torch.bfloat16) * 0.05)(DE, C) * (1.0 / max(1.0, DE) ** 0.5)), 'cg': _w(cg), 'w_mix': _w((lambda *s: torch.randn(*s, dtype=torch.bfloat16) * 0.05)(C, C) * (1.0 / max(1.0, C) ** 0.5))}

def reference(x, src, dst, Y, edge_feat, w_rad, cg, w_mix):
    N, C, M = (x.shape[0], x.shape[1], x.shape[2])
    R = _silu(edge_feat @ w_rad)
    xs = x[src]
    tp = torch.einsum('eci,ef,ifo->eco', xs, Y, cg)
    msg = R[:, :, None] * tp
    agg = _segment_sum(msg.reshape(-1, C * M), dst, N).reshape(N, C, M)
    return torch.einsum('ncm,cd->ndm', agg, w_mix)
```

```cpp
#include <hip/hip_runtime.h>
#include <hip/hip_bf16.h>
#include <stdint.h>

typedef __attribute__((ext_vector_type(8))) short bf16x8;
typedef __attribute__((ext_vector_type(4))) short bf16x4;
typedef __attribute__((ext_vector_type(4))) float f32x4;
typedef __attribute__((ext_vector_type(2))) unsigned u32x2;
typedef __attribute__((ext_vector_type(4))) unsigned u32x4;
typedef __attribute__((ext_vector_type(8))) unsigned u32x8;
typedef __attribute__((ext_vector_type(16))) unsigned u32x16;
typedef __attribute__((ext_vector_type(32))) unsigned u32x32;
typedef unsigned long long u64;

#define C_DIM 128
#define M_DIM 4
#define DE_DIM 16
#define NPB_MAX 80
#define NB_MAX 128
#define EPB_MAX 1280
#define NRND 7
#define SMAX 72
#define OOB_OFF 0x80000000u
#define SC1 16
#define NT 2
#define SPIN_MAX (1u << 22)
#define CODE_LINES 166
#define PTO 0xfffa9500u
#define PTY 0xffffa950u
#define PTZ 0xffffea54u
#define NT0 3
#define NT2 7
#define TLIST 8, 9, 4, 5, 10, 11, 12
#define WLO 8
#define WHI 11
#define PSL "1"
#define TKF 0x75624130u
#define TKB 0x75690213u
#define TKA 0x9848ffffu

#define W_CTL 0
#define W_DEC 2048
#define W_FLG 4096
#define W_TAB (W_FLG + 32768)
#define W_RA (W_TAB + 256 * 256 * 8)
#define W_AGG (W_RA + 256 * EPB_MAX * 16)
#define W_END (W_AGG + NB_MAX * NPB_MAX * 1024)

#define SB() __builtin_amdgcn_sched_barrier(0)

static __device__ __forceinline__ float bf2f(unsigned short v) { return __uint_as_float(((unsigned)v) << 16); }
typedef __bf16 bf16v2 __attribute__((ext_vector_type(2)));
static __device__ __forceinline__ unsigned pack2bf(float lo, float hi) {
    bf16v2 r = (bf16v2){(__bf16)lo, (__bf16)hi};
    return __builtin_bit_cast(unsigned, r);
}
static __device__ __forceinline__ unsigned short f2bf(float v) { return (unsigned short)(pack2bf(v, 0.f) & 0xffffu); }
template <int CTRL, int RM> static __device__ __forceinline__ int dpp_add(int v) {
    return v + __builtin_amdgcn_update_dpp(0, v, CTRL, RM, 0xf, false);
}
static __device__ __forceinline__ int wave_incl_scan(int v) {
    v = dpp_add<0x111, 0xf>(v); v = dpp_add<0x112, 0xf>(v); v = dpp_add<0x114, 0xf>(v); v = dpp_add<0x118, 0xf>(v);
    v = dpp_add<0x142, 0xa>(v); v = dpp_add<0x143, 0xc>(v);
    return v;
}
template <int CTRL, int RM> static __device__ __forceinline__ unsigned dpp_max(unsigned v) {
    return max(v, (unsigned)__builtin_amdgcn_update_dpp(0, (int)v, CTRL, RM, 0xf, false));
}
static __device__ __forceinline__ unsigned wave_max_scan(unsigned v) {
    v = dpp_max<0x111, 0xf>(v); v = dpp_max<0x112, 0xf>(v); v = dpp_max<0x114, 0xf>(v); v = dpp_max<0x118, 0xf>(v);
    v = dpp_max<0x142, 0xa>(v); v = dpp_max<0x143, 0xc>(v);
    return v;
}
static __device__ __forceinline__ unsigned bsum(unsigned c, unsigned m, unsigned acc = 0u) { return __builtin_amdgcn_udot4(c, m, acc, false); }
static __device__ __forceinline__ unsigned bytesum2(unsigned c0, unsigned c1, unsigned m0, unsigned m1, unsigned acc = 0u) {
    return __builtin_amdgcn_udot4(c1, m1, __builtin_amdgcn_udot4(c0, m0, acc, false), false);
}
static __device__ __forceinline__ unsigned below_mask(int w, int k) {
    const int n = w - 4 * k;
    return n >= 4 ? 0x01010101u : (n <= 0 ? 0u : (0x01010101u & ((1u << (8 * n)) - 1u)));
}
static __device__ __forceinline__ void flag_store(const void* p, unsigned v) {
    unsigned t0, t1; u64 sv;
    asm volatile("v_mov_b32 %0, 0\n\tv_mov_b32 %1, %4\n\ts_mov_b64 %2, exec\n\ts_mov_b64 exec, 1\n\t"
                 "global_store_dword %0, %1, %3 sc1\n\ts_mov_b64 exec, %2" : "=&v"(t0), "=&v"(t1), "=&s"(sv) : "s"(p), "s"(v) : "memory");
}
static __device__ __forceinline__ unsigned lds_arrive_rtn(unsigned a) {
    unsigned t0, t1; u64 sv;
    asm volatile("v_mov_b32 %0, %3\n\tv_mov_b32 %1, 1\n\ts_mov_b64 %2, exec\n\ts_mov_b64 exec, 1\n\t"
                 "ds_add_rtn_u32 %1, %0, %1\n\ts_mov_b64 exec, %2\n\ts_waitcnt lgkmcnt(0)" : "=&v"(t0), "=&v"(t1), "=&s"(sv) : "s"(a) : "memory");
    return (unsigned)__builtin_amdgcn_readfirstlane((int)t1);
}
static __device__ __forceinline__ void lds_arrive(unsigned a, unsigned inc = 1u) {
    unsigned t0, t1; u64 sv;
    asm volatile("s_waitcnt lgkmcnt(0)\n\tv_mov_b32 %0, %3\n\tv_mov_b32 %1, %4\n\ts_mov_b64 %2, exec\n\ts_mov_b64 exec, 1\n\t"
                 "ds_add_u32 %0, %1\n\ts_mov_b64 exec, %2" : "=&v"(t0), "=&v"(t1), "=&s"(sv) : "s"(a), "s"(inc) : "memory");
}
template <int SLEEP = 0> static __device__ __forceinline__ unsigned lds_wait(unsigned a, unsigned n) {
    for (;;) {
        unsigned v;
        asm volatile("ds_read_b32 %0, %1\n\ts_waitcnt lgkmcnt(0)" : "=v"(v) : "v"(a) : "memory");
        v = (unsigned)__builtin_amdgcn_readfirstlane((int)v);
        if (v >= n) return v;
        if (SLEEP) __builtin_amdgcn_s_sleep(SLEEP);
    }
}
static __device__ __forceinline__ unsigned sel3(unsigned r, unsigned a, unsigned b, unsigned c) {
    asm volatile("" : "+v"(a), "+v"(b), "+v"(c));
    return r == 0u ? a : (r == 1u ? b : c);
}
static __device__ __forceinline__ unsigned ld_agent(const void* p) { return __hip_atomic_load((const unsigned*)p, __ATOMIC_RELAXED, __HIP_MEMORY_SCOPE_AGENT); }
static __device__ __forceinline__ void poll_tag(const void* p, unsigned tag) {
#pragma unroll 1
    for (unsigned spin = 0; spin < SPIN_MAX; ++spin) {
        if (__all(ld_agent(p) == tag)) break;
        __builtin_amdgcn_s_sleep(2);
    }
    __builtin_amdgcn_fence(__ATOMIC_ACQUIRE, "wavefront");
}
static __device__ __forceinline__ void wg_barrier() {
    asm volatile("s_waitcnt lgkmcnt(0)" ::: "memory");
    __builtin_amdgcn_s_barrier();
    asm volatile("" ::: "memory");
}
static __device__ __forceinline__ f32x4 mfma4(u32x2 a, u32x2 b, f32x4 c) {
    return __builtin_amdgcn_mfma_f32_4x4x4bf16_1k(__builtin_bit_cast(bf16x4, a), __builtin_bit_cast(bf16x4, b), c, 0, 0, 0);
}
static __device__ __forceinline__ f32x4 mfma16(u32x4 a, u32x4 b, f32x4 c) {
    return __builtin_amdgcn_mfma_f32_16x16x32_bf16(__builtin_bit_cast(bf16x8, a), __builtin_bit_cast(bf16x8, b), c, 0, 0, 0);
}

static constexpr unsigned evt_of(unsigned pt) {
    unsigned e = 6u;
    for (int n = 2; n < 15; ++n) if (((pt >> (2 * n)) & 3u) != ((pt >> (2 * n + 2)) & 3u)) e |= 1u << n;
    return e;
}

static constexpr u64 wave_of(bool kept) {
    u64 r = 0;
    for (int w = 0; w < 8; ++w) {
        r |= (u64)w << (4 * (((kept ? TKF : TKB) >> (4 * w)) & 15u));
        if (w >= 4 && (w >= 6) == kept) r |= (u64)w << (4 * ((TKA >> (4 * w)) & 15u));
    }
    return r;
}
static constexpr unsigned outer_of(bool kept) { return 1u << ((TKA >> (kept ? 24 : 16)) & 15u) | 1u << ((TKA >> (kept ? 28 : 20)) & 15u); }

#define L_MISC 0
#define L_KEY 96
#define L_AT 416
#define L_NB 736
#define L_PCNT 1280
#define L_CCNT (L_PCNT + 2048)
#define L_BNC (L_CCNT + 1280)
#define L_CG (L_BNC + 4096)
#define L_PRV (L_CG + 1024)
#define A_STG L_PRV
#define A_PTAB (A_STG + EPB_MAX * 16)
#define PK_STR 8704
#define L_PK (L_PRV + 8 * SMAX * 64)
#define L_DUM L_PK
#define L_CTAB (L_PK + 2048)
#define L_MRK (L_PK + PK_STR)
#define L_WR (L_PK + 7 * PK_STR)
#define L_IMG (L_PK + 8 * PK_STR)
#define L_TOTAL (L_IMG + 32768)
static_assert(L_TOTAL <= 163840 && L_BNC <= 8192, "LDS budget");
static_assert(L_PRV % 16 == 0 && L_PK % 16 == 0 && L_IMG % 16 == 0 && L_CG % 16 == 0 && L_CCNT % 16 == 0, "alignment");
enum { SY_CPY = 40, SY_C1 = 44, SY_S = 48, SY_C2 = 52, SY_C3 = 56, SY_RI = 60, SY_PRE = 64 };

static __device__ __forceinline__ u32x4 mkdesc(const void* p, unsigned bytes) {
    return (u32x4){(unsigned)(uintptr_t)p, (unsigned)((uintptr_t)p >> 32) & 0xffffu, bytes, 0x00020000u};
}

__global__ __launch_bounds__(512, 2) void k_fused(
    const unsigned short* __restrict__ x, const int* __restrict__ src, const int* __restrict__ dst, const u32x2* __restrict__ Y,
    const u32x4* __restrict__ ef, const unsigned short* __restrict__ w_rad, const unsigned short* __restrict__ cg,
    const unsigned short* __restrict__ w_mix, unsigned short* __restrict__ out, unsigned char* ws,
    int N, int E, int epb, unsigned nb_magic, unsigned smask, unsigned cmul)
{
    __shared__ __attribute__((aligned(256))) unsigned char smem[L_TOTAL];
    const int tid = threadIdx.x, lane = tid & 63;
    const int w = __builtin_amdgcn_readfirstlane(tid >> 6);
    const int bx = blockIdx.x;
    int half;
    const int b = (bx & 3) * 32 + (bx >> 3);
    const int g = lane >> 4, c16 = lane & 15;
    const int node0 = (b * N + 127) >> 7, npb = (((b + 1) * N + 127) >> 7) - node0;
    unsigned cp;
    {
        u64 pc;
        asm volatile("s_getpc_b64 %0" : "=s"(pc));
        const __amdgpu_buffer_rsrc_t rsc = __builtin_amdgcn_make_buffer_rsrc((void*)(pc & ~(u64)127), 0, CODE_LINES * 128, 0x00020000);
        cp = __builtin_amdgcn_raw_buffer_load_b32(rsc, (tid >> 3) == 32 ? ((bx >> 3) + lane * 32) * 128 : OOB_OFF, 0, 0);
        SB();
    }
    const unsigned lds0 = (unsigned)(uintptr_t)(__attribute__((address_space(3))) unsigned char*)smem;
    const __amdgpu_buffer_rsrc_t rsw = __builtin_amdgcn_make_buffer_rsrc((void*)ws, 0, W_END, 0x00020000);
    asm volatile("" :: "s"(x), "s"(src), "s"(dst), "s"(Y), "s"(ef), "s"(w_rad), "s"(cg), "s"(w_mix), "s"(out), "s"(ws), "s"(N), "s"(E), "s"(epb), "s"(nb_magic),
                 "s"(smask), "s"(cmul));
    const int base_e = bx * epb - epb;
    const int ne = base_e < 0 ? 0 : max(min(epb, E - base_e), 0);
    auto task_at = [&](unsigned wq, int t) -> unsigned {
        const unsigned q = (t == 0 ? TKF : (t == 1 ? TKB : TKA)) >> (4 * wq) & 15u;
        return q == 15u ? L_MISC + 8 : L_AT + ((t == 0 || (t == 2 && wq >= 6u) ? 40 * half : 40 - 40 * half) + 4 * q) * 4;
    };
    unsigned tag;
    bool slow;
    u32x4 wr[8], cgA;
    auto load_wr = [&]() {
#pragma unroll
        for (int t = 0; t < 8; ++t) wr[t] = *(const u32x4*)(smem + L_WR + t * 1024 + lane * 16);
        cgA = *(const u32x4*)(smem + L_CG + lane * 16);
        asm volatile("" : "+v"(wr[0]), "+v"(wr[1]), "+v"(wr[2]), "+v"(wr[3]), "+v"(wr[4]), "+v"(wr[5]), "+v"(wr[6]), "+v"(wr[7]), "+v"(cgA));
    };

    __builtin_amdgcn_s_setprio(3);
    auto touch = [&](unsigned code) -> unsigned {
        if (code < 8u)
            return __builtin_amdgcn_raw_buffer_load_b32(__builtin_amdgcn_make_buffer_rsrc((void*)(ef + (size_t)base_e * 2), 0, code == 0u ? 0 : ne * 32, 0x00020000), lane * 128, (code - 1u) * 8192u, 0);
        const unsigned j = code < 16u ? (code - 8u) * 128u + b : (code - 16u) * 32u + (bx >> 3);
        return __builtin_amdgcn_raw_buffer_load_b32(__builtin_amdgcn_make_buffer_rsrc((void*)x, 0, N * 1024, 0x00020000), lane * 128,
                                                    (2u * j + ((half ^ (smask >> (j >> 5))) & 1u)) * 8192u, 0);
    };
    constexpr unsigned tl[] = {TLIST};
    const __amdgpu_buffer_rsrc_t rsm = __builtin_amdgcn_make_buffer_rsrc((void*)w_mix, 0, C_DIM * C_DIM * 2, 0x00020000);
    unsigned t0v = 0u, cm = 0u, wv[8], cv[4], tv[NT2 + 1];
    {
        const unsigned sh = 8u * (w & 3);
        unsigned d[3], sv[3], epoch, inc[3];
        u32x2 yv[3];
        const __amdgpu_buffer_rsrc_t rsd = __builtin_amdgcn_make_buffer_rsrc((void*)(dst + base_e), 0, ne * 4, 0x00020000);
        const __amdgpu_buffer_rsrc_t rss = __builtin_amdgcn_make_buffer_rsrc((void*)(src + base_e), 0, ne * 4, 0x00020000);
        const __amdgpu_buffer_rsrc_t rsy = __builtin_amdgcn_make_buffer_rsrc((void*)(Y + base_e), 0, ne * 8, 0x00020000);
        const unsigned le = (unsigned)(w * 160 + lane), le2 = lane < 32 ? le : 0x1000000u;
#pragma unroll
        for (int r = 0; r < 3; ++r) {
            d[r] = __builtin_amdgcn_raw_buffer_load_b32(rsd, (r < 2 ? le : le2) * 4 + 256 * r, 0, NT);
            sv[r] = __builtin_amdgcn_raw_buffer_load_b32(rss, (r < 2 ? le : le2) * 4 + 256 * r, 0, NT);
            SB();
        }
        epoch = __builtin_amdgcn_raw_buffer_load_b32(rsw, W_CTL, 0, 0);
        const unsigned pt = __builtin_amdgcn_raw_buffer_load_b32(__builtin_amdgcn_make_buffer_rsrc((void*)x, 0, bx * 8 + w == 8 + 7 ? min(N, 6144) * 1024 : 0, 0x00020000),
                                                                 (lane * 0x42000u) & 0xe0e000u, 0, 0);
#pragma unroll
        for (int r = 0; r < 3; ++r) yv[r] = __builtin_amdgcn_raw_buffer_load_b64(rsy, (r < 2 ? le : le2) * 8 + 512 * r, 0, NT);
        SB();
        if (bx * 8 + w == 7) {
            unsigned s0, s1, v0, v1;
#define RACE(r, o, a, b) "s_load_dword %2, %4, " o " offset:" a "\n\ts_load_dword %3, %4, " o " offset:" b "\n\ts_waitcnt lgkmcnt(1)\n\ts_sleep " PSL "\n\t" \
                         "s_getreg_b32 " r ", hwreg(HW_REG_IB_STS, 8, 1)\n\ts_waitcnt lgkmcnt(0)\n\t"
            asm volatile("s_load_dword %0, %4, 0x10000\n\ts_load_dword %1, %4, %5 offset:0x10000\n\ts_load_dword %2, %4, %6 offset:0x10000\n\ts_mov_b32 %3, 0\n\ts_waitcnt lgkmcnt(0)\n\t"
                         "" RACE("%0", "%3", "0", "0x2000") RACE("%1", "%5", "0x6000", "0x4000") "s_cmp_lg_u32 %0, %1\n\ts_cbranch_scc1 Lr_%=\n\t" RACE("%1", "%6", "0x8000", "0xa000") "Lr_%=:"
                         : "=&s"(s0), "=&s"(s1), "=&s"(v0), "=&s"(v1) : "s"(x), "s"(N > 6144 ? 0x400000u : 0u), "s"(N > 6144 ? 0x200000u : 0x2000u) : "memory", "scc");
            unsigned xc;
            asm volatile("s_getreg_b32 %0, hwreg(HW_REG_XCC_ID, 0, 2)" : "=s"(xc));
            unsigned hm = (0xf0f0f0u >> (xc + 4u * s1));
            const unsigned tg = epoch * 1664525u + 1013904223u;
            __builtin_amdgcn_raw_buffer_store_b128((u32x4){hm, tg, hm, tg}, rsw, lane < 8 ? W_DEC + lane * 16 : OOB_OFF, 0, SC1);
            for (int i = 0; i < 2; ++i) __builtin_amdgcn_raw_buffer_store_b128((u32x4){0u, tg, 0u, tg}, rsw, W_TAB + lane * 16, i * 1024, SC1);
        }
        {
            const unsigned k0 = (unsigned)tid - L_KEY / 16 < NPB_MAX / 4 ? 127u + L_KEY / 4 - 4u * tid : 0u, k1 = min(k0, 1u);
            *(u32x4*)(smem + tid * 16) = (u32x4){k0, k0 - k1, k0 - 2u * k1, k0 - 3u * k1};
        }
        if (tid < 64 * NRND) *(u32x4*)(smem + L_MRK + tid * 16) = (u32x4){0u, 0u, 0u, 0u};
        {
            const int hm = w >= 4 ? -1 : 0;
            const __amdgpu_buffer_rsrc_t rsa = __builtin_amdgcn_make_buffer_rsrc((void*)w_rad, 0, DE_DIM * C_DIM * 2 & hm, 0x00020000);
            const __amdgpu_buffer_rsrc_t rsg = __builtin_amdgcn_make_buffer_rsrc((void*)cg, 0, 128 & hm, 0x00020000);
#pragma unroll
            for (int j = 0; j < 8; ++j) wv[j] = __builtin_amdgcn_raw_buffer_load_b32(rsa, (g & 1) * 2048 + 64 * (w & 3) + 4 * c16 + 256 * j, 0, 0);
#pragma unroll
            for (int f = 0; f < 4; ++f) cv[f] = __builtin_amdgcn_raw_buffer_load_b32(rsg, (lane & 3) * 32 + (lane & 8u) / 2 + 8 * f, 0, 0);
            t0v = __builtin_amdgcn_raw_buffer_load_b32(__builtin_amdgcn_make_buffer_rsrc((void*)(ef + (size_t)base_e * 2), 0, min(ne * 32, NT0 * 8192), 0x00020000), (511 - tid) * 128, 0, 0);
            cm = __builtin_amdgcn_raw_buffer_load_b32(rsm, (tid >> 3) == 32 ? ((bx >> 3) * 8 + lane) * 128 : OOB_OFF, 0, 0);
        }
#pragma unroll
        for (int r = 0; r < 3; ++r) inc[r] = (int)(r < 2 ? le : le2) + r * 64 < ne ? 1u << sh : 0u;
        asm volatile("" : "+v"(inc[0]), "+v"(inc[1]), "+v"(inc[2]));
        SB();
        wg_barrier();
        if (__builtin_expect(bx == 0, 0)) {
            tag = (unsigned)__builtin_amdgcn_readfirstlane((int)epoch) * 1664525u + 1013904223u;
        } else {
        unsigned bin[3], rank[3], ba[3];
        asm volatile("" : "+v"(d[0]), "+v"(sv[0]), "+v"(d[1]), "+v"(sv[1]), "+v"(d[2]), "+v"(sv[2]));
#pragma unroll
        for (int r = 0; r < 3; ++r) {
            bin[r] = __umulhi(d[r], nb_magic);
            ba[r] = ((((sv[r] >> 3) ^ (smask >> ((sv[r] >> 9) & 31u))) & 1u) * 512u + bin[r] * 4u) & 1023u;
            rank[r] = atomicAdd((unsigned*)(smem + L_PCNT + (w >> 2) * 1024 + ba[r]), inc[r]);
        }
        tag = (unsigned)__builtin_amdgcn_readfirstlane((int)epoch) * 1664525u + 1013904223u;
        asm volatile("" : "+s"(tag));
        unsigned rw0[3], rw1[3];
#pragma unroll
        for (int r = 0; r < 3; ++r) {
            const unsigned sl = sv[r], nl = d[r] - ((__umul24(bin[r], (unsigned)N) + 127u) >> 7);
            rw0[r] = sl | (tag << 14);
            rw1[r] = (nl * 4u + (__umul24(sl, cmul) >> 16)) | ((le + r * 64) << 9) | (tag & 0xfff00000u);
            asm volatile("" : "+v"(rw0[r]), "+v"(rw1[r]));
        }
        wg_barrier();
        u32x2 c[4];
#pragma unroll
        for (int i = 0; i < 4; ++i) c[i] = *(const u32x2*)(smem + L_PCNT + i * 512 + lane * 8);
        const unsigned m0 = below_mask(w, 0), m1 = below_mask(w, 1);
        unsigned t[4], run[4];
#pragma unroll
        for (int i = 0; i < 4; ++i) t[i] = bytesum2(c[i >> 1][i & 1], c[2 + (i >> 1)][i & 1], 0x01010101u, 0x01010101u);
        const unsigned ts = (t[0] + t[1]) | (t[2] + t[3]) << 16;
        const unsigned ti = (unsigned)wave_incl_scan((int)ts), tx = ti - ts;
        run[0] = tx & 0xffffu; run[2] = (tx >> 16) + ((unsigned)__builtin_amdgcn_readlane((int)ti, 63) & 0xffffu);
        run[1] = run[0] + t[0]; run[3] = run[2] + t[2];
        if ((w & 6) == 4) {
            const unsigned ra = (w & 1) == 0 ? run[0] | (t[0] << 16) : run[1] | (t[1] << 16), rb = (w & 1) == 0 ? run[2] | (t[2] << 16) : run[3] | (t[3] << 16);
            __builtin_amdgcn_raw_buffer_store_b128((u32x4){ra, tag, rb, tag}, rsw, W_TAB + bx * 2048 + lane * 16 + (w & 1) * 1024, 0, SC1);
        }
        unsigned o[4];
#pragma unroll
        for (int i = 0; i < 4; ++i) o[i] = bytesum2(c[i >> 1][i & 1], c[2 + (i >> 1)][i & 1], m0, m1, run[i]);
        *(unsigned*)(smem + A_PTAB + w * 512 + lane * 4) = o[0] | (o[1] << 16);
        *(unsigned*)(smem + A_PTAB + w * 512 + 256 + lane * 4) = o[2] | (o[3] << 16);
        unsigned pos[3];
        asm volatile("" ::: "memory");
#pragma unroll
        for (int r = 0; r < 3; ++r) pos[r] = *(const unsigned short*)(smem + A_PTAB + w * 512 + (ba[r] >> 1));
#pragma unroll
        for (int r = 0; r < 3; ++r) pos[r] = inc[r] != 0u ? A_STG + (pos[r] + ((rank[r] >> sh) & 0xffu)) * 16 : L_DUM + lane * 16;
        asm volatile("" : "+v"(pos[0]), "+v"(pos[1]), "+v"(pos[2]));
#pragma unroll
        for (int r = 0; r < 3; ++r) *(u32x4*)(smem + pos[r]) = (u32x4){yv[r].x, rw0[r], yv[r].y, rw1[r]};
        }
        asm volatile("" :: "v"(pt));
        wg_barrier();
    }
    if (w < 4) {
        asm volatile("" :: "v"(t0v), "v"(cm), "v"(wv[0]), "v"(wv[7]), "v"(cv[3]));
        const unsigned sh = 8u * w, mb = 0x01010101u & ((1u << sh) - 1u);
        const unsigned T0 = tag << 14, T1 = tag & 0xfff00000u;
        const unsigned to = W_TAB + tid * 2048 + (b & 1) * 1024 + (b >> 1) * 16;
        u32x4 en;
        u32x2 dec;
        __builtin_amdgcn_s_sleep(8);
#pragma unroll 1
        for (unsigned spin = 0; spin < SPIN_MAX; ++spin) {
            asm volatile("" ::: "memory");
            en = __builtin_amdgcn_raw_buffer_load_b128(rsw, to, 0, SC1);
            dec = __builtin_amdgcn_raw_buffer_load_b64(rsw, W_DEC, 0, SC1);
            if (__all((en.y == tag) & (en.w == tag) & (dec.y == tag))) break;
            __builtin_amdgcn_s_sleep(2);
        }
        half = (__builtin_amdgcn_readfirstlane((int)dec.x) >> (bx & 7)) & 1;
        if (half) en.x = en.z;
        const unsigned rc = en.x >> 16;
        const unsigned incl = (unsigned)wave_incl_scan((int)rc);
        const int twf = __builtin_amdgcn_readlane((int)incl, 63), tw = twf <= 64 * NRND ? twf : 0;
        const bool more = tw > 64 * (NRND - 1);
        *(unsigned*)(smem + ((rc != 0u && incl <= 64u * NRND) ? L_MRK + w * (256 * NRND) + (incl - rc) * 4 : L_DUM + lane * 4)) = (unsigned)lane * EPB_MAX + (en.x & 0xffffu) - (incl - rc);
        unsigned ra[NRND];
        asm volatile("" ::: "memory");
#pragma unroll
        for (int r = 0; r < NRND; ++r) ra[r] = *(const unsigned*)(smem + L_MRK + w * (256 * NRND) + r * 256 + lane * 4);
        unsigned carry = 0u;
        u32x4 A[NRND];
#pragma unroll
        for (int r = 0; r < NRND; ++r) {
            if (r == NRND - 1 && !more) { ra[r] = OOB_OFF >> 1; A[r] = __builtin_amdgcn_raw_buffer_load_b128(rsw, ra[r], 0, SC1); continue; }
            const unsigned m = max(wave_max_scan(ra[r]), carry);
            carry = (unsigned)__builtin_amdgcn_readlane((int)m, 63);
            ra[r] = r * 64 + lane < tw ? (m + (unsigned)(r * 64 + lane)) * 16u : OOB_OFF >> 1;
            A[r] = __builtin_amdgcn_raw_buffer_load_b128(rsw, ra[r], W_RA + 64 * w * (EPB_MAX * 16), SC1);
        }
        SB();
        lds_arrive(lds0 + SY_RI, 16u + half);
        unsigned pe[NRND], inc[NRND];
#pragma unroll
        for (int r = 0; r < NRND; ++r) {
            pe[r] = __umul24(__umulhi(ra[r], 209716u) + 64u * w - 1u, (unsigned)epb * 32u);
            inc[r] = ra[r] < 0x40000000u ? 1u << sh : 0u;
            asm volatile("" : "+v"(pe[r]), "+v"(inc[r]));
        }
        SB();
        bool ok = true;
#pragma unroll
        for (int r = 0; r < NRND; ++r) { A[r].y ^= T0; A[r].w ^= T1; ok = ok & ((inc[r] == 0u) | ((A[r].y < 16384u) & (A[r].w < 0x100000u))); }
        if (!__all(ok)) {
#pragma unroll 1
            for (unsigned spin = 0; spin < SPIN_MAX; ++spin) {
                asm volatile("" ::: "memory");
#pragma unroll
                for (int r = 0; r < NRND; ++r) A[r] = __builtin_amdgcn_raw_buffer_load_b128(rsw, ra[r], W_RA + 64 * w * (EPB_MAX * 16), SC1);
                ok = true;
#pragma unroll
                for (int r = 0; r < NRND; ++r) { A[r].y ^= T0; A[r].w ^= T1; ok = ok & ((inc[r] == 0u) | ((A[r].y < 16384u) & (A[r].w < 0x100000u))); }
                if (__all(ok)) break;
                __builtin_amdgcn_s_sleep(1);
            }
        }
        unsigned rank[NRND], nc[NRND];
#pragma unroll
        for (int r = 0; r < NRND; ++r) {
            nc[r] = (r < 4 || inc[r] != 0u) ? A[r].w & 0x1ffu : (unsigned)lane;
            nc[r] ^= (nc[r] >= 160u) == (half != 0) ? 3u : 0u;
            if (r == NRND - 1 && !more) { rank[r] = 0u; continue; }
            rank[r] = atomicAdd((unsigned*)(smem + L_CCNT + nc[r] * 4), inc[r]);
            atomicAdd((unsigned*)(smem + L_KEY + (nc[r] & 0x1fcu)), min(inc[r], 1u) << 7);
        }
        if (twf > 64 * NRND) *(unsigned*)(smem + L_MISC) = 1u;
        lds_arrive(lds0 + SY_C1);
#pragma unroll
        for (int r = 0; r < NRND; ++r) {
            rank[r] = (rank[r] >> sh) & 0xffu;
            A[r].y = A[r].y << 10;
            A[r].w = ((A[r].w >> 4) & 0xffe0u) + pe[r];
        }
        lds_wait(lds0 + SY_C1, 4);
        {
            const u32x4 c0 = *(const u32x4*)(smem + L_CCNT + lane * 16), c1 = *(const u32x4*)(smem + L_CCNT + (64 + c16) * 16);
            unsigned p0[4], p1[4], d0 = 0u, d1 = 0u;
#pragma unroll
            for (int c = 0; c < 4; ++c) {
                p0[c] = bsum(c0[c], mb, d0); p1[c] = bsum(c1[c], mb, d1);
                if (c < 3) { d0 = bsum(c0[c], 0x01010101u, d0); d1 = bsum(c1[c], 0x01010101u, d1); }
            }
            *(u32x2*)(smem + L_CTAB + w * 640 + lane * 8) = (u32x2){p0[0] | (p0[1] << 16), p0[2] | (p0[3] << 16)};
            *(u32x2*)(smem + L_CTAB + w * 640 + (64 + c16) * 8) = (u32x2){p1[0] | (p1[1] << 16), p1[2] | (p1[3] << 16)};
        }
        asm volatile("" ::: "memory");
#pragma unroll
        for (int r = 0; r < NRND; ++r) { rank[r] += *(const unsigned short*)(smem + L_CTAB + w * 640 + nc[r] * 2); nc[r] &= 0x1fcu; }
#pragma unroll
        for (int r = 0; r < NRND; ++r) asm volatile("" : "+v"(rank[r]), "+v"(nc[r]));
        lds_wait(lds0 + SY_PRE, 4);
        load_wr();
        lds_wait(lds0 + SY_C2, 3);
        unsigned nbv[NRND];
#pragma unroll
        for (int r = 0; r < NRND; ++r) nbv[r] = *(const unsigned*)(smem + L_NB + nc[r]);
        const unsigned slf = *(const unsigned*)(smem + L_MISC);
        slow = __builtin_amdgcn_readfirstlane((int)slf) != 0;
        if (!slow) {
#pragma unroll
            for (int r = 0; r < NRND; ++r) {
                if (r == NRND - 1 && !more) continue;
                const unsigned a = nbv[r] + (rank[r] << 6);
                *(u32x4*)(smem + (inc[r] != 0u ? a : L_DUM + lane * 16)) = (u32x4){A[r].y, A[r].w, A[r].x, A[r].z};
            }
        }
        lds_arrive(lds0 + SY_C3);
    } else {
        const int q = w - 4, t2 = tid - 256;
        {
            const __amdgpu_buffer_rsrc_t rsr = __builtin_amdgcn_make_buffer_rsrc((void*)(ws + W_RA + bx * (EPB_MAX * 16)), 0, (ne * 16 + 127) & ~127, 0x00020000);
            u32x4 va[5];
#pragma unroll
            for (int i = 0; i < 5; ++i) va[i] = *(const u32x4*)(smem + A_STG + t2 * 16 + 4096 * i);
#pragma unroll
            for (int i = 0; i < 5; ++i) __builtin_amdgcn_raw_buffer_store_b128(va[i], rsr, t2 * 16, 4096 * i, SC1);
            lds_arrive(lds0 + SY_CPY);
        }
        __builtin_amdgcn_s_setprio(2);
        u32x4 mv[8];
#pragma unroll
        for (int j = 0; j < 8; ++j)
            mv[j] = __builtin_amdgcn_raw_buffer_load_b128(rsm, g * 512 + c16 * 16 + q * 2048 + (j & 1) * 256, (j >> 1) * 8192, 0);
        SB();
        {
            float f0[8], f1[8];
#pragma unroll
            for (int j = 0; j < 8; ++j) {
                const float a = __uint_as_float(wv[j] << 16) * -1.44269504088896340736f, c = __uint_as_float(wv[j] & 0xffff0000u) * -1.44269504088896340736f;
                const unsigned hp = g < 2 ? 0u : pack2bf(a, c);
                f0[j] = a - __uint_as_float(hp << 16);
                f1[j] = c - __uint_as_float(hp & 0xffff0000u);
            }
            *(u32x4*)(smem + L_WR + (2 * q) * 1024 + lane * 16) = (u32x4){pack2bf(f0[0], f0[1]), pack2bf(f0[2], f0[3]), pack2bf(f0[4], f0[5]), pack2bf(f0[6], f0[7])};
            *(u32x4*)(smem + L_WR + (2 * q + 1) * 1024 + lane * 16) = (u32x4){pack2bf(f1[0], f1[1]), pack2bf(f1[2], f1[3]), pack2bf(f1[4], f1[5]), pack2bf(f1[6], f1[7])};
            float a[4];
#pragma unroll
            for (int f = 0; f < 4; ++f) a[f] = __uint_as_float((lane & 4) ? cv[f] & 0xffff0000u : cv[f] << 16) * -0.69314718055994530942f;
            const unsigned h0 = pack2bf(a[0], a[1]), h1 = pack2bf(a[2], a[3]);
            *(u32x4*)(smem + L_CG + lane * 16) = (u32x4){h0, h1, pack2bf(a[0] - __uint_as_float(h0 << 16), a[1] - __uint_as_float(h0 & 0xffff0000u)),
                                                        pack2bf(a[2] - __uint_as_float(h1 << 16), a[3] - __uint_as_float(h1 & 0xffff0000u))};
        }
        lds_wait(lds0 + SY_CPY, 4);
#pragma unroll
        for (int i = 0; i < SMAX / 8; ++i) *(u32x4*)(smem + L_PRV + (t2 + 256 * i) * 16) = (u32x4){OOB_OFF, OOB_OFF, 0u, 0u};
        SB();
        asm volatile("" :: "v"(cp), "v"(cm), "v"(t0v));
#pragma unroll
        for (int i = 0; i < 4; ++i) {
            u32x4 p0, p1;
#pragma unroll
            for (int j = 0; j < 4; ++j) {
                p0[j] = __builtin_amdgcn_perm(mv[2 * j + 1][i], mv[2 * j][i], 0x05040100u);
                p1[j] = __builtin_amdgcn_perm(mv[2 * j + 1][i], mv[2 * j][i], 0x07060302u);
            }
            const unsigned ia = L_IMG + (c16 >> 2) * 8192 + q * 1024 + g * 256 + (c16 & 3) * 64 + i * 16;
            *(u32x4*)(smem + ia) = p0;
            *(u32x4*)(smem + ia + 4096) = p1;
        }
        lds_arrive(lds0 + SY_PRE);
        if (w == 7) {
            half = (lds_wait<1>(lds0 + SY_RI, 64) & 15u) != 0u;
#pragma unroll
            for (int i = 0; i < NT2; ++i) tv[i] = touch(tl[i]);
            SB();
            lds_wait<1>(lds0 + SY_C2, 3);
            load_wr();
#pragma unroll
            for (int i = 0; i < NT2; ++i) asm volatile("" :: "v"(tv[i]));
        } else {
            lds_wait(lds0 + SY_PRE, 4);
            load_wr();
            half = (lds_wait<1>(lds0 + SY_RI, 16) & 15u) != 0u;
            const int n5 = min(27 * q + (lane >> 1), NPB_MAX - 1);
            unsigned tb, ta[3];
            {
                const bool kp = (n5 >= 40) == (half != 0);
                const int qq = (n5 >= 40 ? n5 - 40 : n5) >> 2;
                const unsigned wq = (unsigned)((kp ? wave_of(true) : wave_of(false)) >> (4 * qq)) & 15u;
                tb = L_PRV + wq * (SMAX * 64) + (n5 & 3) * 16 + ((((kp ? outer_of(true) : outer_of(false)) >> qq) & 1u) ? 2u : (kp ? 0u : 1u));
#pragma unroll
                for (int t = 0; t < 3; ++t) ta[t] = task_at(wq, t);
                asm volatile("" : "+v"(tb), "+v"(ta[0]), "+v"(ta[1]), "+v"(ta[2]));
            }
            lds_wait<1>(lds0 + SY_C1, 3);
            __builtin_amdgcn_s_setprio(3);
            lds_wait(lds0 + SY_C1, 4);
            const unsigned kn = *(const unsigned*)(smem + L_KEY + n5 * 4);
            u32x4 dk[5];
#pragma unroll
            for (int k = 0; k < 5; ++k) dk[k] = *(const u32x4*)(smem + L_KEY + (n5 >= 40 ? 160 : 0) + (lane & 1) * 80 + k * 16);
            int c = 0;
#pragma unroll
            for (int k = 0; k < 5; ++k) c += (int)((kn - dk[k].x) >> 31) + (int)((kn - dk[k].y) >> 31) + (int)((kn - dk[k].z) >> 31) + (int)((kn - dk[k].w) >> 31);
            c += __builtin_amdgcn_update_dpp(0, c, 0xB1, 0xf, 0xf, false);
            *(unsigned*)(smem + L_AT + ((n5 >= 40 ? 40 : 0) + c) * 4) = (unsigned)n5 | (max(kn >> 7, 8u) << 8);
            lds_arrive(lds0 + SY_S); lds_wait(lds0 + SY_S, 3);
            const unsigned at = *(const unsigned*)(smem + L_AT + n5 * 4);
            const unsigned lf = *(const unsigned*)(smem + ta[0]) >> 8, lb = *(const unsigned*)(smem + ta[1]) >> 8, la = *(const unsigned*)(smem + ta[2]) >> 8;
            *(unsigned*)(smem + L_NB + (at & 0xffu) * 4) = (tb & ~3u) + (sel3(tb & 3u, la + lb, 0u, lb) << 6);
            if (lf + lb + la > SMAX) *(unsigned*)(smem + L_MISC) = 1u;
            lds_arrive(lds0 + SY_C2);
        }
        if (w != 7) lds_wait(lds0 + SY_C2, 3);
        slow = __builtin_amdgcn_readfirstlane(*(const int*)(smem + L_MISC)) != 0;
        __builtin_amdgcn_s_setprio(3);
    }
    const unsigned agf = *(const unsigned*)(smem + task_at(w, 0) + g * 4), ag1 = *(const unsigned*)(smem + task_at(w, 1) + g * 4),
                   ag2 = *(const unsigned*)(smem + task_at(w, 2) + (w < 4 ? 0 : g * 4));
    const unsigned lane_off = (unsigned)c16 * 16u;
    const unsigned aX = L_PRV + w * (SMAX * 64) + (c16 & 3) * 64 + g * 16;
    const unsigned aF = L_PRV + w * (SMAX * 64) + (c16 & 3) * 64 + (c16 >> 2) * 16 + 4;
    const unsigned aWw = L_BNC + w * 512 + lane * 8;
    const unsigned aWr = L_BNC + w * 512 + g * 128 + (c16 & 3) * 32 + (c16 >> 2) * 8;
    auto row_of = [=](unsigned at) -> unsigned {
        const unsigned nl = at & 0xffu;
        return nl < (unsigned)npb ? ((unsigned)node0 + nl) * 1024u + lane_off : OOB_OFF;
    };
    if (!slow) {
        u32x32 oxa, oxb;
        u32x4 efA, efN, S;
        const u32x4 dx = mkdesc(x, (unsigned)N * 1024u), de = mkdesc(ef, (unsigned)E * 32u);
        unsigned tmp;
        __builtin_amdgcn_s_waitcnt(0x0F70);
        asm volatile(
            "Lw_%=:\n"
            "ds_read_b32 v220, v219\n"
            "s_waitcnt lgkmcnt(0)\n"
            "v_readfirstlane_b32 %[tmp], v220\n"
            "s_cmp_lt_u32 %[tmp], 4\n"
            "s_cbranch_scc1 Lw_%=\n"
            "ds_read2st64_b32 v[232:233], v211 offset1:1\n"
            "ds_read_b128 v[196:199], v210\n"
            "s_waitcnt lgkmcnt(1)\n"
            "v_add_u32 v232, v232, v229\n"
            "v_add_u32 v233, v233, v229\n"
            "buffer_load_dwordx4 v[192:195], v232, %[rse], 0 offen\n"
            "s_waitcnt lgkmcnt(0)\n"
            "v_add_u32_dpp v220, v196, v214 quad_perm:[0,0,0,0] row_mask:0xf bank_mask:0xf\n"
            "v_add_u32_dpp v221, v196, v214 quad_perm:[1,1,1,1] row_mask:0xf bank_mask:0xf\n"
            "v_add_u32_dpp v222, v196, v214 quad_perm:[2,2,2,2] row_mask:0xf bank_mask:0xf\n"
            "v_add_u32_dpp v223, v196, v214 quad_perm:[3,3,3,3] row_mask:0xf bank_mask:0xf\n"
            "buffer_load_dwordx4 v[32:35], v220, %[rsx], 0 offen\n"
            "buffer_load_dwordx4 v[36:39], v220, %[rsx], 0 offen offset:256\n"
            "buffer_load_dwordx4 v[48:51], v221, %[rsx], 0 offen\n"
            "buffer_load_dwordx4 v[52:55], v221, %[rsx], 0 offen offset:256\n"
            "buffer_load_dwordx4 v[232:235], v233, %[rse], 0 offen\n"
            "buffer_load_dwordx4 v[64:67], v222, %[rsx], 0 offen\n"
            "buffer_load_dwordx4 v[68:71], v222, %[rsx], 0 offen offset:256\n"
            "buffer_load_dwordx4 v[80:83], v223, %[rsx], 0 offen\n"
            "buffer_load_dwordx4 v[84:87], v223, %[rsx], 0 offen offset:256\n"
            "buffer_load_dwordx4 v[40:43], v220, %[rsx], 0 offen offset:512\n"
            "buffer_load_dwordx4 v[44:47], v220, %[rsx], 0 offen offset:768\n"
            "buffer_load_dwordx4 v[56:59], v221, %[rsx], 0 offen offset:512\n"
            "buffer_load_dwordx4 v[60:63], v221, %[rsx], 0 offen offset:768\n"
            "buffer_load_dwordx4 v[72:75], v222, %[rsx], 0 offen offset:512\n"
            "buffer_load_dwordx4 v[76:79], v222, %[rsx], 0 offen offset:768\n"
            "buffer_load_dwordx4 v[88:91], v223, %[rsx], 0 offen offset:512\n"
            "buffer_load_dwordx4 v[92:95], v223, %[rsx], 0 offen offset:768\n"
            : "={v[32:63]}"(oxa), "={v[64:95]}"(oxb), "={v[192:195]}"(efA), "={v[196:199]}"(S), "={v[232:235]}"(efN), [tmp] "=&s"(tmp)
            : [rse] "s"(de), [rsx] "s"(dx), [ax] "{v210}"(lds0 + aX), [af] "{v211}"(lds0 + aF), [lo] "{v214}"(lane_off), [eo] "{v229}"((g & 1) * 16), [sy] "{v219}"(lds0 + SY_C3)
            : "memory", "scc", "v220", "v221", "v222", "v223");
        if (bx == 0 && w == 4) flag_store(ws + W_CTL, tag);
        const int lf = __builtin_amdgcn_readfirstlane((int)agf) >> 8, l1 = __builtin_amdgcn_readfirstlane((int)ag1) >> 8, l2 = __builtin_amdgcn_readfirstlane((int)ag2) >> 8;
        const int e2 = lf + l1 + l2;
        int nst = (e2 + 3) >> 2;
        const unsigned r2 = row_of(ag2);
        u32x32 owr;
        u32x16 omi;
        u32x8 ot;
#pragma unroll
        for (int i = 0; i < 32; ++i) owr[i] = wr[i >> 2][i & 3];
        const unsigned trb = lds0 + L_PK + w * PK_STR + 4096;
        omi[2] = lds0 + aX + 256; omi[3] = lds0 + aF + 256;
        omi[4] = lds0 + aWw; omi[5] = lds0 + aWr; omi[6] = lane_off; omi[7] = W_AGG + row_of(ag1); omi[8] = W_AGG + r2;
        omi[9] = row_of(agf); omi[10] = w >= 6 ? r2 : OOB_OFF; omi[15] = ~tag;
        ot[0] = (lane & 3) == 0 ? 0x3f80u : ((lane & 3) == 1 ? 0x3f800000u : 0u); ot[1] = (lane & 3) == 2 ? 0x3f80u : ((lane & 3) == 3 ? 0x3f800000u : 0u);
        ot[2] = trb + g * (4 * 272) + c16 * 16; ot[3] = trb + c16 * 272 + g * 16; ot[4] = lds0 + L_IMG + lane * 16; ot[5] = (g & 1) * 16; ot[7] = trb - 4096 + lane * 16;
        int rem = l1, lens = w < 4 ? 0xff : (l2 - 4) | 0xff00, fm = w < 4 ? 0x8 : (w < 6 ? 0x80 : 0x28);
        u64 ra;
        asm volatile(
            "s_nop 1\n"
            "v_mfma_f32_4x4x4_16b_bf16 v[204:207], v[202:203], v[198:199], 0\n"
            "v_mov_b64 v[0:1], 0\n"
            "v_mov_b64 v[2:3], 0\n"
            "v_mov_b64 v[4:5], 0\n"
            "v_mov_b64 v[6:7], 0\n"
            "v_mfma_f32_4x4x4_16b_bf16 v[204:207], v[200:201], v[198:199], v[204:207]\n"
            "v_mov_b64 v[8:9], 0\n"
            "v_mov_b64 v[10:11], 0\n"
            "v_mov_b64 v[12:13], 0\n"
            "v_mov_b64 v[14:15], 0\n"
            "v_mov_b64 v[16:17], 0\n"
            "v_mov_b64 v[18:19], 0\n"
            "v_mov_b64 v[20:21], 0\n"
            "v_mov_b64 v[22:23], 0\n"
            "v_mov_b64 v[24:25], 0\n"
            "v_mov_b64 v[26:27], 0\n"
            "v_mov_b64 v[28:29], 0\n"
            "v_mov_b64 v[30:31], 0\n"
            "v_cvt_pk_bf16_f32 v204, v204, v205\n"
            "v_cvt_pk_bf16_f32 v205, v206, v207\n"
            "ds_write_b64 v212, v[204:205]\n"
            "ds_read_b128 v[196:199], v210\n"
            "ds_read_b64 v[208:209], v213\n"
            "s_waitcnt vmcnt(17)\n"
            "v_mfma_f32_16x16x32_bf16 v[176:179], v[192:195], v[96:99], 0\n"
            "v_mfma_f32_16x16x32_bf16 v[180:183], v[192:195], v[100:103], 0\n"
            "v_mfma_f32_16x16x32_bf16 v[184:187], v[192:195], v[104:107], 0\n"
            "v_mfma_f32_16x16x32_bf16 v[188:191], v[192:195], v[108:111], 0\n"
            "v_mfma_f32_16x16x32_bf16 v[128:131], v[192:195], v[112:115], 0\n"
            "v_mfma_f32_16x16x32_bf16 v[132:135], v[192:195], v[116:119], 0\n"
            "s_nop 2\n"
            "v_exp_f32 v144, v176\n"
            "v_exp_f32 v145, v177\n"
            "v_exp_f32 v146, v178\n"
            "v_exp_f32 v147, v179\n"
            "v_exp_f32 v148, v180\n"
            "v_exp_f32 v149, v181\n"
            "v_exp_f32 v150, v182\n"
            "v_exp_f32 v151, v183\n"
            "v_exp_f32 v152, v184\n"
            "v_exp_f32 v153, v185\n"
            "v_exp_f32 v154, v186\n"
            "v_exp_f32 v155, v187\n"
            "v_exp_f32 v156, v188\n"
            "v_exp_f32 v157, v189\n"
            "v_exp_f32 v158, v190\n"
            "v_exp_f32 v159, v191\n"
            "v_pk_add_f32 v[144:145], v[144:145], 1.0 op_sel_hi:[1,0]\n"
            "v_pk_add_f32 v[146:147], v[146:147], 1.0 op_sel_hi:[1,0]\n"
            "v_pk_add_f32 v[148:149], v[148:149], 1.0 op_sel_hi:[1,0]\n"
            "v_pk_add_f32 v[150:151], v[150:151], 1.0 op_sel_hi:[1,0]\n"
            "v_pk_add_f32 v[152:153], v[152:153], 1.0 op_sel_hi:[1,0]\n"
            "v_pk_add_f32 v[154:155], v[154:155], 1.0 op_sel_hi:[1,0]\n"
            "v_pk_add_f32 v[156:157], v[156:157], 1.0 op_sel_hi:[1,0]\n"
            "v_pk_add_f32 v[158:159], v[158:159], 1.0 op_sel_hi:[1,0]\n"
            "v_rcp_f32 v144, v144\n"
            "v_rcp_f32 v145, v145\n"
            "v_rcp_f32 v146, v146\n"
            "v_rcp_f32 v147, v147\n"
            "v_rcp_f32 v148, v148\n"
            "v_rcp_f32 v149, v149\n"
            "v_rcp_f32 v150, v150\n"
            "v_rcp_f32 v151, v151\n"
            "v_rcp_f32 v152, v152\n"
            "v_rcp_f32 v153, v153\n"
            "v_rcp_f32 v154, v154\n"
            "v_rcp_f32 v155, v155\n"
            "v_rcp_f32 v156, v156\n"
            "v_rcp_f32 v157, v157\n"
            "v_rcp_f32 v158, v158\n"
            "v_rcp_f32 v159, v159\n"
            "v_pk_mul_f32 v[144:145], v[144:145], v[176:177]\n"
            "v_pk_mul_f32 v[146:147], v[146:147], v[178:179]\n"
            "v_pk_mul_f32 v[148:149], v[148:149], v[180:181]\n"
            "v_pk_mul_f32 v[150:151], v[150:151], v[182:183]\n"
            "v_pk_mul_f32 v[152:153], v[152:153], v[184:185]\n"
            "v_pk_mul_f32 v[154:155], v[154:155], v[186:187]\n"
            "v_pk_mul_f32 v[156:157], v[156:157], v[188:189]\n"
            "v_pk_mul_f32 v[158:159], v[158:159], v[190:191]\n"
            "s_branch Ldisp_%=\n"
            "Lfast_%=:\n"
            "v_mfma_f32_16x16x32_bf16 v[136:139], v[192:195], v[120:123], 0\n"
            "v_exp_f32 v160, v128\n"
            "v_add_u32 v211, 0x100, v211\n"
            "v_exp_f32 v161, v129\n"
            "s_waitcnt vmcnt(13) lgkmcnt(0)\n"
            "v_mfma_f32_4x4x4_16b_bf16 v[176:179], v[208:209], v[32:33], 0 cbsz:2\n"
            "v_exp_f32 v162, v130\n"
            "v_mfma_f32_4x4x4_16b_bf16 v[180:183], v[208:209], v[34:35], 0 cbsz:2\n"
            "v_exp_f32 v163, v131\n"
            "v_mfma_f32_4x4x4_16b_bf16 v[184:187], v[208:209], v[36:37], 0 cbsz:2\n"
            "v_exp_f32 v164, v132\n"
            "v_mfma_f32_4x4x4_16b_bf16 v[188:191], v[208:209], v[38:39], 0 cbsz:2\n"
            "v_exp_f32 v165, v133\n"
            "v_add_u32_dpp v219, v196, v214 quad_perm:[0,0,0,0] row_mask:0xf bank_mask:0xf\n"
            "v_mfma_f32_4x4x4_16b_bf16 v[204:207], v[202:203], v[198:199], 0\n"
            "v_exp_f32 v166, v134\n"
            "buffer_load_dwordx4 v[32:35], v219, %[rsx], 0 offen\n"
            "buffer_load_dwordx4 v[36:39], v219, %[rsx], 0 offen offset:256\n"
            "v_exp_f32 v167, v135\n"
            "v_pk_fma_f32 v[0:1], v[144:145], v[176:177], v[0:1] op_sel_hi:[0,1,1]\n"
            "v_pk_fma_f32 v[2:3], v[144:145], v[178:179], v[2:3] op_sel_hi:[0,1,1]\n"
            "v_pk_fma_f32 v[4:5], v[148:149], v[180:181], v[4:5] op_sel_hi:[0,1,1]\n"
            "v_pk_fma_f32 v[6:7], v[148:149], v[182:183], v[6:7] op_sel_hi:[0,1,1]\n"
            "v_pk_fma_f32 v[8:9], v[152:153], v[184:185], v[8:9] op_sel_hi:[0,1,1]\n"
            "v_pk_fma_f32 v[10:11], v[152:153], v[186:187], v[10:11] op_sel_hi:[0,1,1]\n"
            "v_pk_fma_f32 v[12:13], v[156:157], v[188:189], v[12:13] op_sel_hi:[0,1,1]\n"
            "v_pk_fma_f32 v[14:15], v[156:157], v[190:191], v[14:15] op_sel_hi:[0,1,1]\n"
            "v_pk_add_f32 v[160:161], v[160:161], 1.0 op_sel_hi:[1,0]\n"
            "v_pk_add_f32 v[162:163], v[162:163], 1.0 op_sel_hi:[1,0]\n"
            "v_pk_add_f32 v[164:165], v[164:165], 1.0 op_sel_hi:[1,0]\n"
            "v_pk_add_f32 v[166:167], v[166:167], 1.0 op_sel_hi:[1,0]\n"
            "v_mfma_f32_16x16x32_bf16 v[140:143], v[192:195], v[124:127], 0\n"
            "ds_read_b32 v230, v211\n"
            "v_rcp_f32 v160, v160\n"
            "v_rcp_f32 v161, v161\n"
            "v_mfma_f32_4x4x4_16b_bf16 v[176:179], v[208:209], v[48:49], 0 cbsz:2 abid:1\n"
            "v_rcp_f32 v162, v162\n"
            "v_mfma_f32_4x4x4_16b_bf16 v[180:183], v[208:209], v[50:51], 0 cbsz:2 abid:1\n"
            "v_rcp_f32 v163, v163\n"
            "v_mfma_f32_4x4x4_16b_bf16 v[184:187], v[208:209], v[52:53], 0 cbsz:2 abid:1\n"
            "v_rcp_f32 v164, v164\n"
            "v_mfma_f32_4x4x4_16b_bf16 v[188:191], v[208:209], v[54:55], 0 cbsz:2 abid:1\n"
            "v_rcp_f32 v165, v165\n"
            "v_add_u32_dpp v220, v196, v214 quad_perm:[1,1,1,1] row_mask:0xf bank_mask:0xf\n"
            "v_mfma_f32_4x4x4_16b_bf16 v[204:207], v[200:201], v[198:199], v[204:207]\n"
            "v_rcp_f32 v166, v166\n"
            "buffer_load_dwordx4 v[48:51], v220, %[rsx], 0 offen\n"
            "buffer_load_dwordx4 v[52:55], v220, %[rsx], 0 offen offset:256\n"
            "v_rcp_f32 v167, v167\n"
            "v_pk_fma_f32 v[0:1], v[144:145], v[176:177], v[0:1] op_sel:[1,0,0]\n"
            "v_pk_fma_f32 v[2:3], v[144:145], v[178:179], v[2:3] op_sel:[1,0,0]\n"
            "v_pk_fma_f32 v[4:5], v[148:149], v[180:181], v[4:5] op_sel:[1,0,0]\n"
            "v_pk_fma_f32 v[6:7], v[148:149], v[182:183], v[6:7] op_sel:[1,0,0]\n"
            "v_pk_fma_f32 v[8:9], v[152:153], v[184:185], v[8:9] op_sel:[1,0,0]\n"
            "v_pk_fma_f32 v[10:11], v[152:153], v[186:187], v[10:11] op_sel:[1,0,0]\n"
            "v_pk_fma_f32 v[12:13], v[156:157], v[188:189], v[12:13] op_sel:[1,0,0]\n"
            "v_pk_fma_f32 v[14:15], v[156:157], v[190:191], v[14:15] op_sel:[1,0,0]\n"
            "v_pk_mul_f32 v[160:161], v[160:161], v[128:129]\n"
            "v_pk_mul_f32 v[162:163], v[162:163], v[130:131]\n"
            "v_pk_mul_f32 v[164:165], v[164:165], v[132:133]\n"
            "v_pk_mul_f32 v[166:167], v[166:167], v[134:135]\n"
            "s_waitcnt vmcnt(12) lgkmcnt(0)\n"
            "v_mov_b64 v[192:193], v[232:233]\n"
            "v_mov_b64 v[194:195], v[234:235]\n"
            "v_add_u32 v230, v230, v229\n"
            "buffer_load_dwordx4 v[232:235], v230, %[rse], 0 offen\n"
            "v_mfma_f32_16x16x32_bf16 v[128:131], v[192:195], v[96:99], 0\n"
            "v_exp_f32 v168, v136\n"
            "v_exp_f32 v169, v137\n"
            "v_mfma_f32_4x4x4_16b_bf16 v[176:179], v[208:209], v[64:65], 0 cbsz:2 abid:2\n"
            "v_exp_f32 v170, v138\n"
            "v_mfma_f32_4x4x4_16b_bf16 v[180:183], v[208:209], v[66:67], 0 cbsz:2 abid:2\n"
            "v_exp_f32 v171, v139\n"
            "v_mfma_f32_4x4x4_16b_bf16 v[184:187], v[208:209], v[68:69], 0 cbsz:2 abid:2\n"
            "v_exp_f32 v172, v140\n"
            "v_mfma_f32_4x4x4_16b_bf16 v[188:191], v[208:209], v[70:71], 0 cbsz:2 abid:2\n"
            "v_exp_f32 v173, v141\n"
            "v_add_u32_dpp v221, v196, v214 quad_perm:[2,2,2,2] row_mask:0xf bank_mask:0xf\n"
            "v_exp_f32 v174, v142\n"
            "buffer_load_dwordx4 v[64:67], v221, %[rsx], 0 offen\n"
            "buffer_load_dwordx4 v[68:71], v221, %[rsx], 0 offen offset:256\n"
            "v_exp_f32 v175, v143\n"
            "v_pk_fma_f32 v[0:1], v[146:147], v[176:177], v[0:1] op_sel_hi:[0,1,1]\n"
            "v_pk_fma_f32 v[2:3], v[146:147], v[178:179], v[2:3] op_sel_hi:[0,1,1]\n"
            "v_pk_fma_f32 v[4:5], v[150:151], v[180:181], v[4:5] op_sel_hi:[0,1,1]\n"
            "v_pk_fma_f32 v[6:7], v[150:151], v[182:183], v[6:7] op_sel_hi:[0,1,1]\n"
            "v_pk_fma_f32 v[8:9], v[154:155], v[184:185], v[8:9] op_sel_hi:[0,1,1]\n"
            "v_pk_fma_f32 v[10:11], v[154:155], v[186:187], v[10:11] op_sel_hi:[0,1,1]\n"
            "v_pk_fma_f32 v[12:13], v[158:159], v[188:189], v[12:13] op_sel_hi:[0,1,1]\n"
            "v_pk_fma_f32 v[14:15], v[158:159], v[190:191], v[14:15] op_sel_hi:[0,1,1]\n"
            "v_pk_add_f32 v[168:169], v[168:169], 1.0 op_sel_hi:[1,0]\n"
            "v_pk_add_f32 v[170:171], v[170:171], 1.0 op_sel_hi:[1,0]\n"
            "v_pk_add_f32 v[172:173], v[172:173], 1.0 op_sel_hi:[1,0]\n"
            "v_pk_add_f32 v[174:175], v[174:175], 1.0 op_sel_hi:[1,0]\n"
            "v_cvt_pk_bf16_f32 v204, v204, v205\n"
            "v_cvt_pk_bf16_f32 v205, v206, v207\n"
            "ds_write_b64 v212, v[204:205]\n"
            "v_mfma_f32_16x16x32_bf16 v[132:135], v[192:195], v[100:103], 0\n"
            "v_rcp_f32 v168, v168\n"
            "v_rcp_f32 v169, v169\n"
            "v_mfma_f32_4x4x4_16b_bf16 v[176:179], v[208:209], v[80:81], 0 cbsz:2 abid:3\n"
            "v_rcp_f32 v170, v170\n"
            "v_mfma_f32_4x4x4_16b_bf16 v[180:183], v[208:209], v[82:83], 0 cbsz:2 abid:3\n"
            "v_rcp_f32 v171, v171\n"
            "v_mfma_f32_4x4x4_16b_bf16 v[184:187], v[208:209], v[84:85], 0 cbsz:2 abid:3\n"
            "v_rcp_f32 v172, v172\n"
            "v_mfma_f32_4x4x4_16b_bf16 v[188:191], v[208:209], v[86:87], 0 cbsz:2 abid:3\n"
            "v_rcp_f32 v173, v173\n"
            "v_add_u32_dpp v222, v196, v214 quad_perm:[3,3,3,3] row_mask:0xf bank_mask:0xf\n"
            "v_rcp_f32 v174, v174\n"
            "buffer_load_dwordx4 v[80:83], v222, %[rsx], 0 offen\n"
            "buffer_load_dwordx4 v[84:87], v222, %[rsx], 0 offen offset:256\n"
            "v_rcp_f32 v175, v175\n"
            "v_pk_fma_f32 v[0:1], v[146:147], v[176:177], v[0:1] op_sel:[1,0,0]\n"
            "v_pk_fma_f32 v[2:3], v[146:147], v[178:179], v[2:3] op_sel:[1,0,0]\n"
            "v_pk_fma_f32 v[4:5], v[150:151], v[180:181], v[4:5] op_sel:[1,0,0]\n"
            "v_pk_fma_f32 v[6:7], v[150:151], v[182:183], v[6:7] op_sel:[1,0,0]\n"
            "v_pk_fma_f32 v[8:9], v[154:155], v[184:185], v[8:9] op_sel:[1,0,0]\n"
            "v_pk_fma_f32 v[10:11], v[154:155], v[186:187], v[10:11] op_sel:[1,0,0]\n"
            "v_pk_fma_f32 v[12:13], v[158:159], v[188:189], v[12:13] op_sel:[1,0,0]\n"
            "v_pk_fma_f32 v[14:15], v[158:159], v[190:191], v[14:15] op_sel:[1,0,0]\n"
            "v_pk_mul_f32 v[168:169], v[168:169], v[136:137]\n"
            "v_pk_mul_f32 v[170:171], v[170:171], v[138:139]\n"
            "v_pk_mul_f32 v[172:173], v[172:173], v[140:141]\n"
            "v_pk_mul_f32 v[174:175], v[174:175], v[142:143]\n"
            "v_add_u32 v210, 0x100, v210\n"
            "ds_read_b128 v[196:199], v210\n"
            "v_mfma_f32_16x16x32_bf16 v[136:139], v[192:195], v[104:107], 0\n"
            "v_exp_f32 v144, v128\n"
            "v_exp_f32 v145, v129\n"
            "s_waitcnt vmcnt(13)\n"
            "v_mfma_f32_4x4x4_16b_bf16 v[176:179], v[208:209], v[40:41], 0 cbsz:2\n"
            "v_exp_f32 v146, v130\n"
            "v_mfma_f32_4x4x4_16b_bf16 v[180:183], v[208:209], v[42:43], 0 cbsz:2\n"
            "v_exp_f32 v147, v131\n"
            "v_mfma_f32_4x4x4_16b_bf16 v[184:187], v[208:209], v[44:45], 0 cbsz:2\n"
            "v_exp_f32 v148, v132\n"
            "v_mfma_f32_4x4x4_16b_bf16 v[188:191], v[208:209], v[46:47], 0 cbsz:2\n"
            "v_exp_f32 v149, v133\n"
            "v_exp_f32 v150, v134\n"
            "buffer_load_dwordx4 v[40:43], v219, %[rsx], 0 offen offset:512\n"
            "buffer_load_dwordx4 v[44:47], v219, %[rsx], 0 offen offset:768\n"
            "v_exp_f32 v151, v135\n"
            "v_pk_fma_f32 v[16:17], v[160:161], v[176:177], v[16:17] op_sel_hi:[0,1,1]\n"
            "v_pk_fma_f32 v[18:19], v[160:161], v[178:179], v[18:19] op_sel_hi:[0,1,1]\n"
            "v_pk_fma_f32 v[20:21], v[164:165], v[180:181], v[20:21] op_sel_hi:[0,1,1]\n"
            "v_pk_fma_f32 v[22:23], v[164:165], v[182:183], v[22:23] op_sel_hi:[0,1,1]\n"
            "v_pk_fma_f32 v[24:25], v[168:169], v[184:185], v[24:25] op_sel_hi:[0,1,1]\n"
            "v_pk_fma_f32 v[26:27], v[168:169], v[186:187], v[26:27] op_sel_hi:[0,1,1]\n"
            "v_pk_fma_f32 v[28:29], v[172:173], v[188:189], v[28:29] op_sel_hi:[0,1,1]\n"
            "v_pk_fma_f32 v[30:31], v[172:173], v[190:191], v[30:31] op_sel_hi:[0,1,1]\n"
            "v_pk_add_f32 v[144:145], v[144:145], 1.0 op_sel_hi:[1,0]\n"
            "v_pk_add_f32 v[146:147], v[146:147], 1.0 op_sel_hi:[1,0]\n"
            "v_pk_add_f32 v[148:149], v[148:149], 1.0 op_sel_hi:[1,0]\n"
            "v_pk_add_f32 v[150:151], v[150:151], 1.0 op_sel_hi:[1,0]\n"
            "v_mfma_f32_16x16x32_bf16 v[140:143], v[192:195], v[108:111], 0\n"
            "v_rcp_f32 v144, v144\n"
            "v_rcp_f32 v145, v145\n"
            "v_mfma_f32_4x4x4_16b_bf16 v[176:179], v[208:209], v[56:57], 0 cbsz:2 abid:1\n"
            "v_rcp_f32 v146, v146\n"
            "v_mfma_f32_4x4x4_16b_bf16 v[180:183], v[208:209], v[58:59], 0 cbsz:2 abid:1\n"
            "v_rcp_f32 v147, v147\n"
            "v_mfma_f32_4x4x4_16b_bf16 v[184:187], v[208:209], v[60:61], 0 cbsz:2 abid:1\n"
            "v_rcp_f32 v148, v148\n"
            "v_mfma_f32_4x4x4_16b_bf16 v[188:191], v[208:209], v[62:63], 0 cbsz:2 abid:1\n"
            "v_rcp_f32 v149, v149\n"
            "v_rcp_f32 v150, v150\n"
            "buffer_load_dwordx4 v[56:59], v220, %[rsx], 0 offen offset:512\n"
            "buffer_load_dwordx4 v[60:63], v220, %[rsx], 0 offen offset:768\n"
            "v_rcp_f32 v151, v151\n"
            "v_pk_fma_f32 v[16:17], v[160:161], v[176:177], v[16:17] op_sel:[1,0,0]\n"
            "v_pk_fma_f32 v[18:19], v[160:161], v[178:179], v[18:19] op_sel:[1,0,0]\n"
            "v_pk_fma_f32 v[20:21], v[164:165], v[180:181], v[20:21] op_sel:[1,0,0]\n"
            "v_pk_fma_f32 v[22:23], v[164:165], v[182:183], v[22:23] op_sel:[1,0,0]\n"
            "v_pk_fma_f32 v[24:25], v[168:169], v[184:185], v[24:25] op_sel:[1,0,0]\n"
            "v_pk_fma_f32 v[26:27], v[168:169], v[186:187], v[26:27] op_sel:[1,0,0]\n"
            "v_pk_fma_f32 v[28:29], v[172:173], v[188:189], v[28:29] op_sel:[1,0,0]\n"
            "v_pk_fma_f32 v[30:31], v[172:173], v[190:191], v[30:31] op_sel:[1,0,0]\n"
            "v_pk_mul_f32 v[144:145], v[144:145], v[128:129]\n"
            "v_pk_mul_f32 v[146:147], v[146:147], v[130:131]\n"
            "v_pk_mul_f32 v[148:149], v[148:149], v[132:133]\n"
            "v_pk_mul_f32 v[150:151], v[150:151], v[134:135]\n"
            "v_mfma_f32_16x16x32_bf16 v[128:131], v[192:195], v[112:115], 0\n"
            "v_exp_f32 v152, v136\n"
            "v_exp_f32 v153, v137\n"
            "s_waitcnt vmcnt(13)\n"
            "v_mfma_f32_4x4x4_16b_bf16 v[176:179], v[208:209], v[72:73], 0 cbsz:2 abid:2\n"
            "v_exp_f32 v154, v138\n"
            "v_mfma_f32_4x4x4_16b_bf16 v[180:183], v[208:209], v[74:75], 0 cbsz:2 abid:2\n"
            "v_exp_f32 v155, v139\n"
            "v_mfma_f32_4x4x4_16b_bf16 v[184:187], v[208:209], v[76:77], 0 cbsz:2 abid:2\n"
            "v_exp_f32 v156, v140\n"
            "v_mfma_f32_4x4x4_16b_bf16 v[188:191], v[208:209], v[78:79], 0 cbsz:2 abid:2\n"
            "v_exp_f32 v157, v141\n"
            "v_exp_f32 v158, v142\n"
            "buffer_load_dwordx4 v[72:75], v221, %[rsx], 0 offen offset:512\n"
            "buffer_load_dwordx4 v[76:79], v221, %[rsx], 0 offen offset:768\n"
            "v_exp_f32 v159, v143\n"
            "v_pk_fma_f32 v[16:17], v[162:163], v[176:177], v[16:17] op_sel_hi:[0,1,1]\n"
            "v_pk_fma_f32 v[18:19], v[162:163], v[178:179], v[18:19] op_sel_hi:[0,1,1]\n"
            "v_pk_fma_f32 v[20:21], v[166:167], v[180:181], v[20:21] op_sel_hi:[0,1,1]\n"
            "v_pk_fma_f32 v[22:23], v[166:167], v[182:183], v[22:23] op_sel_hi:[0,1,1]\n"
            "v_pk_fma_f32 v[24:25], v[170:171], v[184:185], v[24:25] op_sel_hi:[0,1,1]\n"
            "v_pk_fma_f32 v[26:27], v[170:171], v[186:187], v[26:27] op_sel_hi:[0,1,1]\n"
            "v_pk_fma_f32 v[28:29], v[174:175], v[188:189], v[28:29] op_sel_hi:[0,1,1]\n"
            "v_pk_fma_f32 v[30:31], v[174:175], v[190:191], v[30:31] op_sel_hi:[0,1,1]\n"
            "v_pk_add_f32 v[152:153], v[152:153], 1.0 op_sel_hi:[1,0]\n"
            "v_pk_add_f32 v[154:155], v[154:155], 1.0 op_sel_hi:[1,0]\n"
            "v_pk_add_f32 v[156:157], v[156:157], 1.0 op_sel_hi:[1,0]\n"
            "v_pk_add_f32 v[158:159], v[158:159], 1.0 op_sel_hi:[1,0]\n"
            "v_mfma_f32_16x16x32_bf16 v[132:135], v[192:195], v[116:119], 0\n"
            "v_rcp_f32 v152, v152\n"
            "v_rcp_f32 v153, v153\n"
            "v_mfma_f32_4x4x4_16b_bf16 v[176:179], v[208:209], v[88:89], 0 cbsz:2 abid:3\n"
            "v_rcp_f32 v154, v154\n"
            "v_mfma_f32_4x4x4_16b_bf16 v[180:183], v[208:209], v[90:91], 0 cbsz:2 abid:3\n"
            "v_rcp_f32 v155, v155\n"
            "v_mfma_f32_4x4x4_16b_bf16 v[184:187], v[208:209], v[92:93], 0 cbsz:2 abid:3\n"
            "v_rcp_f32 v156, v156\n"
            "v_mfma_f32_4x4x4_16b_bf16 v[188:191], v[208:209], v[94:95], 0 cbsz:2 abid:3\n"
            "ds_read_b64 v[208:209], v213\n"
            "v_rcp_f32 v157, v157\n"
            "v_rcp_f32 v158, v158\n"
            "buffer_load_dwordx4 v[88:91], v222, %[rsx], 0 offen offset:512\n"
            "buffer_load_dwordx4 v[92:95], v222, %[rsx], 0 offen offset:768\n"
            "v_rcp_f32 v159, v159\n"
            "v_pk_fma_f32 v[16:17], v[162:163], v[176:177], v[16:17] op_sel:[1,0,0]\n"
            "v_pk_fma_f32 v[18:19], v[162:163], v[178:179], v[18:19] op_sel:[1,0,0]\n"
            "v_pk_fma_f32 v[20:21], v[166:167], v[180:181], v[20:21] op_sel:[1,0,0]\n"
            "v_pk_fma_f32 v[22:23], v[166:167], v[182:183], v[22:23] op_sel:[1,0,0]\n"
            "v_pk_fma_f32 v[24:25], v[170:171], v[184:185], v[24:25] op_sel:[1,0,0]\n"
            "v_pk_fma_f32 v[26:27], v[170:171], v[186:187], v[26:27] op_sel:[1,0,0]\n"
            "v_pk_fma_f32 v[28:29], v[174:175], v[188:189], v[28:29] op_sel:[1,0,0]\n"
            "v_pk_fma_f32 v[30:31], v[174:175], v[190:191], v[30:31] op_sel:[1,0,0]\n"
            "v_pk_mul_f32 v[152:153], v[152:153], v[136:137]\n"
            "v_pk_mul_f32 v[154:155], v[154:155], v[138:139]\n"
            "v_pk_mul_f32 v[156:157], v[156:157], v[140:141]\n"
            "v_pk_mul_f32 v[158:159], v[158:159], v[142:143]\n"
            "s_sub_u32 %[rem], %[rem], 4\n"
            "Lnext_%=:\n"
            "s_sub_u32 %[nst], %[nst], 1\n"
            "Ldisp_%=:\n"
            "s_bitcmp1_b32 %[evt], %[nst]\n"
            "s_cbranch_scc1 Lprio_%=\n"
            "Ld2_%=:\n"
            "s_cmp_gt_u32 %[rem], 4\n"
            "s_cbranch_scc1 Lfast_%=\n"
            "v_mfma_f32_16x16x32_bf16 v[136:139], v[192:195], v[120:123], 0\n"
            "v_exp_f32 v160, v128\n"
            "v_add_u32 v211, 0x100, v211\n"
            "v_exp_f32 v161, v129\n"
            "s_waitcnt vmcnt(13) lgkmcnt(0)\n"
            "v_mfma_f32_4x4x4_16b_bf16 v[176:179], v[208:209], v[32:33], 0 cbsz:2\n"
            "v_exp_f32 v162, v130\n"
            "v_mfma_f32_4x4x4_16b_bf16 v[180:183], v[208:209], v[34:35], 0 cbsz:2\n"
            "v_exp_f32 v163, v131\n"
            "v_mfma_f32_4x4x4_16b_bf16 v[184:187], v[208:209], v[36:37], 0 cbsz:2\n"
            "v_exp_f32 v164, v132\n"
            "v_mfma_f32_4x4x4_16b_bf16 v[188:191], v[208:209], v[38:39], 0 cbsz:2\n"
            "v_exp_f32 v165, v133\n"
            "v_add_u32_dpp v219, v196, v214 quad_perm:[0,0,0,0] row_mask:0xf bank_mask:0xf\n"
            "v_mfma_f32_4x4x4_16b_bf16 v[204:207], v[202:203], v[198:199], 0\n"
            "v_exp_f32 v166, v134\n"
            "buffer_load_dwordx4 v[32:35], v219, %[rsx], 0 offen\n"
            "buffer_load_dwordx4 v[36:39], v219, %[rsx], 0 offen offset:256\n"
            "v_exp_f32 v167, v135\n"
            "v_pk_fma_f32 v[0:1], v[144:145], v[176:177], v[0:1] op_sel_hi:[0,1,1]\n"
            "v_pk_fma_f32 v[2:3], v[144:145], v[178:179], v[2:3] op_sel_hi:[0,1,1]\n"
            "v_pk_fma_f32 v[4:5], v[148:149], v[180:181], v[4:5] op_sel_hi:[0,1,1]\n"
            "v_pk_fma_f32 v[6:7], v[148:149], v[182:183], v[6:7] op_sel_hi:[0,1,1]\n"
            "v_pk_fma_f32 v[8:9], v[152:153], v[184:185], v[8:9] op_sel_hi:[0,1,1]\n"
            "v_pk_fma_f32 v[10:11], v[152:153], v[186:187], v[10:11] op_sel_hi:[0,1,1]\n"
            "v_pk_fma_f32 v[12:13], v[156:157], v[188:189], v[12:13] op_sel_hi:[0,1,1]\n"
            "v_pk_fma_f32 v[14:15], v[156:157], v[190:191], v[14:15] op_sel_hi:[0,1,1]\n"
            "v_pk_add_f32 v[160:161], v[160:161], 1.0 op_sel_hi:[1,0]\n"
            "v_pk_add_f32 v[162:163], v[162:163], 1.0 op_sel_hi:[1,0]\n"
            "v_pk_add_f32 v[164:165], v[164:165], 1.0 op_sel_hi:[1,0]\n"
            "v_pk_add_f32 v[166:167], v[166:167], 1.0 op_sel_hi:[1,0]\n"
            "s_cmp_eq_u32 %[rem], 1\n"
            "s_cbranch_scc1 Lf00_%=\n"
            "Lr00_%=:\n"
            "v_mfma_f32_16x16x32_bf16 v[140:143], v[192:195], v[124:127], 0\n"
            "ds_read_b32 v230, v211\n"
            "v_rcp_f32 v160, v160\n"
            "v_rcp_f32 v161, v161\n"
            "v_mfma_f32_4x4x4_16b_bf16 v[176:179], v[208:209], v[48:49], 0 cbsz:2 abid:1\n"
            "v_rcp_f32 v162, v162\n"
            "v_mfma_f32_4x4x4_16b_bf16 v[180:183], v[208:209], v[50:51], 0 cbsz:2 abid:1\n"
            "v_rcp_f32 v163, v163\n"
            "v_mfma_f32_4x4x4_16b_bf16 v[184:187], v[208:209], v[52:53], 0 cbsz:2 abid:1\n"
            "v_rcp_f32 v164, v164\n"
            "v_mfma_f32_4x4x4_16b_bf16 v[188:191], v[208:209], v[54:55], 0 cbsz:2 abid:1\n"
            "v_rcp_f32 v165, v165\n"
            "v_add_u32_dpp v220, v196, v214 quad_perm:[1,1,1,1] row_mask:0xf bank_mask:0xf\n"
            "v_mfma_f32_4x4x4_16b_bf16 v[204:207], v[200:201], v[198:199], v[204:207]\n"
            "v_rcp_f32 v166, v166\n"
            "buffer_load_dwordx4 v[48:51], v220, %[rsx], 0 offen\n"
            "buffer_load_dwordx4 v[52:55], v220, %[rsx], 0 offen offset:256\n"
            "v_rcp_f32 v167, v167\n"
            "v_pk_fma_f32 v[0:1], v[144:145], v[176:177], v[0:1] op_sel:[1,0,0]\n"
            "v_pk_fma_f32 v[2:3], v[144:145], v[178:179], v[2:3] op_sel:[1,0,0]\n"
            "v_pk_fma_f32 v[4:5], v[148:149], v[180:181], v[4:5] op_sel:[1,0,0]\n"
            "v_pk_fma_f32 v[6:7], v[148:149], v[182:183], v[6:7] op_sel:[1,0,0]\n"
            "v_pk_fma_f32 v[8:9], v[152:153], v[184:185], v[8:9] op_sel:[1,0,0]\n"
            "v_pk_fma_f32 v[10:11], v[152:153], v[186:187], v[10:11] op_sel:[1,0,0]\n"
            "v_pk_fma_f32 v[12:13], v[156:157], v[188:189], v[12:13] op_sel:[1,0,0]\n"
            "v_pk_fma_f32 v[14:15], v[156:157], v[190:191], v[14:15] op_sel:[1,0,0]\n"
            "v_pk_mul_f32 v[160:161], v[160:161], v[128:129]\n"
            "v_pk_mul_f32 v[162:163], v[162:163], v[130:131]\n"
            "v_pk_mul_f32 v[164:165], v[164:165], v[132:133]\n"
            "v_pk_mul_f32 v[166:167], v[166:167], v[134:135]\n"
            "s_cmp_eq_u32 %[rem], 2\n"
            "s_cbranch_scc1 Lf01_%=\n"
            "Lr01_%=:\n"
            "s_waitcnt vmcnt(12) lgkmcnt(0)\n"
            "v_mov_b64 v[192:193], v[232:233]\n"
            "v_mov_b64 v[194:195], v[234:235]\n"
            "v_add_u32 v230, v230, v229\n"
            "buffer_load_dwordx4 v[232:235], v230, %[rse], 0 offen\n"
            "v_mfma_f32_16x16x32_bf16 v[128:131], v[192:195], v[96:99], 0\n"
            "v_exp_f32 v168, v136\n"
            "v_exp_f32 v169, v137\n"
            "v_mfma_f32_4x4x4_16b_bf16 v[176:179], v[208:209], v[64:65], 0 cbsz:2 abid:2\n"
            "v_exp_f32 v170, v138\n"
            "v_mfma_f32_4x4x4_16b_bf16 v[180:183], v[208:209], v[66:67], 0 cbsz:2 abid:2\n"
            "v_exp_f32 v171, v139\n"
            "v_mfma_f32_4x4x4_16b_bf16 v[184:187], v[208:209], v[68:69], 0 cbsz:2 abid:2\n"
            "v_exp_f32 v172, v140\n"
            "v_mfma_f32_4x4x4_16b_bf16 v[188:191], v[208:209], v[70:71], 0 cbsz:2 abid:2\n"
            "v_exp_f32 v173, v141\n"
            "v_add_u32_dpp v221, v196, v214 quad_perm:[2,2,2,2] row_mask:0xf bank_mask:0xf\n"
            "v_exp_f32 v174, v142\n"
            "buffer_load_dwordx4 v[64:67], v221, %[rsx], 0 offen\n"
            "buffer_load_dwordx4 v[68:71], v221, %[rsx], 0 offen offset:256\n"
            "v_exp_f32 v175, v143\n"
            "v_pk_fma_f32 v[0:1], v[146:147], v[176:177], v[0:1] op_sel_hi:[0,1,1]\n"
            "v_pk_fma_f32 v[2:3], v[146:147], v[178:179], v[2:3] op_sel_hi:[0,1,1]\n"
            "v_pk_fma_f32 v[4:5], v[150:151], v[180:181], v[4:5] op_sel_hi:[0,1,1]\n"
            "v_pk_fma_f32 v[6:7], v[150:151], v[182:183], v[6:7] op_sel_hi:[0,1,1]\n"
            "v_pk_fma_f32 v[8:9], v[154:155], v[184:185], v[8:9] op_sel_hi:[0,1,1]\n"
            "v_pk_fma_f32 v[10:11], v[154:155], v[186:187], v[10:11] op_sel_hi:[0,1,1]\n"
            "v_pk_fma_f32 v[12:13], v[158:159], v[188:189], v[12:13] op_sel_hi:[0,1,1]\n"
            "v_pk_fma_f32 v[14:15], v[158:159], v[190:191], v[14:15] op_sel_hi:[0,1,1]\n"
            "v_pk_add_f32 v[168:169], v[168:169], 1.0 op_sel_hi:[1,0]\n"
            "v_pk_add_f32 v[170:171], v[170:171], 1.0 op_sel_hi:[1,0]\n"
            "v_pk_add_f32 v[172:173], v[172:173], 1.0 op_sel_hi:[1,0]\n"
            "v_pk_add_f32 v[174:175], v[174:175], 1.0 op_sel_hi:[1,0]\n"
            "v_cvt_pk_bf16_f32 v204, v204, v205\n"
            "v_cvt_pk_bf16_f32 v205, v206, v207\n"
            "ds_write_b64 v212, v[204:205]\n"
            "s_cmp_eq_u32 %[rem], 3\n"
            "s_cbranch_scc1 Lf02_%=\n"
            "Lr02_%=:\n"
            "v_mfma_f32_16x16x32_bf16 v[132:135], v[192:195], v[100:103], 0\n"
            "v_rcp_f32 v168, v168\n"
            "v_rcp_f32 v169, v169\n"
            "v_mfma_f32_4x4x4_16b_bf16 v[176:179], v[208:209], v[80:81], 0 cbsz:2 abid:3\n"
            "v_rcp_f32 v170, v170\n"
            "v_mfma_f32_4x4x4_16b_bf16 v[180:183], v[208:209], v[82:83], 0 cbsz:2 abid:3\n"
            "v_rcp_f32 v171, v171\n"
            "v_mfma_f32_4x4x4_16b_bf16 v[184:187], v[208:209], v[84:85], 0 cbsz:2 abid:3\n"
            "v_rcp_f32 v172, v172\n"
            "v_mfma_f32_4x4x4_16b_bf16 v[188:191], v[208:209], v[86:87], 0 cbsz:2 abid:3\n"
            "v_rcp_f32 v173, v173\n"
            "v_add_u32_dpp v222, v196, v214 quad_perm:[3,3,3,3] row_mask:0xf bank_mask:0xf\n"
            "v_rcp_f32 v174, v174\n"
            "buffer_load_dwordx4 v[80:83], v222, %[rsx], 0 offen\n"
            "buffer_load_dwordx4 v[84:87], v222, %[rsx], 0 offen offset:256\n"
            "v_rcp_f32 v175, v175\n"
            "v_pk_fma_f32 v[0:1], v[146:147], v[176:177], v[0:1] op_sel:[1,0,0]\n"
            "v_pk_fma_f32 v[2:3], v[146:147], v[178:179], v[2:3] op_sel:[1,0,0]\n"
            "v_pk_fma_f32 v[4:5], v[150:151], v[180:181], v[4:5] op_sel:[1,0,0]\n"
            "v_pk_fma_f32 v[6:7], v[150:151], v[182:183], v[6:7] op_sel:[1,0,0]\n"
            "v_pk_fma_f32 v[8:9], v[154:155], v[184:185], v[8:9] op_sel:[1,0,0]\n"
            "v_pk_fma_f32 v[10:11], v[154:155], v[186:187], v[10:11] op_sel:[1,0,0]\n"
            "v_pk_fma_f32 v[12:13], v[158:159], v[188:189], v[12:13] op_sel:[1,0,0]\n"
            "v_pk_fma_f32 v[14:15], v[158:159], v[190:191], v[14:15] op_sel:[1,0,0]\n"
            "v_pk_mul_f32 v[168:169], v[168:169], v[136:137]\n"
            "v_pk_mul_f32 v[170:171], v[170:171], v[138:139]\n"
            "v_pk_mul_f32 v[172:173], v[172:173], v[140:141]\n"
            "v_pk_mul_f32 v[174:175], v[174:175], v[142:143]\n"
            "s_cmp_eq_u32 %[rem], 4\n"
            "s_cbranch_scc1 Lf03_%=\n"
            "Lr03_%=:\n"
            "v_add_u32 v210, 0x100, v210\n"
            "ds_read_b128 v[196:199], v210\n"
            "v_mfma_f32_16x16x32_bf16 v[136:139], v[192:195], v[104:107], 0\n"
            "v_exp_f32 v144, v128\n"
            "v_exp_f32 v145, v129\n"
            "s_waitcnt vmcnt(13)\n"
            "v_mfma_f32_4x4x4_16b_bf16 v[176:179], v[208:209], v[40:41], 0 cbsz:2\n"
            "v_exp_f32 v146, v130\n"
            "v_mfma_f32_4x4x4_16b_bf16 v[180:183], v[208:209], v[42:43], 0 cbsz:2\n"
            "v_exp_f32 v147, v131\n"
            "v_mfma_f32_4x4x4_16b_bf16 v[184:187], v[208:209], v[44:45], 0 cbsz:2\n"
            "v_exp_f32 v148, v132\n"
            "v_mfma_f32_4x4x4_16b_bf16 v[188:191], v[208:209], v[46:47], 0 cbsz:2\n"
            "v_exp_f32 v149, v133\n"
            "v_exp_f32 v150, v134\n"
            "buffer_load_dwordx4 v[40:43], v219, %[rsx], 0 offen offset:512\n"
            "buffer_load_dwordx4 v[44:47], v219, %[rsx], 0 offen offset:768\n"
            "v_exp_f32 v151, v135\n"
            "v_pk_fma_f32 v[16:17], v[160:161], v[176:177], v[16:17] op_sel_hi:[0,1,1]\n"
            "v_pk_fma_f32 v[18:19], v[160:161], v[178:179], v[18:19] op_sel_hi:[0,1,1]\n"
            "v_pk_fma_f32 v[20:21], v[164:165], v[180:181], v[20:21] op_sel_hi:[0,1,1]\n"
            "v_pk_fma_f32 v[22:23], v[164:165], v[182:183], v[22:23] op_sel_hi:[0,1,1]\n"
            "v_pk_fma_f32 v[24:25], v[168:169], v[184:185], v[24:25] op_sel_hi:[0,1,1]\n"
            "v_pk_fma_f32 v[26:27], v[168:169], v[186:187], v[26:27] op_sel_hi:[0,1,1]\n"
            "v_pk_fma_f32 v[28:29], v[172:173], v[188:189], v[28:29] op_sel_hi:[0,1,1]\n"
            "v_pk_fma_f32 v[30:31], v[172:173], v[190:191], v[30:31] op_sel_hi:[0,1,1]\n"
            "v_pk_add_f32 v[144:145], v[144:145], 1.0 op_sel_hi:[1,0]\n"
            "v_pk_add_f32 v[146:147], v[146:147], 1.0 op_sel_hi:[1,0]\n"
            "v_pk_add_f32 v[148:149], v[148:149], 1.0 op_sel_hi:[1,0]\n"
            "v_pk_add_f32 v[150:151], v[150:151], 1.0 op_sel_hi:[1,0]\n"
            "s_cmp_eq_u32 %[rem], 1\n"
            "s_cbranch_scc1 Lf10_%=\n"
            "Lr10_%=:\n"
            "v_mfma_f32_16x16x32_bf16 v[140:143], v[192:195], v[108:111], 0\n"
            "v_rcp_f32 v144, v144\n"
            "v_rcp_f32 v145, v145\n"
            "v_mfma_f32_4x4x4_16b_bf16 v[176:179], v[208:209], v[56:57], 0 cbsz:2 abid:1\n"
            "v_rcp_f32 v146, v146\n"
            "v_mfma_f32_4x4x4_16b_bf16 v[180:183], v[208:209], v[58:59], 0 cbsz:2 abid:1\n"
            "v_rcp_f32 v147, v147\n"
            "v_mfma_f32_4x4x4_16b_bf16 v[184:187], v[208:209], v[60:61], 0 cbsz:2 abid:1\n"
            "v_rcp_f32 v148, v148\n"
            "v_mfma_f32_4x4x4_16b_bf16 v[188:191], v[208:209], v[62:63], 0 cbsz:2 abid:1\n"
            "v_rcp_f32 v149, v149\n"
            "v_rcp_f32 v150, v150\n"
            "buffer_load_dwordx4 v[56:59], v220, %[rsx], 0 offen offset:512\n"
            "buffer_load_dwordx4 v[60:63], v220, %[rsx], 0 offen offset:768\n"
            "v_rcp_f32 v151, v151\n"
            "v_pk_fma_f32 v[16:17], v[160:161], v[176:177], v[16:17] op_sel:[1,0,0]\n"
            "v_pk_fma_f32 v[18:19], v[160:161], v[178:179], v[18:19] op_sel:[1,0,0]\n"
            "v_pk_fma_f32 v[20:21], v[164:165], v[180:181], v[20:21] op_sel:[1,0,0]\n"
            "v_pk_fma_f32 v[22:23], v[164:165], v[182:183], v[22:23] op_sel:[1,0,0]\n"
            "v_pk_fma_f32 v[24:25], v[168:169], v[184:185], v[24:25] op_sel:[1,0,0]\n"
            "v_pk_fma_f32 v[26:27], v[168:169], v[186:187], v[26:27] op_sel:[1,0,0]\n"
            "v_pk_fma_f32 v[28:29], v[172:173], v[188:189], v[28:29] op_sel:[1,0,0]\n"
            "v_pk_fma_f32 v[30:31], v[172:173], v[190:191], v[30:31] op_sel:[1,0,0]\n"
            "v_pk_mul_f32 v[144:145], v[144:145], v[128:129]\n"
            "v_pk_mul_f32 v[146:147], v[146:147], v[130:131]\n"
            "v_pk_mul_f32 v[148:149], v[148:149], v[132:133]\n"
            "v_pk_mul_f32 v[150:151], v[150:151], v[134:135]\n"
            "s_cmp_eq_u32 %[rem], 2\n"
            "s_cbranch_scc1 Lf11_%=\n"
            "Lr11_%=:\n"
            "v_mfma_f32_16x16x32_bf16 v[128:131], v[192:195], v[112:115], 0\n"
            "v_exp_f32 v152, v136\n"
            "v_exp_f32 v153, v137\n"
            "s_waitcnt vmcnt(13)\n"
            "v_mfma_f32_4x4x4_16b_bf16 v[176:179], v[208:209], v[72:73], 0 cbsz:2 abid:2\n"
            "v_exp_f32 v154, v138\n"
            "v_mfma_f32_4x4x4_16b_bf16 v[180:183], v[208:209], v[74:75], 0 cbsz:2 abid:2\n"
            "v_exp_f32 v155, v139\n"
            "v_mfma_f32_4x4x4_16b_bf16 v[184:187], v[208:209], v[76:77], 0 cbsz:2 abid:2\n"
            "v_exp_f32 v156, v140\n"
            "v_mfma_f32_4x4x4_16b_bf16 v[188:191], v[208:209], v[78:79], 0 cbsz:2 abid:2\n"
            "v_exp_f32 v157, v141\n"
            "v_exp_f32 v158, v142\n"
            "buffer_load_dwordx4 v[72:75], v221, %[rsx], 0 offen offset:512\n"
            "buffer_load_dwordx4 v[76:79], v221, %[rsx], 0 offen offset:768\n"
            "v_exp_f32 v159, v143\n"
            "v_pk_fma_f32 v[16:17], v[162:163], v[176:177], v[16:17] op_sel_hi:[0,1,1]\n"
            "v_pk_fma_f32 v[18:19], v[162:163], v[178:179], v[18:19] op_sel_hi:[0,1,1]\n"
            "v_pk_fma_f32 v[20:21], v[166:167], v[180:181], v[20:21] op_sel_hi:[0,1,1]\n"
            "v_pk_fma_f32 v[22:23], v[166:167], v[182:183], v[22:23] op_sel_hi:[0,1,1]\n"
            "v_pk_fma_f32 v[24:25], v[170:171], v[184:185], v[24:25] op_sel_hi:[0,1,1]\n"
            "v_pk_fma_f32 v[26:27], v[170:171], v[186:187], v[26:27] op_sel_hi:[0,1,1]\n"
            "v_pk_fma_f32 v[28:29], v[174:175], v[188:189], v[28:29] op_sel_hi:[0,1,1]\n"
            "v_pk_fma_f32 v[30:31], v[174:175], v[190:191], v[30:31] op_sel_hi:[0,1,1]\n"
            "v_pk_add_f32 v[152:153], v[152:153], 1.0 op_sel_hi:[1,0]\n"
            "v_pk_add_f32 v[154:155], v[154:155], 1.0 op_sel_hi:[1,0]\n"
            "v_pk_add_f32 v[156:157], v[156:157], 1.0 op_sel_hi:[1,0]\n"
            "v_pk_add_f32 v[158:159], v[158:159], 1.0 op_sel_hi:[1,0]\n"
            "s_cmp_eq_u32 %[rem], 3\n"
            "s_cbranch_scc1 Lf12_%=\n"
            "Lr12_%=:\n"
            "v_mfma_f32_16x16x32_bf16 v[132:135], v[192:195], v[116:119], 0\n"
            "v_rcp_f32 v152, v152\n"
            "v_rcp_f32 v153, v153\n"
            "v_mfma_f32_4x4x4_16b_bf16 v[176:179], v[208:209], v[88:89], 0 cbsz:2 abid:3\n"
            "v_rcp_f32 v154, v154\n"
            "v_mfma_f32_4x4x4_16b_bf16 v[180:183], v[208:209], v[90:91], 0 cbsz:2 abid:3\n"
            "v_rcp_f32 v155, v155\n"
            "v_mfma_f32_4x4x4_16b_bf16 v[184:187], v[208:209], v[92:93], 0 cbsz:2 abid:3\n"
            "v_rcp_f32 v156, v156\n"
            "v_mfma_f32_4x4x4_16b_bf16 v[188:191], v[208:209], v[94:95], 0 cbsz:2 abid:3\n"
            "ds_read_b64 v[208:209], v213\n"
            "v_rcp_f32 v157, v157\n"
            "v_rcp_f32 v158, v158\n"
            "buffer_load_dwordx4 v[88:91], v222, %[rsx], 0 offen offset:512\n"
            "buffer_load_dwordx4 v[92:95], v222, %[rsx], 0 offen offset:768\n"
            "v_rcp_f32 v159, v159\n"
            "v_pk_fma_f32 v[16:17], v[162:163], v[176:177], v[16:17] op_sel:[1,0,0]\n"
            "v_pk_fma_f32 v[18:19], v[162:163], v[178:179], v[18:19] op_sel:[1,0,0]\n"
            "v_pk_fma_f32 v[20:21], v[166:167], v[180:181], v[20:21] op_sel:[1,0,0]\n"
            "v_pk_fma_f32 v[22:23], v[166:167], v[182:183], v[22:23] op_sel:[1,0,0]\n"
            "v_pk_fma_f32 v[24:25], v[170:171], v[184:185], v[24:25] op_sel:[1,0,0]\n"
            "v_pk_fma_f32 v[26:27], v[170:171], v[186:187], v[26:27] op_sel:[1,0,0]\n"
            "v_pk_fma_f32 v[28:29], v[174:175], v[188:189], v[28:29] op_sel:[1,0,0]\n"
            "v_pk_fma_f32 v[30:31], v[174:175], v[190:191], v[30:31] op_sel:[1,0,0]\n"
            "v_pk_mul_f32 v[152:153], v[152:153], v[136:137]\n"
            "v_pk_mul_f32 v[154:155], v[154:155], v[138:139]\n"
            "v_pk_mul_f32 v[156:157], v[156:157], v[140:141]\n"
            "v_pk_mul_f32 v[158:159], v[158:159], v[142:143]\n"
            "s_cmp_eq_u32 %[rem], 4\n"
            "s_cbranch_scc1 Lf13_%=\n"
            "Lr13_%=:\n"
            "s_and_b32 %[tmp], %[lens], 0xff\n"
            "s_add_u32 %[rem], %[rem], %[tmp]\n"
            "s_lshr_b32 %[lens], %[lens], 8\n"
            "s_and_b32 %[tmp], %[fm], 14\n"
            "s_lshr_b32 %[fm], %[fm], 4\n"
            "s_cmp_lg_u32 %[tmp], 0\n"
            "s_cbranch_scc1 Lb1_%=\n"
            "v_mov_b32 v215, v216\n"
            "s_branch Lnext_%=\n"
            "Lb1_%=:\n"
            "s_cmp_lg_u32 %[tmp], 8\n"
            "s_cbranch_scc1 Lnext_%=\n"
            "s_waitcnt vmcnt(0)\n"
            "v_mov_b32 v176, %[cnt]\n"
            "v_mov_b32 v177, 1\n"
            "s_mov_b64 exec, 1\n"
            "ds_add_rtn_u32 v177, v176, v177\n"
            "s_waitcnt lgkmcnt(0)\n"
            "v_readfirstlane_b32 %[tmp], v177\n"
            "s_mov_b64 exec, 0xff\n"
            "v_mov_b32 v176, %[tag]\n"
            "v_mov_b32 v177, %[tag]\n"
            "v_mov_b32 v178, %[tag]\n"
            "v_mov_b32 v179, %[tag]\n"
            "s_cmp_lg_u32 %[tmp], 7\n"
            "s_cbranch_scc1 Lpub2_%=\n"
            "global_store_dwordx4 v214, v[176:179], %[flg] sc1\n"
            "Lpub2_%=:\n"
            "s_mov_b64 exec, -1\n"
            "s_branch Lnext_%=\n"
            "Lprio_%=:\n"
            "s_cmp_eq_u32 %[nst], 1\n"
            "s_cbranch_scc1 Llast_%=\n"
            "s_lshl_b32 %[tmp], %[nst], 1\n"
            "s_lshr_b32 %[tmp], %[ptab], %[tmp]\n"
            "s_bitcmp1_b32 %[tmp], 1\n"
            "s_cbranch_scc1 Lq2_%=\n"
            "s_bitcmp1_b32 %[tmp], 0\n"
            "s_setprio 0\n"
            "s_cbranch_scc0 Lq9_%=\n"
            "s_setprio 1\n"
            "s_branch Lq9_%=\n"
            "Lq2_%=:\n"
            "s_bitcmp1_b32 %[tmp], 0\n"
            "s_setprio 2\n"
            "s_cbranch_scc0 Lq9_%=\n"
            "s_setprio 3\n"
            "Lq9_%=:\n"
            "s_cmp_lg_u32 %[nst], 2\n"
            "s_cbranch_scc1 Ld2_%=\n"
            "v_mov_b32 v223, 0\n"
            "global_load_dword v223, v223, %[pfl] sc1\n"
            "s_branch Ld2_%=\n"
            "Lf00_%=:\n"
            "s_call_b64 %[ra], Lflush0_%=\n"
            "s_branch Lr00_%=\n"
            "Lf01_%=:\n"
            "s_call_b64 %[ra], Lflush0_%=\n"
            "s_branch Lr01_%=\n"
            "Lf02_%=:\n"
            "s_call_b64 %[ra], Lflush0_%=\n"
            "s_branch Lr02_%=\n"
            "Lf03_%=:\n"
            "s_call_b64 %[ra], Lflush0_%=\n"
            "s_branch Lr03_%=\n"
            "Lflush0_%=:\n"
            "v_cvt_pk_bf16_f32 v176, v0, v1\n"
            "v_cvt_pk_bf16_f32 v177, v2, v3\n"
            "v_cvt_pk_bf16_f32 v178, v4, v5\n"
            "v_cvt_pk_bf16_f32 v179, v6, v7\n"
            "v_cvt_pk_bf16_f32 v180, v8, v9\n"
            "v_cvt_pk_bf16_f32 v181, v10, v11\n"
            "v_cvt_pk_bf16_f32 v182, v12, v13\n"
            "v_cvt_pk_bf16_f32 v183, v14, v15\n"
            "s_bitcmp1_b32 %[fm], 1\n"
            "s_cbranch_scc1 Lpark0_%=\n"
            "buffer_store_dwordx4 v[176:179], v215, %[rsw], 0 offen offset:0 sc1\n"
            "buffer_store_dwordx4 v[180:183], v215, %[rsw], 0 offen offset:256 sc1\n"
            "s_branch Lzero0_%=\n"
            "Lpark0_%=:\n"
            "ds_write_b128 v231, v[176:179] offset:0\n"
            "ds_write_b128 v231, v[180:183] offset:1024\n"
            "Lzero0_%=:\n"
            "v_mov_b64 v[0:1], 0\n"
            "v_mov_b64 v[2:3], 0\n"
            "v_mov_b64 v[4:5], 0\n"
            "v_mov_b64 v[6:7], 0\n"
            "v_mov_b64 v[8:9], 0\n"
            "v_mov_b64 v[10:11], 0\n"
            "v_mov_b64 v[12:13], 0\n"
            "v_mov_b64 v[14:15], 0\n"
            "s_setpc_b64 %[ra]\n"
            "Lf10_%=:\n"
            "s_call_b64 %[ra], Lflush1_%=\n"
            "s_branch Lr10_%=\n"
            "Lf11_%=:\n"
            "s_call_b64 %[ra], Lflush1_%=\n"
            "s_branch Lr11_%=\n"
            "Lf12_%=:\n"
            "s_call_b64 %[ra], Lflush1_%=\n"
            "s_branch Lr12_%=\n"
            "Lf13_%=:\n"
            "s_call_b64 %[ra], Lflush1_%=\n"
            "s_branch Lr13_%=\n"
            "Lflush1_%=:\n"
            "v_cvt_pk_bf16_f32 v176, v16, v17\n"
            "v_cvt_pk_bf16_f32 v177, v18, v19\n"
            "v_cvt_pk_bf16_f32 v178, v20, v21\n"
            "v_cvt_pk_bf16_f32 v179, v22, v23\n"
            "v_cvt_pk_bf16_f32 v180, v24, v25\n"
            "v_cvt_pk_bf16_f32 v181, v26, v27\n"
            "v_cvt_pk_bf16_f32 v182, v28, v29\n"
            "v_cvt_pk_bf16_f32 v183, v30, v31\n"
            "s_bitcmp1_b32 %[fm], 1\n"
            "s_cbranch_scc1 Lpark1_%=\n"
            "buffer_store_dwordx4 v[176:179], v215, %[rsw], 0 offen offset:512 sc1\n"
            "buffer_store_dwordx4 v[180:183], v215, %[rsw], 0 offen offset:768 sc1\n"
            "s_branch Lzero1_%=\n"
            "Lpark1_%=:\n"
            "ds_write_b128 v231, v[176:179] offset:2048\n"
            "ds_write_b128 v231, v[180:183] offset:3072\n"
            "Lzero1_%=:\n"
            "v_mov_b64 v[16:17], 0\n"
            "v_mov_b64 v[18:19], 0\n"
            "v_mov_b64 v[20:21], 0\n"
            "v_mov_b64 v[22:23], 0\n"
            "v_mov_b64 v[24:25], 0\n"
            "v_mov_b64 v[26:27], 0\n"
            "v_mov_b64 v[28:29], 0\n"
            "v_mov_b64 v[30:31], 0\n"
            "s_setpc_b64 %[ra]\n"
            "Lspin_%=:\n"
            "s_mov_b32 %[lens], %[spin]\n"
            "Lsp1_%=:\n"
            "v_readfirstlane_b32 %[nst], v223\n"
            "s_sub_u32 %[lens], %[lens], 1\n"
            "s_cmp_eq_u32 %[lens], 0\n"
            "s_cbranch_scc1 Lsp2_%=\n"
            "s_cmp_eq_u32 %[nst], %[tag]\n"
            "s_cbranch_scc1 Lsp2_%=\n"
            "s_sleep 1\n"
            "v_mov_b32 v223, 0\n"
            "global_load_dword v223, v223, %[pfl] sc1\n"
            "s_waitcnt vmcnt(0)\n"
            "s_branch Lsp1_%=\n"
            "Lsp2_%=:\n"
            "buffer_load_dwordx4 v[32:35], v217, %[rsw], %[oagg] offen offset:0 sc1\n"
            "buffer_load_dwordx4 v[36:39], v217, %[rsw], %[oagg] offen offset:256 sc1\n"
            "buffer_load_dwordx4 v[48:51], v217, %[rsw], %[oagg] offen offset:512 sc1\n"
            "buffer_load_dwordx4 v[52:55], v217, %[rsw], %[oagg] offen offset:768 sc1\n"
            "s_branch Lgo_%=\n"
            "Llast_%=:\n"
            "s_waitcnt vmcnt(16)\n"
            "v_readfirstlane_b32 %[nst], v223\n"
            "v_mov_b32 v222, v217\n"
            "s_cmp_eq_u32 %[nst], %[tag]\n"
            "s_cbranch_scc1 Lp4_%=\n"
            "v_bfrev_b32 v222, 1\n"
            "v_mov_b32 v223, 0\n"
            "global_load_dword v223, v223, %[pfl] sc1\n"
            "Lp4_%=:\n"
            "v_mfma_f32_16x16x32_bf16 v[136:139], v[192:195], v[120:123], 0\n"
            "v_exp_f32 v160, v128\n"
            "v_exp_f32 v161, v129\n"
            "s_waitcnt vmcnt(13) lgkmcnt(0)\n"
            "v_mfma_f32_4x4x4_16b_bf16 v[176:179], v[208:209], v[32:33], 0 cbsz:2\n"
            "v_exp_f32 v162, v130\n"
            "v_mfma_f32_4x4x4_16b_bf16 v[180:183], v[208:209], v[34:35], 0 cbsz:2\n"
            "v_exp_f32 v163, v131\n"
            "v_mfma_f32_4x4x4_16b_bf16 v[184:187], v[208:209], v[36:37], 0 cbsz:2\n"
            "v_exp_f32 v164, v132\n"
            "v_mfma_f32_4x4x4_16b_bf16 v[188:191], v[208:209], v[38:39], 0 cbsz:2\n"
            "v_exp_f32 v165, v133\n"
            "v_exp_f32 v166, v134\n"
            "buffer_load_dwordx4 v[32:35], v222, %[rsw], %[oagg] offen offset:0 sc1\n"
            "buffer_load_dwordx4 v[36:39], v222, %[rsw], %[oagg] offen offset:256 sc1\n"
            "ds_read_b128 v[236:239], v228 offset:0\n"
            "ds_read_b128 v[240:243], v228 offset:1024\n"
            "ds_read_b128 v[244:247], v228 offset:2048\n"
            "ds_read_b128 v[248:251], v228 offset:3072\n"
            "ds_read_b128 v[96:99], v228 offset:4096\n"
            "v_exp_f32 v167, v135\n"
            "v_pk_fma_f32 v[0:1], v[144:145], v[176:177], v[0:1] op_sel_hi:[0,1,1]\n"
            "v_pk_fma_f32 v[2:3], v[144:145], v[178:179], v[2:3] op_sel_hi:[0,1,1]\n"
            "v_pk_fma_f32 v[4:5], v[148:149], v[180:181], v[4:5] op_sel_hi:[0,1,1]\n"
            "v_pk_fma_f32 v[6:7], v[148:149], v[182:183], v[6:7] op_sel_hi:[0,1,1]\n"
            "v_pk_fma_f32 v[8:9], v[152:153], v[184:185], v[8:9] op_sel_hi:[0,1,1]\n"
            "v_pk_fma_f32 v[10:11], v[152:153], v[186:187], v[10:11] op_sel_hi:[0,1,1]\n"
            "v_pk_fma_f32 v[12:13], v[156:157], v[188:189], v[12:13] op_sel_hi:[0,1,1]\n"
            "v_pk_fma_f32 v[14:15], v[156:157], v[190:191], v[14:15] op_sel_hi:[0,1,1]\n"
            "v_pk_add_f32 v[160:161], v[160:161], 1.0 op_sel_hi:[1,0]\n"
            "v_pk_add_f32 v[162:163], v[162:163], 1.0 op_sel_hi:[1,0]\n"
            "v_pk_add_f32 v[164:165], v[164:165], 1.0 op_sel_hi:[1,0]\n"
            "v_pk_add_f32 v[166:167], v[166:167], 1.0 op_sel_hi:[1,0]\n"
            "v_mfma_f32_16x16x32_bf16 v[140:143], v[192:195], v[124:127], 0\n"
            "v_rcp_f32 v160, v160\n"
            "v_rcp_f32 v161, v161\n"
            "v_mfma_f32_4x4x4_16b_bf16 v[176:179], v[208:209], v[48:49], 0 cbsz:2 abid:1\n"
            "v_rcp_f32 v162, v162\n"
            "v_mfma_f32_4x4x4_16b_bf16 v[180:183], v[208:209], v[50:51], 0 cbsz:2 abid:1\n"
            "v_rcp_f32 v163, v163\n"
            "v_mfma_f32_4x4x4_16b_bf16 v[184:187], v[208:209], v[52:53], 0 cbsz:2 abid:1\n"
            "v_rcp_f32 v164, v164\n"
            "v_mfma_f32_4x4x4_16b_bf16 v[188:191], v[208:209], v[54:55], 0 cbsz:2 abid:1\n"
            "v_rcp_f32 v165, v165\n"
            "v_rcp_f32 v166, v166\n"
            "buffer_load_dwordx4 v[48:51], v222, %[rsw], %[oagg] offen offset:512 sc1\n"
            "buffer_load_dwordx4 v[52:55], v222, %[rsw], %[oagg] offen offset:768 sc1\n"
            "ds_read_b128 v[100:103], v228 offset:5120\n"
            "ds_read_b128 v[104:107], v228 offset:6144\n"
            "ds_read_b128 v[108:111], v228 offset:7168\n"
            "ds_read_b128 v[112:115], v228 offset:8192\n"
            "ds_read_b128 v[116:119], v228 offset:9216\n"
            "ds_read_b128 v[196:199], v228 offset:10240\n"
            "v_rcp_f32 v167, v167\n"
            "v_pk_fma_f32 v[0:1], v[144:145], v[176:177], v[0:1] op_sel:[1,0,0]\n"
            "v_pk_fma_f32 v[2:3], v[144:145], v[178:179], v[2:3] op_sel:[1,0,0]\n"
            "v_pk_fma_f32 v[4:5], v[148:149], v[180:181], v[4:5] op_sel:[1,0,0]\n"
            "v_pk_fma_f32 v[6:7], v[148:149], v[182:183], v[6:7] op_sel:[1,0,0]\n"
            "v_pk_fma_f32 v[8:9], v[152:153], v[184:185], v[8:9] op_sel:[1,0,0]\n"
            "v_pk_fma_f32 v[10:11], v[152:153], v[186:187], v[10:11] op_sel:[1,0,0]\n"
            "v_pk_fma_f32 v[12:13], v[156:157], v[188:189], v[12:13] op_sel:[1,0,0]\n"
            "v_pk_fma_f32 v[14:15], v[156:157], v[190:191], v[14:15] op_sel:[1,0,0]\n"
            "v_pk_mul_f32 v[160:161], v[160:161], v[128:129]\n"
            "v_pk_mul_f32 v[162:163], v[162:163], v[130:131]\n"
            "v_pk_mul_f32 v[164:165], v[164:165], v[132:133]\n"
            "v_pk_mul_f32 v[166:167], v[166:167], v[134:135]\n"
            "v_exp_f32 v168, v136\n"
            "v_exp_f32 v169, v137\n"
            "s_waitcnt vmcnt(12)\n"
            "v_mfma_f32_4x4x4_16b_bf16 v[176:179], v[208:209], v[64:65], 0 cbsz:2 abid:2\n"
            "v_exp_f32 v170, v138\n"
            "v_mfma_f32_4x4x4_16b_bf16 v[180:183], v[208:209], v[66:67], 0 cbsz:2 abid:2\n"
            "v_exp_f32 v171, v139\n"
            "v_mfma_f32_4x4x4_16b_bf16 v[184:187], v[208:209], v[68:69], 0 cbsz:2 abid:2\n"
            "v_exp_f32 v172, v140\n"
            "v_mfma_f32_4x4x4_16b_bf16 v[188:191], v[208:209], v[70:71], 0 cbsz:2 abid:2\n"
            "v_exp_f32 v173, v141\n"
            "v_exp_f32 v174, v142\n"
            "ds_read_b128 v[200:203], v228 offset:11264\n"
            "ds_read_b128 v[204:207], v228 offset:12288\n"
            "ds_read_b128 v[232:235], v228 offset:13312\n"
            "v_exp_f32 v175, v143\n"
            "v_pk_fma_f32 v[0:1], v[146:147], v[176:177], v[0:1] op_sel_hi:[0,1,1]\n"
            "v_pk_fma_f32 v[2:3], v[146:147], v[178:179], v[2:3] op_sel_hi:[0,1,1]\n"
            "v_pk_fma_f32 v[4:5], v[150:151], v[180:181], v[4:5] op_sel_hi:[0,1,1]\n"
            "v_pk_fma_f32 v[6:7], v[150:151], v[182:183], v[6:7] op_sel_hi:[0,1,1]\n"
            "v_pk_fma_f32 v[8:9], v[154:155], v[184:185], v[8:9] op_sel_hi:[0,1,1]\n"
            "v_pk_fma_f32 v[10:11], v[154:155], v[186:187], v[10:11] op_sel_hi:[0,1,1]\n"
            "v_pk_fma_f32 v[12:13], v[158:159], v[188:189], v[12:13] op_sel_hi:[0,1,1]\n"
            "v_pk_fma_f32 v[14:15], v[158:159], v[190:191], v[14:15] op_sel_hi:[0,1,1]\n"
            "v_pk_add_f32 v[168:169], v[168:169], 1.0 op_sel_hi:[1,0]\n"
            "v_pk_add_f32 v[170:171], v[170:171], 1.0 op_sel_hi:[1,0]\n"
            "v_pk_add_f32 v[172:173], v[172:173], 1.0 op_sel_hi:[1,0]\n"
            "v_pk_add_f32 v[174:175], v[174:175], 1.0 op_sel_hi:[1,0]\n"
            "v_rcp_f32 v168, v168\n"
            "v_rcp_f32 v169, v169\n"
            "v_mfma_f32_4x4x4_16b_bf16 v[176:179], v[208:209], v[80:81], 0 cbsz:2 abid:3\n"
            "v_rcp_f32 v170, v170\n"
            "v_mfma_f32_4x4x4_16b_bf16 v[180:183], v[208:209], v[82:83], 0 cbsz:2 abid:3\n"
            "v_rcp_f32 v171, v171\n"
            "v_mfma_f32_4x4x4_16b_bf16 v[184:187], v[208:209], v[84:85], 0 cbsz:2 abid:3\n"
            "v_rcp_f32 v172, v172\n"
            "v_mfma_f32_4x4x4_16b_bf16 v[188:191], v[208:209], v[86:87], 0 cbsz:2 abid:3\n"
            "v_rcp_f32 v173, v173\n"
            "v_rcp_f32 v174, v174\n"
            "ds_read_b128 v[120:123], v228 offset:14336\n"
            "ds_read_b128 v[124:127], v228 offset:15360\n"
            "ds_read_b128 v[192:195], v228 offset:16384\n"
            "ds_read_b128 v[128:131], v228 offset:17408\n"
            "ds_read_b128 v[132:135], v228 offset:18432\n"
            "v_rcp_f32 v175, v175\n"
            "v_pk_fma_f32 v[0:1], v[146:147], v[176:177], v[0:1] op_sel:[1,0,0]\n"
            "v_pk_fma_f32 v[2:3], v[146:147], v[178:179], v[2:3] op_sel:[1,0,0]\n"
            "v_pk_fma_f32 v[4:5], v[150:151], v[180:181], v[4:5] op_sel:[1,0,0]\n"
            "v_pk_fma_f32 v[6:7], v[150:151], v[182:183], v[6:7] op_sel:[1,0,0]\n"
            "v_pk_fma_f32 v[8:9], v[154:155], v[184:185], v[8:9] op_sel:[1,0,0]\n"
            "v_pk_fma_f32 v[10:11], v[154:155], v[186:187], v[10:11] op_sel:[1,0,0]\n"
            "v_pk_fma_f32 v[12:13], v[158:159], v[188:189], v[12:13] op_sel:[1,0,0]\n"
            "v_pk_fma_f32 v[14:15], v[158:159], v[190:191], v[14:15] op_sel:[1,0,0]\n"
            "v_pk_mul_f32 v[168:169], v[168:169], v[136:137]\n"
            "v_pk_mul_f32 v[170:171], v[170:171], v[138:139]\n"
            "v_pk_mul_f32 v[172:173], v[172:173], v[140:141]\n"
            "v_pk_mul_f32 v[174:175], v[174:175], v[142:143]\n"
            "s_waitcnt vmcnt(8)\n"
            "v_mfma_f32_4x4x4_16b_bf16 v[176:179], v[208:209], v[40:41], 0 cbsz:2\n"
            "v_mfma_f32_4x4x4_16b_bf16 v[180:183], v[208:209], v[42:43], 0 cbsz:2\n"
            "v_mfma_f32_4x4x4_16b_bf16 v[184:187], v[208:209], v[44:45], 0 cbsz:2\n"
            "v_mfma_f32_4x4x4_16b_bf16 v[188:191], v[208:209], v[46:47], 0 cbsz:2\n"
            "ds_read_b128 v[136:139], v228 offset:19456\n"
            "ds_read_b128 v[140:143], v228 offset:20480\n"
            "ds_read_b128 v[144:147], v228 offset:21504\n"
            "ds_read_b128 v[148:151], v228 offset:22528\n"
            "ds_read_b128 v[152:155], v228 offset:23552\n"
            "ds_read_b128 v[156:159], v228 offset:24576\n"
            "s_nop 1\n"
            "v_pk_fma_f32 v[16:17], v[160:161], v[176:177], v[16:17] op_sel_hi:[0,1,1]\n"
            "v_pk_fma_f32 v[18:19], v[160:161], v[178:179], v[18:19] op_sel_hi:[0,1,1]\n"
            "v_pk_fma_f32 v[20:21], v[164:165], v[180:181], v[20:21] op_sel_hi:[0,1,1]\n"
            "v_pk_fma_f32 v[22:23], v[164:165], v[182:183], v[22:23] op_sel_hi:[0,1,1]\n"
            "v_pk_fma_f32 v[24:25], v[168:169], v[184:185], v[24:25] op_sel_hi:[0,1,1]\n"
            "v_pk_fma_f32 v[26:27], v[168:169], v[186:187], v[26:27] op_sel_hi:[0,1,1]\n"
            "v_pk_fma_f32 v[28:29], v[172:173], v[188:189], v[28:29] op_sel_hi:[0,1,1]\n"
            "v_pk_fma_f32 v[30:31], v[172:173], v[190:191], v[30:31] op_sel_hi:[0,1,1]\n"
            "s_cmp_le_u32 %[njl], 1\n"
            "s_cbranch_scc1 Ltail_%=\n"
            "v_mfma_f32_4x4x4_16b_bf16 v[176:179], v[208:209], v[56:57], 0 cbsz:2 abid:1\n"
            "v_mfma_f32_4x4x4_16b_bf16 v[180:183], v[208:209], v[58:59], 0 cbsz:2 abid:1\n"
            "v_mfma_f32_4x4x4_16b_bf16 v[184:187], v[208:209], v[60:61], 0 cbsz:2 abid:1\n"
            "v_mfma_f32_4x4x4_16b_bf16 v[188:191], v[208:209], v[62:63], 0 cbsz:2 abid:1\n"
            "s_nop 1\n"
            "v_pk_fma_f32 v[16:17], v[160:161], v[176:177], v[16:17] op_sel:[1,0,0]\n"
            "v_pk_fma_f32 v[18:19], v[160:161], v[178:179], v[18:19] op_sel:[1,0,0]\n"
            "v_pk_fma_f32 v[20:21], v[164:165], v[180:181], v[20:21] op_sel:[1,0,0]\n"
            "v_pk_fma_f32 v[22:23], v[164:165], v[182:183], v[22:23] op_sel:[1,0,0]\n"
            "v_pk_fma_f32 v[24:25], v[168:169], v[184:185], v[24:25] op_sel:[1,0,0]\n"
            "v_pk_fma_f32 v[26:27], v[168:169], v[186:187], v[26:27] op_sel:[1,0,0]\n"
            "v_pk_fma_f32 v[28:29], v[172:173], v[188:189], v[28:29] op_sel:[1,0,0]\n"
            "v_pk_fma_f32 v[30:31], v[172:173], v[190:191], v[30:31] op_sel:[1,0,0]\n"
            "s_cmp_le_u32 %[njl], 2\n"
            "s_cbranch_scc1 Ltail_%=\n"
            "s_waitcnt vmcnt(4)\n"
            "v_mfma_f32_4x4x4_16b_bf16 v[176:179], v[208:209], v[72:73], 0 cbsz:2 abid:2\n"
            "v_mfma_f32_4x4x4_16b_bf16 v[180:183], v[208:209], v[74:75], 0 cbsz:2 abid:2\n"
            "v_mfma_f32_4x4x4_16b_bf16 v[184:187], v[208:209], v[76:77], 0 cbsz:2 abid:2\n"
            "v_mfma_f32_4x4x4_16b_bf16 v[188:191], v[208:209], v[78:79], 0 cbsz:2 abid:2\n"
            "s_nop 1\n"
            "v_pk_fma_f32 v[16:17], v[162:163], v[176:177], v[16:17] op_sel_hi:[0,1,1]\n"
            "v_pk_fma_f32 v[18:19], v[162:163], v[178:179], v[18:19] op_sel_hi:[0,1,1]\n"
            "v_pk_fma_f32 v[20:21], v[166:167], v[180:181], v[20:21] op_sel_hi:[0,1,1]\n"
            "v_pk_fma_f32 v[22:23], v[166:167], v[182:183], v[22:23] op_sel_hi:[0,1,1]\n"
            "v_pk_fma_f32 v[24:25], v[170:171], v[184:185], v[24:25] op_sel_hi:[0,1,1]\n"
            "v_pk_fma_f32 v[26:27], v[170:171], v[186:187], v[26:27] op_sel_hi:[0,1,1]\n"
            "v_pk_fma_f32 v[28:29], v[174:175], v[188:189], v[28:29] op_sel_hi:[0,1,1]\n"
            "v_pk_fma_f32 v[30:31], v[174:175], v[190:191], v[30:31] op_sel_hi:[0,1,1]\n"
            "s_cmp_le_u32 %[njl], 3\n"
            "s_cbranch_scc1 Ltail_%=\n"
            "v_mfma_f32_4x4x4_16b_bf16 v[176:179], v[208:209], v[88:89], 0 cbsz:2 abid:3\n"
            "v_mfma_f32_4x4x4_16b_bf16 v[180:183], v[208:209], v[90:91], 0 cbsz:2 abid:3\n"
            "v_mfma_f32_4x4x4_16b_bf16 v[184:187], v[208:209], v[92:93], 0 cbsz:2 abid:3\n"
            "v_mfma_f32_4x4x4_16b_bf16 v[188:191], v[208:209], v[94:95], 0 cbsz:2 abid:3\n"
            "s_nop 1\n"
            "v_pk_fma_f32 v[16:17], v[162:163], v[176:177], v[16:17] op_sel:[1,0,0]\n"
            "v_pk_fma_f32 v[18:19], v[162:163], v[178:179], v[18:19] op_sel:[1,0,0]\n"
            "v_pk_fma_f32 v[20:21], v[166:167], v[180:181], v[20:21] op_sel:[1,0,0]\n"
            "v_pk_fma_f32 v[22:23], v[166:167], v[182:183], v[22:23] op_sel:[1,0,0]\n"
            "v_pk_fma_f32 v[24:25], v[170:171], v[184:185], v[24:25] op_sel:[1,0,0]\n"
            "v_pk_fma_f32 v[26:27], v[170:171], v[186:187], v[26:27] op_sel:[1,0,0]\n"
            "v_pk_fma_f32 v[28:29], v[174:175], v[188:189], v[28:29] op_sel:[1,0,0]\n"
            "v_pk_fma_f32 v[30:31], v[174:175], v[190:191], v[30:31] op_sel:[1,0,0]\n"
            "Ltail_%=:\n"
            "s_setprio 3\n"
            "s_waitcnt vmcnt(4)\n"
            "ds_read_b128 v[72:75], v228 offset:25600\n"
            "ds_read_b128 v[76:79], v228 offset:26624\n"
            "ds_read_b128 v[88:91], v228 offset:27648\n"
            "ds_read_b128 v[92:95], v228 offset:28672\n"
            "ds_read_b128 v[160:163], v228 offset:29696\n"
            "ds_read_b128 v[164:167], v228 offset:30720\n"
            "ds_read_b128 v[168:171], v228 offset:31744\n"
            "s_cmp_lg_u32 %[nst], %[tag]\n"
            "s_cbranch_scc1 Lspin_%=\n"
            "Lgo_%=:\n"
            "buffer_load_dwordx4 v[40:43], v218, %[rsw], %[oagg] offen offset:0 sc1\n"
            "buffer_load_dwordx4 v[44:47], v218, %[rsw], %[oagg] offen offset:256 sc1\n"
            "buffer_load_dwordx4 v[56:59], v218, %[rsw], %[oagg] offen offset:512 sc1\n"
            "buffer_load_dwordx4 v[60:63], v218, %[rsw], %[oagg] offen offset:768 sc1\n"
            "s_waitcnt vmcnt(4)\n"
            "v_mfma_f32_4x4x4_16b_bf16 v[0:3], v[224:225], v[32:33], v[0:3]\n"
            "v_mfma_f32_4x4x4_16b_bf16 v[4:7], v[224:225], v[34:35], v[4:7]\n"
            "v_mfma_f32_4x4x4_16b_bf16 v[8:11], v[224:225], v[36:37], v[8:11]\n"
            "v_mfma_f32_4x4x4_16b_bf16 v[12:15], v[224:225], v[38:39], v[12:15]\n"
            "v_mfma_f32_4x4x4_16b_bf16 v[16:19], v[224:225], v[48:49], v[16:19]\n"
            "v_mfma_f32_4x4x4_16b_bf16 v[20:23], v[224:225], v[50:51], v[20:23]\n"
            "v_mfma_f32_4x4x4_16b_bf16 v[24:27], v[224:225], v[52:53], v[24:27]\n"
            "v_mfma_f32_4x4x4_16b_bf16 v[28:31], v[224:225], v[54:55], v[28:31]\n"
            "s_nop 1\n"
            "v_cvt_pk_bf16_f32 v176, v0, v4\n"
            "v_cvt_pk_bf16_f32 v177, v8, v12\n"
            "v_cvt_pk_bf16_f32 v178, v16, v20\n"
            "v_cvt_pk_bf16_f32 v179, v24, v28\n"
            "ds_write_b128 v226, v[176:179] offset:0\n"
            "v_cvt_pk_bf16_f32 v180, v1, v5\n"
            "v_cvt_pk_bf16_f32 v181, v9, v13\n"
            "v_cvt_pk_bf16_f32 v182, v17, v21\n"
            "v_cvt_pk_bf16_f32 v183, v25, v29\n"
            "ds_write_b128 v226, v[180:183] offset:272\n"
            "v_cvt_pk_bf16_f32 v184, v2, v6\n"
            "v_cvt_pk_bf16_f32 v185, v10, v14\n"
            "v_cvt_pk_bf16_f32 v186, v18, v22\n"
            "v_cvt_pk_bf16_f32 v187, v26, v30\n"
            "ds_write_b128 v226, v[184:187] offset:544\n"
            "v_cvt_pk_bf16_f32 v188, v3, v7\n"
            "v_cvt_pk_bf16_f32 v189, v11, v15\n"
            "v_cvt_pk_bf16_f32 v190, v19, v23\n"
            "v_cvt_pk_bf16_f32 v191, v27, v31\n"
            "ds_write_b128 v226, v[188:191] offset:816\n"
            "ds_read_b128 v[176:179], v227 offset:0\n"
            "ds_read_b128 v[180:183], v227 offset:64\n"
            "ds_read_b128 v[184:187], v227 offset:128\n"
            "ds_read_b128 v[188:191], v227 offset:192\n"
            "s_waitcnt lgkmcnt(3)\n"
            "v_mfma_f32_16x16x32_bf16 v[0:3], v[176:179], v[236:239], 0\n"
            "v_mfma_f32_16x16x32_bf16 v[4:7], v[176:179], v[96:99], 0\n"
            "s_waitcnt lgkmcnt(2)\n"
            "v_mfma_f32_16x16x32_bf16 v[0:3], v[180:183], v[240:243], v[0:3]\n"
            "v_mfma_f32_16x16x32_bf16 v[4:7], v[180:183], v[100:103], v[4:7]\n"
            "s_waitcnt lgkmcnt(1)\n"
            "v_mfma_f32_16x16x32_bf16 v[0:3], v[184:187], v[244:247], v[0:3]\n"
            "v_mfma_f32_16x16x32_bf16 v[4:7], v[184:187], v[104:107], v[4:7]\n"
            "s_waitcnt lgkmcnt(0)\n"
            "v_mfma_f32_16x16x32_bf16 v[0:3], v[188:191], v[248:251], v[0:3]\n"
            "v_mfma_f32_16x16x32_bf16 v[4:7], v[188:191], v[108:111], v[4:7]\n"
            "v_mfma_f32_16x16x32_bf16 v[8:11], v[176:179], v[112:115], 0\n"
            "v_mfma_f32_16x16x32_bf16 v[12:15], v[176:179], v[204:207], 0\n"
            "v_mfma_f32_16x16x32_bf16 v[8:11], v[180:183], v[116:119], v[8:11]\n"
            "v_mfma_f32_16x16x32_bf16 v[12:15], v[180:183], v[232:235], v[12:15]\n"
            "v_mfma_f32_16x16x32_bf16 v[8:11], v[184:187], v[196:199], v[8:11]\n"
            "v_mfma_f32_16x16x32_bf16 v[12:15], v[184:187], v[120:123], v[12:15]\n"
            "v_mfma_f32_16x16x32_bf16 v[8:11], v[188:191], v[200:203], v[8:11]\n"
            "v_mfma_f32_16x16x32_bf16 v[12:15], v[188:191], v[124:127], v[12:15]\n"
            "v_cvt_pk_bf16_f32 v0, v0, v1\n"
            "v_cvt_pk_bf16_f32 v1, v2, v3\n"
            "v_cvt_pk_bf16_f32 v2, v4, v5\n"
            "v_cvt_pk_bf16_f32 v3, v6, v7\n"
            "buffer_store_dwordx4 v[0:3], v217, %[rso], 0 offen offset:0 sc1\n"
            "v_mfma_f32_16x16x32_bf16 v[16:19], v[176:179], v[192:195], 0\n"
            "v_mfma_f32_16x16x32_bf16 v[20:23], v[176:179], v[140:143], 0\n"
            "v_mfma_f32_16x16x32_bf16 v[16:19], v[180:183], v[128:131], v[16:19]\n"
            "v_mfma_f32_16x16x32_bf16 v[20:23], v[180:183], v[144:147], v[20:23]\n"
            "v_mfma_f32_16x16x32_bf16 v[16:19], v[184:187], v[132:135], v[16:19]\n"
            "v_mfma_f32_16x16x32_bf16 v[20:23], v[184:187], v[148:151], v[20:23]\n"
            "v_mfma_f32_16x16x32_bf16 v[16:19], v[188:191], v[136:139], v[16:19]\n"
            "v_mfma_f32_16x16x32_bf16 v[20:23], v[188:191], v[152:155], v[20:23]\n"
            "v_cvt_pk_bf16_f32 v8, v8, v9\n"
            "v_cvt_pk_bf16_f32 v9, v10, v11\n"
            "v_cvt_pk_bf16_f32 v10, v12, v13\n"
            "v_cvt_pk_bf16_f32 v11, v14, v15\n"
            "buffer_store_dwordx4 v[8:11], v217, %[rso], 0 offen offset:256 sc1\n"
            "v_mfma_f32_16x16x32_bf16 v[24:27], v[176:179], v[156:159], 0\n"
            "v_mfma_f32_16x16x32_bf16 v[28:31], v[176:179], v[92:95], 0\n"
            "v_mfma_f32_16x16x32_bf16 v[24:27], v[180:183], v[72:75], v[24:27]\n"
            "v_mfma_f32_16x16x32_bf16 v[28:31], v[180:183], v[160:163], v[28:31]\n"
            "v_mfma_f32_16x16x32_bf16 v[24:27], v[184:187], v[76:79], v[24:27]\n"
            "v_mfma_f32_16x16x32_bf16 v[28:31], v[184:187], v[164:167], v[28:31]\n"
            "v_mfma_f32_16x16x32_bf16 v[24:27], v[188:191], v[88:91], v[24:27]\n"
            "v_mfma_f32_16x16x32_bf16 v[28:31], v[188:191], v[168:171], v[28:31]\n"
            "v_cvt_pk_bf16_f32 v16, v16, v17\n"
            "v_cvt_pk_bf16_f32 v17, v18, v19\n"
            "v_cvt_pk_bf16_f32 v18, v20, v21\n"
            "v_cvt_pk_bf16_f32 v19, v22, v23\n"
            "buffer_store_dwordx4 v[16:19], v217, %[rso], 0 offen offset:512 sc1\n"
            "s_nop 2\n"
            "v_cvt_pk_bf16_f32 v24, v24, v25\n"
            "v_cvt_pk_bf16_f32 v25, v26, v27\n"
            "v_cvt_pk_bf16_f32 v26, v28, v29\n"
            "v_cvt_pk_bf16_f32 v27, v30, v31\n"
            "buffer_store_dwordx4 v[24:27], v217, %[rso], 0 offen offset:768 sc1\n"
            "s_cmp_lt_u32 %[two], 6\n"
            "s_cbranch_scc1 Lend_%=\n"
            "ds_read_b128 v[64:67], v231 offset:0\n"
            "ds_read_b128 v[68:71], v231 offset:1024\n"
            "ds_read_b128 v[80:83], v231 offset:2048\n"
            "ds_read_b128 v[84:87], v231 offset:3072\n"
            "s_waitcnt vmcnt(4) lgkmcnt(0)\n"
            "v_mfma_f32_4x4x4_16b_bf16 v[0:3], v[224:225], v[64:65], 0\n"
            "v_mfma_f32_4x4x4_16b_bf16 v[4:7], v[224:225], v[66:67], 0\n"
            "v_mfma_f32_4x4x4_16b_bf16 v[8:11], v[224:225], v[68:69], 0\n"
            "v_mfma_f32_4x4x4_16b_bf16 v[12:15], v[224:225], v[70:71], 0\n"
            "v_mfma_f32_4x4x4_16b_bf16 v[16:19], v[224:225], v[80:81], 0\n"
            "v_mfma_f32_4x4x4_16b_bf16 v[20:23], v[224:225], v[82:83], 0\n"
            "v_mfma_f32_4x4x4_16b_bf16 v[24:27], v[224:225], v[84:85], 0\n"
            "v_mfma_f32_4x4x4_16b_bf16 v[28:31], v[224:225], v[86:87], 0\n"
            "v_mfma_f32_4x4x4_16b_bf16 v[0:3], v[224:225], v[40:41], v[0:3]\n"
            "v_mfma_f32_4x4x4_16b_bf16 v[4:7], v[224:225], v[42:43], v[4:7]\n"
            "v_mfma_f32_4x4x4_16b_bf16 v[8:11], v[224:225], v[44:45], v[8:11]\n"
            "v_mfma_f32_4x4x4_16b_bf16 v[12:15], v[224:225], v[46:47], v[12:15]\n"
            "v_mfma_f32_4x4x4_16b_bf16 v[16:19], v[224:225], v[56:57], v[16:19]\n"
            "v_mfma_f32_4x4x4_16b_bf16 v[20:23], v[224:225], v[58:59], v[20:23]\n"
            "v_mfma_f32_4x4x4_16b_bf16 v[24:27], v[224:225], v[60:61], v[24:27]\n"
            "v_mfma_f32_4x4x4_16b_bf16 v[28:31], v[224:225], v[62:63], v[28:31]\n"
            "s_nop 1\n"
            "v_cvt_pk_bf16_f32 v176, v0, v4\n"
            "v_cvt_pk_bf16_f32 v177, v8, v12\n"
            "v_cvt_pk_bf16_f32 v178, v16, v20\n"
            "v_cvt_pk_bf16_f32 v179, v24, v28\n"
            "ds_write_b128 v226, v[176:179] offset:0\n"
            "v_cvt_pk_bf16_f32 v180, v1, v5\n"
            "v_cvt_pk_bf16_f32 v181, v9, v13\n"
            "v_cvt_pk_bf16_f32 v182, v17, v21\n"
            "v_cvt_pk_bf16_f32 v183, v25, v29\n"
            "ds_write_b128 v226, v[180:183] offset:272\n"
            "v_cvt_pk_bf16_f32 v184, v2, v6\n"
            "v_cvt_pk_bf16_f32 v185, v10, v14\n"
            "v_cvt_pk_bf16_f32 v186, v18, v22\n"
            "v_cvt_pk_bf16_f32 v187, v26, v30\n"
            "ds_write_b128 v226, v[184:187] offset:544\n"
            "v_cvt_pk_bf16_f32 v188, v3, v7\n"
            "v_cvt_pk_bf16_f32 v189, v11, v15\n"
            "v_cvt_pk_bf16_f32 v190, v19, v23\n"
            "v_cvt_pk_bf16_f32 v191, v27, v31\n"
            "ds_write_b128 v226, v[188:191] offset:816\n"
            "ds_read_b128 v[176:179], v227 offset:0\n"
            "ds_read_b128 v[180:183], v227 offset:64\n"
            "ds_read_b128 v[184:187], v227 offset:128\n"
            "ds_read_b128 v[188:191], v227 offset:192\n"
            "s_waitcnt lgkmcnt(3)\n"
            "v_mfma_f32_16x16x32_bf16 v[0:3], v[176:179], v[236:239], 0\n"
            "v_mfma_f32_16x16x32_bf16 v[4:7], v[176:179], v[96:99], 0\n"
            "s_waitcnt lgkmcnt(2)\n"
            "v_mfma_f32_16x16x32_bf16 v[0:3], v[180:183], v[240:243], v[0:3]\n"
            "v_mfma_f32_16x16x32_bf16 v[4:7], v[180:183], v[100:103], v[4:7]\n"
            "s_waitcnt lgkmcnt(1)\n"
            "v_mfma_f32_16x16x32_bf16 v[0:3], v[184:187], v[244:247], v[0:3]\n"
            "v_mfma_f32_16x16x32_bf16 v[4:7], v[184:187], v[104:107], v[4:7]\n"
            "s_waitcnt lgkmcnt(0)\n"
            "v_mfma_f32_16x16x32_bf16 v[0:3], v[188:191], v[248:251], v[0:3]\n"
            "v_mfma_f32_16x16x32_bf16 v[4:7], v[188:191], v[108:111], v[4:7]\n"
            "v_mfma_f32_16x16x32_bf16 v[8:11], v[176:179], v[112:115], 0\n"
            "v_mfma_f32_16x16x32_bf16 v[12:15], v[176:179], v[204:207], 0\n"
            "v_mfma_f32_16x16x32_bf16 v[8:11], v[180:183], v[116:119], v[8:11]\n"
            "v_mfma_f32_16x16x32_bf16 v[12:15], v[180:183], v[232:235], v[12:15]\n"
            "v_mfma_f32_16x16x32_bf16 v[8:11], v[184:187], v[196:199], v[8:11]\n"
            "v_mfma_f32_16x16x32_bf16 v[12:15], v[184:187], v[120:123], v[12:15]\n"
            "v_mfma_f32_16x16x32_bf16 v[8:11], v[188:191], v[200:203], v[8:11]\n"
            "v_mfma_f32_16x16x32_bf16 v[12:15], v[188:191], v[124:127], v[12:15]\n"
            "v_cvt_pk_bf16_f32 v0, v0, v1\n"
            "v_cvt_pk_bf16_f32 v1, v2, v3\n"
            "v_cvt_pk_bf16_f32 v2, v4, v5\n"
            "v_cvt_pk_bf16_f32 v3, v6, v7\n"
            "buffer_store_dwordx4 v[0:3], v218, %[rso], 0 offen offset:0 sc1\n"
            "v_mfma_f32_16x16x32_bf16 v[16:19], v[176:179], v[192:195], 0\n"
            "v_mfma_f32_16x16x32_bf16 v[20:23], v[176:179], v[140:143], 0\n"
            "v_mfma_f32_16x16x32_bf16 v[16:19], v[180:183], v[128:131], v[16:19]\n"
            "v_mfma_f32_16x16x32_bf16 v[20:23], v[180:183], v[144:147], v[20:23]\n"
            "v_mfma_f32_16x16x32_bf16 v[16:19], v[184:187], v[132:135], v[16:19]\n"
            "v_mfma_f32_16x16x32_bf16 v[20:23], v[184:187], v[148:151], v[20:23]\n"
            "v_mfma_f32_16x16x32_bf16 v[16:19], v[188:191], v[136:139], v[16:19]\n"
            "v_mfma_f32_16x16x32_bf16 v[20:23], v[188:191], v[152:155], v[20:23]\n"
            "v_cvt_pk_bf16_f32 v8, v8, v9\n"
            "v_cvt_pk_bf16_f32 v9, v10, v11\n"
            "v_cvt_pk_bf16_f32 v10, v12, v13\n"
            "v_cvt_pk_bf16_f32 v11, v14, v15\n"
            "buffer_store_dwordx4 v[8:11], v218, %[rso], 0 offen offset:256 sc1\n"
            "v_mfma_f32_16x16x32_bf16 v[24:27], v[176:179], v[156:159], 0\n"
            "v_mfma_f32_16x16x32_bf16 v[28:31], v[176:179], v[92:95], 0\n"
            "v_mfma_f32_16x16x32_bf16 v[24:27], v[180:183], v[72:75], v[24:27]\n"
            "v_mfma_f32_16x16x32_bf16 v[28:31], v[180:183], v[160:163], v[28:31]\n"
            "v_mfma_f32_16x16x32_bf16 v[24:27], v[184:187], v[76:79], v[24:27]\n"
            "v_mfma_f32_16x16x32_bf16 v[28:31], v[184:187], v[164:167], v[28:31]\n"
            "v_mfma_f32_16x16x32_bf16 v[24:27], v[188:191], v[88:91], v[24:27]\n"
            "v_mfma_f32_16x16x32_bf16 v[28:31], v[188:191], v[168:171], v[28:31]\n"
            "v_cvt_pk_bf16_f32 v16, v16, v17\n"
            "v_cvt_pk_bf16_f32 v17, v18, v19\n"
            "v_cvt_pk_bf16_f32 v18, v20, v21\n"
            "v_cvt_pk_bf16_f32 v19, v22, v23\n"
            "buffer_store_dwordx4 v[16:19], v218, %[rso], 0 offen offset:512 sc1\n"
            "s_nop 2\n"
            "v_cvt_pk_bf16_f32 v24, v24, v25\n"
            "v_cvt_pk_bf16_f32 v25, v26, v27\n"
            "v_cvt_pk_bf16_f32 v26, v28, v29\n"
            "v_cvt_pk_bf16_f32 v27, v30, v31\n"
            "buffer_store_dwordx4 v[24:27], v218, %[rso], 0 offen offset:768 sc1\n"
            "Lend_%=:\n"
            "s_waitcnt vmcnt(4)\n"
            : "+{v[32:63]}"(oxa), "+{v[64:95]}"(oxb), "+{v[96:127]}"(owr), "+{v[192:195]}"(efA), "+{v[196:199]}"(S), "+{v[200:203]}"(cgA), "+{v[208:223]}"(omi), "+{v[224:231]}"(ot),
              "+{v[232:235]}"(efN),
              [rem] "+s"(rem), [lens] "+s"(lens), [fm] "+s"(fm), [nst] "+s"(nst), [ra] "=&s"(ra), [tmp] "=&s"(tmp)
            : [rse] "s"(de), [rsx] "s"(dx), [rso] "s"(mkdesc(out, (unsigned)N * 1024u)), [rsw] "s"(mkdesc(ws, W_END)),
              [cnt] "s"(lds0 + L_MISC + 4), [tag] "s"(tag), [flg] "s"(ws + W_FLG + bx * 128), [pfl] "s"(ws + W_FLG + (bx ^ 4) * 128),
              [njl] "s"(((e2 - 1) & 3) + 1), [two] "s"(w), [evt] "s"((unsigned)__builtin_amdgcn_readfirstlane(w < 4 ? evt_of(PTO) : (w < 6 ? evt_of(PTY) : evt_of(PTZ)))), [ptab] "s"((unsigned)__builtin_amdgcn_readfirstlane(w < 4 ? PTO : (w < 6 ? PTY : PTZ))), [oagg] "s"(W_AGG), [spin] "s"(SPIN_MAX)
            : "memory", "vcc", "scc", "v0", "v1", "v2", "v3", "v4", "v5", "v6", "v7", "v8", "v9", "v10", "v11", "v12", "v13", "v14", "v15", "v16", "v17", "v18", "v19", "v20", "v21", "v22", "v23", "v24", "v25", "v26", "v27", "v28", "v29", "v30", "v31", "v128", "v129", "v130", "v131", "v132", "v133", "v134", "v135", "v136", "v137", "v138", "v139", "v140", "v141", "v142", "v143", "v144", "v145", "v146", "v147", "v148", "v149", "v150", "v151", "v152", "v153", "v154", "v155", "v156", "v157", "v158", "v159", "v160", "v161", "v162", "v163", "v164", "v165", "v166", "v167", "v168", "v169", "v170", "v171", "v172", "v173", "v174", "v175", "v176", "v177", "v178", "v179", "v180", "v181", "v182", "v183", "v184", "v185", "v186", "v187", "v188", "v189", "v190", "v191", "v204", "v205", "v206", "v207", "v236", "v237", "v238", "v239", "v240", "v241", "v242", "v243", "v244", "v245", "v246", "v247", "v248", "v249", "v250", "v251");
    } else {
        if (bx == 0 && w == 4) flag_store(ws + W_CTL, tag);
        const __amdgpu_buffer_rsrc_t rso = __builtin_amdgcn_make_buffer_rsrc((void*)out, 0, N * 1024, 0x00020000);
#pragma unroll 1
        for (int it = 0; it < 10; ++it) {
            const int nl = (it < 5 ? 40 - 40 * half + 8 * it : 40 * half + 8 * (it - 5)) + w, node = node0 + nl;
            const bool nv = nl < npb;
            const unsigned ro = nv ? (unsigned)node * 1024u + (unsigned)lane * 16u : OOB_OFF;
            float a8[8] = {0.f, 0.f, 0.f, 0.f, 0.f, 0.f, 0.f, 0.f};
#pragma unroll 1
            for (int e0 = 0; e0 < (nv ? E : 0); e0 += 64) {
                const int ec = min(e0 + lane, E - 1);
                u64 mm = __ballot(e0 + lane < E && dst[ec] == node && (int)(((src[ec] >> 3) ^ (smask >> ((src[ec] >> 9) & 31))) & 1) == half);
#pragma unroll 1
                while (mm) {
                    const int e = e0 + __builtin_ctzll(mm);
                    mm &= mm - 1;
                    const unsigned short* fe = (const unsigned short*)ef + (size_t)e * DE_DIM;
                    const unsigned short* ye = (const unsigned short*)Y + (size_t)e * M_DIM;
                    const unsigned short* xp = x + (size_t)src[e] * (C_DIM * M_DIM) + lane * 8;
                    float z0 = 0.f, z1 = 0.f;
#pragma unroll 1
                    for (int f = 0; f < DE_DIM; ++f) { z0 += bf2f(fe[f]) * bf2f(w_rad[f * C_DIM + 2 * lane]); z1 += bf2f(fe[f]) * bf2f(w_rad[f * C_DIM + 2 * lane + 1]); }
                    const float r0 = z0 / (1.f + __expf(-z0)), r1 = z1 / (1.f + __expf(-z1));
#pragma unroll 1
                    for (int i = 0; i < 4; ++i) {
                        const float x0 = bf2f(xp[i]) * r0, x1 = bf2f(xp[4 + i]) * r1;
#pragma unroll
                        for (int o = 0; o < 4; ++o) {
                            float wv4 = 0.f;
#pragma unroll
                            for (int f = 0; f < 4; ++f) wv4 += bf2f(cg[(i * 4 + f) * 4 + o]) * bf2f(ye[f]);
                            a8[o] += x0 * wv4; a8[4 + o] += x1 * wv4;
                        }
                    }
                }
            }
            if (it >= 5) {
                const u32x4 pv = __builtin_amdgcn_raw_buffer_load_b128(rsw, ro, W_AGG, SC1);
#pragma unroll
                for (int i = 0; i < 4; ++i) { a8[2 * i] += __uint_as_float(pv[i] << 16); a8[2 * i + 1] += __uint_as_float(pv[i] & 0xffff0000u); }
            }
            const u32x4 rv = (u32x4){pack2bf(a8[0], a8[1]), pack2bf(a8[2], a8[3]), pack2bf(a8[4], a8[5]), pack2bf(a8[6], a8[7])};
            if (it < 5) {
                __builtin_amdgcn_raw_buffer_store_b128(rv, rsw, ro, W_AGG, SC1);
                if (it == 4) {
                    asm volatile("s_waitcnt vmcnt(0)" ::: "memory");
                    if (lds_arrive_rtn(lds0 + L_MISC + 4) == 7u) flag_store(ws + W_FLG + bx * 128, tag);
                    poll_tag(ws + W_FLG + (bx ^ 4) * 128, tag);
                }
            } else {
                *(u32x4*)(smem + L_PRV + w * 1024 + lane * 16) = rv;
#pragma unroll
                for (int i = 0; i < 8; ++i) a8[i] = 0.f;
#pragma unroll 1
                for (int c = 0; c < C_DIM; ++c) {
                    const unsigned short* ap = (const unsigned short*)(smem + L_PRV + w * 1024 + c * 8);
                    const float w0 = bf2f(w_mix[c * C_DIM + 2 * lane]), w1 = bf2f(w_mix[c * C_DIM + 2 * lane + 1]);
#pragma unroll
                    for (int m = 0; m < 4; ++m) { a8[m] += bf2f(ap[m]) * w0; a8[4 + m] += bf2f(ap[m]) * w1; }
                }
                __builtin_amdgcn_raw_buffer_store_b128((u32x4){pack2bf(a8[0], a8[1]), pack2bf(a8[2], a8[3]), pack2bf(a8[4], a8[5]), pack2bf(a8[6], a8[7])}, rso, ro, 0, 0);
            }
        }
    }
}

__global__ void kf_zero(float* __restrict__ agg, size_t n) {
    for (size_t i = (size_t)blockIdx.x * blockDim.x + threadIdx.x; i < n; i += (size_t)gridDim.x * blockDim.x) agg[i] = 0.f;
}
__global__ void kf_edges(const unsigned short* __restrict__ x, const int* __restrict__ src, const int* __restrict__ dst,
                         const unsigned short* __restrict__ Y, const unsigned short* __restrict__ ef,
                         const unsigned short* __restrict__ w_rad, const unsigned short* __restrict__ cg,
                         float* __restrict__ agg, int E) {
    const int ch = threadIdx.x;
    for (int e = blockIdx.x; e < E; e += gridDim.x) {
        float z = 0.f;
        for (int f = 0; f < DE_DIM; ++f) z += bf2f(ef[(size_t)e * DE_DIM + f]) * bf2f(w_rad[f * C_DIM + ch]);
        const float r = z / (1.f + __expf(-z));
        float a4[4] = {0.f, 0.f, 0.f, 0.f};
        for (int ii = 0; ii < 4; ++ii) {
            const float xv = bf2f(x[((size_t)src[e] * C_DIM + ch) * M_DIM + ii]) * r;
            for (int o = 0; o < 4; ++o) {
                float wv = 0.f;
                for (int f = 0; f < 4; ++f) wv += bf2f(cg[(ii * 4 + f) * 4 + o]) * bf2f(Y[(size_t)e * 4 + f]);
                a4[o] += xv * wv;
            }
        }
        for (int o = 0; o < 4; ++o) atomicAdd(&agg[((size_t)dst[e] * C_DIM + ch) * M_DIM + o], a4[o]);
    }
}
__global__ void kf_mix(const float* __restrict__ agg, const unsigned short* __restrict__ w_mix, unsigned short* __restrict__ out, int N) {
    const int d = threadIdx.x;
    for (int n = blockIdx.x; n < N; n += gridDim.x) {
        float a4[4] = {0.f, 0.f, 0.f, 0.f};
        for (int c = 0; c < C_DIM; ++c) {
            const float wv = bf2f(w_mix[c * C_DIM + d]);
            for (int m = 0; m < 4; ++m) a4[m] += bf2f(f2bf(agg[((size_t)n * C_DIM + c) * M_DIM + m])) * wv;
        }
        for (int m = 0; m < 4; ++m) out[((size_t)n * C_DIM + d) * M_DIM + m] = f2bf(a4[m]);
    }
}

extern "C" void kernel_launch(void* const* d_in, const int* in_sizes, int n_in,
                              void* d_out, int out_size, void* d_ws, size_t ws_size,
                              hipStream_t stream) {
    const unsigned short* x = (const unsigned short*)d_in[0];
    const int* src = (const int*)d_in[1];
    const int* dst = (const int*)d_in[2];
    const unsigned short* Y = (const unsigned short*)d_in[3];
    const unsigned short* ef = (const unsigned short*)d_in[4];
    const unsigned short* w_rad = (const unsigned short*)d_in[5];
    const unsigned short* cg = (const unsigned short*)d_in[6];
    const unsigned short* w_mix = (const unsigned short*)d_in[7];
    unsigned short* out = (unsigned short*)d_out;
    const int N = in_sizes[0] / (C_DIM * M_DIM);
    const int E = in_sizes[1];
    const int epb = (E + 254) / 255;
    const bool fast = N >= 256 && N <= NB_MAX * NPB_MAX && E > 0 && epb <= EPB_MAX && ws_size >= (size_t)W_END;
    if (!fast) {
        float* agg = (float*)d_ws;
        if (N <= 0) return;
        kf_zero<<<1024, 256, 0, stream>>>(agg, (size_t)N * C_DIM * M_DIM);
        if (E > 0) kf_edges<<<4096, C_DIM, 0, stream>>>(x, src, dst, Y, ef, w_rad, cg, agg, E);
        kf_mix<<<2048, C_DIM, 0, stream>>>(agg, w_mix, out, N);
        return;
    }
    const unsigned nb_magic = (unsigned)(((1ull << 39) + N - 1) / N);
    unsigned smask = 0;
    const int ng = (N + 511) / 512;
    int pw = 1;
    while (2 * pw * 4 < ng + 4) pw *= 2;
    for (int i = 0; i < ng; ++i) smask |= (unsigned)(i < 4 * pw ? ((i >> 2) ^ (i >= WLO * ng / 20 && i < WHI * ng / 20)) & 1 : i & 1) << i;
    k_fused<<<256, 512, 0, stream>>>(x, src, dst, (const u32x2*)Y, (const u32x4*)ef, w_rad, cg, w_mix, out, (unsigned char*)d_ws,
                                     N, E, epb, nb_magic, smask, (unsigned)(4 * 65536 / N));
}
```

```cpp
#include <hip/hip_runtime.h>
#include <hip/hip_bf16.h>
#include <stdint.h>

typedef __attribute__((ext_vector_type(8))) short bf16x8;
typedef __attribute__((ext_vector_type(4))) short bf16x4;
typedef __attribute__((ext_vector_type(4))) float f32x4;
typedef __attribute__((ext_vector_type(2))) unsigned u32x2;
typedef __attribute__((ext_vector_type(4))) unsigned u32x4;
typedef __attribute__((ext_vector_type(8))) unsigned u32x8;
typedef __attribute__((ext_vector_type(16))) unsigned u32x16;
typedef __attribute__((ext_vector_type(32))) unsigned u32x32;
typedef unsigned long long u64;

#define C_DIM 128
#define M_DIM 4
#define DE_DIM 16
#define NPB_MAX 80
#define NB_MAX 128
#define EPB_MAX 1280
#define NRND 7
#define SMAX 72
#define OOB_OFF 0x80000000u
#define SC1 16
#define NT 2
#define SPIN_MAX (1u << 22)
#define CODE_LINES 166
#define PTO 0xfffa9540u
#define PTY 0xffffa950u
#define PTZ 0xffffea54u
#define NT0 3
#define NT2 5
#define TLIST 8, 9, 4, 5, 10
#define WLO 8
#define WHI 11
#define CPRIO 2
#define WRPOS 1
#define MVPOS 0
#define MVN 64
#define PSL "1"
#define TKF 0x98765432u
#define TKB 0x23456789u
#define TKA 0x1010ffffu
#define EXTRA 0, 0, 7, 9

#define W_CTL 0
#define W_DEC 2048
#define W_FLG 4096
#define W_TAB (W_FLG + 32768)
#define W_RA (W_TAB + 256 * 256 * 8)
#define W_AGG (W_RA + 256 * EPB_MAX * 16)
#define W_END (W_AGG + NB_MAX * NPB_MAX * 1024)

#define SB() __builtin_amdgcn_sched_barrier(0)

static __device__ __forceinline__ float bf2f(unsigned short v) { return __uint_as_float(((unsigned)v) << 16); }
typedef __bf16 bf16v2 __attribute__((ext_vector_type(2)));
static __device__ __forceinline__ unsigned pack2bf(float lo, float hi) {
    bf16v2 r = (bf16v2){(__bf16)lo, (__bf16)hi};
    return __builtin_bit_cast(unsigned, r);
}
static __device__ __forceinline__ unsigned short f2bf(float v) { return (unsigned short)(pack2bf(v, 0.f) & 0xffffu); }
template <int CTRL, int RM> static __device__ __forceinline__ int dpp_add(int v) {
    return v + __builtin_amdgcn_update_dpp(0, v, CTRL, RM, 0xf, false);
}
static __device__ __forceinline__ int wave_incl_scan(int v) {
    v = dpp_add<0x111, 0xf>(v); v = dpp_add<0x112, 0xf>(v); v = dpp_add<0x114, 0xf>(v); v = dpp_add<0x118, 0xf>(v);
    v = dpp_add<0x142, 0xa>(v); v = dpp_add<0x143, 0xc>(v);
    return v;
}
template <int CTRL, int RM> static __device__ __forceinline__ unsigned dpp_max(unsigned v) {
    return max(v, (unsigned)__builtin_amdgcn_update_dpp(0, (int)v, CTRL, RM, 0xf, false));
}
static __device__ __forceinline__ unsigned wave_max_scan(unsigned v) {
    v = dpp_max<0x111, 0xf>(v); v = dpp_max<0x112, 0xf>(v); v = dpp_max<0x114, 0xf>(v); v = dpp_max<0x118, 0xf>(v);
    v = dpp_max<0x142, 0xa>(v); v = dpp_max<0x143, 0xc>(v);
    return v;
}
#define MAXS(c) "v_max_u32_dpp %0, %0, %0 " c "\n\tv_max_u32_dpp %1, %1, %1 " c "\n\tv_max_u32_dpp %2, %2, %2 " c "\n\t"
static __device__ __forceinline__ void wave_max_scan3(unsigned& a, unsigned& b, unsigned& c) {
    asm volatile("s_nop 1\n\t" MAXS("row_shr:1 row_mask:0xf bank_mask:0xf bound_ctrl:1") MAXS("row_shr:2 row_mask:0xf bank_mask:0xf bound_ctrl:1")
                 MAXS("row_shr:4 row_mask:0xf bank_mask:0xf bound_ctrl:1") MAXS("row_shr:8 row_mask:0xf bank_mask:0xf bound_ctrl:1")
                 MAXS("row_bcast:15 row_mask:0xa bank_mask:0xf") MAXS("row_bcast:31 row_mask:0xc bank_mask:0xf") "s_nop 0" : "+v"(a), "+v"(b), "+v"(c));
}
static __device__ __forceinline__ unsigned bsum(unsigned c, unsigned m, unsigned acc = 0u) { return __builtin_amdgcn_udot4(c, m, acc, false); }
static __device__ __forceinline__ unsigned bytesum2(unsigned c0, unsigned c1, unsigned m0, unsigned m1, unsigned acc = 0u) {
    return __builtin_amdgcn_udot4(c1, m1, __builtin_amdgcn_udot4(c0, m0, acc, false), false);
}
static __device__ __forceinline__ unsigned below_mask(int w, int k) {
    const int n = w - 4 * k;
    return n >= 4 ? 0x01010101u : (n <= 0 ? 0u : (0x01010101u & ((1u << (8 * n)) - 1u)));
}
static __device__ __forceinline__ void flag_store(const void* p, unsigned v) {
    unsigned t0, t1; u64 sv;
    asm volatile("v_mov_b32 %0, 0\n\tv_mov_b32 %1, %4\n\ts_mov_b64 %2, exec\n\ts_mov_b64 exec, 1\n\t"
                 "global_store_dword %0, %1, %3 sc1\n\ts_mov_b64 exec, %2" : "=&v"(t0), "=&v"(t1), "=&s"(sv) : "s"(p), "s"(v) : "memory");
}
static __device__ __forceinline__ unsigned lds_arrive_rtn(unsigned a) {
    unsigned t0, t1; u64 sv;
    asm volatile("v_mov_b32 %0, %3\n\tv_mov_b32 %1, 1\n\ts_mov_b64 %2, exec\n\ts_mov_b64 exec, 1\n\t"
                 "ds_add_rtn_u32 %1, %0, %1\n\ts_mov_b64 exec, %2\n\ts_waitcnt lgkmcnt(0)" : "=&v"(t0), "=&v"(t1), "=&s"(sv) : "s"(a) : "memory");
    return (unsigned)__builtin_amdgcn_readfirstlane((int)t1);
}
static __device__ __forceinline__ void lds_arrive(unsigned a, unsigned inc = 1u) {
    unsigned t0, t1; u64 sv;
    asm volatile("" "v_mov_b32 %0, %3\n\tv_mov_b32 %1, %4\n\ts_mov_b64 %2, exec\n\ts_mov_b64 exec, 1\n\t"
                 "ds_add_u32 %0, %1\n\ts_mov_b64 exec, %2" : "=&v"(t0), "=&v"(t1), "=&s"(sv) : "s"(a), "s"(inc) : "memory");
}
template <int SLEEP = 0, unsigned MASK = ~0u> static __device__ __forceinline__ unsigned lds_wait(unsigned a, unsigned n) {
    for (;;) {
        unsigned v;
        asm volatile("ds_read_b32 %0, %1\n\ts_waitcnt lgkmcnt(0)" : "=v"(v) : "v"(a) : "memory");
        v = (unsigned)__builtin_amdgcn_readfirstlane((int)v);
        if ((v & MASK) >= n) return v;
        if (SLEEP) __builtin_amdgcn_s_sleep(SLEEP);
    }
}
static __device__ __forceinline__ unsigned sel3(unsigned r, unsigned a, unsigned b, unsigned c) {
    asm volatile("" : "+v"(a), "+v"(b), "+v"(c));
    return r == 0u ? a : (r == 1u ? b : c);
}
static __device__ __forceinline__ unsigned ld_agent(const void* p) { return __hip_atomic_load((const unsigned*)p, __ATOMIC_RELAXED, __HIP_MEMORY_SCOPE_AGENT); }
static __device__ __forceinline__ void poll_tag(const void* p, unsigned tag) {
#pragma unroll 1
    for (unsigned spin = 0; spin < SPIN_MAX; ++spin) {
        if (__all(ld_agent(p) == tag)) break;
        __builtin_amdgcn_s_sleep(2);
    }
    __builtin_amdgcn_fence(__ATOMIC_ACQUIRE, "wavefront");
}
static __device__ __forceinline__ void wg_barrier() {
    asm volatile("s_waitcnt lgkmcnt(0)" ::: "memory");
    __builtin_amdgcn_s_barrier();
    asm volatile("" ::: "memory");
}
static __device__ __forceinline__ f32x4 mfma4(u32x2 a, u32x2 b, f32x4 c) {
    return __builtin_amdgcn_mfma_f32_4x4x4bf16_1k(__builtin_bit_cast(bf16x4, a), __builtin_bit_cast(bf16x4, b), c, 0, 0, 0);
}
static __device__ __forceinline__ f32x4 mfma16(u32x4 a, u32x4 b, f32x4 c) {
    return __builtin_amdgcn_mfma_f32_16x16x32_bf16(__builtin_bit_cast(bf16x8, a), __builtin_bit_cast(bf16x8, b), c, 0, 0, 0);
}

static constexpr unsigned evt_of(unsigned pt) {
    unsigned e = 6u;
    for (int n = 2; n < 15; ++n) if (((pt >> (2 * n)) & 3u) != ((pt >> (2 * n + 2)) & 3u)) e |= 1u << n;
    return e;
}

static constexpr u64 wave_of(bool kept) {
    u64 r = 0;
    for (int w = 0; w < 8; ++w) {
        r |= (u64)w << (4 * (((kept ? TKF : TKB) >> (4 * w)) & 15u));
        if (w >= 4 && (w >= 6) == kept) r |= (u64)w << (4 * ((TKA >> (4 * w)) & 15u));
    }
    return r;
}
static constexpr unsigned outer_of(bool kept) { return 1u << ((TKA >> (kept ? 24 : 16)) & 15u) | 1u << ((TKA >> (kept ? 28 : 20)) & 15u); }

static constexpr u64 len_pos(bool kept) {
    u64 r = 0;
    for (int w = 0; w < 8; ++w) {
        r |= (u64)(kept ? 8 + w : w) << (4 * (((kept ? TKF : TKB) >> (4 * w)) & 15u));
        if (w >= 4 && (w >= 6) == kept) r |= (u64)(kept ? w : 4 + w) << (4 * ((TKA >> (4 * w)) & 15u));
    }
    return r;
}
static constexpr unsigned ring_const() {
    constexpr int e[4] = {EXTRA};
    unsigned r = 0;
    for (int i = 0; i < 4; ++i) r |= (unsigned)((4 + (-3 * e[i] - e[(i + 3) & 3] + e[(i + 2) & 3] + 3 * e[(i + 1) & 3]) / 2) & 255) << (8 * i);
    return r;
}

#define L_MISC 0
#define L_KEY 96
#define L_AT 416
#define L_LEN 1056
#define L_PART 1088
#define L_PCNT 1280
#define L_NB L_PCNT
#define L_CCNT (L_PCNT + 2048)
#define L_BNC (L_CCNT + 1280)
#define L_CG (L_BNC + 4096)
#define L_PRV (L_CG + 1024)
#define A_STG L_PRV
#define A_PTAB (A_STG + EPB_MAX * 16)
#define PK_STR 8704
#define L_PK (L_PRV + 8 * SMAX * 64)
#define L_DUM L_PK
#define L_CTAB (L_PK + 2048)
#define L_MRK (L_PK + PK_STR)
#define L_WR (L_PK + 7 * PK_STR)
#define L_IMG (L_PK + 8 * PK_STR)
#define L_TOTAL (L_IMG + 32768)
static_assert(L_TOTAL <= 163840 && L_BNC <= 8192, "LDS budget");
static_assert(L_PRV % 16 == 0 && L_PK % 16 == 0 && L_IMG % 16 == 0 && L_CG % 16 == 0 && L_CCNT % 16 == 0, "alignment");
enum { SY_CPY = 40, SY_C1 = 44, SY_S = 48, SY_C2 = 52, SY_C3 = 56, SY_RI = 60, SY_PRE = 64 };

static __device__ __forceinline__ u32x4 mkdesc(const void* p, unsigned bytes) {
    return (u32x4){(unsigned)(uintptr_t)p, (unsigned)((uintptr_t)p >> 32) & 0xffffu, bytes, 0x00020000u};
}

__global__ __launch_bounds__(512, 2) void k_fused(
    const unsigned short* __restrict__ x, const int* __restrict__ src, const int* __restrict__ dst, const u32x2* __restrict__ Y,
    const u32x4* __restrict__ ef, const unsigned short* __restrict__ w_rad, const unsigned short* __restrict__ cg,
    const unsigned short* __restrict__ w_mix, unsigned short* __restrict__ out, unsigned char* ws,
    int N, int E, int epb, unsigned nb_magic, unsigned smask, unsigned cmul)
{
    __shared__ __attribute__((aligned(256))) unsigned char smem[L_TOTAL];
    const int tid = threadIdx.x, lane = tid & 63;
    const int w = __builtin_amdgcn_readfirstlane(tid >> 6);
    const int bx = blockIdx.x;
    int half;
    const int b = (bx & 3) * 32 + (bx >> 3);
    const int g = lane >> 4, c16 = lane & 15;
    const int node0 = (b * N + 127) >> 7, npb = (((b + 1) * N + 127) >> 7) - node0;
    unsigned cp;
    {
        u64 pc;
        asm volatile("s_getpc_b64 %0" : "=s"(pc));
        const __amdgpu_buffer_rsrc_t rsc = __builtin_amdgcn_make_buffer_rsrc((void*)(pc & ~(u64)127), 0, CODE_LINES * 128, 0x00020000);
        cp = __builtin_amdgcn_raw_buffer_load_b32(rsc, (tid >> 3) == 32 ? ((bx >> 3) + lane * 32) * 128 : OOB_OFF, 0, 0);
        SB();
    }
    const unsigned lds0 = (unsigned)(uintptr_t)(__attribute__((address_space(3))) unsigned char*)smem;
    const __amdgpu_buffer_rsrc_t rsw = __builtin_amdgcn_make_buffer_rsrc((void*)ws, 0, W_END, 0x00020000);
    asm volatile("" :: "s"(x), "s"(src), "s"(dst), "s"(Y), "s"(ef), "s"(w_rad), "s"(cg), "s"(w_mix), "s"(out), "s"(ws), "s"(N), "s"(E), "s"(epb), "s"(nb_magic),
                 "s"(smask), "s"(cmul));
    const int base_e = bx * epb - epb;
    const int ne = base_e < 0 ? 0 : max(min(epb, E - base_e), 0);
    auto task_at = [&](unsigned wq, int t) -> unsigned {
        const unsigned q = (t == 0 ? TKF : (t == 1 ? TKB : TKA)) >> (4 * wq) & 15u;
        return q == 15u ? L_MISC + 8 : L_AT + ((t == 0 || (t == 2 && wq >= 6u) ? 40 * half : 40 - 40 * half) + 4 * q) * 4;
    };
    unsigned tag;
    bool slow;
    u32x4 wr[8], cgA;
    auto load_wr = [&]() {
#pragma unroll
        for (int t = 0; t < 8; ++t) wr[t] = *(const u32x4*)(smem + L_WR + t * 1024 + lane * 16);
        cgA = *(const u32x4*)(smem + L_CG + lane * 16);
        asm volatile("" : "+v"(wr[0]), "+v"(wr[1]), "+v"(wr[2]), "+v"(wr[3]), "+v"(wr[4]), "+v"(wr[5]), "+v"(wr[6]), "+v"(wr[7]), "+v"(cgA));
    };

    __builtin_amdgcn_s_setprio(3);
    auto touch = [&](unsigned code) -> unsigned {
        if (code < 8u)
            return __builtin_amdgcn_raw_buffer_load_b32(__builtin_amdgcn_make_buffer_rsrc((void*)(ef + (size_t)base_e * 2), 0, code == 0u ? 0 : ne * 32, 0x00020000), lane * 128, (code - 1u) * 8192u, 0);
        const unsigned j = code < 16u ? (code - 8u) * 128u + b : (code - 16u) * 32u + (bx >> 3);
        return __builtin_amdgcn_raw_buffer_load_b32(__builtin_amdgcn_make_buffer_rsrc((void*)x, 0, N * 1024, 0x00020000), lane * 128,
                                                    (2u * j + ((half ^ (smask >> (j >> 5))) & 1u)) * 8192u, 0);
    };
    constexpr unsigned tl[] = {TLIST};
    const __amdgpu_buffer_rsrc_t rsm = __builtin_amdgcn_make_buffer_rsrc((void*)w_mix, 0, C_DIM * C_DIM * 2, 0x00020000);
    u32x4 mv[8];
    auto load_mv = [&]() {
#pragma unroll
        for (int j = 0; j < 8; ++j)
            mv[j] = __builtin_amdgcn_raw_buffer_load_b128(rsm, w < 4 ? OOB_OFF : g * 512 + c16 * 16 + (w & 3) * 2048 + (j & 1) * 256, (j >> 1) * 8192, 0);
        SB();
    };
    auto make_img = [&]() {
#pragma unroll
        for (int i = 0; i < 4; ++i) {
            u32x4 p0, p1;
#pragma unroll
            for (int j = 0; j < 4; ++j) {
                p0[j] = __builtin_amdgcn_perm(mv[2 * j + 1][i], mv[2 * j][i], 0x05040100u);
                p1[j] = __builtin_amdgcn_perm(mv[2 * j + 1][i], mv[2 * j][i], 0x07060302u);
            }
            const unsigned ia = L_IMG + (c16 >> 2) * 8192 + (w & 3) * 1024 + g * 256 + (c16 & 3) * 64 + i * 16;
            *(u32x4*)(smem + ia) = p0;
            *(u32x4*)(smem + ia + 4096) = p1;
        }
    };
    unsigned t0v = 0u, cm = 0u, wv[8], cv[4], tv[NT2 + 1];
    {
        const unsigned sh = 8u * (w & 3);
        unsigned d[3], sv[3], epoch, inc[3];
        u32x2 yv[3];
        const __amdgpu_buffer_rsrc_t rsd = __builtin_amdgcn_make_buffer_rsrc((void*)(dst + base_e), 0, ne * 4, 0x00020000);
        const __amdgpu_buffer_rsrc_t rss = __builtin_amdgcn_make_buffer_rsrc((void*)(src + base_e), 0, ne * 4, 0x00020000);
        const __amdgpu_buffer_rsrc_t rsy = __builtin_amdgcn_make_buffer_rsrc((void*)(Y + base_e), 0, ne * 8, 0x00020000);
        const unsigned le = (unsigned)(w * 160 + lane), le2 = lane < 32 ? le : 0x1000000u;
#pragma unroll
        for (int r = 0; r < 3; ++r) {
            d[r] = __builtin_amdgcn_raw_buffer_load_b32(rsd, (r < 2 ? le : le2) * 4 + 256 * r, 0, NT);
            sv[r] = __builtin_amdgcn_raw_buffer_load_b32(rss, (r < 2 ? le : le2) * 4 + 256 * r, 0, NT);
            SB();
        }
        epoch = __builtin_amdgcn_raw_buffer_load_b32(rsw, W_CTL, 0, 0);
        const unsigned pt = __builtin_amdgcn_raw_buffer_load_b32(__builtin_amdgcn_make_buffer_rsrc((void*)x, 0, bx * 8 + w == 8 + 7 ? min(N, 6144) * 1024 : 0, 0x00020000),
                                                                 (lane * 0x42000u) & 0xe0e000u, 0, 0);
#pragma unroll
        for (int r = 0; r < 3; ++r) yv[r] = __builtin_amdgcn_raw_buffer_load_b64(rsy, (r < 2 ? le : le2) * 8 + 512 * r, 0, NT);
        SB();
        if (bx * 8 + w == 7) {
            unsigned s0, s1, v0, v1;
#define RACE(r, o, a, b) "s_load_dword %2, %4, " o " offset:" a "\n\ts_load_dword %3, %4, " o " offset:" b "\n\ts_waitcnt lgkmcnt(1)\n\ts_sleep " PSL "\n\t" \
                         "s_getreg_b32 " r ", hwreg(HW_REG_IB_STS, 8, 1)\n\ts_waitcnt lgkmcnt(0)\n\t"
            asm volatile("s_load_dword %0, %4, 0x10000\n\ts_load_dword %1, %4, %5 offset:0x10000\n\ts_load_dword %2, %4, %6 offset:0x10000\n\ts_mov_b32 %3, 0\n\ts_waitcnt lgkmcnt(0)\n\t"
                         "" RACE("%0", "%3", "0", "0x2000") RACE("%1", "%5", "0x6000", "0x4000") "s_cmp_lg_u32 %0, %1\n\ts_cbranch_scc1 Lr_%=\n\t" RACE("%1", "%6", "0x8000", "0xa000") "Lr_%=:"
                         : "=&s"(s0), "=&s"(s1), "=&s"(v0), "=&s"(v1) : "s"(x), "s"(N > 6144 ? 0x400000u : 0u), "s"(N > 6144 ? 0x200000u : 0x2000u) : "memory", "scc");
            unsigned xc;
            asm volatile("s_getreg_b32 %0, hwreg(HW_REG_XCC_ID, 0, 2)" : "=s"(xc));
            unsigned hm = (0xf0f0f0u >> (xc + 4u * s1));
            const unsigned tg = epoch * 1664525u + 1013904223u;
            __builtin_amdgcn_raw_buffer_store_b128((u32x4){hm, tg, hm, tg}, rsw, lane < 8 ? W_DEC + lane * 16 : OOB_OFF, 0, SC1);
            for (int i = 0; i < 2; ++i) __builtin_amdgcn_raw_buffer_store_b128((u32x4){0u, tg, 0u, tg}, rsw, W_TAB + lane * 16, i * 1024, SC1);
        }
        {
            const unsigned k0 = (unsigned)tid - L_KEY / 16 < NPB_MAX / 4 ? 127u + L_KEY / 4 - 4u * tid : 0u, k1 = min(k0, 1u);
            *(u32x4*)(smem + tid * 16) = (u32x4){k0, k0 - k1, k0 - 2u * k1, k0 - 3u * k1};
        }
        if (tid < 64 * NRND) *(u32x4*)(smem + L_MRK + tid * 16) = (u32x4){0u, 0u, 0u, 0u};
        {
            const int hm = w >= 4 ? -1 : 0;
            const __amdgpu_buffer_rsrc_t rsa = __builtin_amdgcn_make_buffer_rsrc((void*)w_rad, 0, DE_DIM * C_DIM * 2 & hm, 0x00020000);
            const __amdgpu_buffer_rsrc_t rsg = __builtin_amdgcn_make_buffer_rsrc((void*)cg, 0, 128 & hm, 0x00020000);
#pragma unroll
            for (int j = 0; j < 8; ++j) wv[j] = __builtin_amdgcn_raw_buffer_load_b32(rsa, (g & 1) * 2048 + 64 * (w & 3) + 4 * c16 + 256 * j, 0, 0);
#pragma unroll
            for (int f = 0; f < 4; ++f) cv[f] = __builtin_amdgcn_raw_buffer_load_b32(rsg, (lane & 3) * 32 + (lane & 8u) / 2 + 8 * f, 0, 0);
            t0v = __builtin_amdgcn_raw_buffer_load_b32(__builtin_amdgcn_make_buffer_rsrc((void*)(ef + (size_t)base_e * 2), 0, min(ne * 32, NT0 * 8192), 0x00020000), (511 - tid) * 128, 0, 0);
            if (MVPOS != 1) cm = __builtin_amdgcn_raw_buffer_load_b32(rsm, (tid >> 3) == 32 ? ((bx >> 3) * 8 + lane) * 128 : OOB_OFF, 0, 0);
            if (MVPOS == 1 && w >= 4) load_mv();
        }
#pragma unroll
        for (int r = 0; r < 3; ++r) inc[r] = (int)(r < 2 ? le : le2) + r * 64 < ne ? 1u << sh : 0u;
        asm volatile("" : "+v"(inc[0]), "+v"(inc[1]), "+v"(inc[2]));
        SB();
        wg_barrier();
        if (bx != 0) {
        unsigned bin[3], rank[3], ba[3];
        asm volatile("" : "+v"(d[0]), "+v"(sv[0]), "+v"(d[1]), "+v"(sv[1]), "+v"(d[2]), "+v"(sv[2]));
#pragma unroll
        for (int r = 0; r < 3; ++r) {
            bin[r] = __umulhi(d[r], nb_magic);
            ba[r] = ((((sv[r] >> 3) ^ (smask >> ((sv[r] >> 9) & 31u))) & 1u) * 512u + bin[r] * 4u) & 1023u;
            rank[r] = atomicAdd((unsigned*)(smem + L_PCNT + (w >> 2) * 1024 + ba[r]), inc[r]);
        }
        unsigned tag = (unsigned)__builtin_amdgcn_readfirstlane((int)epoch) * 1664525u + 1013904223u;
        asm volatile("" : "+s"(tag));
        unsigned ta = W_TAB + bx * 2048 + lane * 16 + (w & 1) * 1024;
        asm volatile("" : "+v"(ta));
        if ((w & 6) != 4) __builtin_amdgcn_s_setprio(2);
        wg_barrier();
        u32x2 c[4];
#pragma unroll
        for (int i = 0; i < 4; ++i) c[i] = *(const u32x2*)(smem + L_PCNT + i * 512 + lane * 8);
        const unsigned m0 = below_mask(w, 0), m1 = below_mask(w, 1);
        unsigned t[4], run[4];
#pragma unroll
        for (int i = 0; i < 4; ++i) t[i] = bsum(c[i >> 1][i & 1], 0x01010101u);
        asm volatile("" : "+v"(t[0]), "+v"(t[1]), "+v"(t[2]), "+v"(t[3]));
#pragma unroll
        for (int i = 0; i < 4; ++i) t[i] = bsum(c[2 + (i >> 1)][i & 1], 0x01010101u, t[i]);
        asm volatile("" : "+v"(t[0]), "+v"(t[1]), "+v"(t[2]), "+v"(t[3]));
        const unsigned ts = (t[0] + t[1]) | (t[2] + t[3]) << 16;
        const unsigned ti = (unsigned)wave_incl_scan((int)ts), tx = ti - ts;
        run[0] = tx & 0xffffu; run[2] = (tx >> 16) + ((unsigned)__builtin_amdgcn_readlane((int)ti, 63) & 0xffffu);
        run[1] = run[0] + t[0]; run[3] = run[2] + t[2];
        if ((w & 6) == 4) {
            const unsigned ra = (w & 1) == 0 ? run[0] | (t[0] << 16) : run[1] | (t[1] << 16), rb = (w & 1) == 0 ? run[2] | (t[2] << 16) : run[3] | (t[3] << 16);
            __builtin_amdgcn_raw_buffer_store_b128((u32x4){ra, tag, rb, tag}, rsw, ta, 0, SC1);
        }
        __builtin_amdgcn_s_setprio(3);
        unsigned o[4];
#pragma unroll
        for (int i = 0; i < 4; ++i) o[i] = bytesum2(c[i >> 1][i & 1], c[2 + (i >> 1)][i & 1], m0, m1, run[i]);
        *(unsigned*)(smem + A_PTAB + w * 512 + lane * 4) = o[0] | (o[1] << 16);
        *(unsigned*)(smem + A_PTAB + w * 512 + 256 + lane * 4) = o[2] | (o[3] << 16);
        unsigned pos[3];
        asm volatile("" ::: "memory");
#pragma unroll
        for (int r = 0; r < 3; ++r) pos[r] = *(const unsigned short*)(smem + A_PTAB + w * 512 + (ba[r] >> 1));
        SB();
        unsigned rw0[3], rw1[3];
#pragma unroll
        for (int r = 0; r < 3; ++r) {
            const unsigned sl = sv[r], nl = d[r] - ((__umul24(bin[r], (unsigned)N) + 127u) >> 7);
            rw0[r] = sl | (tag << 14);
            rw1[r] = (nl * 4u + ((__umul24(sl, cmul) >> 16) ^ ((nl >= 40u) == (ba[r] >= 512u) ? 3u : 0u))) | ((le + r * 64) << 9) | (tag & 0xfff00000u);
            asm volatile("" : "+v"(rw0[r]), "+v"(rw1[r]));
        }
#pragma unroll
        for (int r = 0; r < 3; ++r) pos[r] = A_STG + (pos[r] + ((rank[r] >> sh) & 0xffu)) * 16;
        asm volatile("" : "+v"(pos[0]), "+v"(pos[1]), "+v"(pos[2]));
#pragma unroll
        for (int r = 0; r < 3; ++r) pos[r] = inc[r] != 0u ? pos[r] : L_DUM + lane * 16;
#pragma unroll
        for (int r = 0; r < 3; ++r) *(u32x4*)(smem + pos[r]) = (u32x4){yv[r].x, rw0[r], yv[r].y, rw1[r]};
        }
        asm volatile("" : "+v"(epoch) : "v"(pt));
        tag = (unsigned)__builtin_amdgcn_readfirstlane((int)epoch) * 1664525u + 1013904223u;
        wg_barrier();
    }
    if (w < 4) {
        asm volatile("" :: "v"(t0v), "v"(cm), "v"(wv[0]), "v"(wv[7]), "v"(cv[3]));
        const unsigned sh = 8u * w, mb = 0x01010101u & ((1u << sh) - 1u);
        const unsigned T0 = tag << 14, T1 = tag & 0xfff00000u;
        const unsigned to = W_TAB + tid * 2048 + (b & 1) * 1024 + (b >> 1) * 16;
        u32x4 en;
        u32x2 dec;
        __builtin_amdgcn_s_sleep(8);
#pragma unroll 1
        for (unsigned spin = 0; spin < SPIN_MAX; ++spin) {
            asm volatile("" ::: "memory");
            en = __builtin_amdgcn_raw_buffer_load_b128(rsw, to, 0, SC1);
            dec = __builtin_amdgcn_raw_buffer_load_b64(rsw, W_DEC, 0, SC1);
            if (__all((en.y == tag) & (en.w == tag) & (dec.y == tag))) break;
            __builtin_amdgcn_s_sleep(2);
        }
        half = (__builtin_amdgcn_readfirstlane((int)dec.x) >> (bx & 7)) & 1;
        if (half) en.x = en.z;
        const unsigned rc = en.x >> 16;
        const unsigned incl = (unsigned)wave_incl_scan((int)rc);
        const int twf = __builtin_amdgcn_readlane((int)incl, 63), tw = twf <= 64 * NRND ? twf : 0;
        const bool more = tw > 64 * (NRND - 1);
        unsigned ma = L_MRK + w * (256 * NRND) + (incl - rc) * 4;
        asm volatile("" : "+v"(ma));
        *(unsigned*)(smem + ((rc != 0u) & (incl <= 64u * NRND) ? ma : L_DUM + lane * 4)) = (unsigned)lane * EPB_MAX + (en.x & 0xffffu) - (incl - rc);
        unsigned ra[NRND];
        asm volatile("" ::: "memory");
#pragma unroll
        for (int r = 0; r < NRND; ++r) ra[r] = *(const unsigned*)(smem + L_MRK + w * (256 * NRND) + r * 256 + lane * 4);
        unsigned carry = 0u;
        u32x4 A[NRND];
#pragma unroll
        for (int r = 0; r < NRND; ++r) {
            if (r == NRND - 1) { if (more) ra[r] = wave_max_scan(ra[r]); asm volatile("" : "+v"(ra[r])); }
            else if (r % 3 == 0) wave_max_scan3(ra[r], ra[r + 1], ra[r + 2]);
            const unsigned m = max(ra[r], carry);
            carry = max(carry, (unsigned)__builtin_amdgcn_readlane((int)ra[r], 63));
            ra[r] = r * 64 + lane < tw ? (m + (unsigned)(r * 64 + lane)) * 16u : OOB_OFF >> 1;
            A[r] = __builtin_amdgcn_raw_buffer_load_b128(rsw, ra[r], W_RA + 64 * w * (EPB_MAX * 16), SC1);
        }
        SB();
        lds_arrive(lds0 + SY_RI, 16u + half);
        unsigned pe[NRND], inc[NRND], inck[NRND], lim[NRND];
#pragma unroll
        for (int r = 0; r < NRND; ++r) {
            pe[r] = __umul24(__umulhi(ra[r], 209716u) + 64u * w - 1u, (unsigned)epb * 32u);
            inc[r] = ra[r] < 0x40000000u ? 1u << sh : 0u;
            inck[r] = ra[r] < 0x40000000u ? 128u : 0u;
            lim[r] = ra[r] < 0x40000000u ? 16383u : ~0u;
            asm volatile("" : "+v"(pe[r]), "+v"(inc[r]), "+v"(inck[r]), "+v"(lim[r]));
        }
        if (twf > 64 * NRND) lds_arrive(lds0 + SY_C2, 256u);
        if (WRPOS == 1) { lds_wait(lds0 + SY_PRE, 4); load_wr(); }
        SB();
        unsigned rank[NRND], nc[NRND];
#pragma unroll
        for (int r = 0; r < NRND; ++r) {
            if (r == NRND - 1 && !more) { rank[r] = 0u; nc[r] = 0u; continue; }
            A[r].y ^= T0; A[r].w ^= T1;
            if (__builtin_expect(__builtin_amdgcn_ballot_w64((A[r].y | (A[r].w >> 6)) > lim[r]) != 0ull, 0)) {
#pragma unroll 1
                for (unsigned spin = 0; spin < SPIN_MAX; ++spin) {
                    asm volatile("" ::: "memory");
                    A[r] = __builtin_amdgcn_raw_buffer_load_b128(rsw, ra[r], W_RA + 64 * w * (EPB_MAX * 16), SC1);
                    A[r].y ^= T0; A[r].w ^= T1;
                    if (__builtin_amdgcn_ballot_w64((A[r].y | (A[r].w >> 6)) > lim[r]) == 0ull) break;
                }
            }
            nc[r] = (r < 4 || inc[r] != 0u) ? A[r].w & 0x1ffu : (unsigned)lane;
            rank[r] = atomicAdd((unsigned*)(smem + L_CCNT + nc[r] * 4), inc[r]);
            atomicAdd((unsigned*)(smem + L_KEY + (nc[r] & 0x1fcu)), inck[r]);
            SB();
        }
        lds_arrive(lds0 + SY_C1);
        if (CPRIO != 3) __builtin_amdgcn_s_setprio(CPRIO);
#pragma unroll
        for (int r = 0; r < NRND; ++r) {
            rank[r] = (rank[r] >> sh) & 0xffu;
            A[r] = (u32x4){A[r].y << 10, ((A[r].w >> 4) & 0xffe0u) + pe[r], A[r].x, A[r].z};
            asm volatile("" : "+v"(A[r]), "+v"(rank[r]));
        }
        lds_wait(lds0 + SY_C1, 4);
        {
            const u32x4 c0 = *(const u32x4*)(smem + L_CCNT + lane * 16), c1 = *(const u32x4*)(smem + L_CCNT + (64 + c16) * 16);
            unsigned p0[4], p1[4], d0 = 0u, d1 = 0u;
#pragma unroll
            for (int c = 0; c < 4; ++c) {
                p0[c] = bsum(c0[c], mb, d0); p1[c] = bsum(c1[c], mb, d1);
                if (c < 3) { d0 = bsum(c0[c], 0x01010101u, d0); d1 = bsum(c1[c], 0x01010101u, d1); }
            }
            *(u32x2*)(smem + L_CTAB + w * 640 + lane * 8) = (u32x2){p0[0] | (p0[1] << 16), p0[2] | (p0[3] << 16)};
            *(u32x2*)(smem + L_CTAB + w * 640 + (64 + c16) * 8) = (u32x2){p1[0] | (p1[1] << 16), p1[2] | (p1[3] << 16)};
        }
        asm volatile("" ::: "memory");
#pragma unroll
        for (int r = 0; r < NRND; ++r) {
            rank[r] = (rank[r] + *(const unsigned short*)(smem + L_CTAB + w * 640 + nc[r] * 2)) << 6;
            rank[r] = inc[r] != 0u ? rank[r] : L_DUM + lane * 16;
            nc[r] = inc[r] != 0u ? L_NB + (nc[r] & 0x1fcu) * 2 : L_MISC + 8;
        }
#pragma unroll
        for (int r = 0; r < NRND; ++r) asm volatile("" : "+v"(rank[r]), "+v"(nc[r]));
        if (WRPOS == 0) { lds_wait(lds0 + SY_PRE, 4); load_wr(); }
        slow = lds_wait<0, 255>(lds0 + SY_C2, 3) >= 256u;
        if (CPRIO != 3) __builtin_amdgcn_s_setprio(3);
        if (!slow) {
            u32x2 nbv[NRND];
#pragma unroll
            for (int r = 0; r < NRND; ++r) nbv[r] = *(const u32x2*)(smem + nc[r]);
#pragma unroll
            for (int r = 0; r < NRND; ++r) {
                if (r == NRND - 1 && !more) continue;
                const unsigned a = (nbv[r].x & 0xffffu) + rank[r];
                *(u32x4*)(smem + a + (a >= (nbv[r].x >> 16) ? nbv[r].y : 0u)) = A[r];
            }
        }
        lds_arrive(lds0 + SY_C3);
    } else {
        const int q = w - 4, t2 = tid - 256;
        {
            const __amdgpu_buffer_rsrc_t rsr = __builtin_amdgcn_make_buffer_rsrc((void*)(ws + W_RA + bx * (EPB_MAX * 16)), 0, (ne * 16 + 127) & ~127, 0x00020000);
            u32x4 va[5];
#pragma unroll
            for (int i = 0; i < 5; ++i) va[i] = *(const u32x4*)(smem + A_STG + t2 * 16 + 4096 * i);
#pragma unroll
            for (int i = 0; i < 5; ++i) __builtin_amdgcn_raw_buffer_store_b128(va[i], rsr, t2 * 16, 4096 * i, SC1);
            lds_arrive(lds0 + SY_CPY);
        }
        __builtin_amdgcn_s_setprio(2);
        if (MVPOS == 0) load_mv();
        {
            float f0[8], f1[8];
#pragma unroll
            for (int j = 0; j < 8; ++j) {
                const float a = __uint_as_float(wv[j] << 16) * -1.44269504088896340736f, c = __uint_as_float(wv[j] & 0xffff0000u) * -1.44269504088896340736f;
                const unsigned hp = g < 2 ? 0u : pack2bf(a, c);
                f0[j] = a - __uint_as_float(hp << 16);
                f1[j] = c - __uint_as_float(hp & 0xffff0000u);
            }
            *(u32x4*)(smem + L_WR + (2 * q) * 1024 + lane * 16) = (u32x4){pack2bf(f0[0], f0[1]), pack2bf(f0[2], f0[3]), pack2bf(f0[4], f0[5]), pack2bf(f0[6], f0[7])};
            *(u32x4*)(smem + L_WR + (2 * q + 1) * 1024 + lane * 16) = (u32x4){pack2bf(f1[0], f1[1]), pack2bf(f1[2], f1[3]), pack2bf(f1[4], f1[5]), pack2bf(f1[6], f1[7])};
            float a[4];
#pragma unroll
            for (int f = 0; f < 4; ++f) a[f] = __uint_as_float((lane & 4) ? cv[f] & 0xffff0000u : cv[f] << 16) * -0.69314718055994530942f;
            const unsigned h0 = pack2bf(a[0], a[1]), h1 = pack2bf(a[2], a[3]);
            *(u32x4*)(smem + L_CG + lane * 16) = (u32x4){h0, h1, pack2bf(a[0] - __uint_as_float(h0 << 16), a[1] - __uint_as_float(h0 & 0xffff0000u)),
                                                        pack2bf(a[2] - __uint_as_float(h1 << 16), a[3] - __uint_as_float(h1 & 0xffff0000u))};
        }
        lds_wait(lds0 + SY_CPY, 4);
#pragma unroll
        for (int i = 0; i < SMAX / 8; ++i) *(u32x4*)(smem + L_PRV + (t2 + 256 * i) * 16) = (u32x4){OOB_OFF, OOB_OFF, 0u, 0u};
        SB();
        asm volatile("" :: "v"(cp), "v"(cm), "v"(t0v));
        if (MVPOS < 2) make_img();
        lds_arrive(lds0 + SY_PRE);
        if (w == 7) {
            half = (lds_wait<1>(lds0 + SY_RI, 64) & 15u) != 0u;
            if (MVPOS >= 2) load_mv();
#pragma unroll
            for (int i = 0; i < NT2; ++i) tv[i] = touch(tl[i]);
            SB();
            if (MVPOS == 2) make_img();
            lds_wait<1, 255>(lds0 + SY_C2, 3);
            load_wr();
#pragma unroll
            for (int i = 0; i < NT2; ++i) asm volatile("" :: "v"(tv[i]));
        } else {
            lds_wait(lds0 + SY_PRE, 4);
            load_wr();
            half = (lds_wait<1>(lds0 + SY_RI, MVPOS >= 2 ? MVN : 16) & 15u) != 0u;
            if (MVPOS >= 2) load_mv();
            const int n5 = min(27 * q + (lane >> 1), NPB_MAX - 1);
            const bool kp = (n5 >= 40) == (half != 0);
            u64 lpos = kp ? len_pos(true) : len_pos(false);
            unsigned wq, role, ri8, cc, cd, tb, td;
            int k0;
            {
                const int qq = (n5 >= 40 ? n5 - 40 : n5) >> 2;
                wq = (unsigned)((kp ? wave_of(true) : wave_of(false)) >> (4 * qq)) & 15u;
                role = (((kp ? outer_of(true) : outer_of(false)) >> qq) & 1u) ? 2u : (kp ? 0u : 1u);
                ri8 = ((wq < 4u ? wq - 1u : wq) & 3u) * 8u;
                cc = __builtin_rotateleft32(0xff0103fdu, ri8); cd = __builtin_rotateleft32(0xfe000204u, ri8);
                k0 = (int)(signed char)(ring_const() >> ri8);
                tb = L_PRV + wq * (SMAX * 64) + (n5 & 3) * 16;
                td = (wq - ((wq + 1u) & 3u)) * SMAX;
                asm volatile("" : "+v"(lpos), "+v"(wq), "+v"(role), "+v"(ri8), "+v"(cc), "+v"(cd), "+v"(k0), "+v"(tb), "+v"(td));
            }
            lds_wait<1>(lds0 + SY_C1, 3);
            __builtin_amdgcn_s_setprio(3);
            lds_wait(lds0 + SY_C1, 4);
            const unsigned kn = *(const unsigned*)(smem + L_KEY + n5 * 4);
            u32x4 dk[5];
#pragma unroll
            for (int k = 0; k < 5; ++k) dk[k] = *(const u32x4*)(smem + L_KEY + (n5 >= 40 ? 160 : 0) + (lane & 1) * 80 + k * 16);
            int c = 0;
#pragma unroll
            for (int k = 0; k < 5; ++k) c += (int)((kn - dk[k].x) >> 31) + (int)((kn - dk[k].y) >> 31) + (int)((kn - dk[k].z) >> 31) + (int)((kn - dk[k].w) >> 31);
            c += __builtin_amdgcn_update_dpp(0, c, 0xB1, 0xf, 0xf, false);
            *(unsigned*)(smem + L_AT + ((n5 >= 40 ? 40 : 0) + c) * 4) = (unsigned)n5 | (max(kn >> 7, 8u) << 8);
            smem[(c & 3) == 0 ? L_LEN + (kp ? 4 : 0) + ((unsigned)(lpos >> c) & 15u) : L_LEN + 20] = (unsigned char)min(max(kn >> 7, 8u), 63u);
            lds_arrive(lds0 + SY_S); lds_wait(lds0 + SY_S, 3);
            const unsigned at = *(const unsigned*)(smem + L_AT + n5 * 4);
            const u32x4 ln = *(const u32x4*)(smem + L_LEN);
            const unsigned lh = *(const unsigned*)(smem + L_LEN + 16);
            const int p8 = __builtin_amdgcn_sdot4((int)(ln.x + ln.y), (int)cc, __builtin_amdgcn_sdot4((int)(ln.w + lh), (int)cc, __builtin_amdgcn_sdot4((int)ln.z, (int)cd, k0, false), false), false);
            const unsigned ld = (ln.z >> ri8) & 255u, p2 = (unsigned)min(max(p8 >> 3, 4), (int)ld - 4), p1 = ld - p2;
            const unsigned lb = ((wq < 4u ? ln.x : ln.y) >> (8u * (wq & 3u))) & 255u, part = wq < 4u ? p1 : p2;
            const unsigned a1 = tb + (sel3(role, lb + part, wq < 4u ? part : 0u, kp ? lb : 0u - td) << 6);
            *(u32x2*)(smem + L_NB + (at & 0xffu) * 8) = (u32x2){a1 | (a1 + ((kp ? p2 : p1) << 6)) << 16, role == 2u ? (kp ? 0u - td - lb - p2 : td + lb - p1) << 6 : 0u};
            *(unsigned*)(smem + L_PART + wq * 4) = part;
            lds_arrive(lds0 + SY_C2, __any(((at >> 8) > (role == 0u ? SMAX - lb - part : 62u)) | (lb + part > SMAX)) ? 257u : 1u);
            if (MVPOS == 2) make_img();
        }
        slow = lds_wait<0, 255>(lds0 + SY_C2, 3) >= 256u;
        __builtin_amdgcn_s_setprio(3);
    }
    const unsigned lane_off = (unsigned)c16 * 16u;
    const unsigned aX = L_PRV + w * (SMAX * 64) + (c16 & 3) * 64 + g * 16;
    const unsigned aF = L_PRV + w * (SMAX * 64) + (c16 & 3) * 64 + (c16 >> 2) * 16 + 4;
    const unsigned aWw = L_BNC + w * 512 + lane * 8;
    const unsigned aWr = L_BNC + w * 512 + g * 128 + (c16 & 3) * 32 + (c16 >> 2) * 8;
    auto row_of = [=](unsigned at) -> unsigned {
        const unsigned nl = at & 0xffu;
        return nl < (unsigned)npb ? ((unsigned)node0 + nl) * 1024u + lane_off : OOB_OFF;
    };
    if (!slow) {
        u32x32 oxa, oxb;
        u32x4 efA, efN, S;
        const u32x4 dx = mkdesc(x, (unsigned)N * 1024u), de = mkdesc(ef, (unsigned)E * 32u);
        unsigned tmp;
        __builtin_amdgcn_s_waitcnt(0x0F70);
        asm volatile(
            "Lw_%=:\n"
            "ds_read_b32 v220, v219\n"
            "s_waitcnt lgkmcnt(0)\n"
            "v_readfirstlane_b32 %[tmp], v220\n"
            "s_cmp_lt_u32 %[tmp], 4\n"
            "s_cbranch_scc1 Lw_%=\n"
            "ds_read2st64_b32 v[232:233], v211 offset1:1\n"
            "ds_read_b128 v[196:199], v210\n"
            "s_waitcnt lgkmcnt(1)\n"
            "v_add_u32 v232, v232, v229\n"
            "v_add_u32 v233, v233, v229\n"
            "buffer_load_dwordx4 v[192:195], v232, %[rse], 0 offen\n"
            "s_waitcnt lgkmcnt(0)\n"
            "v_add_u32_dpp v220, v196, v214 quad_perm:[0,0,0,0] row_mask:0xf bank_mask:0xf\n"
            "v_add_u32_dpp v221, v196, v214 quad_perm:[1,1,1,1] row_mask:0xf bank_mask:0xf\n"
            "v_add_u32_dpp v222, v196, v214 quad_perm:[2,2,2,2] row_mask:0xf bank_mask:0xf\n"
            "v_add_u32_dpp v223, v196, v214 quad_perm:[3,3,3,3] row_mask:0xf bank_mask:0xf\n"
            "buffer_load_dwordx4 v[32:35], v220, %[rsx], 0 offen\n"
            "buffer_load_dwordx4 v[36:39], v220, %[rsx], 0 offen offset:256\n"
            "buffer_load_dwordx4 v[48:51], v221, %[rsx], 0 offen\n"
            "buffer_load_dwordx4 v[52:55], v221, %[rsx], 0 offen offset:256\n"
            "buffer_load_dwordx4 v[232:235], v233, %[rse], 0 offen\n"
            "buffer_load_dwordx4 v[64:67], v222, %[rsx], 0 offen\n"
            "buffer_load_dwordx4 v[68:71], v222, %[rsx], 0 offen offset:256\n"
            "buffer_load_dwordx4 v[80:83], v223, %[rsx], 0 offen\n"
            "buffer_load_dwordx4 v[84:87], v223, %[rsx], 0 offen offset:256\n"
            "buffer_load_dwordx4 v[40:43], v220, %[rsx], 0 offen offset:512\n"
            "buffer_load_dwordx4 v[44:47], v220, %[rsx], 0 offen offset:768\n"
            "buffer_load_dwordx4 v[56:59], v221, %[rsx], 0 offen offset:512\n"
            "buffer_load_dwordx4 v[60:63], v221, %[rsx], 0 offen offset:768\n"
            "buffer_load_dwordx4 v[72:75], v222, %[rsx], 0 offen offset:512\n"
            "buffer_load_dwordx4 v[76:79], v222, %[rsx], 0 offen offset:768\n"
            "buffer_load_dwordx4 v[88:91], v223, %[rsx], 0 offen offset:512\n"
            "buffer_load_dwordx4 v[92:95], v223, %[rsx], 0 offen offset:768\n"
            : "={v[32:63]}"(oxa), "={v[64:95]}"(oxb), "={v[192:195]}"(efA), "={v[196:199]}"(S), "={v[232:235]}"(efN), [tmp] "=&s"(tmp)
            : [rse] "s"(de), [rsx] "s"(dx), [ax] "{v210}"(lds0 + aX), [af] "{v211}"(lds0 + aF), [lo] "{v214}"(lane_off), [eo] "{v229}"((g & 1) * 16), [sy] "{v219}"(lds0 + SY_C3)
            : "memory", "scc", "v220", "v221", "v222", "v223");
        const unsigned agf = *(const unsigned*)(smem + task_at(w, 0) + g * 4), ag1 = *(const unsigned*)(smem + task_at(w, 1) + g * 4),
                       ag2 = *(const unsigned*)(smem + task_at(w, 2) + (w < 4 ? 0 : g * 4)), agp = *(const unsigned*)(smem + L_PART + w * 4);
        if (MVPOS == 3 && w >= 4) make_img();
        if (bx == 0 && w == 4) flag_store(ws + W_CTL, tag);
        const int lf = __builtin_amdgcn_readfirstlane((int)agf) >> 8, l1 = __builtin_amdgcn_readfirstlane((int)ag1) >> 8, l2 = __builtin_amdgcn_readfirstlane((int)agp);
        const int e2 = lf + l1 + l2;
        int nst = (e2 + 3) >> 2;
        const unsigned r2 = row_of(ag2);
        u32x32 owr;
        u32x16 omi;
        u32x8 ot;
#pragma unroll
        for (int i = 0; i < 32; ++i) owr[i] = wr[i >> 2][i & 3];
        const int pw = (w + (w >> 2)) & 3;
        const unsigned trb = lds0 + L_PK + w * PK_STR + 4096;
        omi[2] = lds0 + aX + 256; omi[3] = lds0 + aF + 256;
        omi[4] = lds0 + aWw; omi[5] = lds0 + aWr; omi[6] = lane_off; omi[7] = W_AGG + row_of(ag1); omi[8] = W_AGG + r2;
        omi[9] = row_of(agf); omi[10] = w >= 6 ? r2 : OOB_OFF; omi[15] = ~tag;
        ot[0] = (lane & 3) == 0 ? 0x3f80u : ((lane & 3) == 1 ? 0x3f800000u : 0u); ot[1] = (lane & 3) == 2 ? 0x3f80u : ((lane & 3) == 3 ? 0x3f800000u : 0u);
        ot[2] = trb + g * (4 * 272) + c16 * 16; ot[3] = trb + c16 * 272 + g * 16; ot[4] = lds0 + L_IMG + lane * 16; ot[5] = (g & 1) * 16; ot[7] = lds0 + L_PK + pw * PK_STR + lane * 16;
        int rem = w < 4 ? l2 : l1, lens = ((w < 4 ? l1 : l2) - 4) | 0xff00, fm = w < 4 ? 0x82 : (w < 6 ? 0x90 : 0x38);
        u64 ra;
        asm volatile(
            "s_nop 1\n"
            "v_mfma_f32_4x4x4_16b_bf16 v[204:207], v[202:203], v[198:199], 0\n"
            "v_mov_b64 v[0:1], 0\n"
            "v_mov_b64 v[2:3], 0\n"
            "v_mov_b64 v[4:5], 0\n"
            "v_mov_b64 v[6:7], 0\n"
            "v_mfma_f32_4x4x4_16b_bf16 v[204:207], v[200:201], v[198:199], v[204:207]\n"
            "v_mov_b64 v[8:9], 0\n"
            "v_mov_b64 v[10:11], 0\n"
            "v_mov_b64 v[12:13], 0\n"
            "v_mov_b64 v[14:15], 0\n"
            "v_mov_b64 v[16:17], 0\n"
            "v_mov_b64 v[18:19], 0\n"
            "v_mov_b64 v[20:21], 0\n"
            "v_mov_b64 v[22:23], 0\n"
            "v_mov_b64 v[24:25], 0\n"
            "v_mov_b64 v[26:27], 0\n"
            "v_mov_b64 v[28:29], 0\n"
            "v_mov_b64 v[30:31], 0\n"
            "v_cvt_pk_bf16_f32 v204, v204, v205\n"
            "v_cvt_pk_bf16_f32 v205, v206, v207\n"
            "ds_write_b64 v212, v[204:205]\n"
            "ds_read_b128 v[196:199], v210\n"
            "ds_read_b64 v[208:209], v213\n"
            "s_waitcnt vmcnt(17)\n"
            "v_mfma_f32_16x16x32_bf16 v[176:179], v[192:195], v[96:99], 0\n"
            "v_mfma_f32_16x16x32_bf16 v[180:183], v[192:195], v[100:103], 0\n"
            "v_mfma_f32_16x16x32_bf16 v[184:187], v[192:195], v[104:107], 0\n"
            "v_mfma_f32_16x16x32_bf16 v[188:191], v[192:195], v[108:111], 0\n"
            "v_mfma_f32_16x16x32_bf16 v[128:131], v[192:195], v[112:115], 0\n"
            "v_mfma_f32_16x16x32_bf16 v[132:135], v[192:195], v[116:119], 0\n"
            "s_nop 2\n"
            "v_exp_f32 v144, v176\n"
            "v_exp_f32 v145, v177\n"
            "v_exp_f32 v146, v178\n"
            "v_exp_f32 v147, v179\n"
            "v_exp_f32 v148, v180\n"
            "v_exp_f32 v149, v181\n"
            "v_exp_f32 v150, v182\n"
            "v_exp_f32 v151, v183\n"
            "v_exp_f32 v152, v184\n"
            "v_exp_f32 v153, v185\n"
            "v_exp_f32 v154, v186\n"
            "v_exp_f32 v155, v187\n"
            "v_exp_f32 v156, v188\n"
            "v_exp_f32 v157, v189\n"
            "v_exp_f32 v158, v190\n"
            "v_exp_f32 v159, v191\n"
            "v_pk_add_f32 v[144:145], v[144:145], 1.0 op_sel_hi:[1,0]\n"
            "v_pk_add_f32 v[146:147], v[146:147], 1.0 op_sel_hi:[1,0]\n"
            "v_pk_add_f32 v[148:149], v[148:149], 1.0 op_sel_hi:[1,0]\n"
            "v_pk_add_f32 v[150:151], v[150:151], 1.0 op_sel_hi:[1,0]\n"
            "v_pk_add_f32 v[152:153], v[152:153], 1.0 op_sel_hi:[1,0]\n"
            "v_pk_add_f32 v[154:155], v[154:155], 1.0 op_sel_hi:[1,0]\n"
            "v_pk_add_f32 v[156:157], v[156:157], 1.0 op_sel_hi:[1,0]\n"
            "v_pk_add_f32 v[158:159], v[158:159], 1.0 op_sel_hi:[1,0]\n"
            "v_rcp_f32 v144, v144\n"
            "v_rcp_f32 v145, v145\n"
            "v_rcp_f32 v146, v146\n"
            "v_rcp_f32 v147, v147\n"
            "v_rcp_f32 v148, v148\n"
            "v_rcp_f32 v149, v149\n"
            "v_rcp_f32 v150, v150\n"
            "v_rcp_f32 v151, v151\n"
            "v_rcp_f32 v152, v152\n"
            "v_rcp_f32 v153, v153\n"
            "v_rcp_f32 v154, v154\n"
            "v_rcp_f32 v155, v155\n"
            "v_rcp_f32 v156, v156\n"
            "v_rcp_f32 v157, v157\n"
            "v_rcp_f32 v158, v158\n"
            "v_rcp_f32 v159, v159\n"
            "v_pk_mul_f32 v[144:145], v[144:145], v[176:177]\n"
            "v_pk_mul_f32 v[146:147], v[146:147], v[178:179]\n"
            "v_pk_mul_f32 v[148:149], v[148:149], v[180:181]\n"
            "v_pk_mul_f32 v[150:151], v[150:151], v[182:183]\n"
            "v_pk_mul_f32 v[152:153], v[152:153], v[184:185]\n"
            "v_pk_mul_f32 v[154:155], v[154:155], v[186:187]\n"
            "v_pk_mul_f32 v[156:157], v[156:157], v[188:189]\n"
            "v_pk_mul_f32 v[158:159], v[158:159], v[190:191]\n"
            "s_branch Ldisp_%=\n"
            "Lfast_%=:\n"
            "v_mfma_f32_16x16x32_bf16 v[136:139], v[192:195], v[120:123], 0\n"
            "v_exp_f32 v160, v128\n"
            "v_add_u32 v211, 0x100, v211\n"
            "v_exp_f32 v161, v129\n"
            "s_waitcnt vmcnt(13) lgkmcnt(0)\n"
            "v_mfma_f32_4x4x4_16b_bf16 v[176:179], v[208:209], v[32:33], 0 cbsz:2\n"
            "v_exp_f32 v162, v130\n"
            "v_mfma_f32_4x4x4_16b_bf16 v[180:183], v[208:209], v[34:35], 0 cbsz:2\n"
            "v_exp_f32 v163, v131\n"
            "v_mfma_f32_4x4x4_16b_bf16 v[184:187], v[208:209], v[36:37], 0 cbsz:2\n"
            "v_exp_f32 v164, v132\n"
            "v_mfma_f32_4x4x4_16b_bf16 v[188:191], v[208:209], v[38:39], 0 cbsz:2\n"
            "v_exp_f32 v165, v133\n"
            "v_add_u32_dpp v219, v196, v214 quad_perm:[0,0,0,0] row_mask:0xf bank_mask:0xf\n"
            "v_mfma_f32_4x4x4_16b_bf16 v[204:207], v[202:203], v[198:199], 0\n"
            "v_exp_f32 v166, v134\n"
            "buffer_load_dwordx4 v[32:35], v219, %[rsx], 0 offen\n"
            "buffer_load_dwordx4 v[36:39], v219, %[rsx], 0 offen offset:256\n"
            "v_exp_f32 v167, v135\n"
            "v_pk_fma_f32 v[0:1], v[144:145], v[176:177], v[0:1] op_sel_hi:[0,1,1]\n"
            "v_pk_fma_f32 v[2:3], v[144:145], v[178:179], v[2:3] op_sel_hi:[0,1,1]\n"
            "v_pk_fma_f32 v[4:5], v[148:149], v[180:181], v[4:5] op_sel_hi:[0,1,1]\n"
            "v_pk_fma_f32 v[6:7], v[148:149], v[182:183], v[6:7] op_sel_hi:[0,1,1]\n"
            "v_pk_fma_f32 v[8:9], v[152:153], v[184:185], v[8:9] op_sel_hi:[0,1,1]\n"
            "v_pk_fma_f32 v[10:11], v[152:153], v[186:187], v[10:11] op_sel_hi:[0,1,1]\n"
            "v_pk_fma_f32 v[12:13], v[156:157], v[188:189], v[12:13] op_sel_hi:[0,1,1]\n"
            "v_pk_fma_f32 v[14:15], v[156:157], v[190:191], v[14:15] op_sel_hi:[0,1,1]\n"
            "v_pk_add_f32 v[160:161], v[160:161], 1.0 op_sel_hi:[1,0]\n"
            "v_pk_add_f32 v[162:163], v[162:163], 1.0 op_sel_hi:[1,0]\n"
            "v_pk_add_f32 v[164:165], v[164:165], 1.0 op_sel_hi:[1,0]\n"
            "v_pk_add_f32 v[166:167], v[166:167], 1.0 op_sel_hi:[1,0]\n"
            "v_mfma_f32_16x16x32_bf16 v[140:143], v[192:195], v[124:127], 0\n"
            "ds_read_b32 v230, v211\n"
            "v_rcp_f32 v160, v160\n"
            "v_rcp_f32 v161, v161\n"
            "v_mfma_f32_4x4x4_16b_bf16 v[176:179], v[208:209], v[48:49], 0 cbsz:2 abid:1\n"
            "v_rcp_f32 v162, v162\n"
            "v_mfma_f32_4x4x4_16b_bf16 v[180:183], v[208:209], v[50:51], 0 cbsz:2 abid:1\n"
            "v_rcp_f32 v163, v163\n"
            "v_mfma_f32_4x4x4_16b_bf16 v[184:187], v[208:209], v[52:53], 0 cbsz:2 abid:1\n"
            "v_rcp_f32 v164, v164\n"
            "v_mfma_f32_4x4x4_16b_bf16 v[188:191], v[208:209], v[54:55], 0 cbsz:2 abid:1\n"
            "v_rcp_f32 v165, v165\n"
            "v_add_u32_dpp v220, v196, v214 quad_perm:[1,1,1,1] row_mask:0xf bank_mask:0xf\n"
            "v_mfma_f32_4x4x4_16b_bf16 v[204:207], v[200:201], v[198:199], v[204:207]\n"
            "v_rcp_f32 v166, v166\n"
            "buffer_load_dwordx4 v[48:51], v220, %[rsx], 0 offen\n"
            "buffer_load_dwordx4 v[52:55], v220, %[rsx], 0 offen offset:256\n"
            "v_rcp_f32 v167, v167\n"
            "v_pk_fma_f32 v[0:1], v[144:145], v[176:177], v[0:1] op_sel:[1,0,0]\n"
            "v_pk_fma_f32 v[2:3], v[144:145], v[178:179], v[2:3] op_sel:[1,0,0]\n"
            "v_pk_fma_f32 v[4:5], v[148:149], v[180:181], v[4:5] op_sel:[1,0,0]\n"
            "v_pk_fma_f32 v[6:7], v[148:149], v[182:183], v[6:7] op_sel:[1,0,0]\n"
            "v_pk_fma_f32 v[8:9], v[152:153], v[184:185], v[8:9] op_sel:[1,0,0]\n"
            "v_pk_fma_f32 v[10:11], v[152:153], v[186:187], v[10:11] op_sel:[1,0,0]\n"
            "v_pk_fma_f32 v[12:13], v[156:157], v[188:189], v[12:13] op_sel:[1,0,0]\n"
            "v_pk_fma_f32 v[14:15], v[156:157], v[190:191], v[14:15] op_sel:[1,0,0]\n"
            "v_pk_mul_f32 v[160:161], v[160:161], v[128:129]\n"
            "v_pk_mul_f32 v[162:163], v[162:163], v[130:131]\n"
            "v_pk_mul_f32 v[164:165], v[164:165], v[132:133]\n"
            "v_pk_mul_f32 v[166:167], v[166:167], v[134:135]\n"
            "s_waitcnt vmcnt(12) lgkmcnt(0)\n"
            "v_mov_b64 v[192:193], v[232:233]\n"
            "v_mov_b64 v[194:195], v[234:235]\n"
            "v_add_u32 v230, v230, v229\n"
            "buffer_load_dwordx4 v[232:235], v230, %[rse], 0 offen\n"
            "v_mfma_f32_16x16x32_bf16 v[128:131], v[192:195], v[96:99], 0\n"
            "v_exp_f32 v168, v136\n"
            "v_exp_f32 v169, v137\n"
            "v_mfma_f32_4x4x4_16b_bf16 v[176:179], v[208:209], v[64:65], 0 cbsz:2 abid:2\n"
            "v_exp_f32 v170, v138\n"
            "v_mfma_f32_4x4x4_16b_bf16 v[180:183], v[208:209], v[66:67], 0 cbsz:2 abid:2\n"
            "v_exp_f32 v171, v139\n"
            "v_mfma_f32_4x4x4_16b_bf16 v[184:187], v[208:209], v[68:69], 0 cbsz:2 abid:2\n"
            "v_exp_f32 v172, v140\n"
            "v_mfma_f32_4x4x4_16b_bf16 v[188:191], v[208:209], v[70:71], 0 cbsz:2 abid:2\n"
            "v_exp_f32 v173, v141\n"
            "v_add_u32_dpp v221, v196, v214 quad_perm:[2,2,2,2] row_mask:0xf bank_mask:0xf\n"
            "v_exp_f32 v174, v142\n"
            "buffer_load_dwordx4 v[64:67], v221, %[rsx], 0 offen\n"
            "buffer_load_dwordx4 v[68:71], v221, %[rsx], 0 offen offset:256\n"
            "v_exp_f32 v175, v143\n"
            "v_pk_fma_f32 v[0:1], v[146:147], v[176:177], v[0:1] op_sel_hi:[0,1,1]\n"
            "v_pk_fma_f32 v[2:3], v[146:147], v[178:179], v[2:3] op_sel_hi:[0,1,1]\n"
            "v_pk_fma_f32 v[4:5], v[150:151], v[180:181], v[4:5] op_sel_hi:[0,1,1]\n"
            "v_pk_fma_f32 v[6:7], v[150:151], v[182:183], v[6:7] op_sel_hi:[0,1,1]\n"
            "v_pk_fma_f32 v[8:9], v[154:155], v[184:185], v[8:9] op_sel_hi:[0,1,1]\n"
            "v_pk_fma_f32 v[10:11], v[154:155], v[186:187], v[10:11] op_sel_hi:[0,1,1]\n"
            "v_pk_fma_f32 v[12:13], v[158:159], v[188:189], v[12:13] op_sel_hi:[0,1,1]\n"
            "v_pk_fma_f32 v[14:15], v[158:159], v[190:191], v[14:15] op_sel_hi:[0,1,1]\n"
            "v_pk_add_f32 v[168:169], v[168:169], 1.0 op_sel_hi:[1,0]\n"
            "v_pk_add_f32 v[170:171], v[170:171], 1.0 op_sel_hi:[1,0]\n"
            "v_pk_add_f32 v[172:173], v[172:173], 1.0 op_sel_hi:[1,0]\n"
            "v_pk_add_f32 v[174:175], v[174:175], 1.0 op_sel_hi:[1,0]\n"
            "v_cvt_pk_bf16_f32 v204, v204, v205\n"
            "v_cvt_pk_bf16_f32 v205, v206, v207\n"
            "ds_write_b64 v212, v[204:205]\n"
            "v_mfma_f32_16x16x32_bf16 v[132:135], v[192:195], v[100:103], 0\n"
            "v_rcp_f32 v168, v168\n"
            "v_rcp_f32 v169, v169\n"
            "v_mfma_f32_4x4x4_16b_bf16 v[176:179], v[208:209], v[80:81], 0 cbsz:2 abid:3\n"
            "v_rcp_f32 v170, v170\n"
            "v_mfma_f32_4x4x4_16b_bf16 v[180:183], v[208:209], v[82:83], 0 cbsz:2 abid:3\n"
            "v_rcp_f32 v171, v171\n"
            "v_mfma_f32_4x4x4_16b_bf16 v[184:187], v[208:209], v[84:85], 0 cbsz:2 abid:3\n"
            "v_rcp_f32 v172, v172\n"
            "v_mfma_f32_4x4x4_16b_bf16 v[188:191], v[208:209], v[86:87], 0 cbsz:2 abid:3\n"
            "v_rcp_f32 v173, v173\n"
            "v_add_u32_dpp v222, v196, v214 quad_perm:[3,3,3,3] row_mask:0xf bank_mask:0xf\n"
            "v_rcp_f32 v174, v174\n"
            "buffer_load_dwordx4 v[80:83], v222, %[rsx], 0 offen\n"
            "buffer_load_dwordx4 v[84:87], v222, %[rsx], 0 offen offset:256\n"
            "v_rcp_f32 v175, v175\n"
            "v_pk_fma_f32 v[0:1], v[146:147], v[176:177], v[0:1] op_sel:[1,0,0]\n"
            "v_pk_fma_f32 v[2:3], v[146:147], v[178:179], v[2:3] op_sel:[1,0,0]\n"
            "v_pk_fma_f32 v[4:5], v[150:151], v[180:181], v[4:5] op_sel:[1,0,0]\n"
            "v_pk_fma_f32 v[6:7], v[150:151], v[182:183], v[6:7] op_sel:[1,0,0]\n"
            "v_pk_fma_f32 v[8:9], v[154:155], v[184:185], v[8:9] op_sel:[1,0,0]\n"
            "v_pk_fma_f32 v[10:11], v[154:155], v[186:187], v[10:11] op_sel:[1,0,0]\n"
            "v_pk_fma_f32 v[12:13], v[158:159], v[188:189], v[12:13] op_sel:[1,0,0]\n"
            "v_pk_fma_f32 v[14:15], v[158:159], v[190:191], v[14:15] op_sel:[1,0,0]\n"
            "v_pk_mul_f32 v[168:169], v[168:169], v[136:137]\n"
            "v_pk_mul_f32 v[170:171], v[170:171], v[138:139]\n"
            "v_pk_mul_f32 v[172:173], v[172:173], v[140:141]\n"
            "v_pk_mul_f32 v[174:175], v[174:175], v[142:143]\n"
            "v_add_u32 v210, 0x100, v210\n"
            "ds_read_b128 v[196:199], v210\n"
            "v_mfma_f32_16x16x32_bf16 v[136:139], v[192:195], v[104:107], 0\n"
            "v_exp_f32 v144, v128\n"
            "v_exp_f32 v145, v129\n"
            "s_waitcnt vmcnt(13)\n"
            "v_mfma_f32_4x4x4_16b_bf16 v[176:179], v[208:209], v[40:41], 0 cbsz:2\n"
            "v_exp_f32 v146, v130\n"
            "v_mfma_f32_4x4x4_16b_bf16 v[180:183], v[208:209], v[42:43], 0 cbsz:2\n"
            "v_exp_f32 v147, v131\n"
            "v_mfma_f32_4x4x4_16b_bf16 v[184:187], v[208:209], v[44:45], 0 cbsz:2\n"
            "v_exp_f32 v148, v132\n"
            "v_mfma_f32_4x4x4_16b_bf16 v[188:191], v[208:209], v[46:47], 0 cbsz:2\n"
            "v_exp_f32 v149, v133\n"
            "v_exp_f32 v150, v134\n"
            "buffer_load_dwordx4 v[40:43], v219, %[rsx], 0 offen offset:512\n"
            "buffer_load_dwordx4 v[44:47], v219, %[rsx], 0 offen offset:768\n"
            "v_exp_f32 v151, v135\n"
            "v_pk_fma_f32 v[16:17], v[160:161], v[176:177], v[16:17] op_sel_hi:[0,1,1]\n"
            "v_pk_fma_f32 v[18:19], v[160:161], v[178:179], v[18:19] op_sel_hi:[0,1,1]\n"
            "v_pk_fma_f32 v[20:21], v[164:165], v[180:181], v[20:21] op_sel_hi:[0,1,1]\n"
            "v_pk_fma_f32 v[22:23], v[164:165], v[182:183], v[22:23] op_sel_hi:[0,1,1]\n"
            "v_pk_fma_f32 v[24:25], v[168:169], v[184:185], v[24:25] op_sel_hi:[0,1,1]\n"
            "v_pk_fma_f32 v[26:27], v[168:169], v[186:187], v[26:27] op_sel_hi:[0,1,1]\n"
            "v_pk_fma_f32 v[28:29], v[172:173], v[188:189], v[28:29] op_sel_hi:[0,1,1]\n"
            "v_pk_fma_f32 v[30:31], v[172:173], v[190:191], v[30:31] op_sel_hi:[0,1,1]\n"
            "v_pk_add_f32 v[144:145], v[144:145], 1.0 op_sel_hi:[1,0]\n"
            "v_pk_add_f32 v[146:147], v[146:147], 1.0 op_sel_hi:[1,0]\n"
            "v_pk_add_f32 v[148:149], v[148:149], 1.0 op_sel_hi:[1,0]\n"
            "v_pk_add_f32 v[150:151], v[150:151], 1.0 op_sel_hi:[1,0]\n"
            "v_mfma_f32_16x16x32_bf16 v[140:143], v[192:195], v[108:111], 0\n"
            "v_rcp_f32 v144, v144\n"
            "v_rcp_f32 v145, v145\n"
            "v_mfma_f32_4x4x4_16b_bf16 v[176:179], v[208:209], v[56:57], 0 cbsz:2 abid:1\n"
            "v_rcp_f32 v146, v146\n"
            "v_mfma_f32_4x4x4_16b_bf16 v[180:183], v[208:209], v[58:59], 0 cbsz:2 abid:1\n"
            "v_rcp_f32 v147, v147\n"
            "v_mfma_f32_4x4x4_16b_bf16 v[184:187], v[208:209], v[60:61], 0 cbsz:2 abid:1\n"
            "v_rcp_f32 v148, v148\n"
            "v_mfma_f32_4x4x4_16b_bf16 v[188:191], v[208:209], v[62:63], 0 cbsz:2 abid:1\n"
            "v_rcp_f32 v149, v149\n"
            "v_rcp_f32 v150, v150\n"
            "buffer_load_dwordx4 v[56:59], v220, %[rsx], 0 offen offset:512\n"
            "buffer_load_dwordx4 v[60:63], v220, %[rsx], 0 offen offset:768\n"
            "v_rcp_f32 v151, v151\n"
            "v_pk_fma_f32 v[16:17], v[160:161], v[176:177], v[16:17] op_sel:[1,0,0]\n"
            "v_pk_fma_f32 v[18:19], v[160:161], v[178:179], v[18:19] op_sel:[1,0,0]\n"
            "v_pk_fma_f32 v[20:21], v[164:165], v[180:181], v[20:21] op_sel:[1,0,0]\n"
            "v_pk_fma_f32 v[22:23], v[164:165], v[182:183], v[22:23] op_sel:[1,0,0]\n"
            "v_pk_fma_f32 v[24:25], v[168:169], v[184:185], v[24:25] op_sel:[1,0,0]\n"
            "v_pk_fma_f32 v[26:27], v[168:169], v[186:187], v[26:27] op_sel:[1,0,0]\n"
            "v_pk_fma_f32 v[28:29], v[172:173], v[188:189], v[28:29] op_sel:[1,0,0]\n"
            "v_pk_fma_f32 v[30:31], v[172:173], v[190:191], v[30:31] op_sel:[1,0,0]\n"
            "v_pk_mul_f32 v[144:145], v[144:145], v[128:129]\n"
            "v_pk_mul_f32 v[146:147], v[146:147], v[130:131]\n"
            "v_pk_mul_f32 v[148:149], v[148:149], v[132:133]\n"
            "v_pk_mul_f32 v[150:151], v[150:151], v[134:135]\n"
            "v_mfma_f32_16x16x32_bf16 v[128:131], v[192:195], v[112:115], 0\n"
            "v_exp_f32 v152, v136\n"
            "v_exp_f32 v153, v137\n"
            "s_waitcnt vmcnt(13)\n"
            "v_mfma_f32_4x4x4_16b_bf16 v[176:179], v[208:209], v[72:73], 0 cbsz:2 abid:2\n"
            "v_exp_f32 v154, v138\n"
            "v_mfma_f32_4x4x4_16b_bf16 v[180:183], v[208:209], v[74:75], 0 cbsz:2 abid:2\n"
            "v_exp_f32 v155, v139\n"
            "v_mfma_f32_4x4x4_16b_bf16 v[184:187], v[208:209], v[76:77], 0 cbsz:2 abid:2\n"
            "v_exp_f32 v156, v140\n"
            "v_mfma_f32_4x4x4_16b_bf16 v[188:191], v[208:209], v[78:79], 0 cbsz:2 abid:2\n"
            "v_exp_f32 v157, v141\n"
            "v_exp_f32 v158, v142\n"
            "buffer_load_dwordx4 v[72:75], v221, %[rsx], 0 offen offset:512\n"
            "buffer_load_dwordx4 v[76:79], v221, %[rsx], 0 offen offset:768\n"
            "v_exp_f32 v159, v143\n"
            "v_pk_fma_f32 v[16:17], v[162:163], v[176:177], v[16:17] op_sel_hi:[0,1,1]\n"
            "v_pk_fma_f32 v[18:19], v[162:163], v[178:179], v[18:19] op_sel_hi:[0,1,1]\n"
            "v_pk_fma_f32 v[20:21], v[166:167], v[180:181], v[20:21] op_sel_hi:[0,1,1]\n"
            "v_pk_fma_f32 v[22:23], v[166:167], v[182:183], v[22:23] op_sel_hi:[0,1,1]\n"
            "v_pk_fma_f32 v[24:25], v[170:171], v[184:185], v[24:25] op_sel_hi:[0,1,1]\n"
            "v_pk_fma_f32 v[26:27], v[170:171], v[186:187], v[26:27] op_sel_hi:[0,1,1]\n"
            "v_pk_fma_f32 v[28:29], v[174:175], v[188:189], v[28:29] op_sel_hi:[0,1,1]\n"
            "v_pk_fma_f32 v[30:31], v[174:175], v[190:191], v[30:31] op_sel_hi:[0,1,1]\n"
            "v_pk_add_f32 v[152:153], v[152:153], 1.0 op_sel_hi:[1,0]\n"
            "v_pk_add_f32 v[154:155], v[154:155], 1.0 op_sel_hi:[1,0]\n"
            "v_pk_add_f32 v[156:157], v[156:157], 1.0 op_sel_hi:[1,0]\n"
            "v_pk_add_f32 v[158:159], v[158:159], 1.0 op_sel_hi:[1,0]\n"
            "v_mfma_f32_16x16x32_bf16 v[132:135], v[192:195], v[116:119], 0\n"
            "v_rcp_f32 v152, v152\n"
            "v_rcp_f32 v153, v153\n"
            "v_mfma_f32_4x4x4_16b_bf16 v[176:179], v[208:209], v[88:89], 0 cbsz:2 abid:3\n"
            "v_rcp_f32 v154, v154\n"
            "v_mfma_f32_4x4x4_16b_bf16 v[180:183], v[208:209], v[90:91], 0 cbsz:2 abid:3\n"
            "v_rcp_f32 v155, v155\n"
            "v_mfma_f32_4x4x4_16b_bf16 v[184:187], v[208:209], v[92:93], 0 cbsz:2 abid:3\n"
            "v_rcp_f32 v156, v156\n"
            "v_mfma_f32_4x4x4_16b_bf16 v[188:191], v[208:209], v[94:95], 0 cbsz:2 abid:3\n"
            "ds_read_b64 v[208:209], v213\n"
            "v_rcp_f32 v157, v157\n"
            "v_rcp_f32 v158, v158\n"
            "buffer_load_dwordx4 v[88:91], v222, %[rsx], 0 offen offset:512\n"
            "buffer_load_dwordx4 v[92:95], v222, %[rsx], 0 offen offset:768\n"
            "v_rcp_f32 v159, v159\n"
            "v_pk_fma_f32 v[16:17], v[162:163], v[176:177], v[16:17] op_sel:[1,0,0]\n"
            "v_pk_fma_f32 v[18:19], v[162:163], v[178:179], v[18:19] op_sel:[1,0,0]\n"
            "v_pk_fma_f32 v[20:21], v[166:167], v[180:181], v[20:21] op_sel:[1,0,0]\n"
            "v_pk_fma_f32 v[22:23], v[166:167], v[182:183], v[22:23] op_sel:[1,0,0]\n"
            "v_pk_fma_f32 v[24:25], v[170:171], v[184:185], v[24:25] op_sel:[1,0,0]\n"
            "v_pk_fma_f32 v[26:27], v[170:171], v[186:187], v[26:27] op_sel:[1,0,0]\n"
            "v_pk_fma_f32 v[28:29], v[174:175], v[188:189], v[28:29] op_sel:[1,0,0]\n"
            "v_pk_fma_f32 v[30:31], v[174:175], v[190:191], v[30:31] op_sel:[1,0,0]\n"
            "v_pk_mul_f32 v[152:153], v[152:153], v[136:137]\n"
            "v_pk_mul_f32 v[154:155], v[154:155], v[138:139]\n"
            "v_pk_mul_f32 v[156:157], v[156:157], v[140:141]\n"
            "v_pk_mul_f32 v[158:159], v[158:159], v[142:143]\n"
            "s_sub_u32 %[rem], %[rem], 4\n"
            "Lnext_%=:\n"
            "s_sub_u32 %[nst], %[nst], 1\n"
            "Ldisp_%=:\n"
            "s_bitcmp1_b32 %[evt], %[nst]\n"
            "s_cbranch_scc1 Lprio_%=\n"
            "Ld2_%=:\n"
            "s_cmp_gt_u32 %[rem], 4\n"
            "s_cbranch_scc1 Lfast_%=\n"
            "v_mfma_f32_16x16x32_bf16 v[136:139], v[192:195], v[120:123], 0\n"
            "v_exp_f32 v160, v128\n"
            "v_add_u32 v211, 0x100, v211\n"
            "v_exp_f32 v161, v129\n"
            "s_waitcnt vmcnt(13) lgkmcnt(0)\n"
            "v_mfma_f32_4x4x4_16b_bf16 v[176:179], v[208:209], v[32:33], 0 cbsz:2\n"
            "v_exp_f32 v162, v130\n"
            "v_mfma_f32_4x4x4_16b_bf16 v[180:183], v[208:209], v[34:35], 0 cbsz:2\n"
            "v_exp_f32 v163, v131\n"
            "v_mfma_f32_4x4x4_16b_bf16 v[184:187], v[208:209], v[36:37], 0 cbsz:2\n"
            "v_exp_f32 v164, v132\n"
            "v_mfma_f32_4x4x4_16b_bf16 v[188:191], v[208:209], v[38:39], 0 cbsz:2\n"
            "v_exp_f32 v165, v133\n"
            "v_add_u32_dpp v219, v196, v214 quad_perm:[0,0,0,0] row_mask:0xf bank_mask:0xf\n"
            "v_mfma_f32_4x4x4_16b_bf16 v[204:207], v[202:203], v[198:199], 0\n"
            "v_exp_f32 v166, v134\n"
            "buffer_load_dwordx4 v[32:35], v219, %[rsx], 0 offen\n"
            "buffer_load_dwordx4 v[36:39], v219, %[rsx], 0 offen offset:256\n"
            "v_exp_f32 v167, v135\n"
            "v_pk_fma_f32 v[0:1], v[144:145], v[176:177], v[0:1] op_sel_hi:[0,1,1]\n"
            "v_pk_fma_f32 v[2:3], v[144:145], v[178:179], v[2:3] op_sel_hi:[0,1,1]\n"
            "v_pk_fma_f32 v[4:5], v[148:149], v[180:181], v[4:5] op_sel_hi:[0,1,1]\n"
            "v_pk_fma_f32 v[6:7], v[148:149], v[182:183], v[6:7] op_sel_hi:[0,1,1]\n"
            "v_pk_fma_f32 v[8:9], v[152:153], v[184:185], v[8:9] op_sel_hi:[0,1,1]\n"
            "v_pk_fma_f32 v[10:11], v[152:153], v[186:187], v[10:11] op_sel_hi:[0,1,1]\n"
            "v_pk_fma_f32 v[12:13], v[156:157], v[188:189], v[12:13] op_sel_hi:[0,1,1]\n"
            "v_pk_fma_f32 v[14:15], v[156:157], v[190:191], v[14:15] op_sel_hi:[0,1,1]\n"
            "v_pk_add_f32 v[160:161], v[160:161], 1.0 op_sel_hi:[1,0]\n"
            "v_pk_add_f32 v[162:163], v[162:163], 1.0 op_sel_hi:[1,0]\n"
            "v_pk_add_f32 v[164:165], v[164:165], 1.0 op_sel_hi:[1,0]\n"
            "v_pk_add_f32 v[166:167], v[166:167], 1.0 op_sel_hi:[1,0]\n"
            "s_cmp_eq_u32 %[rem], 1\n"
            "s_cbranch_scc1 Lf00_%=\n"
            "Lr00_%=:\n"
            "v_mfma_f32_16x16x32_bf16 v[140:143], v[192:195], v[124:127], 0\n"
            "ds_read_b32 v230, v211\n"
            "v_rcp_f32 v160, v160\n"
            "v_rcp_f32 v161, v161\n"
            "v_mfma_f32_4x4x4_16b_bf16 v[176:179], v[208:209], v[48:49], 0 cbsz:2 abid:1\n"
            "v_rcp_f32 v162, v162\n"
            "v_mfma_f32_4x4x4_16b_bf16 v[180:183], v[208:209], v[50:51], 0 cbsz:2 abid:1\n"
            "v_rcp_f32 v163, v163\n"
            "v_mfma_f32_4x4x4_16b_bf16 v[184:187], v[208:209], v[52:53], 0 cbsz:2 abid:1\n"
            "v_rcp_f32 v164, v164\n"
            "v_mfma_f32_4x4x4_16b_bf16 v[188:191], v[208:209], v[54:55], 0 cbsz:2 abid:1\n"
            "v_rcp_f32 v165, v165\n"
            "v_add_u32_dpp v220, v196, v214 quad_perm:[1,1,1,1] row_mask:0xf bank_mask:0xf\n"
            "v_mfma_f32_4x4x4_16b_bf16 v[204:207], v[200:201], v[198:199], v[204:207]\n"
            "v_rcp_f32 v166, v166\n"
            "buffer_load_dwordx4 v[48:51], v220, %[rsx], 0 offen\n"
            "buffer_load_dwordx4 v[52:55], v220, %[rsx], 0 offen offset:256\n"
            "v_rcp_f32 v167, v167\n"
            "v_pk_fma_f32 v[0:1], v[144:145], v[176:177], v[0:1] op_sel:[1,0,0]\n"
            "v_pk_fma_f32 v[2:3], v[144:145], v[178:179], v[2:3] op_sel:[1,0,0]\n"
            "v_pk_fma_f32 v[4:5], v[148:149], v[180:181], v[4:5] op_sel:[1,0,0]\n"
            "v_pk_fma_f32 v[6:7], v[148:149], v[182:183], v[6:7] op_sel:[1,0,0]\n"
            "v_pk_fma_f32 v[8:9], v[152:153], v[184:185], v[8:9] op_sel:[1,0,0]\n"
            "v_pk_fma_f32 v[10:11], v[152:153], v[186:187], v[10:11] op_sel:[1,0,0]\n"
            "v_pk_fma_f32 v[12:13], v[156:157], v[188:189], v[12:13] op_sel:[1,0,0]\n"
            "v_pk_fma_f32 v[14:15], v[156:157], v[190:191], v[14:15] op_sel:[1,0,0]\n"
            "v_pk_mul_f32 v[160:161], v[160:161], v[128:129]\n"
            "v_pk_mul_f32 v[162:163], v[162:163], v[130:131]\n"
            "v_pk_mul_f32 v[164:165], v[164:165], v[132:133]\n"
            "v_pk_mul_f32 v[166:167], v[166:167], v[134:135]\n"
            "s_cmp_eq_u32 %[rem], 2\n"
            "s_cbranch_scc1 Lf01_%=\n"
            "Lr01_%=:\n"
            "s_waitcnt vmcnt(12) lgkmcnt(0)\n"
            "v_mov_b64 v[192:193], v[232:233]\n"
            "v_mov_b64 v[194:195], v[234:235]\n"
            "v_add_u32 v230, v230, v229\n"
            "buffer_load_dwordx4 v[232:235], v230, %[rse], 0 offen\n"
            "v_mfma_f32_16x16x32_bf16 v[128:131], v[192:195], v[96:99], 0\n"
            "v_exp_f32 v168, v136\n"
            "v_exp_f32 v169, v137\n"
            "v_mfma_f32_4x4x4_16b_bf16 v[176:179], v[208:209], v[64:65], 0 cbsz:2 abid:2\n"
            "v_exp_f32 v170, v138\n"
            "v_mfma_f32_4x4x4_16b_bf16 v[180:183], v[208:209], v[66:67], 0 cbsz:2 abid:2\n"
            "v_exp_f32 v171, v139\n"
            "v_mfma_f32_4x4x4_16b_bf16 v[184:187], v[208:209], v[68:69], 0 cbsz:2 abid:2\n"
            "v_exp_f32 v172, v140\n"
            "v_mfma_f32_4x4x4_16b_bf16 v[188:191], v[208:209], v[70:71], 0 cbsz:2 abid:2\n"
            "v_exp_f32 v173, v141\n"
            "v_add_u32_dpp v221, v196, v214 quad_perm:[2,2,2,2] row_mask:0xf bank_mask:0xf\n"
            "v_exp_f32 v174, v142\n"
            "buffer_load_dwordx4 v[64:67], v221, %[rsx], 0 offen\n"
            "buffer_load_dwordx4 v[68:71], v221, %[rsx], 0 offen offset:256\n"
            "v_exp_f32 v175, v143\n"
            "v_pk_fma_f32 v[0:1], v[146:147], v[176:177], v[0:1] op_sel_hi:[0,1,1]\n"
            "v_pk_fma_f32 v[2:3], v[146:147], v[178:179], v[2:3] op_sel_hi:[0,1,1]\n"
            "v_pk_fma_f32 v[4:5], v[150:151], v[180:181], v[4:5] op_sel_hi:[0,1,1]\n"
            "v_pk_fma_f32 v[6:7], v[150:151], v[182:183], v[6:7] op_sel_hi:[0,1,1]\n"
            "v_pk_fma_f32 v[8:9], v[154:155], v[184:185], v[8:9] op_sel_hi:[0,1,1]\n"
            "v_pk_fma_f32 v[10:11], v[154:155], v[186:187], v[10:11] op_sel_hi:[0,1,1]\n"
            "v_pk_fma_f32 v[12:13], v[158:159], v[188:189], v[12:13] op_sel_hi:[0,1,1]\n"
            "v_pk_fma_f32 v[14:15], v[158:159], v[190:191], v[14:15] op_sel_hi:[0,1,1]\n"
            "v_pk_add_f32 v[168:169], v[168:169], 1.0 op_sel_hi:[1,0]\n"
            "v_pk_add_f32 v[170:171], v[170:171], 1.0 op_sel_hi:[1,0]\n"
            "v_pk_add_f32 v[172:173], v[172:173], 1.0 op_sel_hi:[1,0]\n"
            "v_pk_add_f32 v[174:175], v[174:175], 1.0 op_sel_hi:[1,0]\n"
            "v_cvt_pk_bf16_f32 v204, v204, v205\n"
            "v_cvt_pk_bf16_f32 v205, v206, v207\n"
            "ds_write_b64 v212, v[204:205]\n"
            "s_cmp_eq_u32 %[rem], 3\n"
            "s_cbranch_scc1 Lf02_%=\n"
            "Lr02_%=:\n"
            "v_mfma_f32_16x16x32_bf16 v[132:135], v[192:195], v[100:103], 0\n"
            "v_rcp_f32 v168, v168\n"
            "v_rcp_f32 v169, v169\n"
            "v_mfma_f32_4x4x4_16b_bf16 v[176:179], v[208:209], v[80:81], 0 cbsz:2 abid:3\n"
            "v_rcp_f32 v170, v170\n"
            "v_mfma_f32_4x4x4_16b_bf16 v[180:183], v[208:209], v[82:83], 0 cbsz:2 abid:3\n"
            "v_rcp_f32 v171, v171\n"
            "v_mfma_f32_4x4x4_16b_bf16 v[184:187], v[208:209], v[84:85], 0 cbsz:2 abid:3\n"
            "v_rcp_f32 v172, v172\n"
            "v_mfma_f32_4x4x4_16b_bf16 v[188:191], v[208:209], v[86:87], 0 cbsz:2 abid:3\n"
            "v_rcp_f32 v173, v173\n"
            "v_add_u32_dpp v222, v196, v214 quad_perm:[3,3,3,3] row_mask:0xf bank_mask:0xf\n"
            "v_rcp_f32 v174, v174\n"
            "buffer_load_dwordx4 v[80:83], v222, %[rsx], 0 offen\n"
            "buffer_load_dwordx4 v[84:87], v222, %[rsx], 0 offen offset:256\n"
            "v_rcp_f32 v175, v175\n"
            "v_pk_fma_f32 v[0:1], v[146:147], v[176:177], v[0:1] op_sel:[1,0,0]\n"
            "v_pk_fma_f32 v[2:3], v[146:147], v[178:179], v[2:3] op_sel:[1,0,0]\n"
            "v_pk_fma_f32 v[4:5], v[150:151], v[180:181], v[4:5] op_sel:[1,0,0]\n"
            "v_pk_fma_f32 v[6:7], v[150:151], v[182:183], v[6:7] op_sel:[1,0,0]\n"
            "v_pk_fma_f32 v[8:9], v[154:155], v[184:185], v[8:9] op_sel:[1,0,0]\n"
            "v_pk_fma_f32 v[10:11], v[154:155], v[186:187], v[10:11] op_sel:[1,0,0]\n"
            "v_pk_fma_f32 v[12:13], v[158:159], v[188:189], v[12:13] op_sel:[1,0,0]\n"
            "v_pk_fma_f32 v[14:15], v[158:159], v[190:191], v[14:15] op_sel:[1,0,0]\n"
            "v_pk_mul_f32 v[168:169], v[168:169], v[136:137]\n"
            "v_pk_mul_f32 v[170:171], v[170:171], v[138:139]\n"
            "v_pk_mul_f32 v[172:173], v[172:173], v[140:141]\n"
            "v_pk_mul_f32 v[174:175], v[174:175], v[142:143]\n"
            "s_cmp_eq_u32 %[rem], 4\n"
            "s_cbranch_scc1 Lf03_%=\n"
            "Lr03_%=:\n"
            "v_add_u32 v210, 0x100, v210\n"
            "ds_read_b128 v[196:199], v210\n"
            "v_mfma_f32_16x16x32_bf16 v[136:139], v[192:195], v[104:107], 0\n"
            "v_exp_f32 v144, v128\n"
            "v_exp_f32 v145, v129\n"
            "s_waitcnt vmcnt(13)\n"
            "v_mfma_f32_4x4x4_16b_bf16 v[176:179], v[208:209], v[40:41], 0 cbsz:2\n"
            "v_exp_f32 v146, v130\n"
            "v_mfma_f32_4x4x4_16b_bf16 v[180:183], v[208:209], v[42:43], 0 cbsz:2\n"
            "v_exp_f32 v147, v131\n"
            "v_mfma_f32_4x4x4_16b_bf16 v[184:187], v[208:209], v[44:45], 0 cbsz:2\n"
            "v_exp_f32 v148, v132\n"
            "v_mfma_f32_4x4x4_16b_bf16 v[188:191], v[208:209], v[46:47], 0 cbsz:2\n"
            "v_exp_f32 v149, v133\n"
            "v_exp_f32 v150, v134\n"
            "buffer_load_dwordx4 v[40:43], v219, %[rsx], 0 offen offset:512\n"
            "buffer_load_dwordx4 v[44:47], v219, %[rsx], 0 offen offset:768\n"
            "v_exp_f32 v151, v135\n"
            "v_pk_fma_f32 v[16:17], v[160:161], v[176:177], v[16:17] op_sel_hi:[0,1,1]\n"
            "v_pk_fma_f32 v[18:19], v[160:161], v[178:179], v[18:19] op_sel_hi:[0,1,1]\n"
            "v_pk_fma_f32 v[20:21], v[164:165], v[180:181], v[20:21] op_sel_hi:[0,1,1]\n"
            "v_pk_fma_f32 v[22:23], v[164:165], v[182:183], v[22:23] op_sel_hi:[0,1,1]\n"
            "v_pk_fma_f32 v[24:25], v[168:169], v[184:185], v[24:25] op_sel_hi:[0,1,1]\n"
            "v_pk_fma_f32 v[26:27], v[168:169], v[186:187], v[26:27] op_sel_hi:[0,1,1]\n"
            "v_pk_fma_f32 v[28:29], v[172:173], v[188:189], v[28:29] op_sel_hi:[0,1,1]\n"
            "v_pk_fma_f32 v[30:31], v[172:173], v[190:191], v[30:31] op_sel_hi:[0,1,1]\n"
            "v_pk_add_f32 v[144:145], v[144:145], 1.0 op_sel_hi:[1,0]\n"
            "v_pk_add_f32 v[146:147], v[146:147], 1.0 op_sel_hi:[1,0]\n"
            "v_pk_add_f32 v[148:149], v[148:149], 1.0 op_sel_hi:[1,0]\n"
            "v_pk_add_f32 v[150:151], v[150:151], 1.0 op_sel_hi:[1,0]\n"
            "s_cmp_eq_u32 %[rem], 1\n"
            "s_cbranch_scc1 Lf10_%=\n"
            "Lr10_%=:\n"
            "v_mfma_f32_16x16x32_bf16 v[140:143], v[192:195], v[108:111], 0\n"
            "v_rcp_f32 v144, v144\n"
            "v_rcp_f32 v145, v145\n"
            "v_mfma_f32_4x4x4_16b_bf16 v[176:179], v[208:209], v[56:57], 0 cbsz:2 abid:1\n"
            "v_rcp_f32 v146, v146\n"
            "v_mfma_f32_4x4x4_16b_bf16 v[180:183], v[208:209], v[58:59], 0 cbsz:2 abid:1\n"
            "v_rcp_f32 v147, v147\n"
            "v_mfma_f32_4x4x4_16b_bf16 v[184:187], v[208:209], v[60:61], 0 cbsz:2 abid:1\n"
            "v_rcp_f32 v148, v148\n"
            "v_mfma_f32_4x4x4_16b_bf16 v[188:191], v[208:209], v[62:63], 0 cbsz:2 abid:1\n"
            "v_rcp_f32 v149, v149\n"
            "v_rcp_f32 v150, v150\n"
            "buffer_load_dwordx4 v[56:59], v220, %[rsx], 0 offen offset:512\n"
            "buffer_load_dwordx4 v[60:63], v220, %[rsx], 0 offen offset:768\n"
            "v_rcp_f32 v151, v151\n"
            "v_pk_fma_f32 v[16:17], v[160:161], v[176:177], v[16:17] op_sel:[1,0,0]\n"
            "v_pk_fma_f32 v[18:19], v[160:161], v[178:179], v[18:19] op_sel:[1,0,0]\n"
            "v_pk_fma_f32 v[20:21], v[164:165], v[180:181], v[20:21] op_sel:[1,0,0]\n"
            "v_pk_fma_f32 v[22:23], v[164:165], v[182:183], v[22:23] op_sel:[1,0,0]\n"
            "v_pk_fma_f32 v[24:25], v[168:169], v[184:185], v[24:25] op_sel:[1,0,0]\n"
            "v_pk_fma_f32 v[26:27], v[168:169], v[186:187], v[26:27] op_sel:[1,0,0]\n"
            "v_pk_fma_f32 v[28:29], v[172:173], v[188:189], v[28:29] op_sel:[1,0,0]\n"
            "v_pk_fma_f32 v[30:31], v[172:173], v[190:191], v[30:31] op_sel:[1,0,0]\n"
            "v_pk_mul_f32 v[144:145], v[144:145], v[128:129]\n"
            "v_pk_mul_f32 v[146:147], v[146:147], v[130:131]\n"
            "v_pk_mul_f32 v[148:149], v[148:149], v[132:133]\n"
            "v_pk_mul_f32 v[150:151], v[150:151], v[134:135]\n"
            "s_cmp_eq_u32 %[rem], 2\n"
            "s_cbranch_scc1 Lf11_%=\n"
            "Lr11_%=:\n"
            "v_mfma_f32_16x16x32_bf16 v[128:131], v[192:195], v[112:115], 0\n"
            "v_exp_f32 v152, v136\n"
            "v_exp_f32 v153, v137\n"
            "s_waitcnt vmcnt(13)\n"
            "v_mfma_f32_4x4x4_16b_bf16 v[176:179], v[208:209], v[72:73], 0 cbsz:2 abid:2\n"
            "v_exp_f32 v154, v138\n"
            "v_mfma_f32_4x4x4_16b_bf16 v[180:183], v[208:209], v[74:75], 0 cbsz:2 abid:2\n"
            "v_exp_f32 v155, v139\n"
            "v_mfma_f32_4x4x4_16b_bf16 v[184:187], v[208:209], v[76:77], 0 cbsz:2 abid:2\n"
            "v_exp_f32 v156, v140\n"
            "v_mfma_f32_4x4x4_16b_bf16 v[188:191], v[208:209], v[78:79], 0 cbsz:2 abid:2\n"
            "v_exp_f32 v157, v141\n"
            "v_exp_f32 v158, v142\n"
            "buffer_load_dwordx4 v[72:75], v221, %[rsx], 0 offen offset:512\n"
            "buffer_load_dwordx4 v[76:79], v221, %[rsx], 0 offen offset:768\n"
            "v_exp_f32 v159, v143\n"
            "v_pk_fma_f32 v[16:17], v[162:163], v[176:177], v[16:17] op_sel_hi:[0,1,1]\n"
            "v_pk_fma_f32 v[18:19], v[162:163], v[178:179], v[18:19] op_sel_hi:[0,1,1]\n"
            "v_pk_fma_f32 v[20:21], v[166:167], v[180:181], v[20:21] op_sel_hi:[0,1,1]\n"
            "v_pk_fma_f32 v[22:23], v[166:167], v[182:183], v[22:23] op_sel_hi:[0,1,1]\n"
            "v_pk_fma_f32 v[24:25], v[170:171], v[184:185], v[24:25] op_sel_hi:[0,1,1]\n"
            "v_pk_fma_f32 v[26:27], v[170:171], v[186:187], v[26:27] op_sel_hi:[0,1,1]\n"
            "v_pk_fma_f32 v[28:29], v[174:175], v[188:189], v[28:29] op_sel_hi:[0,1,1]\n"
            "v_pk_fma_f32 v[30:31], v[174:175], v[190:191], v[30:31] op_sel_hi:[0,1,1]\n"
            "v_pk_add_f32 v[152:153], v[152:153], 1.0 op_sel_hi:[1,0]\n"
            "v_pk_add_f32 v[154:155], v[154:155], 1.0 op_sel_hi:[1,0]\n"
            "v_pk_add_f32 v[156:157], v[156:157], 1.0 op_sel_hi:[1,0]\n"
            "v_pk_add_f32 v[158:159], v[158:159], 1.0 op_sel_hi:[1,0]\n"
            "s_cmp_eq_u32 %[rem], 3\n"
            "s_cbranch_scc1 Lf12_%=\n"
            "Lr12_%=:\n"
            "v_mfma_f32_16x16x32_bf16 v[132:135], v[192:195], v[116:119], 0\n"
            "v_rcp_f32 v152, v152\n"
            "v_rcp_f32 v153, v153\n"
            "v_mfma_f32_4x4x4_16b_bf16 v[176:179], v[208:209], v[88:89], 0 cbsz:2 abid:3\n"
            "v_rcp_f32 v154, v154\n"
            "v_mfma_f32_4x4x4_16b_bf16 v[180:183], v[208:209], v[90:91], 0 cbsz:2 abid:3\n"
            "v_rcp_f32 v155, v155\n"
            "v_mfma_f32_4x4x4_16b_bf16 v[184:187], v[208:209], v[92:93], 0 cbsz:2 abid:3\n"
            "v_rcp_f32 v156, v156\n"
            "v_mfma_f32_4x4x4_16b_bf16 v[188:191], v[208:209], v[94:95], 0 cbsz:2 abid:3\n"
            "ds_read_b64 v[208:209], v213\n"
            "v_rcp_f32 v157, v157\n"
            "v_rcp_f32 v158, v158\n"
            "buffer_load_dwordx4 v[88:91], v222, %[rsx], 0 offen offset:512\n"
            "buffer_load_dwordx4 v[92:95], v222, %[rsx], 0 offen offset:768\n"
            "v_rcp_f32 v159, v159\n"
            "v_pk_fma_f32 v[16:17], v[162:163], v[176:177], v[16:17] op_sel:[1,0,0]\n"
            "v_pk_fma_f32 v[18:19], v[162:163], v[178:179], v[18:19] op_sel:[1,0,0]\n"
            "v_pk_fma_f32 v[20:21], v[166:167], v[180:181], v[20:21] op_sel:[1,0,0]\n"
            "v_pk_fma_f32 v[22:23], v[166:167], v[182:183], v[22:23] op_sel:[1,0,0]\n"
            "v_pk_fma_f32 v[24:25], v[170:171], v[184:185], v[24:25] op_sel:[1,0,0]\n"
            "v_pk_fma_f32 v[26:27], v[170:171], v[186:187], v[26:27] op_sel:[1,0,0]\n"
            "v_pk_fma_f32 v[28:29], v[174:175], v[188:189], v[28:29] op_sel:[1,0,0]\n"
            "v_pk_fma_f32 v[30:31], v[174:175], v[190:191], v[30:31] op_sel:[1,0,0]\n"
            "v_pk_mul_f32 v[152:153], v[152:153], v[136:137]\n"
            "v_pk_mul_f32 v[154:155], v[154:155], v[138:139]\n"
            "v_pk_mul_f32 v[156:157], v[156:157], v[140:141]\n"
            "v_pk_mul_f32 v[158:159], v[158:159], v[142:143]\n"
            "s_cmp_eq_u32 %[rem], 4\n"
            "s_cbranch_scc1 Lf13_%=\n"
            "Lr13_%=:\n"
            "s_and_b32 %[tmp], %[lens], 0xff\n"
            "s_add_u32 %[rem], %[rem], %[tmp]\n"
            "s_lshr_b32 %[lens], %[lens], 8\n"
            "s_and_b32 %[tmp], %[fm], 14\n"
            "s_lshr_b32 %[fm], %[fm], 4\n"
            "s_cmp_lg_u32 %[tmp], 0\n"
            "s_cbranch_scc1 Lb1_%=\n"
            "v_mov_b32 v215, v216\n"
            "s_branch Lnext_%=\n"
            "Lb1_%=:\n"
            "s_cmp_lg_u32 %[tmp], 8\n"
            "s_cbranch_scc1 Lnext_%=\n"
            "s_waitcnt vmcnt(0)\n"
            "v_mov_b32 v176, %[cnt]\n"
            "v_mov_b32 v177, 1\n"
            "s_mov_b64 exec, 1\n"
            "ds_add_rtn_u32 v177, v176, v177\n"
            "s_waitcnt lgkmcnt(0)\n"
            "v_readfirstlane_b32 %[tmp], v177\n"
            "s_mov_b64 exec, 0xff\n"
            "v_mov_b32 v176, %[tag]\n"
            "v_mov_b32 v177, %[tag]\n"
            "v_mov_b32 v178, %[tag]\n"
            "v_mov_b32 v179, %[tag]\n"
            "s_cmp_lg_u32 %[tmp], 7\n"
            "s_cbranch_scc1 Lpub2_%=\n"
            "global_store_dwordx4 v214, v[176:179], %[flg] sc1\n"
            "Lpub2_%=:\n"
            "s_mov_b64 exec, -1\n"
            "s_branch Lnext_%=\n"
            "Lprio_%=:\n"
            "s_cmp_eq_u32 %[nst], 1\n"
            "s_cbranch_scc1 Llast_%=\n"
            "s_lshl_b32 %[tmp], %[nst], 1\n"
            "s_lshr_b32 %[tmp], %[ptab], %[tmp]\n"
            "s_bitcmp1_b32 %[tmp], 1\n"
            "s_cbranch_scc1 Lq2_%=\n"
            "s_bitcmp1_b32 %[tmp], 0\n"
            "s_setprio 0\n"
            "s_cbranch_scc0 Lq9_%=\n"
            "s_setprio 1\n"
            "s_branch Lq9_%=\n"
            "Lq2_%=:\n"
            "s_bitcmp1_b32 %[tmp], 0\n"
            "s_setprio 2\n"
            "s_cbranch_scc0 Lq9_%=\n"
            "s_setprio 3\n"
            "Lq9_%=:\n"
            "s_cmp_lg_u32 %[nst], 2\n"
            "s_cbranch_scc1 Ld2_%=\n"
            "v_mov_b32 v223, 0\n"
            "global_load_dword v223, v223, %[pfl] sc1\n"
            "s_branch Ld2_%=\n"
            "Lf00_%=:\n"
            "s_call_b64 %[ra], Lflush0_%=\n"
            "s_branch Lr00_%=\n"
            "Lf01_%=:\n"
            "s_call_b64 %[ra], Lflush0_%=\n"
            "s_branch Lr01_%=\n"
            "Lf02_%=:\n"
            "s_call_b64 %[ra], Lflush0_%=\n"
            "s_branch Lr02_%=\n"
            "Lf03_%=:\n"
            "s_call_b64 %[ra], Lflush0_%=\n"
            "s_branch Lr03_%=\n"
            "Lflush0_%=:\n"
            "v_mov_b32 v184, %[pfa]\n"
            "s_bitcmp0_b32 %[fm], 0\n"
            "s_cbranch_scc1 Lfa0_%=\n"
            "Lfw_%=:\n"
            "ds_read_b32 v185, v184\n"
            "ds_read_b128 v[176:179], v231 offset:0\n"
            "ds_read_b128 v[180:183], v231 offset:1024\n"
            "s_waitcnt lgkmcnt(0)\n"
            "v_cmp_eq_u32 vcc, 0, v185\n"
            "s_cbranch_vccnz Lfw_%=\n"
            "v_mfma_f32_4x4x4_16b_bf16 v[0:3], v[224:225], v[176:177], v[0:3]\n"
            "v_mfma_f32_4x4x4_16b_bf16 v[4:7], v[224:225], v[178:179], v[4:7]\n"
            "v_mfma_f32_4x4x4_16b_bf16 v[8:11], v[224:225], v[180:181], v[8:11]\n"
            "v_mfma_f32_4x4x4_16b_bf16 v[12:15], v[224:225], v[182:183], v[12:15]\n"
            "s_nop 1\n"
            "Lfa0_%=:\n"
            "v_cvt_pk_bf16_f32 v176, v0, v1\n"
            "v_cvt_pk_bf16_f32 v177, v2, v3\n"
            "v_cvt_pk_bf16_f32 v178, v4, v5\n"
            "v_cvt_pk_bf16_f32 v179, v6, v7\n"
            "v_cvt_pk_bf16_f32 v180, v8, v9\n"
            "v_cvt_pk_bf16_f32 v181, v10, v11\n"
            "v_cvt_pk_bf16_f32 v182, v12, v13\n"
            "v_cvt_pk_bf16_f32 v183, v14, v15\n"
            "s_bitcmp1_b32 %[fm], 1\n"
            "s_cbranch_scc1 Lpark0_%=\n"
            "buffer_store_dwordx4 v[176:179], v215, %[rsw], 0 offen offset:0 sc1\n"
            "buffer_store_dwordx4 v[180:183], v215, %[rsw], 0 offen offset:256 sc1\n"
            "s_branch Lzero0_%=\n"
            "Lpark0_%=:\n"
            "ds_write_b128 v231, v[176:179] offset:0\n"
            "ds_write_b128 v231, v[180:183] offset:1024\n"
            "Lzero0_%=:\n"
            "v_mov_b64 v[0:1], 0\n"
            "v_mov_b64 v[2:3], 0\n"
            "v_mov_b64 v[4:5], 0\n"
            "v_mov_b64 v[6:7], 0\n"
            "v_mov_b64 v[8:9], 0\n"
            "v_mov_b64 v[10:11], 0\n"
            "v_mov_b64 v[12:13], 0\n"
            "v_mov_b64 v[14:15], 0\n"
            "s_setpc_b64 %[ra]\n"
            "Lf10_%=:\n"
            "s_call_b64 %[ra], Lflush1_%=\n"
            "s_branch Lr10_%=\n"
            "Lf11_%=:\n"
            "s_call_b64 %[ra], Lflush1_%=\n"
            "s_branch Lr11_%=\n"
            "Lf12_%=:\n"
            "s_call_b64 %[ra], Lflush1_%=\n"
            "s_branch Lr12_%=\n"
            "Lf13_%=:\n"
            "s_call_b64 %[ra], Lflush1_%=\n"
            "s_branch Lr13_%=\n"
            "Lflush1_%=:\n"
            "v_mov_b32 v184, %[pfa]\n"
            "s_bitcmp0_b32 %[fm], 0\n"
            "s_cbranch_scc1 Lfa1_%=\n"
            "ds_read_b128 v[176:179], v231 offset:2048\n"
            "ds_read_b128 v[180:183], v231 offset:3072\n"
            "s_waitcnt lgkmcnt(0)\n"
            "v_mfma_f32_4x4x4_16b_bf16 v[16:19], v[224:225], v[176:177], v[16:19]\n"
            "v_mfma_f32_4x4x4_16b_bf16 v[20:23], v[224:225], v[178:179], v[20:23]\n"
            "v_mfma_f32_4x4x4_16b_bf16 v[24:27], v[224:225], v[180:181], v[24:27]\n"
            "v_mfma_f32_4x4x4_16b_bf16 v[28:31], v[224:225], v[182:183], v[28:31]\n"
            "s_nop 1\n"
            "Lfa1_%=:\n"
            "v_cvt_pk_bf16_f32 v176, v16, v17\n"
            "v_cvt_pk_bf16_f32 v177, v18, v19\n"
            "v_cvt_pk_bf16_f32 v178, v20, v21\n"
            "v_cvt_pk_bf16_f32 v179, v22, v23\n"
            "v_cvt_pk_bf16_f32 v180, v24, v25\n"
            "v_cvt_pk_bf16_f32 v181, v26, v27\n"
            "v_cvt_pk_bf16_f32 v182, v28, v29\n"
            "v_cvt_pk_bf16_f32 v183, v30, v31\n"
            "s_bitcmp1_b32 %[fm], 1\n"
            "s_cbranch_scc1 Lpark1_%=\n"
            "buffer_store_dwordx4 v[176:179], v215, %[rsw], 0 offen offset:512 sc1\n"
            "buffer_store_dwordx4 v[180:183], v215, %[rsw], 0 offen offset:768 sc1\n"
            "s_branch Lzero1_%=\n"
            "Lpark1_%=:\n"
            "ds_write_b128 v231, v[176:179] offset:2048\n"
            "ds_write_b128 v231, v[180:183] offset:3072\n"
            "ds_write_b32 v184, v184\n"
            "Lzero1_%=:\n"
            "v_mov_b64 v[16:17], 0\n"
            "v_mov_b64 v[18:19], 0\n"
            "v_mov_b64 v[20:21], 0\n"
            "v_mov_b64 v[22:23], 0\n"
            "v_mov_b64 v[24:25], 0\n"
            "v_mov_b64 v[26:27], 0\n"
            "v_mov_b64 v[28:29], 0\n"
            "v_mov_b64 v[30:31], 0\n"
            "s_setpc_b64 %[ra]\n"
            "Lspin_%=:\n"
            "s_mov_b32 %[lens], %[spin]\n"
            "Lsp1_%=:\n"
            "v_readfirstlane_b32 %[nst], v223\n"
            "s_sub_u32 %[lens], %[lens], 1\n"
            "s_cmp_eq_u32 %[lens], 0\n"
            "s_cbranch_scc1 Lsp2_%=\n"
            "s_cmp_eq_u32 %[nst], %[tag]\n"
            "s_cbranch_scc1 Lsp2_%=\n"
            "s_sleep 1\n"
            "v_mov_b32 v223, 0\n"
            "global_load_dword v223, v223, %[pfl] sc1\n"
            "s_waitcnt vmcnt(0)\n"
            "s_branch Lsp1_%=\n"
            "Lsp2_%=:\n"
            "buffer_load_dwordx4 v[32:35], v217, %[rsw], %[oagg] offen offset:0 sc1\n"
            "buffer_load_dwordx4 v[36:39], v217, %[rsw], %[oagg] offen offset:256 sc1\n"
            "buffer_load_dwordx4 v[48:51], v217, %[rsw], %[oagg] offen offset:512 sc1\n"
            "buffer_load_dwordx4 v[52:55], v217, %[rsw], %[oagg] offen offset:768 sc1\n"
            "s_branch Lgo_%=\n"
            "Llast_%=:\n"
            "s_waitcnt vmcnt(16)\n"
            "v_readfirstlane_b32 %[nst], v223\n"
            "v_mov_b32 v222, v217\n"
            "s_cmp_eq_u32 %[nst], %[tag]\n"
            "s_cbranch_scc1 Lp4_%=\n"
            "v_bfrev_b32 v222, 1\n"
            "v_mov_b32 v223, 0\n"
            "global_load_dword v223, v223, %[pfl] sc1\n"
            "Lp4_%=:\n"
            "v_mfma_f32_16x16x32_bf16 v[136:139], v[192:195], v[120:123], 0\n"
            "v_exp_f32 v160, v128\n"
            "v_exp_f32 v161, v129\n"
            "s_waitcnt vmcnt(13) lgkmcnt(0)\n"
            "v_mfma_f32_4x4x4_16b_bf16 v[176:179], v[208:209], v[32:33], 0 cbsz:2\n"
            "v_exp_f32 v162, v130\n"
            "v_mfma_f32_4x4x4_16b_bf16 v[180:183], v[208:209], v[34:35], 0 cbsz:2\n"
            "v_exp_f32 v163, v131\n"
            "v_mfma_f32_4x4x4_16b_bf16 v[184:187], v[208:209], v[36:37], 0 cbsz:2\n"
            "v_exp_f32 v164, v132\n"
            "v_mfma_f32_4x4x4_16b_bf16 v[188:191], v[208:209], v[38:39], 0 cbsz:2\n"
            "v_exp_f32 v165, v133\n"
            "v_exp_f32 v166, v134\n"
            "buffer_load_dwordx4 v[32:35], v222, %[rsw], %[oagg] offen offset:0 sc1\n"
            "buffer_load_dwordx4 v[36:39], v222, %[rsw], %[oagg] offen offset:256 sc1\n"
            "ds_read_b128 v[236:239], v228 offset:0\n"
            "ds_read_b128 v[240:243], v228 offset:1024\n"
            "ds_read_b128 v[244:247], v228 offset:2048\n"
            "ds_read_b128 v[248:251], v228 offset:3072\n"
            "ds_read_b128 v[96:99], v228 offset:4096\n"
            "v_exp_f32 v167, v135\n"
            "v_pk_fma_f32 v[0:1], v[144:145], v[176:177], v[0:1] op_sel_hi:[0,1,1]\n"
            "v_pk_fma_f32 v[2:3], v[144:145], v[178:179], v[2:3] op_sel_hi:[0,1,1]\n"
            "v_pk_fma_f32 v[4:5], v[148:149], v[180:181], v[4:5] op_sel_hi:[0,1,1]\n"
            "v_pk_fma_f32 v[6:7], v[148:149], v[182:183], v[6:7] op_sel_hi:[0,1,1]\n"
            "v_pk_fma_f32 v[8:9], v[152:153], v[184:185], v[8:9] op_sel_hi:[0,1,1]\n"
            "v_pk_fma_f32 v[10:11], v[152:153], v[186:187], v[10:11] op_sel_hi:[0,1,1]\n"
            "v_pk_fma_f32 v[12:13], v[156:157], v[188:189], v[12:13] op_sel_hi:[0,1,1]\n"
            "v_pk_fma_f32 v[14:15], v[156:157], v[190:191], v[14:15] op_sel_hi:[0,1,1]\n"
            "v_pk_add_f32 v[160:161], v[160:161], 1.0 op_sel_hi:[1,0]\n"
            "v_pk_add_f32 v[162:163], v[162:163], 1.0 op_sel_hi:[1,0]\n"
            "v_pk_add_f32 v[164:165], v[164:165], 1.0 op_sel_hi:[1,0]\n"
            "v_pk_add_f32 v[166:167], v[166:167], 1.0 op_sel_hi:[1,0]\n"
            "v_mfma_f32_16x16x32_bf16 v[140:143], v[192:195], v[124:127], 0\n"
            "v_rcp_f32 v160, v160\n"
            "v_rcp_f32 v161, v161\n"
            "v_mfma_f32_4x4x4_16b_bf16 v[176:179], v[208:209], v[48:49], 0 cbsz:2 abid:1\n"
            "v_rcp_f32 v162, v162\n"
            "v_mfma_f32_4x4x4_16b_bf16 v[180:183], v[208:209], v[50:51], 0 cbsz:2 abid:1\n"
            "v_rcp_f32 v163, v163\n"
            "v_mfma_f32_4x4x4_16b_bf16 v[184:187], v[208:209], v[52:53], 0 cbsz:2 abid:1\n"
            "v_rcp_f32 v164, v164\n"
            "v_mfma_f32_4x4x4_16b_bf16 v[188:191], v[208:209], v[54:55], 0 cbsz:2 abid:1\n"
            "v_rcp_f32 v165, v165\n"
            "v_rcp_f32 v166, v166\n"
            "buffer_load_dwordx4 v[48:51], v222, %[rsw], %[oagg] offen offset:512 sc1\n"
            "buffer_load_dwordx4 v[52:55], v222, %[rsw], %[oagg] offen offset:768 sc1\n"
            "ds_read_b128 v[100:103], v228 offset:5120\n"
            "ds_read_b128 v[104:107], v228 offset:6144\n"
            "ds_read_b128 v[108:111], v228 offset:7168\n"
            "ds_read_b128 v[112:115], v228 offset:8192\n"
            "ds_read_b128 v[116:119], v228 offset:9216\n"
            "ds_read_b128 v[196:199], v228 offset:10240\n"
            "v_rcp_f32 v167, v167\n"
            "v_pk_fma_f32 v[0:1], v[144:145], v[176:177], v[0:1] op_sel:[1,0,0]\n"
            "v_pk_fma_f32 v[2:3], v[144:145], v[178:179], v[2:3] op_sel:[1,0,0]\n"
            "v_pk_fma_f32 v[4:5], v[148:149], v[180:181], v[4:5] op_sel:[1,0,0]\n"
            "v_pk_fma_f32 v[6:7], v[148:149], v[182:183], v[6:7] op_sel:[1,0,0]\n"
            "v_pk_fma_f32 v[8:9], v[152:153], v[184:185], v[8:9] op_sel:[1,0,0]\n"
            "v_pk_fma_f32 v[10:11], v[152:153], v[186:187], v[10:11] op_sel:[1,0,0]\n"
            "v_pk_fma_f32 v[12:13], v[156:157], v[188:189], v[12:13] op_sel:[1,0,0]\n"
            "v_pk_fma_f32 v[14:15], v[156:157], v[190:191], v[14:15] op_sel:[1,0,0]\n"
            "v_pk_mul_f32 v[160:161], v[160:161], v[128:129]\n"
            "v_pk_mul_f32 v[162:163], v[162:163], v[130:131]\n"
            "v_pk_mul_f32 v[164:165], v[164:165], v[132:133]\n"
            "v_pk_mul_f32 v[166:167], v[166:167], v[134:135]\n"
            "v_exp_f32 v168, v136\n"
            "v_exp_f32 v169, v137\n"
            "s_waitcnt vmcnt(12)\n"
            "v_mfma_f32_4x4x4_16b_bf16 v[176:179], v[208:209], v[64:65], 0 cbsz:2 abid:2\n"
            "v_exp_f32 v170, v138\n"
            "v_mfma_f32_4x4x4_16b_bf16 v[180:183], v[208:209], v[66:67], 0 cbsz:2 abid:2\n"
            "v_exp_f32 v171, v139\n"
            "v_mfma_f32_4x4x4_16b_bf16 v[184:187], v[208:209], v[68:69], 0 cbsz:2 abid:2\n"
            "v_exp_f32 v172, v140\n"
            "v_mfma_f32_4x4x4_16b_bf16 v[188:191], v[208:209], v[70:71], 0 cbsz:2 abid:2\n"
            "v_exp_f32 v173, v141\n"
            "v_exp_f32 v174, v142\n"
            "ds_read_b128 v[200:203], v228 offset:11264\n"
            "ds_read_b128 v[204:207], v228 offset:12288\n"
            "ds_read_b128 v[232:235], v228 offset:13312\n"
            "v_exp_f32 v175, v143\n"
            "v_pk_fma_f32 v[0:1], v[146:147], v[176:177], v[0:1] op_sel_hi:[0,1,1]\n"
            "v_pk_fma_f32 v[2:3], v[146:147], v[178:179], v[2:3] op_sel_hi:[0,1,1]\n"
            "v_pk_fma_f32 v[4:5], v[150:151], v[180:181], v[4:5] op_sel_hi:[0,1,1]\n"
            "v_pk_fma_f32 v[6:7], v[150:151], v[182:183], v[6:7] op_sel_hi:[0,1,1]\n"
            "v_pk_fma_f32 v[8:9], v[154:155], v[184:185], v[8:9] op_sel_hi:[0,1,1]\n"
            "v_pk_fma_f32 v[10:11], v[154:155], v[186:187], v[10:11] op_sel_hi:[0,1,1]\n"
            "v_pk_fma_f32 v[12:13], v[158:159], v[188:189], v[12:13] op_sel_hi:[0,1,1]\n"
            "v_pk_fma_f32 v[14:15], v[158:159], v[190:191], v[14:15] op_sel_hi:[0,1,1]\n"
            "v_pk_add_f32 v[168:169], v[168:169], 1.0 op_sel_hi:[1,0]\n"
            "v_pk_add_f32 v[170:171], v[170:171], 1.0 op_sel_hi:[1,0]\n"
            "v_pk_add_f32 v[172:173], v[172:173], 1.0 op_sel_hi:[1,0]\n"
            "v_pk_add_f32 v[174:175], v[174:175], 1.0 op_sel_hi:[1,0]\n"
            "v_rcp_f32 v168, v168\n"
            "v_rcp_f32 v169, v169\n"
            "v_mfma_f32_4x4x4_16b_bf16 v[176:179], v[208:209], v[80:81], 0 cbsz:2 abid:3\n"
            "v_rcp_f32 v170, v170\n"
            "v_mfma_f32_4x4x4_16b_bf16 v[180:183], v[208:209], v[82:83], 0 cbsz:2 abid:3\n"
            "v_rcp_f32 v171, v171\n"
            "v_mfma_f32_4x4x4_16b_bf16 v[184:187], v[208:209], v[84:85], 0 cbsz:2 abid:3\n"
            "v_rcp_f32 v172, v172\n"
            "v_mfma_f32_4x4x4_16b_bf16 v[188:191], v[208:209], v[86:87], 0 cbsz:2 abid:3\n"
            "v_rcp_f32 v173, v173\n"
            "v_rcp_f32 v174, v174\n"
            "ds_read_b128 v[120:123], v228 offset:14336\n"
            "ds_read_b128 v[124:127], v228 offset:15360\n"
            "ds_read_b128 v[192:195], v228 offset:16384\n"
            "ds_read_b128 v[128:131], v228 offset:17408\n"
            "ds_read_b128 v[132:135], v228 offset:18432\n"
            "v_rcp_f32 v175, v175\n"
            "v_pk_fma_f32 v[0:1], v[146:147], v[176:177], v[0:1] op_sel:[1,0,0]\n"
            "v_pk_fma_f32 v[2:3], v[146:147], v[178:179], v[2:3] op_sel:[1,0,0]\n"
            "v_pk_fma_f32 v[4:5], v[150:151], v[180:181], v[4:5] op_sel:[1,0,0]\n"
            "v_pk_fma_f32 v[6:7], v[150:151], v[182:183], v[6:7] op_sel:[1,0,0]\n"
            "v_pk_fma_f32 v[8:9], v[154:155], v[184:185], v[8:9] op_sel:[1,0,0]\n"
            "v_pk_fma_f32 v[10:11], v[154:155], v[186:187], v[10:11] op_sel:[1,0,0]\n"
            "v_pk_fma_f32 v[12:13], v[158:159], v[188:189], v[12:13] op_sel:[1,0,0]\n"
            "v_pk_fma_f32 v[14:15], v[158:159], v[190:191], v[14:15] op_sel:[1,0,0]\n"
            "v_pk_mul_f32 v[168:169], v[168:169], v[136:137]\n"
            "v_pk_mul_f32 v[170:171], v[170:171], v[138:139]\n"
            "v_pk_mul_f32 v[172:173], v[172:173], v[140:141]\n"
            "v_pk_mul_f32 v[174:175], v[174:175], v[142:143]\n"
            "s_waitcnt vmcnt(8)\n"
            "v_mfma_f32_4x4x4_16b_bf16 v[176:179], v[208:209], v[40:41], 0 cbsz:2\n"
            "v_mfma_f32_4x4x4_16b_bf16 v[180:183], v[208:209], v[42:43], 0 cbsz:2\n"
            "v_mfma_f32_4x4x4_16b_bf16 v[184:187], v[208:209], v[44:45], 0 cbsz:2\n"
            "v_mfma_f32_4x4x4_16b_bf16 v[188:191], v[208:209], v[46:47], 0 cbsz:2\n"
            "ds_read_b128 v[136:139], v228 offset:19456\n"
            "ds_read_b128 v[140:143], v228 offset:20480\n"
            "ds_read_b128 v[144:147], v228 offset:21504\n"
            "ds_read_b128 v[148:151], v228 offset:22528\n"
            "ds_read_b128 v[152:155], v228 offset:23552\n"
            "ds_read_b128 v[156:159], v228 offset:24576\n"
            "s_nop 1\n"
            "v_pk_fma_f32 v[16:17], v[160:161], v[176:177], v[16:17] op_sel_hi:[0,1,1]\n"
            "v_pk_fma_f32 v[18:19], v[160:161], v[178:179], v[18:19] op_sel_hi:[0,1,1]\n"
            "v_pk_fma_f32 v[20:21], v[164:165], v[180:181], v[20:21] op_sel_hi:[0,1,1]\n"
            "v_pk_fma_f32 v[22:23], v[164:165], v[182:183], v[22:23] op_sel_hi:[0,1,1]\n"
            "v_pk_fma_f32 v[24:25], v[168:169], v[184:185], v[24:25] op_sel_hi:[0,1,1]\n"
            "v_pk_fma_f32 v[26:27], v[168:169], v[186:187], v[26:27] op_sel_hi:[0,1,1]\n"
            "v_pk_fma_f32 v[28:29], v[172:173], v[188:189], v[28:29] op_sel_hi:[0,1,1]\n"
            "v_pk_fma_f32 v[30:31], v[172:173], v[190:191], v[30:31] op_sel_hi:[0,1,1]\n"
            "s_cmp_le_u32 %[njl], 1\n"
            "s_cbranch_scc1 Ltail_%=\n"
            "v_mfma_f32_4x4x4_16b_bf16 v[176:179], v[208:209], v[56:57], 0 cbsz:2 abid:1\n"
            "v_mfma_f32_4x4x4_16b_bf16 v[180:183], v[208:209], v[58:59], 0 cbsz:2 abid:1\n"
            "v_mfma_f32_4x4x4_16b_bf16 v[184:187], v[208:209], v[60:61], 0 cbsz:2 abid:1\n"
            "v_mfma_f32_4x4x4_16b_bf16 v[188:191], v[208:209], v[62:63], 0 cbsz:2 abid:1\n"
            "s_nop 1\n"
            "v_pk_fma_f32 v[16:17], v[160:161], v[176:177], v[16:17] op_sel:[1,0,0]\n"
            "v_pk_fma_f32 v[18:19], v[160:161], v[178:179], v[18:19] op_sel:[1,0,0]\n"
            "v_pk_fma_f32 v[20:21], v[164:165], v[180:181], v[20:21] op_sel:[1,0,0]\n"
            "v_pk_fma_f32 v[22:23], v[164:165], v[182:183], v[22:23] op_sel:[1,0,0]\n"
            "v_pk_fma_f32 v[24:25], v[168:169], v[184:185], v[24:25] op_sel:[1,0,0]\n"
            "v_pk_fma_f32 v[26:27], v[168:169], v[186:187], v[26:27] op_sel:[1,0,0]\n"
            "v_pk_fma_f32 v[28:29], v[172:173], v[188:189], v[28:29] op_sel:[1,0,0]\n"
            "v_pk_fma_f32 v[30:31], v[172:173], v[190:191], v[30:31] op_sel:[1,0,0]\n"
            "s_cmp_le_u32 %[njl], 2\n"
            "s_cbranch_scc1 Ltail_%=\n"
            "s_waitcnt vmcnt(4)\n"
            "v_mfma_f32_4x4x4_16b_bf16 v[176:179], v[208:209], v[72:73], 0 cbsz:2 abid:2\n"
            "v_mfma_f32_4x4x4_16b_bf16 v[180:183], v[208:209], v[74:75], 0 cbsz:2 abid:2\n"
            "v_mfma_f32_4x4x4_16b_bf16 v[184:187], v[208:209], v[76:77], 0 cbsz:2 abid:2\n"
            "v_mfma_f32_4x4x4_16b_bf16 v[188:191], v[208:209], v[78:79], 0 cbsz:2 abid:2\n"
            "s_nop 1\n"
            "v_pk_fma_f32 v[16:17], v[162:163], v[176:177], v[16:17] op_sel_hi:[0,1,1]\n"
            "v_pk_fma_f32 v[18:19], v[162:163], v[178:179], v[18:19] op_sel_hi:[0,1,1]\n"
            "v_pk_fma_f32 v[20:21], v[166:167], v[180:181], v[20:21] op_sel_hi:[0,1,1]\n"
            "v_pk_fma_f32 v[22:23], v[166:167], v[182:183], v[22:23] op_sel_hi:[0,1,1]\n"
            "v_pk_fma_f32 v[24:25], v[170:171], v[184:185], v[24:25] op_sel_hi:[0,1,1]\n"
            "v_pk_fma_f32 v[26:27], v[170:171], v[186:187], v[26:27] op_sel_hi:[0,1,1]\n"
            "v_pk_fma_f32 v[28:29], v[174:175], v[188:189], v[28:29] op_sel_hi:[0,1,1]\n"
            "v_pk_fma_f32 v[30:31], v[174:175], v[190:191], v[30:31] op_sel_hi:[0,1,1]\n"
            "s_cmp_le_u32 %[njl], 3\n"
            "s_cbranch_scc1 Ltail_%=\n"
            "v_mfma_f32_4x4x4_16b_bf16 v[176:179], v[208:209], v[88:89], 0 cbsz:2 abid:3\n"
            "v_mfma_f32_4x4x4_16b_bf16 v[180:183], v[208:209], v[90:91], 0 cbsz:2 abid:3\n"
            "v_mfma_f32_4x4x4_16b_bf16 v[184:187], v[208:209], v[92:93], 0 cbsz:2 abid:3\n"
            "v_mfma_f32_4x4x4_16b_bf16 v[188:191], v[208:209], v[94:95], 0 cbsz:2 abid:3\n"
            "s_nop 1\n"
            "v_pk_fma_f32 v[16:17], v[162:163], v[176:177], v[16:17] op_sel:[1,0,0]\n"
            "v_pk_fma_f32 v[18:19], v[162:163], v[178:179], v[18:19] op_sel:[1,0,0]\n"
            "v_pk_fma_f32 v[20:21], v[166:167], v[180:181], v[20:21] op_sel:[1,0,0]\n"
            "v_pk_fma_f32 v[22:23], v[166:167], v[182:183], v[22:23] op_sel:[1,0,0]\n"
            "v_pk_fma_f32 v[24:25], v[170:171], v[184:185], v[24:25] op_sel:[1,0,0]\n"
            "v_pk_fma_f32 v[26:27], v[170:171], v[186:187], v[26:27] op_sel:[1,0,0]\n"
            "v_pk_fma_f32 v[28:29], v[174:175], v[188:189], v[28:29] op_sel:[1,0,0]\n"
            "v_pk_fma_f32 v[30:31], v[174:175], v[190:191], v[30:31] op_sel:[1,0,0]\n"
            "Ltail_%=:\n"
            "s_setprio 3\n"
            "s_waitcnt vmcnt(4)\n"
            "ds_read_b128 v[72:75], v228 offset:25600\n"
            "ds_read_b128 v[76:79], v228 offset:26624\n"
            "ds_read_b128 v[88:91], v228 offset:27648\n"
            "ds_read_b128 v[92:95], v228 offset:28672\n"
            "ds_read_b128 v[160:163], v228 offset:29696\n"
            "ds_read_b128 v[164:167], v228 offset:30720\n"
            "ds_read_b128 v[168:171], v228 offset:31744\n"
            "s_cmp_lg_u32 %[nst], %[tag]\n"
            "s_cbranch_scc1 Lspin_%=\n"
            "Lgo_%=:\n"
            "buffer_load_dwordx4 v[40:43], v218, %[rsw], %[oagg] offen offset:0 sc1\n"
            "buffer_load_dwordx4 v[44:47], v218, %[rsw], %[oagg] offen offset:256 sc1\n"
            "buffer_load_dwordx4 v[56:59], v218, %[rsw], %[oagg] offen offset:512 sc1\n"
            "buffer_load_dwordx4 v[60:63], v218, %[rsw], %[oagg] offen offset:768 sc1\n"
            "s_waitcnt vmcnt(4)\n"
            "v_mfma_f32_4x4x4_16b_bf16 v[0:3], v[224:225], v[32:33], v[0:3]\n"
            "v_mfma_f32_4x4x4_16b_bf16 v[4:7], v[224:225], v[34:35], v[4:7]\n"
            "v_mfma_f32_4x4x4_16b_bf16 v[8:11], v[224:225], v[36:37], v[8:11]\n"
            "v_mfma_f32_4x4x4_16b_bf16 v[12:15], v[224:225], v[38:39], v[12:15]\n"
            "v_mfma_f32_4x4x4_16b_bf16 v[16:19], v[224:225], v[48:49], v[16:19]\n"
            "v_mfma_f32_4x4x4_16b_bf16 v[20:23], v[224:225], v[50:51], v[20:23]\n"
            "v_mfma_f32_4x4x4_16b_bf16 v[24:27], v[224:225], v[52:53], v[24:27]\n"
            "v_mfma_f32_4x4x4_16b_bf16 v[28:31], v[224:225], v[54:55], v[28:31]\n"
            "s_nop 1\n"
            "v_cvt_pk_bf16_f32 v176, v0, v4\n"
            "v_cvt_pk_bf16_f32 v177, v8, v12\n"
            "v_cvt_pk_bf16_f32 v178, v16, v20\n"
            "v_cvt_pk_bf16_f32 v179, v24, v28\n"
            "ds_write_b128 v226, v[176:179] offset:0\n"
            "v_cvt_pk_bf16_f32 v180, v1, v5\n"
            "v_cvt_pk_bf16_f32 v181, v9, v13\n"
            "v_cvt_pk_bf16_f32 v182, v17, v21\n"
            "v_cvt_pk_bf16_f32 v183, v25, v29\n"
            "ds_write_b128 v226, v[180:183] offset:272\n"
            "v_cvt_pk_bf16_f32 v184, v2, v6\n"
            "v_cvt_pk_bf16_f32 v185, v10, v14\n"
            "v_cvt_pk_bf16_f32 v186, v18, v22\n"
            "v_cvt_pk_bf16_f32 v187, v26, v30\n"
            "ds_write_b128 v226, v[184:187] offset:544\n"
            "v_cvt_pk_bf16_f32 v188, v3, v7\n"
            "v_cvt_pk_bf16_f32 v189, v11, v15\n"
            "v_cvt_pk_bf16_f32 v190, v19, v23\n"
            "v_cvt_pk_bf16_f32 v191, v27, v31\n"
            "ds_write_b128 v226, v[188:191] offset:816\n"
            "ds_read_b128 v[176:179], v227 offset:0\n"
            "ds_read_b128 v[180:183], v227 offset:64\n"
            "ds_read_b128 v[184:187], v227 offset:128\n"
            "ds_read_b128 v[188:191], v227 offset:192\n"
            "s_waitcnt lgkmcnt(3)\n"
            "v_mfma_f32_16x16x32_bf16 v[0:3], v[176:179], v[236:239], 0\n"
            "v_mfma_f32_16x16x32_bf16 v[4:7], v[176:179], v[96:99], 0\n"
            "s_waitcnt lgkmcnt(2)\n"
            "v_mfma_f32_16x16x32_bf16 v[0:3], v[180:183], v[240:243], v[0:3]\n"
            "v_mfma_f32_16x16x32_bf16 v[4:7], v[180:183], v[100:103], v[4:7]\n"
            "s_waitcnt lgkmcnt(1)\n"
            "v_mfma_f32_16x16x32_bf16 v[0:3], v[184:187], v[244:247], v[0:3]\n"
            "v_mfma_f32_16x16x32_bf16 v[4:7], v[184:187], v[104:107], v[4:7]\n"
            "s_waitcnt lgkmcnt(0)\n"
            "v_mfma_f32_16x16x32_bf16 v[0:3], v[188:191], v[248:251], v[0:3]\n"
            "v_mfma_f32_16x16x32_bf16 v[4:7], v[188:191], v[108:111], v[4:7]\n"
            "v_mfma_f32_16x16x32_bf16 v[8:11], v[176:179], v[112:115], 0\n"
            "v_mfma_f32_16x16x32_bf16 v[12:15], v[176:179], v[204:207], 0\n"
            "v_mfma_f32_16x16x32_bf16 v[8:11], v[180:183], v[116:119], v[8:11]\n"
            "v_mfma_f32_16x16x32_bf16 v[12:15], v[180:183], v[232:235], v[12:15]\n"
            "v_mfma_f32_16x16x32_bf16 v[8:11], v[184:187], v[196:199], v[8:11]\n"
            "v_mfma_f32_16x16x32_bf16 v[12:15], v[184:187], v[120:123], v[12:15]\n"
            "v_mfma_f32_16x16x32_bf16 v[8:11], v[188:191], v[200:203], v[8:11]\n"
            "v_mfma_f32_16x16x32_bf16 v[12:15], v[188:191], v[124:127], v[12:15]\n"
            "v_cvt_pk_bf16_f32 v0, v0, v1\n"
            "v_cvt_pk_bf16_f32 v1, v2, v3\n"
            "v_cvt_pk_bf16_f32 v2, v4, v5\n"
            "v_cvt_pk_bf16_f32 v3, v6, v7\n"
            "buffer_store_dwordx4 v[0:3], v217, %[rso], 0 offen offset:0 sc1\n"
            "v_mfma_f32_16x16x32_bf16 v[16:19], v[176:179], v[192:195], 0\n"
            "v_mfma_f32_16x16x32_bf16 v[20:23], v[176:179], v[140:143], 0\n"
            "v_mfma_f32_16x16x32_bf16 v[16:19], v[180:183], v[128:131], v[16:19]\n"
            "v_mfma_f32_16x16x32_bf16 v[20:23], v[180:183], v[144:147], v[20:23]\n"
            "v_mfma_f32_16x16x32_bf16 v[16:19], v[184:187], v[132:135], v[16:19]\n"
            "v_mfma_f32_16x16x32_bf16 v[20:23], v[184:187], v[148:151], v[20:23]\n"
            "v_mfma_f32_16x16x32_bf16 v[16:19], v[188:191], v[136:139], v[16:19]\n"
            "v_mfma_f32_16x16x32_bf16 v[20:23], v[188:191], v[152:155], v[20:23]\n"
            "v_cvt_pk_bf16_f32 v8, v8, v9\n"
            "v_cvt_pk_bf16_f32 v9, v10, v11\n"
            "v_cvt_pk_bf16_f32 v10, v12, v13\n"
            "v_cvt_pk_bf16_f32 v11, v14, v15\n"
            "buffer_store_dwordx4 v[8:11], v217, %[rso], 0 offen offset:256 sc1\n"
            "v_mfma_f32_16x16x32_bf16 v[24:27], v[176:179], v[156:159], 0\n"
            "v_mfma_f32_16x16x32_bf16 v[28:31], v[176:179], v[92:95], 0\n"
            "v_mfma_f32_16x16x32_bf16 v[24:27], v[180:183], v[72:75], v[24:27]\n"
            "v_mfma_f32_16x16x32_bf16 v[28:31], v[180:183], v[160:163], v[28:31]\n"
            "v_mfma_f32_16x16x32_bf16 v[24:27], v[184:187], v[76:79], v[24:27]\n"
            "v_mfma_f32_16x16x32_bf16 v[28:31], v[184:187], v[164:167], v[28:31]\n"
            "v_mfma_f32_16x16x32_bf16 v[24:27], v[188:191], v[88:91], v[24:27]\n"
            "v_mfma_f32_16x16x32_bf16 v[28:31], v[188:191], v[168:171], v[28:31]\n"
            "v_cvt_pk_bf16_f32 v16, v16, v17\n"
            "v_cvt_pk_bf16_f32 v17, v18, v19\n"
            "v_cvt_pk_bf16_f32 v18, v20, v21\n"
            "v_cvt_pk_bf16_f32 v19, v22, v23\n"
            "buffer_store_dwordx4 v[16:19], v217, %[rso], 0 offen offset:512 sc1\n"
            "s_nop 2\n"
            "v_cvt_pk_bf16_f32 v24, v24, v25\n"
            "v_cvt_pk_bf16_f32 v25, v26, v27\n"
            "v_cvt_pk_bf16_f32 v26, v28, v29\n"
            "v_cvt_pk_bf16_f32 v27, v30, v31\n"
            "buffer_store_dwordx4 v[24:27], v217, %[rso], 0 offen offset:768 sc1\n"
            "s_cmp_lt_u32 %[two], 6\n"
            "s_cbranch_scc1 Lend_%=\n"
            "ds_read_b128 v[64:67], v231 offset:0\n"
            "ds_read_b128 v[68:71], v231 offset:1024\n"
            "ds_read_b128 v[80:83], v231 offset:2048\n"
            "ds_read_b128 v[84:87], v231 offset:3072\n"
            "s_waitcnt vmcnt(4) lgkmcnt(0)\n"
            "v_mfma_f32_4x4x4_16b_bf16 v[0:3], v[224:225], v[64:65], 0\n"
            "v_mfma_f32_4x4x4_16b_bf16 v[4:7], v[224:225], v[66:67], 0\n"
            "v_mfma_f32_4x4x4_16b_bf16 v[8:11], v[224:225], v[68:69], 0\n"
            "v_mfma_f32_4x4x4_16b_bf16 v[12:15], v[224:225], v[70:71], 0\n"
            "v_mfma_f32_4x4x4_16b_bf16 v[16:19], v[224:225], v[80:81], 0\n"
            "v_mfma_f32_4x4x4_16b_bf16 v[20:23], v[224:225], v[82:83], 0\n"
            "v_mfma_f32_4x4x4_16b_bf16 v[24:27], v[224:225], v[84:85], 0\n"
            "v_mfma_f32_4x4x4_16b_bf16 v[28:31], v[224:225], v[86:87], 0\n"
            "v_mfma_f32_4x4x4_16b_bf16 v[0:3], v[224:225], v[40:41], v[0:3]\n"
            "v_mfma_f32_4x4x4_16b_bf16 v[4:7], v[224:225], v[42:43], v[4:7]\n"
            "v_mfma_f32_4x4x4_16b_bf16 v[8:11], v[224:225], v[44:45], v[8:11]\n"
            "v_mfma_f32_4x4x4_16b_bf16 v[12:15], v[224:225], v[46:47], v[12:15]\n"
            "v_mfma_f32_4x4x4_16b_bf16 v[16:19], v[224:225], v[56:57], v[16:19]\n"
            "v_mfma_f32_4x4x4_16b_bf16 v[20:23], v[224:225], v[58:59], v[20:23]\n"
            "v_mfma_f32_4x4x4_16b_bf16 v[24:27], v[224:225], v[60:61], v[24:27]\n"
            "v_mfma_f32_4x4x4_16b_bf16 v[28:31], v[224:225], v[62:63], v[28:31]\n"
            "s_nop 1\n"
            "v_cvt_pk_bf16_f32 v176, v0, v4\n"
            "v_cvt_pk_bf16_f32 v177, v8, v12\n"
            "v_cvt_pk_bf16_f32 v178, v16, v20\n"
            "v_cvt_pk_bf16_f32 v179, v24, v28\n"
            "ds_write_b128 v226, v[176:179] offset:0\n"
            "v_cvt_pk_bf16_f32 v180, v1, v5\n"
            "v_cvt_pk_bf16_f32 v181, v9, v13\n"
            "v_cvt_pk_bf16_f32 v182, v17, v21\n"
            "v_cvt_pk_bf16_f32 v183, v25, v29\n"
            "ds_write_b128 v226, v[180:183] offset:272\n"
            "v_cvt_pk_bf16_f32 v184, v2, v6\n"
            "v_cvt_pk_bf16_f32 v185, v10, v14\n"
            "v_cvt_pk_bf16_f32 v186, v18, v22\n"
            "v_cvt_pk_bf16_f32 v187, v26, v30\n"
            "ds_write_b128 v226, v[184:187] offset:544\n"
            "v_cvt_pk_bf16_f32 v188, v3, v7\n"
            "v_cvt_pk_bf16_f32 v189, v11, v15\n"
            "v_cvt_pk_bf16_f32 v190, v19, v23\n"
            "v_cvt_pk_bf16_f32 v191, v27, v31\n"
            "ds_write_b128 v226, v[188:191] offset:816\n"
            "ds_read_b128 v[176:179], v227 offset:0\n"
            "ds_read_b128 v[180:183], v227 offset:64\n"
            "ds_read_b128 v[184:187], v227 offset:128\n"
            "ds_read_b128 v[188:191], v227 offset:192\n"
            "s_waitcnt lgkmcnt(3)\n"
            "v_mfma_f32_16x16x32_bf16 v[0:3], v[176:179], v[236:239], 0\n"
            "v_mfma_f32_16x16x32_bf16 v[4:7], v[176:179], v[96:99], 0\n"
            "s_waitcnt lgkmcnt(2)\n"
            "v_mfma_f32_16x16x32_bf16 v[0:3], v[180:183], v[240:243], v[0:3]\n"
            "v_mfma_f32_16x16x32_bf16 v[4:7], v[180:183], v[100:103], v[4:7]\n"
            "s_waitcnt lgkmcnt(1)\n"
            "v_mfma_f32_16x16x32_bf16 v[0:3], v[184:187], v[244:247], v[0:3]\n"
            "v_mfma_f32_16x16x32_bf16 v[4:7], v[184:187], v[104:107], v[4:7]\n"
            "s_waitcnt lgkmcnt(0)\n"
            "v_mfma_f32_16x16x32_bf16 v[0:3], v[188:191], v[248:251], v[0:3]\n"
            "v_mfma_f32_16x16x32_bf16 v[4:7], v[188:191], v[108:111], v[4:7]\n"
            "v_mfma_f32_16x16x32_bf16 v[8:11], v[176:179], v[112:115], 0\n"
            "v_mfma_f32_16x16x32_bf16 v[12:15], v[176:179], v[204:207], 0\n"
            "v_mfma_f32_16x16x32_bf16 v[8:11], v[180:183], v[116:119], v[8:11]\n"
            "v_mfma_f32_16x16x32_bf16 v[12:15], v[180:183], v[232:235], v[12:15]\n"
            "v_mfma_f32_16x16x32_bf16 v[8:11], v[184:187], v[196:199], v[8:11]\n"
            "v_mfma_f32_16x16x32_bf16 v[12:15], v[184:187], v[120:123], v[12:15]\n"
            "v_mfma_f32_16x16x32_bf16 v[8:11], v[188:191], v[200:203], v[8:11]\n"
            "v_mfma_f32_16x16x32_bf16 v[12:15], v[188:191], v[124:127], v[12:15]\n"
            "v_cvt_pk_bf16_f32 v0, v0, v1\n"
            "v_cvt_pk_bf16_f32 v1, v2, v3\n"
            "v_cvt_pk_bf16_f32 v2, v4, v5\n"
            "v_cvt_pk_bf16_f32 v3, v6, v7\n"
            "buffer_store_dwordx4 v[0:3], v218, %[rso], 0 offen offset:0 sc1\n"
            "v_mfma_f32_16x16x32_bf16 v[16:19], v[176:179], v[192:195], 0\n"
            "v_mfma_f32_16x16x32_bf16 v[20:23], v[176:179], v[140:143], 0\n"
            "v_mfma_f32_16x16x32_bf16 v[16:19], v[180:183], v[128:131], v[16:19]\n"
            "v_mfma_f32_16x16x32_bf16 v[20:23], v[180:183], v[144:147], v[20:23]\n"
            "v_mfma_f32_16x16x32_bf16 v[16:19], v[184:187], v[132:135], v[16:19]\n"
            "v_mfma_f32_16x16x32_bf16 v[20:23], v[184:187], v[148:151], v[20:23]\n"
            "v_mfma_f32_16x16x32_bf16 v[16:19], v[188:191], v[136:139], v[16:19]\n"
            "v_mfma_f32_16x16x32_bf16 v[20:23], v[188:191], v[152:155], v[20:23]\n"
            "v_cvt_pk_bf16_f32 v8, v8, v9\n"
            "v_cvt_pk_bf16_f32 v9, v10, v11\n"
            "v_cvt_pk_bf16_f32 v10, v12, v13\n"
            "v_cvt_pk_bf16_f32 v11, v14, v15\n"
            "buffer_store_dwordx4 v[8:11], v218, %[rso], 0 offen offset:256 sc1\n"
            "v_mfma_f32_16x16x32_bf16 v[24:27], v[176:179], v[156:159], 0\n"
            "v_mfma_f32_16x16x32_bf16 v[28:31], v[176:179], v[92:95], 0\n"
            "v_mfma_f32_16x16x32_bf16 v[24:27], v[180:183], v[72:75], v[24:27]\n"
            "v_mfma_f32_16x16x32_bf16 v[28:31], v[180:183], v[160:163], v[28:31]\n"
            "v_mfma_f32_16x16x32_bf16 v[24:27], v[184:187], v[76:79], v[24:27]\n"
            "v_mfma_f32_16x16x32_bf16 v[28:31], v[184:187], v[164:167], v[28:31]\n"
            "v_mfma_f32_16x16x32_bf16 v[24:27], v[188:191], v[88:91], v[24:27]\n"
            "v_mfma_f32_16x16x32_bf16 v[28:31], v[188:191], v[168:171], v[28:31]\n"
            "v_cvt_pk_bf16_f32 v16, v16, v17\n"
            "v_cvt_pk_bf16_f32 v17, v18, v19\n"
            "v_cvt_pk_bf16_f32 v18, v20, v21\n"
            "v_cvt_pk_bf16_f32 v19, v22, v23\n"
            "buffer_store_dwordx4 v[16:19], v218, %[rso], 0 offen offset:512 sc1\n"
            "s_nop 2\n"
            "v_cvt_pk_bf16_f32 v24, v24, v25\n"
            "v_cvt_pk_bf16_f32 v25, v26, v27\n"
            "v_cvt_pk_bf16_f32 v26, v28, v29\n"
            "v_cvt_pk_bf16_f32 v27, v30, v31\n"
            "buffer_store_dwordx4 v[24:27], v218, %[rso], 0 offen offset:768 sc1\n"
            "Lend_%=:\n"
            "s_waitcnt vmcnt(4)\n"
            : "+{v[32:63]}"(oxa), "+{v[64:95]}"(oxb), "+{v[96:127]}"(owr), "+{v[192:195]}"(efA), "+{v[196:199]}"(S), "+{v[200:203]}"(cgA), "+{v[208:223]}"(omi), "+{v[224:231]}"(ot),
              "+{v[232:235]}"(efN),
              [rem] "+s"(rem), [lens] "+s"(lens), [fm] "+s"(fm), [nst] "+s"(nst), [ra] "=&s"(ra), [tmp] "=&s"(tmp)
            : [rse] "s"(de), [rsx] "s"(dx), [rso] "s"(mkdesc(out, (unsigned)N * 1024u)), [rsw] "s"(mkdesc(ws, W_END)),
              [cnt] "s"(lds0 + L_MISC + 4), [pfa] "s"(lds0 + L_MISC + 16 + pw * 4), [tag] "s"(tag), [flg] "s"(ws + W_FLG + bx * 128), [pfl] "s"(ws + W_FLG + (bx ^ 4) * 128),
              [njl] "s"(((e2 - 1) & 3) + 1), [two] "s"(w), [evt] "s"((unsigned)__builtin_amdgcn_readfirstlane(w < 4 ? evt_of(PTO) : (w < 6 ? evt_of(PTY) : evt_of(PTZ)))), [ptab] "s"((unsigned)__builtin_amdgcn_readfirstlane(w < 4 ? PTO : (w < 6 ? PTY : PTZ))), [oagg] "s"(W_AGG), [spin] "s"(SPIN_MAX)
            : "memory", "vcc", "scc", "v0", "v1", "v2", "v3", "v4", "v5", "v6", "v7", "v8", "v9", "v10", "v11", "v12", "v13", "v14", "v15", "v16", "v17", "v18", "v19", "v20", "v21", "v22", "v23", "v24", "v25", "v26", "v27", "v28", "v29", "v30", "v31", "v128", "v129", "v130", "v131", "v132", "v133", "v134", "v135", "v136", "v137", "v138", "v139", "v140", "v141", "v142", "v143", "v144", "v145", "v146", "v147", "v148", "v149", "v150", "v151", "v152", "v153", "v154", "v155", "v156", "v157", "v158", "v159", "v160", "v161", "v162", "v163", "v164", "v165", "v166", "v167", "v168", "v169", "v170", "v171", "v172", "v173", "v174", "v175", "v176", "v177", "v178", "v179", "v180", "v181", "v182", "v183", "v184", "v185", "v186", "v187", "v188", "v189", "v190", "v191", "v204", "v205", "v206", "v207", "v236", "v237", "v238", "v239", "v240", "v241", "v242", "v243", "v244", "v245", "v246", "v247", "v248", "v249", "v250", "v251");
    } else {
        if (bx == 0 && w == 4) flag_store(ws + W_CTL, tag);
        const __amdgpu_buffer_rsrc_t rso = __builtin_amdgcn_make_buffer_rsrc((void*)out, 0, N * 1024, 0x00020000);
#pragma unroll 1
        for (int it = 0; it < 10; ++it) {
            const int nl = (it < 5 ? 40 - 40 * half + 8 * it : 40 * half + 8 * (it - 5)) + w, node = node0 + nl;
            const bool nv = nl < npb;
            const unsigned ro = nv ? (unsigned)node * 1024u + (unsigned)lane * 16u : OOB_OFF;
            float a8[8] = {0.f, 0.f, 0.f, 0.f, 0.f, 0.f, 0.f, 0.f};
#pragma unroll 1
            for (int e0 = 0; e0 < (nv ? E : 0); e0 += 64) {
                const int ec = min(e0 + lane, E - 1);
                u64 mm = __ballot(e0 + lane < E && dst[ec] == node && (int)(((src[ec] >> 3) ^ (smask >> ((src[ec] >> 9) & 31))) & 1) == half);
#pragma unroll 1
                while (mm) {
                    const int e = e0 + __builtin_ctzll(mm);
                    mm &= mm - 1;
                    const unsigned short* fe = (const unsigned short*)ef + (size_t)e * DE_DIM;
                    const unsigned short* ye = (const unsigned short*)Y + (size_t)e * M_DIM;
                    const unsigned short* xp = x + (size_t)src[e] * (C_DIM * M_DIM) + lane * 8;
                    float z0 = 0.f, z1 = 0.f;
#pragma unroll 1
                    for (int f = 0; f < DE_DIM; ++f) { z0 += bf2f(fe[f]) * bf2f(w_rad[f * C_DIM + 2 * lane]); z1 += bf2f(fe[f]) * bf2f(w_rad[f * C_DIM + 2 * lane + 1]); }
                    const float r0 = z0 / (1.f + __expf(-z0)), r1 = z1 / (1.f + __expf(-z1));
#pragma unroll 1
                    for (int i = 0; i < 4; ++i) {
                        const float x0 = bf2f(xp[i]) * r0, x1 = bf2f(xp[4 + i]) * r1;
#pragma unroll
                        for (int o = 0; o < 4; ++o) {
                            float wv4 = 0.f;
#pragma unroll
                            for (int f = 0; f < 4; ++f) wv4 += bf2f(cg[(i * 4 + f) * 4 + o]) * bf2f(ye[f]);
                            a8[o] += x0 * wv4; a8[4 + o] += x1 * wv4;
                        }
                    }
                }
            }
            if (it >= 5) {
                const u32x4 pv = __builtin_amdgcn_raw_buffer_load_b128(rsw, ro, W_AGG, SC1);
#pragma unroll
                for (int i = 0; i < 4; ++i) { a8[2 * i] += __uint_as_float(pv[i] << 16); a8[2 * i + 1] += __uint_as_float(pv[i] & 0xffff0000u); }
            }
            const u32x4 rv = (u32x4){pack2bf(a8[0], a8[1]), pack2bf(a8[2], a8[3]), pack2bf(a8[4], a8[5]), pack2bf(a8[6], a8[7])};
            if (it < 5) {
                __builtin_amdgcn_raw_buffer_store_b128(rv, rsw, ro, W_AGG, SC1);
                if (it == 4) {
                    asm volatile("s_waitcnt vmcnt(0)" ::: "memory");
                    if (lds_arrive_rtn(lds0 + L_MISC + 4) == 7u) flag_store(ws + W_FLG + bx * 128, tag);
                    poll_tag(ws + W_FLG + (bx ^ 4) * 128, tag);
                }
            } else {
                *(u32x4*)(smem + L_PRV + w * 1024 + lane * 16) = rv;
#pragma unroll
                for (int i = 0; i < 8; ++i) a8[i] = 0.f;
#pragma unroll 1
                for (int c = 0; c < C_DIM; ++c) {
                    const unsigned short* ap = (const unsigned short*)(smem + L_PRV + w * 1024 + c * 8);
                    const float w0 = bf2f(w_mix[c * C_DIM + 2 * lane]), w1 = bf2f(w_mix[c * C_DIM + 2 * lane + 1]);
#pragma unroll
                    for (int m = 0; m < 4; ++m) { a8[m] += bf2f(ap[m]) * w0; a8[4 + m] += bf2f(ap[m]) * w1; }
                }
                __builtin_amdgcn_raw_buffer_store_b128((u32x4){pack2bf(a8[0], a8[1]), pack2bf(a8[2], a8[3]), pack2bf(a8[4], a8[5]), pack2bf(a8[6], a8[7])}, rso, ro, 0, 0);
            }
        }
    }
}

__global__ void kf_zero(float* __restrict__ agg, size_t n) {
    for (size_t i = (size_t)blockIdx.x * blockDim.x + threadIdx.x; i < n; i += (size_t)gridDim.x * blockDim.x) agg[i] = 0.f;
}
__global__ void kf_edges(const unsigned short* __restrict__ x, const int* __restrict__ src, const int* __restrict__ dst,
                         const unsigned short* __restrict__ Y, const unsigned short* __restrict__ ef,
                         const unsigned short* __restrict__ w_rad, const unsigned short* __restrict__ cg,
                         float* __restrict__ agg, int E) {
    const int ch = threadIdx.x;
    for (int e = blockIdx.x; e < E; e += gridDim.x) {
        float z = 0.f;
        for (int f = 0; f < DE_DIM; ++f) z += bf2f(ef[(size_t)e * DE_DIM + f]) * bf2f(w_rad[f * C_DIM + ch]);
        const float r = z / (1.f + __expf(-z));
        float a4[4] = {0.f, 0.f, 0.f, 0.f};
        for (int ii = 0; ii < 4; ++ii) {
            const float xv = bf2f(x[((size_t)src[e] * C_DIM + ch) * M_DIM + ii]) * r;
            for (int o = 0; o < 4; ++o) {
                float wv = 0.f;
                for (int f = 0; f < 4; ++f) wv += bf2f(cg[(ii * 4 + f) * 4 + o]) * bf2f(Y[(size_t)e * 4 + f]);
                a4[o] += xv * wv;
            }
        }
        for (int o = 0; o < 4; ++o) atomicAdd(&agg[((size_t)dst[e] * C_DIM + ch) * M_DIM + o], a4[o]);
    }
}
__global__ void kf_mix(const float* __restrict__ agg, const unsigned short* __restrict__ w_mix, unsigned short* __restrict__ out, int N) {
    const int d = threadIdx.x;
    for (int n = blockIdx.x; n < N; n += gridDim.x) {
        float a4[4] = {0.f, 0.f, 0.f, 0.f};
        for (int c = 0; c < C_DIM; ++c) {
            const float wv = bf2f(w_mix[c * C_DIM + d]);
            for (int m = 0; m < 4; ++m) a4[m] += bf2f(f2bf(agg[((size_t)n * C_DIM + c) * M_DIM + m])) * wv;
        }
        for (int m = 0; m < 4; ++m) out[((size_t)n * C_DIM + d) * M_DIM + m] = f2bf(a4[m]);
    }
}

extern "C" void kernel_launch(void* const* d_in, const int* in_sizes, int n_in,
                              void* d_out, int out_size, void* d_ws, size_t ws_size,
                              hipStream_t stream) {
    const unsigned short* x = (const unsigned short*)d_in[0];
    const int* src = (const int*)d_in[1];
    const int* dst = (const int*)d_in[2];
    const unsigned short* Y = (const unsigned short*)d_in[3];
    const unsigned short* ef = (const unsigned short*)d_in[4];
    const unsigned short* w_rad = (const unsigned short*)d_in[5];
    const unsigned short* cg = (const unsigned short*)d_in[6];
    const unsigned short* w_mix = (const unsigned short*)d_in[7];
    unsigned short* out = (unsigned short*)d_out;
    const int N = in_sizes[0] / (C_DIM * M_DIM);
    const int E = in_sizes[1];
    const int epb = (E + 254) / 255;
    const bool fast = N >= 256 && N <= NB_MAX * NPB_MAX && E > 0 && epb <= EPB_MAX && ws_size >= (size_t)W_END;
    if (!fast) {
        float* agg = (float*)d_ws;
        if (N <= 0) return;
        kf_zero<<<1024, 256, 0, stream>>>(agg, (size_t)N * C_DIM * M_DIM);
        if (E > 0) kf_edges<<<4096, C_DIM, 0, stream>>>(x, src, dst, Y, ef, w_rad, cg, agg, E);
        kf_mix<<<2048, C_DIM, 0, stream>>>(agg, w_mix, out, N);
        return;
    }
    const unsigned nb_magic = (unsigned)(((1ull << 39) + N - 1) / N);
    unsigned smask = 0;
    const int ng = (N + 511) / 512;
    int pw = 1;
    while (2 * pw * 4 < ng + 4) pw *= 2;
    for (int i = 0; i < ng; ++i) smask |= (unsigned)(i < 4 * pw ? ((i >> 2) ^ (i >= WLO * ng / 20 && i < WHI * ng / 20)) & 1 : i & 1) << i;
    k_fused<<<256, 512, 0, stream>>>(x, src, dst, (const u32x2*)Y, (const u32x4*)ef, w_rad, cg, w_mix, out, (unsigned char*)d_ws,
                                     N, E, epb, nb_magic, smask, (unsigned)(4 * 65536 / N));
}
```

```cpp
#include <hip/hip_runtime.h>
#include <hip/hip_bf16.h>
#include <stdint.h>

typedef __attribute__((ext_vector_type(8))) short bf16x8;
typedef __attribute__((ext_vector_type(4))) short bf16x4;
typedef __attribute__((ext_vector_type(4))) float f32x4;
typedef __attribute__((ext_vector_type(2))) unsigned u32x2;
typedef __attribute__((ext_vector_type(4))) unsigned u32x4;
typedef __attribute__((ext_vector_type(8))) unsigned u32x8;
typedef __attribute__((ext_vector_type(16))) unsigned u32x16;
typedef __attribute__((ext_vector_type(32))) unsigned u32x32;
typedef unsigned long long u64;

#define C_DIM 128
#define M_DIM 4
#define DE_DIM 16
#define NPB_MAX 80
#define NB_MAX 128
#define EPB_MAX 1280
#define NRND 7
#define LS 47
#define DEG_MAX 42
#define OOB_OFF 0x80000000u
#define SC1 16
#define NT 2
#define SPIN_MAX (1u << 22)
#define CODE_LINES 152
#define PTO 0xfffa9540u
#define PTY 0xffffa950u
#define PTZ 0xffffea54u
#define NT0 3
#define NT2 4
#define TLIST 4, 5, 10, 12
#define WLO 8
#define WHI 11
#define TNEAR 0
#define HPRIO 3
#define PAYR 3
#define PBIAS 12
#define PRW "5"
#define PSL "1"
#define TKF 0x98765432u
#define TKB 0x23456789u
#define TKA 0x1010ffffu
#define EXTRA 0, 0, 5, 7

#define W_CTL 0
#define W_DEC 2048
#define W_FLG 4096
#define W_TAB (W_FLG + 32768)
#define W_RA (W_TAB + 256 * 256 * 8)
#define W_AGG (W_RA + 256 * EPB_MAX * 16)
#define W_END (W_AGG + NB_MAX * NPB_MAX * 1024)

#define SB() __builtin_amdgcn_sched_barrier(0)

static __device__ __forceinline__ float bf2f(unsigned short v) { return __uint_as_float(((unsigned)v) << 16); }
typedef __bf16 bf16v2 __attribute__((ext_vector_type(2)));
static __device__ __forceinline__ unsigned pack2bf(float lo, float hi) {
    bf16v2 r = (bf16v2){(__bf16)lo, (__bf16)hi};
    return __builtin_bit_cast(unsigned, r);
}
static __device__ __forceinline__ unsigned short f2bf(float v) { return (unsigned short)(pack2bf(v, 0.f) & 0xffffu); }
template <int CTRL, int RM> static __device__ __forceinline__ int dpp_add(int v) {
    return v + __builtin_amdgcn_update_dpp(0, v, CTRL, RM, 0xf, false);
}
static __device__ __forceinline__ int wave_incl_scan(int v) {
    v = dpp_add<0x111, 0xf>(v); v = dpp_add<0x112, 0xf>(v); v = dpp_add<0x114, 0xf>(v); v = dpp_add<0x118, 0xf>(v);
    v = dpp_add<0x142, 0xa>(v); v = dpp_add<0x143, 0xc>(v);
    return v;
}
template <int CTRL, int RM> static __device__ __forceinline__ unsigned dpp_max(unsigned v) {
    return max(v, (unsigned)__builtin_amdgcn_update_dpp(0, (int)v, CTRL, RM, 0xf, false));
}
static __device__ __forceinline__ unsigned wave_max_scan(unsigned v) {
    v = dpp_max<0x111, 0xf>(v); v = dpp_max<0x112, 0xf>(v); v = dpp_max<0x114, 0xf>(v); v = dpp_max<0x118, 0xf>(v);
    v = dpp_max<0x142, 0xa>(v); v = dpp_max<0x143, 0xc>(v);
    return v;
}
#define MAXS(c) "v_max_u32_dpp %0, %0, %0 " c "\n\tv_max_u32_dpp %1, %1, %1 " c "\n\tv_max_u32_dpp %2, %2, %2 " c "\n\t"
static __device__ __forceinline__ void wave_max_scan3(unsigned& a, unsigned& b, unsigned& c) {
    asm volatile("s_nop 1\n\t" MAXS("row_shr:1 row_mask:0xf bank_mask:0xf bound_ctrl:1") MAXS("row_shr:2 row_mask:0xf bank_mask:0xf bound_ctrl:1")
                 MAXS("row_shr:4 row_mask:0xf bank_mask:0xf bound_ctrl:1") MAXS("row_shr:8 row_mask:0xf bank_mask:0xf bound_ctrl:1")
                 MAXS("row_bcast:15 row_mask:0xa bank_mask:0xf") MAXS("row_bcast:31 row_mask:0xc bank_mask:0xf") "s_nop 0" : "+v"(a), "+v"(b), "+v"(c));
}
static __device__ __forceinline__ unsigned bsum(unsigned c, unsigned m, unsigned acc = 0u) { return __builtin_amdgcn_udot4(c, m, acc, false); }
static __device__ __forceinline__ unsigned bytesum2(unsigned c0, unsigned c1, unsigned m0, unsigned m1, unsigned acc = 0u) {
    return __builtin_amdgcn_udot4(c1, m1, __builtin_amdgcn_udot4(c0, m0, acc, false), false);
}
static __device__ __forceinline__ unsigned below_mask(int w, int k) {
    const int n = w - 4 * k;
    return n >= 4 ? 0x01010101u : (n <= 0 ? 0u : (0x01010101u & ((1u << (8 * n)) - 1u)));
}
static __device__ __forceinline__ void flag_store(const void* p, unsigned v) {
    unsigned t0, t1; u64 sv;
    asm volatile("v_mov_b32 %0, 0\n\tv_mov_b32 %1, %4\n\ts_mov_b64 %2, exec\n\ts_mov_b64 exec, 1\n\t"
                 "global_store_dword %0, %1, %3 sc1\n\ts_mov_b64 exec, %2" : "=&v"(t0), "=&v"(t1), "=&s"(sv) : "s"(p), "s"(v) : "memory");
}
static __device__ __forceinline__ unsigned lds_arrive_rtn(unsigned a) {
    unsigned t0, t1; u64 sv;
    asm volatile("v_mov_b32 %0, %3\n\tv_mov_b32 %1, 1\n\ts_mov_b64 %2, exec\n\ts_mov_b64 exec, 1\n\t"
                 "ds_add_rtn_u32 %1, %0, %1\n\ts_mov_b64 exec, %2\n\ts_waitcnt lgkmcnt(0)" : "=&v"(t0), "=&v"(t1), "=&s"(sv) : "s"(a) : "memory");
    return (unsigned)__builtin_amdgcn_readfirstlane((int)t1);
}
static __device__ __forceinline__ void lds_arrive(unsigned a, unsigned inc = 1u) {
    unsigned t0, t1; u64 sv;
    asm volatile("" "v_mov_b32 %0, %3\n\tv_mov_b32 %1, %4\n\ts_mov_b64 %2, exec\n\ts_mov_b64 exec, 1\n\t"
                 "ds_add_u32 %0, %1\n\ts_mov_b64 exec, %2" : "=&v"(t0), "=&v"(t1), "=&s"(sv) : "s"(a), "s"(inc) : "memory");
}
template <int SLEEP = 0, unsigned MASK = ~0u> static __device__ __forceinline__ unsigned lds_wait(unsigned a, unsigned n) {
    for (;;) {
        unsigned v;
        asm volatile("ds_read_b32 %0, %1\n\ts_waitcnt lgkmcnt(0)" : "=v"(v) : "v"(a) : "memory");
        v = (unsigned)__builtin_amdgcn_readfirstlane((int)v);
        if ((v & MASK) >= n) return v;
        if (SLEEP) __builtin_amdgcn_s_sleep(SLEEP);
    }
}
static __device__ __forceinline__ unsigned ld_agent(const void* p) { return __hip_atomic_load((const unsigned*)p, __ATOMIC_RELAXED, __HIP_MEMORY_SCOPE_AGENT); }
static __device__ __forceinline__ void poll_tag(const void* p, unsigned tag) {
#pragma unroll 1
    for (unsigned spin = 0; spin < SPIN_MAX; ++spin) {
        if (__all(ld_agent(p) == tag)) break;
        __builtin_amdgcn_s_sleep(2);
    }
    __builtin_amdgcn_fence(__ATOMIC_ACQUIRE, "wavefront");
}
static __device__ __forceinline__ void wg_barrier() {
    asm volatile("s_waitcnt lgkmcnt(0)" ::: "memory");
    __builtin_amdgcn_s_barrier();
    asm volatile("" ::: "memory");
}
static __device__ __forceinline__ f32x4 mfma4(u32x2 a, u32x2 b, f32x4 c) {
    return __builtin_amdgcn_mfma_f32_4x4x4bf16_1k(__builtin_bit_cast(bf16x4, a), __builtin_bit_cast(bf16x4, b), c, 0, 0, 0);
}
static __device__ __forceinline__ f32x4 mfma16(u32x4 a, u32x4 b, f32x4 c) {
    return __builtin_amdgcn_mfma_f32_16x16x32_bf16(__builtin_bit_cast(bf16x8, a), __builtin_bit_cast(bf16x8, b), c, 0, 0, 0);
}

static constexpr unsigned evt_of(unsigned pt) {
    unsigned e = 6u;
    for (int n = 2; n < 15; ++n) if (((pt >> (2 * n)) & 3u) != ((pt >> (2 * n + 2)) & 3u)) e |= 1u << n;
    return e;
}

static constexpr u64 len_pos(bool kept) {
    u64 r = 0;
    for (int w = 0; w < 8; ++w) {
        r |= (u64)(kept ? 8 + w : w) << (4 * (((kept ? TKF : TKB) >> (4 * w)) & 15u));
        if (w >= 4 && (w >= 6) == kept) r |= (u64)(kept ? w : 4 + w) << (4 * ((TKA >> (4 * w)) & 15u));
    }
    return r;
}
static constexpr unsigned ring_const() {
    constexpr int e[4] = {EXTRA};
    unsigned r = 0;
    for (int i = 0; i < 4; ++i) r |= (unsigned)((4 - PBIAS + (-3 * e[i] - e[(i + 3) & 3] + e[(i + 2) & 3] + 3 * e[(i + 1) & 3]) / 2) & 255) << (8 * i);
    return r;
}

#define L_MISC 0
#define L_KEY 96
#define L_AT 416
#define L_LEN 1056
#define L_PCNT 1280
#define L_CCNT (L_PCNT + 2048)
#define L_BNC (L_CCNT + 1280)
#define L_CG (L_BNC + 4096)
#define L_PRV (L_CG + 1024)
#define A_STG L_PRV
#define A_PTAB (A_STG + EPB_MAX * 16)
#define L_PK (L_PRV + 15 * 4096)
#define L_DUM L_PK
#define L_CTAB (L_PK + 2048)
#define L_MRK (L_PK + 8192)
#define TR_STR 4352
#define L_TR (L_PK + 16384)
#define L_WR L_TR
#define L_IMG (L_TR + 8 * TR_STR)
#define L_TOTAL (L_IMG + 32768)
static_assert(L_TOTAL <= 163840 && L_BNC <= 8192 && NPB_MAX * LS * 16 <= 15 * 4096, "LDS budget");
static_assert(L_PRV % 16 == 0 && L_PK % 16 == 0 && L_IMG % 16 == 0 && L_CG % 16 == 0 && L_CCNT % 16 == 0, "alignment");
enum { SY_CPY = 40, SY_C1 = 44, SY_S = 48, SY_C3 = 56, SY_RI = 60, SY_PRE = 64 };

static __device__ __forceinline__ u32x4 mkdesc(const void* p, unsigned bytes) {
    return (u32x4){(unsigned)(uintptr_t)p, (unsigned)((uintptr_t)p >> 32) & 0xffffu, bytes, 0x00020000u};
}

__global__ __launch_bounds__(512, 2) void k_fused(
    const unsigned short* __restrict__ x, const int* __restrict__ src, const int* __restrict__ dst, const u32x2* __restrict__ Y,
    const u32x4* __restrict__ ef, const unsigned short* __restrict__ w_rad, const unsigned short* __restrict__ cg,
    const unsigned short* __restrict__ w_mix, unsigned short* __restrict__ out, unsigned char* ws,
    int N, int E, int epb, unsigned nb_magic, unsigned smask, unsigned cmul, unsigned po0, unsigned po1)
{
    __shared__ __attribute__((aligned(256))) unsigned char smem[L_TOTAL];
    const int tid = threadIdx.x, lane = tid & 63;
    const int w = __builtin_amdgcn_readfirstlane(tid >> 6);
    const int bx = blockIdx.x;
    int half;
    const int b = (bx & 3) * 32 + (bx >> 3);
    const int g = lane >> 4, c16 = lane & 15;
    const int node0 = (b * N + 127) >> 7, npb = (((b + 1) * N + 127) >> 7) - node0;
    unsigned cp;
    {
        u64 pc;
        asm volatile("s_getpc_b64 %0" : "=s"(pc));
        const __amdgpu_buffer_rsrc_t rsc = __builtin_amdgcn_make_buffer_rsrc((void*)(pc & ~(u64)127), 0, CODE_LINES * 128, 0x00020000);
        cp = __builtin_amdgcn_raw_buffer_load_b32(rsc, (tid >> 3) == 32 ? ((bx >> 3) + lane * 32) * 128 : OOB_OFF, 0, 0);
        SB();
    }
    const unsigned lds0 = (unsigned)(uintptr_t)(__attribute__((address_space(3))) unsigned char*)smem;
    const __amdgpu_buffer_rsrc_t rsw = __builtin_amdgcn_make_buffer_rsrc((void*)ws, 0, W_END, 0x00020000);
    asm volatile("" :: "s"(x), "s"(src), "s"(dst), "s"(Y), "s"(ef), "s"(w_rad), "s"(cg), "s"(w_mix), "s"(out), "s"(ws), "s"(N), "s"(E), "s"(epb), "s"(nb_magic),
                 "s"(smask), "s"(cmul), "s"(po0), "s"(po1));
    const int base_e = bx * epb - epb;
    const int ne = base_e < 0 ? 0 : max(min(epb, E - base_e), 0);
    const int wd = 4 + ((w - 1 + (w >> 2)) & 3);
    auto task_at = [&](int t) -> unsigned {
        const unsigned q = (t == 0 ? TKF >> (4 * w) : (t == 1 ? TKB >> (4 * w) : TKA >> (4 * wd))) & 15u;
        return L_AT + ((t == 0 || (t == 2 && wd >= 6) ? 40 * half : 40 - 40 * half) + 4 * q) * 4;
    };
    const unsigned ri8 = (wd & 3) * 8;
    unsigned ta[6], cc, cd;
    int k0;
    auto set_ta = [&]() {
#pragma unroll
        for (int i = 0; i < 3; ++i) {
            const unsigned t = lds0 + task_at(i == 2 ? 0 : ((w < 4) == (i == 0) ? 2 : 1));
            ta[2 * i] = t + g * 4; ta[2 * i + 1] = t + (c16 & 12);
            asm volatile("" : "+v"(ta[2 * i]), "+v"(ta[2 * i + 1]));
        }
        cc = __builtin_rotateleft32(0xff0103fdu, ri8); cd = __builtin_rotateleft32(0xfe000204u, ri8); k0 = (int)(signed char)(ring_const() >> ri8);
        asm volatile("" : "+v"(cc), "+v"(cd), "+v"(k0));
    };
    unsigned tag;
    bool slow;
    u32x4 wr[8], cgA;
    auto load_wr = [&]() {
#pragma unroll
        for (int t = 0; t < 8; ++t) wr[t] = *(const u32x4*)(smem + L_WR + t * 1024 + lane * 16);
        cgA = *(const u32x4*)(smem + L_CG + lane * 16);
        asm volatile("" : "+v"(wr[0]), "+v"(wr[1]), "+v"(wr[2]), "+v"(wr[3]), "+v"(wr[4]), "+v"(wr[5]), "+v"(wr[6]), "+v"(wr[7]), "+v"(cgA));
    };

    __builtin_amdgcn_s_setprio(3);
    auto touch = [&](unsigned code) -> unsigned {
        if (code < 8u)
            return __builtin_amdgcn_raw_buffer_load_b32(__builtin_amdgcn_make_buffer_rsrc((void*)(ef + (size_t)base_e * 2), 0, code == 0u ? 0 : ne * 32, 0x00020000), lane * 128, (code - 1u) * 8192u, 0);
        const unsigned j = code < 16u ? (code - 8u) * 128u + b : (code - 16u) * 32u + (bx >> 3);
        return __builtin_amdgcn_raw_buffer_load_b32(__builtin_amdgcn_make_buffer_rsrc((void*)x, 0, N * 1024, 0x00020000), lane * 128,
                                                    (2u * j + ((half ^ (TNEAR ? j >> 7 : smask >> (j >> 5))) & 1u)) * 8192u, 0);
    };
    constexpr unsigned tl[] = {TLIST};
    const __amdgpu_buffer_rsrc_t rsm = __builtin_amdgcn_make_buffer_rsrc((void*)w_mix, 0, C_DIM * C_DIM * 2, 0x00020000);
    u32x4 mv[8];
    auto load_mv = [&]() {
#pragma unroll
        for (int j = 0; j < 8; ++j)
            mv[j] = __builtin_amdgcn_raw_buffer_load_b128(rsm, g * 512 + c16 * 16 + (w & 3) * 2048 + (j & 1) * 256, (j >> 1) * 8192, 0);
        SB();
    };
    auto make_img = [&]() {
#pragma unroll
        for (int i = 0; i < 4; ++i) {
            u32x4 p0, p1;
#pragma unroll
            for (int j = 0; j < 4; ++j) {
                p0[j] = __builtin_amdgcn_perm(mv[2 * j + 1][i], mv[2 * j][i], 0x05040100u);
                p1[j] = __builtin_amdgcn_perm(mv[2 * j + 1][i], mv[2 * j][i], 0x07060302u);
            }
            const unsigned ia = L_IMG + (c16 >> 2) * 8192 + (w & 3) * 1024 + g * 256 + (c16 & 3) * 64 + i * 16;
            *(u32x4*)(smem + ia) = p0;
            *(u32x4*)(smem + ia + 4096) = p1;
        }
    };
    unsigned t0v = 0u, cm = 0u, wv[8], cv[4], tv[NT2] = {};
    {
        const unsigned sh = 8u * (w & 3);
        unsigned d[3], sv[3], epoch, inc[3];
        u32x2 yv[3];
        const __amdgpu_buffer_rsrc_t rsd = __builtin_amdgcn_make_buffer_rsrc((void*)(dst + base_e), 0, ne * 4, 0x00020000);
        const __amdgpu_buffer_rsrc_t rss = __builtin_amdgcn_make_buffer_rsrc((void*)(src + base_e), 0, ne * 4, 0x00020000);
        const __amdgpu_buffer_rsrc_t rsy = __builtin_amdgcn_make_buffer_rsrc((void*)(Y + base_e), 0, ne * 8, 0x00020000);
        const unsigned le = (unsigned)(w * 160 + lane), le2 = lane < 32 ? le : 0x1000000u;
#pragma unroll
        for (int r = 0; r < 3; ++r) {
            d[r] = __builtin_amdgcn_raw_buffer_load_b32(rsd, (r < 2 ? le : le2) * 4 + 256 * r, 0, NT);
            sv[r] = __builtin_amdgcn_raw_buffer_load_b32(rss, (r < 2 ? le : le2) * 4 + 256 * r, 0, NT);
            SB();
        }
        epoch = __builtin_amdgcn_raw_buffer_load_b32(rsw, W_CTL, 0, 0);
        const unsigned pt = __builtin_amdgcn_raw_buffer_load_b32(__builtin_amdgcn_make_buffer_rsrc((void*)x, 0, bx * 8 + w == 8 + 7 ? N * 1024 : 0, 0x00020000),
                                                                 (lane < 32 ? po0 : po1) + (lane & 3) * 8192, 0, 0);
#pragma unroll
        for (int r = 0; r < 3; ++r) yv[r] = __builtin_amdgcn_raw_buffer_load_b64(rsy, (r < 2 ? le : le2) * 8 + 512 * r, 0, NT);
        SB();
        if (bx * 8 + w == 7) {
            unsigned s0, s1, v0, v1;
#define RACE(r, o, a, b) "s_load_dword %2, %4, " o " offset:" a "\n\ts_load_dword %3, %4, " o " offset:" b "\n\ts_waitcnt lgkmcnt(1)\n\ts_sleep " PSL "\n\t" \
                         "s_getreg_b32 " r ", hwreg(HW_REG_IB_STS, 8, 1)\n\ts_waitcnt lgkmcnt(0)\n\t"
            asm volatile("s_load_dword %0, %4, %5 offset:0x10000\n\ts_load_dword %1, %4, %6 offset:0x10000\n\ts_waitcnt lgkmcnt(0)\n\ts_sleep " PRW "\n\t"
                         RACE("%0", "%5", "0", "0x2000") RACE("%1", "%6", "0x2000", "0") "s_cmp_lg_u32 %0, %1\n\ts_cbranch_scc1 Lr_%=\n\t" RACE("%1", "%5", "0x6000", "0x4000") "Lr_%=:"
                         : "=&s"(s0), "=&s"(s1), "=&s"(v0), "=&s"(v1) : "s"(x), "s"(po0), "s"(po1) : "memory", "scc");
            unsigned xc;
            asm volatile("s_getreg_b32 %0, hwreg(HW_REG_XCC_ID, 0, 2)" : "=s"(xc));
            unsigned hm = (0xf0f0f0u >> (xc + 4u * s1));
            const unsigned tg = epoch * 1664525u + 1013904223u;
            __builtin_amdgcn_raw_buffer_store_b128((u32x4){hm, tg, hm, tg}, rsw, lane < 8 ? W_DEC + lane * 16 : OOB_OFF, 0, SC1);
            for (int i = 0; i < 2; ++i) __builtin_amdgcn_raw_buffer_store_b128((u32x4){0u, tg, 0u, tg}, rsw, W_TAB + lane * 16, i * 1024, SC1);
        }
        {
            const unsigned k0 = (unsigned)tid - L_KEY / 16 < NPB_MAX / 4 ? 127u + L_KEY / 4 - 4u * tid : 0u, k1 = min(k0, 1u);
            *(u32x4*)(smem + tid * 16) = (u32x4){k0, k0 - k1, k0 - 2u * k1, k0 - 3u * k1};
        }
        if (tid < 64 * NRND) *(u32x4*)(smem + L_MRK + tid * 16) = (u32x4){0u, 0u, 0u, 0u};
        {
            const int hm = w >= 4 ? -1 : 0;
            const __amdgpu_buffer_rsrc_t rsa = __builtin_amdgcn_make_buffer_rsrc((void*)w_rad, 0, DE_DIM * C_DIM * 2 & hm, 0x00020000);
            const __amdgpu_buffer_rsrc_t rsg = __builtin_amdgcn_make_buffer_rsrc((void*)cg, 0, 128 & hm, 0x00020000);
#pragma unroll
            for (int j = 0; j < 8; ++j) wv[j] = __builtin_amdgcn_raw_buffer_load_b32(rsa, (g & 1) * 2048 + 64 * (w & 3) + 4 * c16 + 256 * j, 0, 0);
#pragma unroll
            for (int f = 0; f < 4; ++f) cv[f] = __builtin_amdgcn_raw_buffer_load_b32(rsg, (lane & 3) * 32 + (lane & 8u) / 2 + 8 * f, 0, 0);
            t0v = __builtin_amdgcn_raw_buffer_load_b32(__builtin_amdgcn_make_buffer_rsrc((void*)(ef + (size_t)base_e * 2), 0, min(ne * 32, NT0 * 8192), 0x00020000), (511 - tid) * 128, 0, 0);
            cm = __builtin_amdgcn_raw_buffer_load_b32(rsm, (tid >> 3) == 32 ? ((bx >> 3) * 8 + lane) * 128 : OOB_OFF, 0, 0);
        }
#pragma unroll
        for (int r = 0; r < 3; ++r) inc[r] = (int)(r < 2 ? le : le2) + r * 64 < ne ? 1u << sh : 0u;
        asm volatile("" : "+v"(inc[0]), "+v"(inc[1]), "+v"(inc[2]));
        SB();
        wg_barrier();
        if (bx != 0) {
        unsigned bin[3], rank[3], ba[3];
        asm volatile("" : "+v"(d[0]), "+v"(sv[0]), "+v"(d[1]), "+v"(sv[1]), "+v"(d[2]), "+v"(sv[2]));
#pragma unroll
        for (int r = 0; r < 3; ++r) {
            bin[r] = __umulhi(d[r], nb_magic);
            ba[r] = ((((sv[r] >> 3) ^ (smask >> ((sv[r] >> 9) & 31u))) & 1u) * 512u + bin[r] * 4u) & 1023u;
            rank[r] = atomicAdd((unsigned*)(smem + L_PCNT + (w >> 2) * 1024 + ba[r]), inc[r]);
        }
        unsigned tag = (unsigned)__builtin_amdgcn_readfirstlane((int)epoch) * 1664525u + 1013904223u;
        asm volatile("" : "+s"(tag));
        unsigned rw0[3], rw1[3];
#pragma unroll
        for (int r = 0; r < 3; ++r) {
            const unsigned sl = sv[r], nl = d[r] - ((__umul24(bin[r], (unsigned)N) + 127u) >> 7);
            rw0[r] = sl | (tag << 14);
            rw1[r] = (nl * 4u + ((__umul24(sl, cmul) >> 16) ^ ((nl >= 40u) == (ba[r] >= 512u) ? 3u : 0u))) | ((le + r * 64) << 9) | (tag & 0xfff00000u);
            asm volatile("" : "+v"(rw0[r]), "+v"(rw1[r]));
        }
        unsigned pa[3];
#pragma unroll
        for (int r = 0; r < 3; ++r) { pa[r] = A_PTAB + w * 512 + (ba[r] >> 1); asm volatile("" : "+v"(pa[r])); }
        unsigned m0 = below_mask(w, 0), m1 = below_mask(w, 1);
        asm volatile("" : "+s"(m0), "+s"(m1));
        unsigned tba = W_TAB + bx * 2048 + lane * 16 + (w & 1) * 1024;
        asm volatile("" : "+v"(tba));
        wg_barrier();
        u32x2 c[4];
#pragma unroll
        for (int i = 0; i < 4; ++i) c[i] = *(const u32x2*)(smem + L_PCNT + i * 512 + lane * 8);
        unsigned t[4], run[4];
#pragma unroll
        for (int i = 0; i < 4; ++i) t[i] = bsum(c[i >> 1][i & 1], 0x01010101u);
        asm volatile("" : "+v"(t[0]), "+v"(t[1]), "+v"(t[2]), "+v"(t[3]));
#pragma unroll
        for (int i = 0; i < 4; ++i) t[i] = bsum(c[2 + (i >> 1)][i & 1], 0x01010101u, t[i]);
        asm volatile("" : "+v"(t[0]), "+v"(t[1]), "+v"(t[2]), "+v"(t[3]));
        const unsigned ts = (t[0] + t[1]) | (t[2] + t[3]) << 16;
        const unsigned ti = (unsigned)wave_incl_scan((int)ts), tx = ti - ts;
        run[0] = tx & 0xffffu; run[2] = (tx >> 16) + ((unsigned)__builtin_amdgcn_readlane((int)ti, 63) & 0xffffu);
        run[1] = run[0] + t[0]; run[3] = run[2] + t[2];
        unsigned o[4];
#pragma unroll
        for (int i = 0; i < 4; ++i) o[i] = bytesum2(c[i >> 1][i & 1], c[2 + (i >> 1)][i & 1], m0, m1, run[i]);
        *(unsigned*)(smem + A_PTAB + w * 512 + lane * 4) = o[0] | (o[1] << 16);
        *(unsigned*)(smem + A_PTAB + w * 512 + 256 + lane * 4) = o[2] | (o[3] << 16);
        unsigned pos[3];
        asm volatile("" ::: "memory");
#pragma unroll
        for (int r = 0; r < 3; ++r) pos[r] = *(const unsigned short*)(smem + pa[r]);
        SB();
        if ((w & 6) == 4) {
            const unsigned ra = (w & 1) == 0 ? run[0] | (t[0] << 16) : run[1] | (t[1] << 16), rb = (w & 1) == 0 ? run[2] | (t[2] << 16) : run[3] | (t[3] << 16);
            __builtin_amdgcn_raw_buffer_store_b128((u32x4){ra, tag, rb, tag}, rsw, tba, 0, SC1);
        }
#pragma unroll
        for (int r = 0; r < 3; ++r) pos[r] = A_STG + (pos[r] + ((rank[r] >> sh) & 0xffu)) * 16;
        asm volatile("" : "+v"(pos[0]), "+v"(pos[1]), "+v"(pos[2]));
#pragma unroll
        for (int r = 0; r < 3; ++r) pos[r] = inc[r] != 0u ? pos[r] : L_DUM + lane * 16;
#pragma unroll
        for (int r = 0; r < 3; ++r) *(u32x4*)(smem + pos[r]) = (u32x4){yv[r].x, rw0[r], yv[r].y, rw1[r]};
        }
        asm volatile("" : "+v"(epoch) : "v"(pt));
        tag = (unsigned)__builtin_amdgcn_readfirstlane((int)epoch) * 1664525u + 1013904223u;
        wg_barrier();
        const __amdgpu_buffer_rsrc_t rsr = __builtin_amdgcn_make_buffer_rsrc((void*)(ws + W_RA + bx * (EPB_MAX * 16)), 0, (ne * 16 + 127) & ~127, 0x00020000);
        u32x4 va[3];
#pragma unroll
        for (int i = 0; i < 3; ++i) va[i] = *(const u32x4*)(smem + A_STG + (tid ^ 256) * 16 + 8192 * i);
#pragma unroll
        for (int i = 0; i < 3; ++i) __builtin_amdgcn_raw_buffer_store_b128(va[i], rsr, (tid ^ 256) * 16, 8192 * i, SC1);
        lds_arrive(lds0 + SY_CPY);
    }
    u32x4 dx = mkdesc(x, (unsigned)N * 1024u), de = mkdesc(ef, (unsigned)E * 32u);
    asm volatile("" : "+s"(dx), "+s"(de));
    if (w < 4) {
        asm volatile("" :: "v"(t0v), "v"(cm), "v"(wv[0]), "v"(wv[7]), "v"(cv[3]));
        const unsigned sh = 8u * w, mb = 0x01010101u & ((1u << sh) - 1u);
        const unsigned T0 = tag << 14, T1 = tag & 0xfff00000u;
        const unsigned to = W_TAB + tid * 2048 + (b & 1) * 1024 + (b >> 1) * 16;
        u32x4 en;
        u32x2 dec;
        {
            __builtin_amdgcn_s_sleep(4);
            en = __builtin_amdgcn_raw_buffer_load_b128(rsw, to, 0, SC1);
            dec = __builtin_amdgcn_raw_buffer_load_b64(rsw, W_DEC, 0, SC1);
        }
#pragma unroll 1
        for (unsigned spin = 0; spin < SPIN_MAX; ++spin) {
            if (__builtin_amdgcn_ballot_w64(((en.y ^ tag) | (en.w ^ tag) | (dec.y ^ tag)) != 0u) == 0ull) break;
            __builtin_amdgcn_s_sleep(2);
            asm volatile("" ::: "memory");
            en = __builtin_amdgcn_raw_buffer_load_b128(rsw, to, 0, SC1);
            dec = __builtin_amdgcn_raw_buffer_load_b64(rsw, W_DEC, 0, SC1);
        }
        half = (__builtin_amdgcn_readfirstlane((int)dec.x) >> (bx & 7)) & 1;
        if (half) en.x = en.z;
        const unsigned rc = en.x >> 16;
        const unsigned incl = (unsigned)wave_incl_scan((int)rc);
        const int twf = __builtin_amdgcn_readlane((int)incl, 63), tw = twf <= 64 * NRND ? twf : 0;
        const bool more = tw > 64 * (NRND - 1);
        unsigned ma = L_MRK + w * (256 * NRND) + (incl - rc) * 4;
        asm volatile("" : "+v"(ma));
        *(unsigned*)(smem + (((rc != 0u) & (incl <= 64u * NRND)) ? ma : L_DUM + lane * 4)) = (unsigned)lane * EPB_MAX + (en.x & 0xffffu) - (incl - rc);
        unsigned ra[NRND];
        asm volatile("" ::: "memory");
#pragma unroll
        for (int r = 0; r < NRND; ++r) ra[r] = *(const unsigned*)(smem + L_MRK + w * (256 * NRND) + r * 256 + lane * 4);
        unsigned carry = 0u;
        u32x4 A[NRND];
#pragma unroll
        for (int r = 0; r < NRND; ++r) {
            if (r == NRND - 1) { if (more) ra[r] = wave_max_scan(ra[r]); asm volatile("" : "+v"(ra[r])); }
            else if (r % 3 == 0) wave_max_scan3(ra[r], ra[r + 1], ra[r + 2]);
            const unsigned m = max(ra[r], carry);
            carry = max(carry, (unsigned)__builtin_amdgcn_readlane((int)ra[r], 63));
            ra[r] = r * 64 + lane < tw ? (m + (unsigned)(r * 64 + lane)) * 16u : OOB_OFF >> 1;
            A[r] = __builtin_amdgcn_raw_buffer_load_b128(rsw, ra[r], W_RA + 64 * w * (EPB_MAX * 16), SC1);
        }
        SB();
        lds_arrive(lds0 + SY_RI, 16u + half);
        unsigned pe[NRND], inc[NRND], inck[NRND], lim[NRND];
#pragma unroll
        for (int r = 0; r < NRND; ++r) {
            pe[r] = __umul24(__umulhi(ra[r], 209716u) + 64u * w - 1u, (unsigned)epb * 32u);
            inc[r] = ra[r] < 0x40000000u ? 1u << sh : 0u;
            inck[r] = ra[r] < 0x40000000u ? 128u : 0u;
            lim[r] = ra[r] < 0x40000000u ? 16383u : ~0u;
            asm volatile("" : "+v"(pe[r]), "+v"(inc[r]), "+v"(inck[r]), "+v"(lim[r]));
        }
        if (twf > 64 * NRND) lds_arrive(lds0 + SY_S, 256u);
        set_ta();
        lds_wait(lds0 + SY_PRE, 4);
        load_wr();
        SB();
        unsigned rank[NRND], nc[NRND];
        auto slot = [&](int r) {
            nc[r] = inc[r] != 0u ? L_CTAB + w * 640 + nc[r] * 2 : L_MISC + 8;
            A[r] = (u32x4){A[r].y << 10, ((A[r].w >> 4) & 0xffe0u) + pe[r], A[r].x, A[r].z};
            asm volatile("" : "+v"(nc[r]), "+v"(A[r]));
            SB();
        };
#pragma unroll
        for (int r = 0; r < NRND; ++r) {
            if (r == NRND - 1 && !more) { rank[r] = 0u; nc[r] = 0u; continue; }
            A[r].y ^= T0; A[r].w ^= T1;
            if (__builtin_expect(__builtin_amdgcn_ballot_w64((A[r].y | (A[r].w >> 6)) > lim[r]) != 0ull, 0)) {
#pragma unroll 1
                for (unsigned spin = 0; spin < SPIN_MAX; ++spin) {
                    asm volatile("" ::: "memory");
                    A[r] = __builtin_amdgcn_raw_buffer_load_b128(rsw, ra[r], W_RA + 64 * w * (EPB_MAX * 16), SC1);
                    A[r].y ^= T0; A[r].w ^= T1;
                    if (__builtin_amdgcn_ballot_w64((A[r].y | (A[r].w >> 6)) > lim[r]) == 0ull) break;
                }
            }
            nc[r] = (r < 4 || inc[r] != 0u) ? A[r].w & 0x1ffu : (unsigned)lane;
            rank[r] = atomicAdd((unsigned*)(smem + L_CCNT + nc[r] * 4), inc[r]);
            atomicAdd((unsigned*)(smem + L_KEY + (nc[r] & 0x1fcu)), inck[r]);
            SB();
            if (r < PAYR) slot(r);
        }
        lds_arrive(lds0 + SY_C1);
        unsigned d0 = lane * LS, d1 = (64 + c16) * LS, ca0 = L_CTAB + w * 640 + lane * 8, ca1 = L_CTAB + w * 640 + (64 + c16) * 8;
        asm volatile("" : "+v"(d0), "+v"(d1), "+v"(ca0), "+v"(ca1));
#pragma unroll
        for (int r = PAYR; r < NRND; ++r) slot(r);
#pragma unroll
        for (int r = 0; r < NRND; ++r) {
            rank[r] = inc[r] != 0u ? (rank[r] >> sh) & 0xffu : (L_DUM - L_PRV) / 16 + lane;
            asm volatile("" : "+v"(rank[r]));
        }
        {
            u32x4 c0, c1;
            unsigned cn;
            do {
                asm volatile("ds_read_b32 %0, %3 offset:%c6\n\tds_read_b128 %1, %4 offset:%c7\n\tds_read_b128 %2, %5 offset:%c8\n\ts_waitcnt lgkmcnt(0)"
                             : "=&v"(cn), "=&v"(c0), "=&v"(c1) : "v"(lds0), "v"(lds0 + lane * 16), "v"(lds0 + c16 * 16), "i"(SY_C1), "i"(L_CCNT), "i"(L_CCNT + 1024) : "memory");
            } while (__builtin_amdgcn_readfirstlane((int)cn) < 4);
            unsigned p0[4], p1[4];
#pragma unroll
            for (int c = 0; c < 4; ++c) {
                p0[c] = bsum(c0[c], mb, d0); p1[c] = bsum(c1[c], mb, d1);
                if (c < 3) { d0 = bsum(c0[c], 0x01010101u, d0); d1 = bsum(c1[c], 0x01010101u, d1); }
            }
            *(u32x2*)(smem + ca0) = (u32x2){p0[0] | (p0[1] << 16), p0[2] | (p0[3] << 16)};
            *(u32x2*)(smem + ca1) = (u32x2){p1[0] | (p1[1] << 16), p1[2] | (p1[3] << 16)};
        }
        asm volatile("" ::: "memory");
#pragma unroll
        for (int r = 0; r < NRND; ++r) nc[r] = *(const unsigned short*)(smem + nc[r]);
        SB();
#pragma unroll
        for (int r = 0; r < NRND; ++r) {
            if (r == NRND - 1 && !more) continue;
            *(u32x4*)(smem + L_PRV + (nc[r] + rank[r]) * 16) = A[r];
        }
        lds_arrive(lds0 + SY_C3);
    } else {
        const int q = w - 4, t2 = tid - 256;
        __builtin_amdgcn_s_setprio(2);
        load_mv();
        {
            float f0[8], f1[8];
#pragma unroll
            for (int j = 0; j < 8; ++j) {
                const float a = __uint_as_float(wv[j] << 16) * -1.44269504088896340736f, c = __uint_as_float(wv[j] & 0xffff0000u) * -1.44269504088896340736f;
                const unsigned hp = g < 2 ? 0u : pack2bf(a, c);
                f0[j] = a - __uint_as_float(hp << 16);
                f1[j] = c - __uint_as_float(hp & 0xffff0000u);
            }
            *(u32x4*)(smem + L_WR + (2 * q) * 1024 + lane * 16) = (u32x4){pack2bf(f0[0], f0[1]), pack2bf(f0[2], f0[3]), pack2bf(f0[4], f0[5]), pack2bf(f0[6], f0[7])};
            *(u32x4*)(smem + L_WR + (2 * q + 1) * 1024 + lane * 16) = (u32x4){pack2bf(f1[0], f1[1]), pack2bf(f1[2], f1[3]), pack2bf(f1[4], f1[5]), pack2bf(f1[6], f1[7])};
            float a[4];
#pragma unroll
            for (int f = 0; f < 4; ++f) a[f] = __uint_as_float((lane & 4) ? cv[f] & 0xffff0000u : cv[f] << 16) * -0.69314718055994530942f;
            const unsigned h0 = pack2bf(a[0], a[1]), h1 = pack2bf(a[2], a[3]);
            *(u32x4*)(smem + L_CG + lane * 16) = (u32x4){h0, h1, pack2bf(a[0] - __uint_as_float(h0 << 16), a[1] - __uint_as_float(h0 & 0xffff0000u)),
                                                        pack2bf(a[2] - __uint_as_float(h1 << 16), a[3] - __uint_as_float(h1 & 0xffff0000u))};
        }
        lds_wait(lds0 + SY_CPY, 8);
#pragma unroll
        for (int i = 0; i < 15; ++i) *(u32x4*)(smem + L_PRV + (t2 + 256 * i) * 16) = (u32x4){OOB_OFF, OOB_OFF, 0u, 0u};
        SB();
        asm volatile("" :: "v"(cp), "v"(cm), "v"(t0v));
        make_img();
        lds_arrive(lds0 + SY_PRE);
        lds_wait(lds0 + SY_PRE, 4);
        load_wr();
        if (w == 7) {
            lds_arrive(lds0 + SY_C3);
            half = (lds_wait<1>(lds0 + SY_RI, 64) & 15u) != 0u;
#pragma unroll
            for (int i = 0; i < NT2; ++i) tv[i] = touch(tl[i]);
            SB();
            set_ta();
        } else {
            half = (lds_wait<1>(lds0 + SY_RI, 16) & 15u) != 0u;
            set_ta();
            const int n5 = min(27 * q + (lane >> 1), NPB_MAX - 1);
            const bool kp = (n5 >= 40) == (half != 0);
            u64 lpos = kp ? len_pos(true) : len_pos(false);
            unsigned an = lds0 + n5 * 4, ak = lds0 + L_KEY + (n5 >= 40 ? 160 : 0) + (lane & 1) * 80;
            unsigned aa = L_AT + (n5 >= 40 ? 160 : 0), al = L_LEN + (kp ? 4 : 0), av = (unsigned)n5 | (lds0 + L_PRV + n5 * (LS * 16)) << 8;
            asm volatile("" : "+v"(an), "+v"(ak), "+v"(aa), "+v"(al), "+v"(av), "+v"(lpos));
            lds_wait<1>(lds0 + SY_C1, 3);
            __builtin_amdgcn_s_setprio(HPRIO);
            unsigned kn, cn;
            u32x4 dk[5];
            do {
                asm volatile("ds_read_b32 %0, %7 offset:%c10\n\tds_read_b32 %1, %8 offset:%c11\n\tds_read_b128 %2, %9\n\tds_read_b128 %3, %9 offset:16\n\tds_read_b128 %4, %9 offset:32\n\t"
                             "ds_read_b128 %5, %9 offset:48\n\tds_read_b128 %6, %9 offset:64\n\ts_waitcnt lgkmcnt(0)"
                             : "=&v"(cn), "=&v"(kn), "=&v"(dk[0]), "=&v"(dk[1]), "=&v"(dk[2]), "=&v"(dk[3]), "=&v"(dk[4]) : "v"(lds0), "v"(an), "v"(ak), "i"(SY_C1), "i"(L_KEY) : "memory");
            } while (__builtin_amdgcn_readfirstlane((int)cn) < 4);
            unsigned sg[4] = {0u, 0u, 0u, 0u};
#pragma unroll
            for (int k = 0; k < 20; ++k) sg[k & 3] = __builtin_amdgcn_alignbit(sg[k & 3], kn - dk[k >> 2][k & 3], 31);
            int c = __builtin_popcount(sg[0] | sg[1] << 8 | sg[2] << 16 | sg[3] << 24);
            c += __builtin_amdgcn_update_dpp(0, c, 0xB1, 0xf, 0xf, false);
            *(unsigned*)(smem + aa + c * 4) = av;
            smem[(c & 3) == 0 ? al + ((unsigned)(lpos >> c) & 15u) : L_LEN + 20] = (unsigned char)max(kn >> 7, c < 8 ? 12u : 8u);
            lds_arrive(lds0 + SY_S, __builtin_amdgcn_ballot_w64((kn >> 7) > DEG_MAX) != 0ull ? 257u : 1u);
        }
        __builtin_amdgcn_s_setprio(3);
    }
    unsigned ax[3], af[3], lh, cn;
    u32x4 ln;
    {
        if (w == 7) lds_wait<2, 255>(lds0 + SY_S, 1);
        do {
            asm volatile("ds_read_b32 %0, %9 offset:%c16\n\tds_read_b128 %1, %9 offset:%c17\n\tds_read_b32 %2, %9 offset:%c18\n\tds_read_b32 %3, %10\n\tds_read_b32 %4, %11\n\t"
                         "ds_read_b32 %5, %12\n\tds_read_b32 %6, %13\n\tds_read_b32 %7, %14\n\tds_read_b32 %8, %15\n\ts_waitcnt lgkmcnt(0)"
                         : "=&v"(cn), "=&v"(ln), "=&v"(lh), "=&v"(ax[0]), "=&v"(af[0]), "=&v"(ax[1]), "=&v"(af[1]), "=&v"(ax[2]), "=&v"(af[2])
                         : "v"(lds0), "v"(ta[0]), "v"(ta[1]), "v"(ta[2]), "v"(ta[3]), "v"(ta[4]), "v"(ta[5]),
                           "i"(SY_S), "i"(L_LEN), "i"(L_LEN + 16) : "memory");
            cn = (unsigned)__builtin_amdgcn_readfirstlane((int)cn);
        } while ((cn & 255u) < 3u);
        slow = cn >= 256u;
    }
    const int p8 = __builtin_amdgcn_sdot4((int)(ln.x + ln.y), (int)cc, __builtin_amdgcn_sdot4((int)(ln.w + lh), (int)cc,
                   __builtin_amdgcn_sdot4((int)ln.z, (int)cd, k0, false), false), false);
    const int ld = (__builtin_amdgcn_readfirstlane((int)ln.z) >> ri8) & 255, p2 = min(max(__builtin_amdgcn_readfirstlane(p8) >> 3, 4), ld - 8);
    const int l2 = w < 4 ? ld - p2 : p2, od = (w < 4) == (wd >= 6) ? ld - l2 : 0;
    const unsigned aX = (ax[0] >> 8) + ((w < 4 ? od : 0) + (c16 & 3)) * 16, aF = (af[0] >> 8) + ((w < 4 ? od : 0) + (c16 & 3)) * 16 + 4;
    const unsigned lane_off = (unsigned)c16 * 16u;
    const unsigned aWw = L_BNC + w * 512 + lane * 8;
    const unsigned aWr = L_BNC + w * 512 + g * 128 + (c16 & 3) * 32 + (c16 >> 2) * 8;
    auto row_of = [=](unsigned at) -> unsigned {
        const unsigned nl = at & 0xffu;
        return nl < (unsigned)npb ? ((unsigned)node0 + nl) * 1024u + lane_off : OOB_OFF;
    };
#pragma unroll
    for (int i = 0; i < NT2; ++i) asm volatile("" :: "v"(tv[i]));
    if (!slow) {
        u32x32 oxa, oxb;
        u32x4 efA, efN, S;
        unsigned tmp;
        __builtin_amdgcn_s_waitcnt(0x0F70);
        asm volatile(
            "Lw2_%=:\n"
            "ds_read_b32 v220, v219\n"
            "ds_read2_b32 v[232:233], v211 offset1:16\n"
            "ds_read_b128 v[196:199], v210\n"
            "s_waitcnt lgkmcnt(1)\n"
            "v_readfirstlane_b32 %[tmp], v220\n"
            "s_cmp_lt_u32 %[tmp], 5\n"
            "s_cbranch_scc1 Lw2_%=\n"
            "v_add_u32 v232, v232, v229\n"
            "v_add_u32 v233, v233, v229\n"
            "buffer_load_dwordx4 v[192:195], v232, %[rse], 0 offen\n"
            "s_waitcnt lgkmcnt(0)\n"
            "v_add_u32_dpp v220, v196, v214 quad_perm:[0,0,0,0] row_mask:0xf bank_mask:0xf\n"
            "v_add_u32_dpp v221, v196, v214 quad_perm:[1,1,1,1] row_mask:0xf bank_mask:0xf\n"
            "v_add_u32_dpp v222, v196, v214 quad_perm:[2,2,2,2] row_mask:0xf bank_mask:0xf\n"
            "v_add_u32_dpp v223, v196, v214 quad_perm:[3,3,3,3] row_mask:0xf bank_mask:0xf\n"
            "buffer_load_dwordx4 v[32:35], v220, %[rsx], 0 offen\n"
            "buffer_load_dwordx4 v[36:39], v220, %[rsx], 0 offen offset:256\n"
            "buffer_load_dwordx4 v[48:51], v221, %[rsx], 0 offen\n"
            "buffer_load_dwordx4 v[52:55], v221, %[rsx], 0 offen offset:256\n"
            "buffer_load_dwordx4 v[232:235], v233, %[rse], 0 offen\n"
            "buffer_load_dwordx4 v[64:67], v222, %[rsx], 0 offen\n"
            "buffer_load_dwordx4 v[68:71], v222, %[rsx], 0 offen offset:256\n"
            "buffer_load_dwordx4 v[80:83], v223, %[rsx], 0 offen\n"
            "buffer_load_dwordx4 v[84:87], v223, %[rsx], 0 offen offset:256\n"
            "buffer_load_dwordx4 v[40:43], v220, %[rsx], 0 offen offset:512\n"
            "buffer_load_dwordx4 v[44:47], v220, %[rsx], 0 offen offset:768\n"
            "buffer_load_dwordx4 v[56:59], v221, %[rsx], 0 offen offset:512\n"
            "buffer_load_dwordx4 v[60:63], v221, %[rsx], 0 offen offset:768\n"
            "buffer_load_dwordx4 v[72:75], v222, %[rsx], 0 offen offset:512\n"
            "buffer_load_dwordx4 v[76:79], v222, %[rsx], 0 offen offset:768\n"
            "buffer_load_dwordx4 v[88:91], v223, %[rsx], 0 offen offset:512\n"
            "buffer_load_dwordx4 v[92:95], v223, %[rsx], 0 offen offset:768\n"
            : "={v[32:63]}"(oxa), "={v[64:95]}"(oxb), "={v[192:195]}"(efA), "={v[196:199]}"(S), "={v[232:235]}"(efN), [tmp] "=&s"(tmp)
            : [rse] "s"(de), [rsx] "s"(dx), [ax] "{v210}"(aX), [af] "{v211}"(aF), [lo] "{v214}"(lane_off), [eo] "{v229}"((g & 1) * 16), [sy] "{v219}"(lds0 + SY_C3)
            : "memory", "scc", "v220", "v221", "v222", "v223");
        if (bx == 0 && w == 4) flag_store(ws + W_CTL, tag);
        const int lf = (__builtin_amdgcn_readfirstlane((int)(w < 4 ? ln.w : lh)) >> (8 * (w & 3))) & 255, l1 = (__builtin_amdgcn_readfirstlane((int)(w < 4 ? ln.x : ln.y)) >> (8 * (w & 3))) & 255;
        const int e2 = lf + l1 + l2;
        int nst = (e2 + 3) >> 2;
        const unsigned r2 = row_of(ax[1]);
        u32x32 owr;
        u32x16 omi;
        u32x8 ot;
#pragma unroll
        for (int i = 0; i < 32; ++i) owr[i] = wr[i >> 2][i & 3];
        const int pw = (w + (w >> 2)) & 3;
        const unsigned trb = lds0 + L_TR + w * TR_STR;
        omi[2] = aX + 64; omi[3] = aF + 64;
        omi[4] = lds0 + aWw; omi[5] = lds0 + aWr; omi[6] = lane_off; omi[7] = W_AGG + row_of(ax[0]); omi[8] = W_AGG + r2;
        omi[9] = row_of(ax[2]); omi[10] = w >= 6 ? r2 : OOB_OFF; omi[15] = ~tag;
        ot[0] = (lane & 3) == 0 ? 0x3f80u : ((lane & 3) == 1 ? 0x3f800000u : 0u); ot[1] = (lane & 3) == 2 ? 0x3f80u : ((lane & 3) == 3 ? 0x3f800000u : 0u);
        ot[2] = trb + g * (4 * 272) + c16 * 16; ot[3] = trb + c16 * 272 + g * 16; ot[4] = lds0 + L_IMG + lane * 16; ot[5] = (g & 1) * 16; ot[7] = lds0 + L_PK + pw * 4096 + lane * 16;
        int rem = w < 4 ? l2 : l1, lens = ((w < 4 ? l1 : l2) - 4) | 0xff00, fm = w < 4 ? 0x802 : (w < 6 ? 0x810 : 0xb0), pb = 0;
        const int j1 = (w < 4 ? -od - l2 : od - l1) * 16, j2 = (w < 4 ? -l1 : -od - l2) * 16;
        u32x4 jmp = (u32x4){(ax[1] >> 8) - (ax[0] >> 8) + j1, (af[1] >> 8) - (af[0] >> 8) + j1, (ax[2] >> 8) - (ax[1] >> 8) + j2, (af[2] >> 8) - (af[1] >> 8) + j2};
        unsigned evt = (unsigned)__builtin_amdgcn_readfirstlane(w < 4 ? evt_of(PTO) : (w < 6 ? evt_of(PTY) : evt_of(PTZ))) | 6u << (nst - (rem >> 2)) | 6u << (nst - ((l1 + l2) >> 2));
        u64 ra;
        asm volatile(
            "s_nop 1\n"
            "v_mfma_f32_4x4x4_16b_bf16 v[204:207], v[202:203], v[198:199], 0\n"
            "v_mov_b64 v[0:1], 0\n"
            "v_mov_b64 v[2:3], 0\n"
            "v_mov_b64 v[4:5], 0\n"
            "v_mov_b64 v[6:7], 0\n"
            "v_mfma_f32_4x4x4_16b_bf16 v[204:207], v[200:201], v[198:199], v[204:207]\n"
            "v_mov_b64 v[8:9], 0\n"
            "v_mov_b64 v[10:11], 0\n"
            "v_mov_b64 v[12:13], 0\n"
            "v_mov_b64 v[14:15], 0\n"
            "v_mov_b64 v[16:17], 0\n"
            "v_mov_b64 v[18:19], 0\n"
            "v_mov_b64 v[20:21], 0\n"
            "v_mov_b64 v[22:23], 0\n"
            "v_mov_b64 v[24:25], 0\n"
            "v_mov_b64 v[26:27], 0\n"
            "v_mov_b64 v[28:29], 0\n"
            "v_mov_b64 v[30:31], 0\n"
            "v_cvt_pk_bf16_f32 v204, v204, v205\n"
            "v_cvt_pk_bf16_f32 v205, v206, v207\n"
            "ds_write_b64 v212, v[204:205]\n"
            "ds_read_b128 v[196:199], v210\n"
            "ds_read_b64 v[208:209], v213\n"
            "s_waitcnt vmcnt(17)\n"
            "v_mfma_f32_16x16x32_bf16 v[176:179], v[192:195], v[96:99], 0\n"
            "v_mfma_f32_16x16x32_bf16 v[180:183], v[192:195], v[100:103], 0\n"
            "v_mfma_f32_16x16x32_bf16 v[184:187], v[192:195], v[104:107], 0\n"
            "v_mfma_f32_16x16x32_bf16 v[188:191], v[192:195], v[108:111], 0\n"
            "v_mfma_f32_16x16x32_bf16 v[128:131], v[192:195], v[112:115], 0\n"
            "v_mfma_f32_16x16x32_bf16 v[132:135], v[192:195], v[116:119], 0\n"
            "s_nop 2\n"
            "v_exp_f32 v144, v176\n"
            "v_exp_f32 v145, v177\n"
            "v_exp_f32 v146, v178\n"
            "v_exp_f32 v147, v179\n"
            "v_exp_f32 v148, v180\n"
            "v_exp_f32 v149, v181\n"
            "v_exp_f32 v150, v182\n"
            "v_exp_f32 v151, v183\n"
            "v_exp_f32 v152, v184\n"
            "v_exp_f32 v153, v185\n"
            "v_exp_f32 v154, v186\n"
            "v_exp_f32 v155, v187\n"
            "v_exp_f32 v156, v188\n"
            "v_exp_f32 v157, v189\n"
            "v_exp_f32 v158, v190\n"
            "v_exp_f32 v159, v191\n"
            "v_pk_add_f32 v[144:145], v[144:145], 1.0 op_sel_hi:[1,0]\n"
            "v_pk_add_f32 v[146:147], v[146:147], 1.0 op_sel_hi:[1,0]\n"
            "v_pk_add_f32 v[148:149], v[148:149], 1.0 op_sel_hi:[1,0]\n"
            "v_pk_add_f32 v[150:151], v[150:151], 1.0 op_sel_hi:[1,0]\n"
            "v_pk_add_f32 v[152:153], v[152:153], 1.0 op_sel_hi:[1,0]\n"
            "v_pk_add_f32 v[154:155], v[154:155], 1.0 op_sel_hi:[1,0]\n"
            "v_pk_add_f32 v[156:157], v[156:157], 1.0 op_sel_hi:[1,0]\n"
            "v_pk_add_f32 v[158:159], v[158:159], 1.0 op_sel_hi:[1,0]\n"
            "v_rcp_f32 v144, v144\n"
            "v_rcp_f32 v145, v145\n"
            "v_rcp_f32 v146, v146\n"
            "v_rcp_f32 v147, v147\n"
            "v_rcp_f32 v148, v148\n"
            "v_rcp_f32 v149, v149\n"
            "v_rcp_f32 v150, v150\n"
            "v_rcp_f32 v151, v151\n"
            "v_rcp_f32 v152, v152\n"
            "v_rcp_f32 v153, v153\n"
            "v_rcp_f32 v154, v154\n"
            "v_rcp_f32 v155, v155\n"
            "v_rcp_f32 v156, v156\n"
            "v_rcp_f32 v157, v157\n"
            "v_rcp_f32 v158, v158\n"
            "v_rcp_f32 v159, v159\n"
            "v_pk_mul_f32 v[144:145], v[144:145], v[176:177]\n"
            "v_pk_mul_f32 v[146:147], v[146:147], v[178:179]\n"
            "v_pk_mul_f32 v[148:149], v[148:149], v[180:181]\n"
            "v_pk_mul_f32 v[150:151], v[150:151], v[182:183]\n"
            "v_pk_mul_f32 v[152:153], v[152:153], v[184:185]\n"
            "v_pk_mul_f32 v[154:155], v[154:155], v[186:187]\n"
            "v_pk_mul_f32 v[156:157], v[156:157], v[188:189]\n"
            "v_pk_mul_f32 v[158:159], v[158:159], v[190:191]\n"
            "s_branch Ldisp_%=\n"
            "Lfast_%=:\n"
            "v_mfma_f32_16x16x32_bf16 v[136:139], v[192:195], v[120:123], 0\n"
            "v_exp_f32 v160, v128\n"
            "v_add_u32 v211, 64, v211\n"
            "v_exp_f32 v161, v129\n"
            "s_waitcnt vmcnt(13) lgkmcnt(0)\n"
            "v_mfma_f32_4x4x4_16b_bf16 v[176:179], v[208:209], v[32:33], 0 cbsz:2\n"
            "v_exp_f32 v162, v130\n"
            "v_mfma_f32_4x4x4_16b_bf16 v[180:183], v[208:209], v[34:35], 0 cbsz:2\n"
            "v_exp_f32 v163, v131\n"
            "v_mfma_f32_4x4x4_16b_bf16 v[184:187], v[208:209], v[36:37], 0 cbsz:2\n"
            "v_exp_f32 v164, v132\n"
            "v_mfma_f32_4x4x4_16b_bf16 v[188:191], v[208:209], v[38:39], 0 cbsz:2\n"
            "v_exp_f32 v165, v133\n"
            "v_add_u32_dpp v219, v196, v214 quad_perm:[0,0,0,0] row_mask:0xf bank_mask:0xf\n"
            "v_mfma_f32_4x4x4_16b_bf16 v[204:207], v[202:203], v[198:199], 0\n"
            "v_exp_f32 v166, v134\n"
            "buffer_load_dwordx4 v[32:35], v219, %[rsx], 0 offen\n"
            "buffer_load_dwordx4 v[36:39], v219, %[rsx], 0 offen offset:256\n"
            "v_exp_f32 v167, v135\n"
            "v_pk_fma_f32 v[0:1], v[144:145], v[176:177], v[0:1] op_sel_hi:[0,1,1]\n"
            "v_pk_fma_f32 v[2:3], v[144:145], v[178:179], v[2:3] op_sel_hi:[0,1,1]\n"
            "v_pk_fma_f32 v[4:5], v[148:149], v[180:181], v[4:5] op_sel_hi:[0,1,1]\n"
            "v_pk_fma_f32 v[6:7], v[148:149], v[182:183], v[6:7] op_sel_hi:[0,1,1]\n"
            "v_pk_fma_f32 v[8:9], v[152:153], v[184:185], v[8:9] op_sel_hi:[0,1,1]\n"
            "v_pk_fma_f32 v[10:11], v[152:153], v[186:187], v[10:11] op_sel_hi:[0,1,1]\n"
            "v_pk_fma_f32 v[12:13], v[156:157], v[188:189], v[12:13] op_sel_hi:[0,1,1]\n"
            "v_pk_fma_f32 v[14:15], v[156:157], v[190:191], v[14:15] op_sel_hi:[0,1,1]\n"
            "v_pk_add_f32 v[160:161], v[160:161], 1.0 op_sel_hi:[1,0]\n"
            "v_pk_add_f32 v[162:163], v[162:163], 1.0 op_sel_hi:[1,0]\n"
            "v_pk_add_f32 v[164:165], v[164:165], 1.0 op_sel_hi:[1,0]\n"
            "v_pk_add_f32 v[166:167], v[166:167], 1.0 op_sel_hi:[1,0]\n"
            "v_mfma_f32_16x16x32_bf16 v[140:143], v[192:195], v[124:127], 0\n"
            "ds_read_b32 v230, v211\n"
            "v_rcp_f32 v160, v160\n"
            "v_rcp_f32 v161, v161\n"
            "v_mfma_f32_4x4x4_16b_bf16 v[176:179], v[208:209], v[48:49], 0 cbsz:2 abid:1\n"
            "v_rcp_f32 v162, v162\n"
            "v_mfma_f32_4x4x4_16b_bf16 v[180:183], v[208:209], v[50:51], 0 cbsz:2 abid:1\n"
            "v_rcp_f32 v163, v163\n"
            "v_mfma_f32_4x4x4_16b_bf16 v[184:187], v[208:209], v[52:53], 0 cbsz:2 abid:1\n"
            "v_rcp_f32 v164, v164\n"
            "v_mfma_f32_4x4x4_16b_bf16 v[188:191], v[208:209], v[54:55], 0 cbsz:2 abid:1\n"
            "v_rcp_f32 v165, v165\n"
            "v_add_u32_dpp v220, v196, v214 quad_perm:[1,1,1,1] row_mask:0xf bank_mask:0xf\n"
            "v_mfma_f32_4x4x4_16b_bf16 v[204:207], v[200:201], v[198:199], v[204:207]\n"
            "v_rcp_f32 v166, v166\n"
            "buffer_load_dwordx4 v[48:51], v220, %[rsx], 0 offen\n"
            "buffer_load_dwordx4 v[52:55], v220, %[rsx], 0 offen offset:256\n"
            "v_rcp_f32 v167, v167\n"
            "v_pk_fma_f32 v[0:1], v[144:145], v[176:177], v[0:1] op_sel:[1,0,0]\n"
            "v_pk_fma_f32 v[2:3], v[144:145], v[178:179], v[2:3] op_sel:[1,0,0]\n"
            "v_pk_fma_f32 v[4:5], v[148:149], v[180:181], v[4:5] op_sel:[1,0,0]\n"
            "v_pk_fma_f32 v[6:7], v[148:149], v[182:183], v[6:7] op_sel:[1,0,0]\n"
            "v_pk_fma_f32 v[8:9], v[152:153], v[184:185], v[8:9] op_sel:[1,0,0]\n"
            "v_pk_fma_f32 v[10:11], v[152:153], v[186:187], v[10:11] op_sel:[1,0,0]\n"
            "v_pk_fma_f32 v[12:13], v[156:157], v[188:189], v[12:13] op_sel:[1,0,0]\n"
            "v_pk_fma_f32 v[14:15], v[156:157], v[190:191], v[14:15] op_sel:[1,0,0]\n"
            "v_pk_mul_f32 v[160:161], v[160:161], v[128:129]\n"
            "v_pk_mul_f32 v[162:163], v[162:163], v[130:131]\n"
            "v_pk_mul_f32 v[164:165], v[164:165], v[132:133]\n"
            "v_pk_mul_f32 v[166:167], v[166:167], v[134:135]\n"
            "s_waitcnt vmcnt(12) lgkmcnt(0)\n"
            "v_mov_b64 v[192:193], v[232:233]\n"
            "v_mov_b64 v[194:195], v[234:235]\n"
            "v_add_u32 v230, v230, v229\n"
            "buffer_load_dwordx4 v[232:235], v230, %[rse], 0 offen\n"
            "v_mfma_f32_16x16x32_bf16 v[128:131], v[192:195], v[96:99], 0\n"
            "v_exp_f32 v168, v136\n"
            "v_exp_f32 v169, v137\n"
            "v_mfma_f32_4x4x4_16b_bf16 v[176:179], v[208:209], v[64:65], 0 cbsz:2 abid:2\n"
            "v_exp_f32 v170, v138\n"
            "v_mfma_f32_4x4x4_16b_bf16 v[180:183], v[208:209], v[66:67], 0 cbsz:2 abid:2\n"
            "v_exp_f32 v171, v139\n"
            "v_mfma_f32_4x4x4_16b_bf16 v[184:187], v[208:209], v[68:69], 0 cbsz:2 abid:2\n"
            "v_exp_f32 v172, v140\n"
            "v_mfma_f32_4x4x4_16b_bf16 v[188:191], v[208:209], v[70:71], 0 cbsz:2 abid:2\n"
            "v_exp_f32 v173, v141\n"
            "v_add_u32_dpp v221, v196, v214 quad_perm:[2,2,2,2] row_mask:0xf bank_mask:0xf\n"
            "v_exp_f32 v174, v142\n"
            "buffer_load_dwordx4 v[64:67], v221, %[rsx], 0 offen\n"
            "buffer_load_dwordx4 v[68:71], v221, %[rsx], 0 offen offset:256\n"
            "v_exp_f32 v175, v143\n"
            "v_pk_fma_f32 v[0:1], v[146:147], v[176:177], v[0:1] op_sel_hi:[0,1,1]\n"
            "v_pk_fma_f32 v[2:3], v[146:147], v[178:179], v[2:3] op_sel_hi:[0,1,1]\n"
            "v_pk_fma_f32 v[4:5], v[150:151], v[180:181], v[4:5] op_sel_hi:[0,1,1]\n"
            "v_pk_fma_f32 v[6:7], v[150:151], v[182:183], v[6:7] op_sel_hi:[0,1,1]\n"
            "v_pk_fma_f32 v[8:9], v[154:155], v[184:185], v[8:9] op_sel_hi:[0,1,1]\n"
            "v_pk_fma_f32 v[10:11], v[154:155], v[186:187], v[10:11] op_sel_hi:[0,1,1]\n"
            "v_pk_fma_f32 v[12:13], v[158:159], v[188:189], v[12:13] op_sel_hi:[0,1,1]\n"
            "v_pk_fma_f32 v[14:15], v[158:159], v[190:191], v[14:15] op_sel_hi:[0,1,1]\n"
            "v_pk_add_f32 v[168:169], v[168:169], 1.0 op_sel_hi:[1,0]\n"
            "v_pk_add_f32 v[170:171], v[170:171], 1.0 op_sel_hi:[1,0]\n"
            "v_pk_add_f32 v[172:173], v[172:173], 1.0 op_sel_hi:[1,0]\n"
            "v_pk_add_f32 v[174:175], v[174:175], 1.0 op_sel_hi:[1,0]\n"
            "v_cvt_pk_bf16_f32 v204, v204, v205\n"
            "v_cvt_pk_bf16_f32 v205, v206, v207\n"
            "ds_write_b64 v212, v[204:205]\n"
            "v_mfma_f32_16x16x32_bf16 v[132:135], v[192:195], v[100:103], 0\n"
            "v_rcp_f32 v168, v168\n"
            "v_rcp_f32 v169, v169\n"
            "v_mfma_f32_4x4x4_16b_bf16 v[176:179], v[208:209], v[80:81], 0 cbsz:2 abid:3\n"
            "v_rcp_f32 v170, v170\n"
            "v_mfma_f32_4x4x4_16b_bf16 v[180:183], v[208:209], v[82:83], 0 cbsz:2 abid:3\n"
            "v_rcp_f32 v171, v171\n"
            "v_mfma_f32_4x4x4_16b_bf16 v[184:187], v[208:209], v[84:85], 0 cbsz:2 abid:3\n"
            "v_rcp_f32 v172, v172\n"
            "v_mfma_f32_4x4x4_16b_bf16 v[188:191], v[208:209], v[86:87], 0 cbsz:2 abid:3\n"
            "v_rcp_f32 v173, v173\n"
            "v_add_u32_dpp v222, v196, v214 quad_perm:[3,3,3,3] row_mask:0xf bank_mask:0xf\n"
            "v_rcp_f32 v174, v174\n"
            "buffer_load_dwordx4 v[80:83], v222, %[rsx], 0 offen\n"
            "buffer_load_dwordx4 v[84:87], v222, %[rsx], 0 offen offset:256\n"
            "v_rcp_f32 v175, v175\n"
            "v_pk_fma_f32 v[0:1], v[146:147], v[176:177], v[0:1] op_sel:[1,0,0]\n"
            "v_pk_fma_f32 v[2:3], v[146:147], v[178:179], v[2:3] op_sel:[1,0,0]\n"
            "v_pk_fma_f32 v[4:5], v[150:151], v[180:181], v[4:5] op_sel:[1,0,0]\n"
            "v_pk_fma_f32 v[6:7], v[150:151], v[182:183], v[6:7] op_sel:[1,0,0]\n"
            "v_pk_fma_f32 v[8:9], v[154:155], v[184:185], v[8:9] op_sel:[1,0,0]\n"
            "v_pk_fma_f32 v[10:11], v[154:155], v[186:187], v[10:11] op_sel:[1,0,0]\n"
            "v_pk_fma_f32 v[12:13], v[158:159], v[188:189], v[12:13] op_sel:[1,0,0]\n"
            "v_pk_fma_f32 v[14:15], v[158:159], v[190:191], v[14:15] op_sel:[1,0,0]\n"
            "v_pk_mul_f32 v[168:169], v[168:169], v[136:137]\n"
            "v_pk_mul_f32 v[170:171], v[170:171], v[138:139]\n"
            "v_pk_mul_f32 v[172:173], v[172:173], v[140:141]\n"
            "v_pk_mul_f32 v[174:175], v[174:175], v[142:143]\n"
            "v_add_u32 v210, 64, v210\n"
            "ds_read_b128 v[196:199], v210\n"
            "v_mfma_f32_16x16x32_bf16 v[136:139], v[192:195], v[104:107], 0\n"
            "v_exp_f32 v144, v128\n"
            "v_exp_f32 v145, v129\n"
            "s_waitcnt vmcnt(13)\n"
            "v_mfma_f32_4x4x4_16b_bf16 v[176:179], v[208:209], v[40:41], 0 cbsz:2\n"
            "v_exp_f32 v146, v130\n"
            "v_mfma_f32_4x4x4_16b_bf16 v[180:183], v[208:209], v[42:43], 0 cbsz:2\n"
            "v_exp_f32 v147, v131\n"
            "v_mfma_f32_4x4x4_16b_bf16 v[184:187], v[208:209], v[44:45], 0 cbsz:2\n"
            "v_exp_f32 v148, v132\n"
            "v_mfma_f32_4x4x4_16b_bf16 v[188:191], v[208:209], v[46:47], 0 cbsz:2\n"
            "v_exp_f32 v149, v133\n"
            "v_exp_f32 v150, v134\n"
            "buffer_load_dwordx4 v[40:43], v219, %[rsx], 0 offen offset:512\n"
            "buffer_load_dwordx4 v[44:47], v219, %[rsx], 0 offen offset:768\n"
            "v_exp_f32 v151, v135\n"
            "v_pk_fma_f32 v[16:17], v[160:161], v[176:177], v[16:17] op_sel_hi:[0,1,1]\n"
            "v_pk_fma_f32 v[18:19], v[160:161], v[178:179], v[18:19] op_sel_hi:[0,1,1]\n"
            "v_pk_fma_f32 v[20:21], v[164:165], v[180:181], v[20:21] op_sel_hi:[0,1,1]\n"
            "v_pk_fma_f32 v[22:23], v[164:165], v[182:183], v[22:23] op_sel_hi:[0,1,1]\n"
            "v_pk_fma_f32 v[24:25], v[168:169], v[184:185], v[24:25] op_sel_hi:[0,1,1]\n"
            "v_pk_fma_f32 v[26:27], v[168:169], v[186:187], v[26:27] op_sel_hi:[0,1,1]\n"
            "v_pk_fma_f32 v[28:29], v[172:173], v[188:189], v[28:29] op_sel_hi:[0,1,1]\n"
            "v_pk_fma_f32 v[30:31], v[172:173], v[190:191], v[30:31] op_sel_hi:[0,1,1]\n"
            "v_pk_add_f32 v[144:145], v[144:145], 1.0 op_sel_hi:[1,0]\n"
            "v_pk_add_f32 v[146:147], v[146:147], 1.0 op_sel_hi:[1,0]\n"
            "v_pk_add_f32 v[148:149], v[148:149], 1.0 op_sel_hi:[1,0]\n"
            "v_pk_add_f32 v[150:151], v[150:151], 1.0 op_sel_hi:[1,0]\n"
            "v_mfma_f32_16x16x32_bf16 v[140:143], v[192:195], v[108:111], 0\n"
            "v_rcp_f32 v144, v144\n"
            "v_rcp_f32 v145, v145\n"
            "v_mfma_f32_4x4x4_16b_bf16 v[176:179], v[208:209], v[56:57], 0 cbsz:2 abid:1\n"
            "v_rcp_f32 v146, v146\n"
            "v_mfma_f32_4x4x4_16b_bf16 v[180:183], v[208:209], v[58:59], 0 cbsz:2 abid:1\n"
            "v_rcp_f32 v147, v147\n"
            "v_mfma_f32_4x4x4_16b_bf16 v[184:187], v[208:209], v[60:61], 0 cbsz:2 abid:1\n"
            "v_rcp_f32 v148, v148\n"
            "v_mfma_f32_4x4x4_16b_bf16 v[188:191], v[208:209], v[62:63], 0 cbsz:2 abid:1\n"
            "v_rcp_f32 v149, v149\n"
            "v_rcp_f32 v150, v150\n"
            "buffer_load_dwordx4 v[56:59], v220, %[rsx], 0 offen offset:512\n"
            "buffer_load_dwordx4 v[60:63], v220, %[rsx], 0 offen offset:768\n"
            "v_rcp_f32 v151, v151\n"
            "v_pk_fma_f32 v[16:17], v[160:161], v[176:177], v[16:17] op_sel:[1,0,0]\n"
            "v_pk_fma_f32 v[18:19], v[160:161], v[178:179], v[18:19] op_sel:[1,0,0]\n"
            "v_pk_fma_f32 v[20:21], v[164:165], v[180:181], v[20:21] op_sel:[1,0,0]\n"
            "v_pk_fma_f32 v[22:23], v[164:165], v[182:183], v[22:23] op_sel:[1,0,0]\n"
            "v_pk_fma_f32 v[24:25], v[168:169], v[184:185], v[24:25] op_sel:[1,0,0]\n"
            "v_pk_fma_f32 v[26:27], v[168:169], v[186:187], v[26:27] op_sel:[1,0,0]\n"
            "v_pk_fma_f32 v[28:29], v[172:173], v[188:189], v[28:29] op_sel:[1,0,0]\n"
            "v_pk_fma_f32 v[30:31], v[172:173], v[190:191], v[30:31] op_sel:[1,0,0]\n"
            "v_pk_mul_f32 v[144:145], v[144:145], v[128:129]\n"
            "v_pk_mul_f32 v[146:147], v[146:147], v[130:131]\n"
            "v_pk_mul_f32 v[148:149], v[148:149], v[132:133]\n"
            "v_pk_mul_f32 v[150:151], v[150:151], v[134:135]\n"
            "v_mfma_f32_16x16x32_bf16 v[128:131], v[192:195], v[112:115], 0\n"
            "v_exp_f32 v152, v136\n"
            "v_exp_f32 v153, v137\n"
            "s_waitcnt vmcnt(13)\n"
            "v_mfma_f32_4x4x4_16b_bf16 v[176:179], v[208:209], v[72:73], 0 cbsz:2 abid:2\n"
            "v_exp_f32 v154, v138\n"
            "v_mfma_f32_4x4x4_16b_bf16 v[180:183], v[208:209], v[74:75], 0 cbsz:2 abid:2\n"
            "v_exp_f32 v155, v139\n"
            "v_mfma_f32_4x4x4_16b_bf16 v[184:187], v[208:209], v[76:77], 0 cbsz:2 abid:2\n"
            "v_exp_f32 v156, v140\n"
            "v_mfma_f32_4x4x4_16b_bf16 v[188:191], v[208:209], v[78:79], 0 cbsz:2 abid:2\n"
            "v_exp_f32 v157, v141\n"
            "v_exp_f32 v158, v142\n"
            "buffer_load_dwordx4 v[72:75], v221, %[rsx], 0 offen offset:512\n"
            "buffer_load_dwordx4 v[76:79], v221, %[rsx], 0 offen offset:768\n"
            "v_exp_f32 v159, v143\n"
            "v_pk_fma_f32 v[16:17], v[162:163], v[176:177], v[16:17] op_sel_hi:[0,1,1]\n"
            "v_pk_fma_f32 v[18:19], v[162:163], v[178:179], v[18:19] op_sel_hi:[0,1,1]\n"
            "v_pk_fma_f32 v[20:21], v[166:167], v[180:181], v[20:21] op_sel_hi:[0,1,1]\n"
            "v_pk_fma_f32 v[22:23], v[166:167], v[182:183], v[22:23] op_sel_hi:[0,1,1]\n"
            "v_pk_fma_f32 v[24:25], v[170:171], v[184:185], v[24:25] op_sel_hi:[0,1,1]\n"
            "v_pk_fma_f32 v[26:27], v[170:171], v[186:187], v[26:27] op_sel_hi:[0,1,1]\n"
            "v_pk_fma_f32 v[28:29], v[174:175], v[188:189], v[28:29] op_sel_hi:[0,1,1]\n"
            "v_pk_fma_f32 v[30:31], v[174:175], v[190:191], v[30:31] op_sel_hi:[0,1,1]\n"
            "v_pk_add_f32 v[152:153], v[152:153], 1.0 op_sel_hi:[1,0]\n"
            "v_pk_add_f32 v[154:155], v[154:155], 1.0 op_sel_hi:[1,0]\n"
            "v_pk_add_f32 v[156:157], v[156:157], 1.0 op_sel_hi:[1,0]\n"
            "v_pk_add_f32 v[158:159], v[158:159], 1.0 op_sel_hi:[1,0]\n"
            "v_mfma_f32_16x16x32_bf16 v[132:135], v[192:195], v[116:119], 0\n"
            "v_rcp_f32 v152, v152\n"
            "v_rcp_f32 v153, v153\n"
            "v_mfma_f32_4x4x4_16b_bf16 v[176:179], v[208:209], v[88:89], 0 cbsz:2 abid:3\n"
            "v_rcp_f32 v154, v154\n"
            "v_mfma_f32_4x4x4_16b_bf16 v[180:183], v[208:209], v[90:91], 0 cbsz:2 abid:3\n"
            "v_rcp_f32 v155, v155\n"
            "v_mfma_f32_4x4x4_16b_bf16 v[184:187], v[208:209], v[92:93], 0 cbsz:2 abid:3\n"
            "v_rcp_f32 v156, v156\n"
            "v_mfma_f32_4x4x4_16b_bf16 v[188:191], v[208:209], v[94:95], 0 cbsz:2 abid:3\n"
            "ds_read_b64 v[208:209], v213\n"
            "v_rcp_f32 v157, v157\n"
            "v_rcp_f32 v158, v158\n"
            "buffer_load_dwordx4 v[88:91], v222, %[rsx], 0 offen offset:512\n"
            "buffer_load_dwordx4 v[92:95], v222, %[rsx], 0 offen offset:768\n"
            "v_rcp_f32 v159, v159\n"
            "v_pk_fma_f32 v[16:17], v[162:163], v[176:177], v[16:17] op_sel:[1,0,0]\n"
            "v_pk_fma_f32 v[18:19], v[162:163], v[178:179], v[18:19] op_sel:[1,0,0]\n"
            "v_pk_fma_f32 v[20:21], v[166:167], v[180:181], v[20:21] op_sel:[1,0,0]\n"
            "v_pk_fma_f32 v[22:23], v[166:167], v[182:183], v[22:23] op_sel:[1,0,0]\n"
            "v_pk_fma_f32 v[24:25], v[170:171], v[184:185], v[24:25] op_sel:[1,0,0]\n"
            "v_pk_fma_f32 v[26:27], v[170:171], v[186:187], v[26:27] op_sel:[1,0,0]\n"
            "v_pk_fma_f32 v[28:29], v[174:175], v[188:189], v[28:29] op_sel:[1,0,0]\n"
            "v_pk_fma_f32 v[30:31], v[174:175], v[190:191], v[30:31] op_sel:[1,0,0]\n"
            "v_pk_mul_f32 v[152:153], v[152:153], v[136:137]\n"
            "v_pk_mul_f32 v[154:155], v[154:155], v[138:139]\n"
            "v_pk_mul_f32 v[156:157], v[156:157], v[140:141]\n"
            "v_pk_mul_f32 v[158:159], v[158:159], v[142:143]\n"
            "s_sub_u32 %[rem], %[rem], 4\n"
            "Lnext_%=:\n"
            "s_sub_u32 %[nst], %[nst], 1\n"
            "Ldisp_%=:\n"
            "s_bitcmp1_b32 %[evt], %[nst]\n"
            "s_cbranch_scc1 Lprio_%=\n"
            "Ld2_%=:\n"
            "s_cmp_gt_u32 %[rem], 4\n"
            "s_cbranch_scc1 Lfast_%=\n"
            "v_mfma_f32_16x16x32_bf16 v[136:139], v[192:195], v[120:123], 0\n"
            "v_exp_f32 v160, v128\n"
            "v_add_u32 v211, 64, v211\n"
            "v_exp_f32 v161, v129\n"
            "s_waitcnt vmcnt(13) lgkmcnt(0)\n"
            "v_mfma_f32_4x4x4_16b_bf16 v[176:179], v[208:209], v[32:33], 0 cbsz:2\n"
            "v_exp_f32 v162, v130\n"
            "v_mfma_f32_4x4x4_16b_bf16 v[180:183], v[208:209], v[34:35], 0 cbsz:2\n"
            "v_exp_f32 v163, v131\n"
            "v_mfma_f32_4x4x4_16b_bf16 v[184:187], v[208:209], v[36:37], 0 cbsz:2\n"
            "v_exp_f32 v164, v132\n"
            "v_mfma_f32_4x4x4_16b_bf16 v[188:191], v[208:209], v[38:39], 0 cbsz:2\n"
            "v_exp_f32 v165, v133\n"
            "v_add_u32_dpp v219, v196, v214 quad_perm:[0,0,0,0] row_mask:0xf bank_mask:0xf\n"
            "v_mfma_f32_4x4x4_16b_bf16 v[204:207], v[202:203], v[198:199], 0\n"
            "v_exp_f32 v166, v134\n"
            "buffer_load_dwordx4 v[32:35], v219, %[rsx], 0 offen\n"
            "buffer_load_dwordx4 v[36:39], v219, %[rsx], 0 offen offset:256\n"
            "v_exp_f32 v167, v135\n"
            "v_pk_fma_f32 v[0:1], v[144:145], v[176:177], v[0:1] op_sel_hi:[0,1,1]\n"
            "v_pk_fma_f32 v[2:3], v[144:145], v[178:179], v[2:3] op_sel_hi:[0,1,1]\n"
            "v_pk_fma_f32 v[4:5], v[148:149], v[180:181], v[4:5] op_sel_hi:[0,1,1]\n"
            "v_pk_fma_f32 v[6:7], v[148:149], v[182:183], v[6:7] op_sel_hi:[0,1,1]\n"
            "v_pk_fma_f32 v[8:9], v[152:153], v[184:185], v[8:9] op_sel_hi:[0,1,1]\n"
            "v_pk_fma_f32 v[10:11], v[152:153], v[186:187], v[10:11] op_sel_hi:[0,1,1]\n"
            "v_pk_fma_f32 v[12:13], v[156:157], v[188:189], v[12:13] op_sel_hi:[0,1,1]\n"
            "v_pk_fma_f32 v[14:15], v[156:157], v[190:191], v[14:15] op_sel_hi:[0,1,1]\n"
            "v_pk_add_f32 v[160:161], v[160:161], 1.0 op_sel_hi:[1,0]\n"
            "v_pk_add_f32 v[162:163], v[162:163], 1.0 op_sel_hi:[1,0]\n"
            "v_pk_add_f32 v[164:165], v[164:165], 1.0 op_sel_hi:[1,0]\n"
            "v_pk_add_f32 v[166:167], v[166:167], 1.0 op_sel_hi:[1,0]\n"
            "s_cmp_eq_u32 %[rem], 1\n"
            "s_cbranch_scc1 Lf00_%=\n"
            "Lr00_%=:\n"
            "v_mfma_f32_16x16x32_bf16 v[140:143], v[192:195], v[124:127], 0\n"
            "ds_read_b32 v230, v211\n"
            "v_rcp_f32 v160, v160\n"
            "v_rcp_f32 v161, v161\n"
            "v_mfma_f32_4x4x4_16b_bf16 v[176:179], v[208:209], v[48:49], 0 cbsz:2 abid:1\n"
            "v_rcp_f32 v162, v162\n"
            "v_mfma_f32_4x4x4_16b_bf16 v[180:183], v[208:209], v[50:51], 0 cbsz:2 abid:1\n"
            "v_rcp_f32 v163, v163\n"
            "v_mfma_f32_4x4x4_16b_bf16 v[184:187], v[208:209], v[52:53], 0 cbsz:2 abid:1\n"
            "v_rcp_f32 v164, v164\n"
            "v_mfma_f32_4x4x4_16b_bf16 v[188:191], v[208:209], v[54:55], 0 cbsz:2 abid:1\n"
            "v_rcp_f32 v165, v165\n"
            "v_add_u32_dpp v220, v196, v214 quad_perm:[1,1,1,1] row_mask:0xf bank_mask:0xf\n"
            "v_mfma_f32_4x4x4_16b_bf16 v[204:207], v[200:201], v[198:199], v[204:207]\n"
            "v_rcp_f32 v166, v166\n"
            "buffer_load_dwordx4 v[48:51], v220, %[rsx], 0 offen\n"
            "buffer_load_dwordx4 v[52:55], v220, %[rsx], 0 offen offset:256\n"
            "v_rcp_f32 v167, v167\n"
            "v_pk_fma_f32 v[0:1], v[144:145], v[176:177], v[0:1] op_sel:[1,0,0]\n"
            "v_pk_fma_f32 v[2:3], v[144:145], v[178:179], v[2:3] op_sel:[1,0,0]\n"
            "v_pk_fma_f32 v[4:5], v[148:149], v[180:181], v[4:5] op_sel:[1,0,0]\n"
            "v_pk_fma_f32 v[6:7], v[148:149], v[182:183], v[6:7] op_sel:[1,0,0]\n"
            "v_pk_fma_f32 v[8:9], v[152:153], v[184:185], v[8:9] op_sel:[1,0,0]\n"
            "v_pk_fma_f32 v[10:11], v[152:153], v[186:187], v[10:11] op_sel:[1,0,0]\n"
            "v_pk_fma_f32 v[12:13], v[156:157], v[188:189], v[12:13] op_sel:[1,0,0]\n"
            "v_pk_fma_f32 v[14:15], v[156:157], v[190:191], v[14:15] op_sel:[1,0,0]\n"
            "v_pk_mul_f32 v[160:161], v[160:161], v[128:129]\n"
            "v_pk_mul_f32 v[162:163], v[162:163], v[130:131]\n"
            "v_pk_mul_f32 v[164:165], v[164:165], v[132:133]\n"
            "v_pk_mul_f32 v[166:167], v[166:167], v[134:135]\n"
            "s_cmp_eq_u32 %[rem], 2\n"
            "s_cbranch_scc1 Lf01_%=\n"
            "Lr01_%=:\n"
            "s_waitcnt vmcnt(12) lgkmcnt(0)\n"
            "v_mov_b64 v[192:193], v[232:233]\n"
            "v_mov_b64 v[194:195], v[234:235]\n"
            "v_add_u32 v230, v230, v229\n"
            "buffer_load_dwordx4 v[232:235], v230, %[rse], 0 offen\n"
            "v_mfma_f32_16x16x32_bf16 v[128:131], v[192:195], v[96:99], 0\n"
            "v_exp_f32 v168, v136\n"
            "v_exp_f32 v169, v137\n"
            "v_mfma_f32_4x4x4_16b_bf16 v[176:179], v[208:209], v[64:65], 0 cbsz:2 abid:2\n"
            "v_exp_f32 v170, v138\n"
            "v_mfma_f32_4x4x4_16b_bf16 v[180:183], v[208:209], v[66:67], 0 cbsz:2 abid:2\n"
            "v_exp_f32 v171, v139\n"
            "v_mfma_f32_4x4x4_16b_bf16 v[184:187], v[208:209], v[68:69], 0 cbsz:2 abid:2\n"
            "v_exp_f32 v172, v140\n"
            "v_mfma_f32_4x4x4_16b_bf16 v[188:191], v[208:209], v[70:71], 0 cbsz:2 abid:2\n"
            "v_exp_f32 v173, v141\n"
            "v_add_u32_dpp v221, v196, v214 quad_perm:[2,2,2,2] row_mask:0xf bank_mask:0xf\n"
            "v_exp_f32 v174, v142\n"
            "buffer_load_dwordx4 v[64:67], v221, %[rsx], 0 offen\n"
            "buffer_load_dwordx4 v[68:71], v221, %[rsx], 0 offen offset:256\n"
            "v_exp_f32 v175, v143\n"
            "v_pk_fma_f32 v[0:1], v[146:147], v[176:177], v[0:1] op_sel_hi:[0,1,1]\n"
            "v_pk_fma_f32 v[2:3], v[146:147], v[178:179], v[2:3] op_sel_hi:[0,1,1]\n"
            "v_pk_fma_f32 v[4:5], v[150:151], v[180:181], v[4:5] op_sel_hi:[0,1,1]\n"
            "v_pk_fma_f32 v[6:7], v[150:151], v[182:183], v[6:7] op_sel_hi:[0,1,1]\n"
            "v_pk_fma_f32 v[8:9], v[154:155], v[184:185], v[8:9] op_sel_hi:[0,1,1]\n"
            "v_pk_fma_f32 v[10:11], v[154:155], v[186:187], v[10:11] op_sel_hi:[0,1,1]\n"
            "v_pk_fma_f32 v[12:13], v[158:159], v[188:189], v[12:13] op_sel_hi:[0,1,1]\n"
            "v_pk_fma_f32 v[14:15], v[158:159], v[190:191], v[14:15] op_sel_hi:[0,1,1]\n"
            "v_pk_add_f32 v[168:169], v[168:169], 1.0 op_sel_hi:[1,0]\n"
            "v_pk_add_f32 v[170:171], v[170:171], 1.0 op_sel_hi:[1,0]\n"
            "v_pk_add_f32 v[172:173], v[172:173], 1.0 op_sel_hi:[1,0]\n"
            "v_pk_add_f32 v[174:175], v[174:175], 1.0 op_sel_hi:[1,0]\n"
            "v_cvt_pk_bf16_f32 v204, v204, v205\n"
            "v_cvt_pk_bf16_f32 v205, v206, v207\n"
            "ds_write_b64 v212, v[204:205]\n"
            "s_cmp_eq_u32 %[rem], 3\n"
            "s_cbranch_scc1 Lf02_%=\n"
            "Lr02_%=:\n"
            "v_mfma_f32_16x16x32_bf16 v[132:135], v[192:195], v[100:103], 0\n"
            "v_rcp_f32 v168, v168\n"
            "v_rcp_f32 v169, v169\n"
            "v_mfma_f32_4x4x4_16b_bf16 v[176:179], v[208:209], v[80:81], 0 cbsz:2 abid:3\n"
            "v_rcp_f32 v170, v170\n"
            "v_mfma_f32_4x4x4_16b_bf16 v[180:183], v[208:209], v[82:83], 0 cbsz:2 abid:3\n"
            "v_rcp_f32 v171, v171\n"
            "v_mfma_f32_4x4x4_16b_bf16 v[184:187], v[208:209], v[84:85], 0 cbsz:2 abid:3\n"
            "v_rcp_f32 v172, v172\n"
            "v_mfma_f32_4x4x4_16b_bf16 v[188:191], v[208:209], v[86:87], 0 cbsz:2 abid:3\n"
            "v_rcp_f32 v173, v173\n"
            "v_add_u32_dpp v222, v196, v214 quad_perm:[3,3,3,3] row_mask:0xf bank_mask:0xf\n"
            "v_rcp_f32 v174, v174\n"
            "buffer_load_dwordx4 v[80:83], v222, %[rsx], 0 offen\n"
            "buffer_load_dwordx4 v[84:87], v222, %[rsx], 0 offen offset:256\n"
            "v_rcp_f32 v175, v175\n"
            "v_pk_fma_f32 v[0:1], v[146:147], v[176:177], v[0:1] op_sel:[1,0,0]\n"
            "v_pk_fma_f32 v[2:3], v[146:147], v[178:179], v[2:3] op_sel:[1,0,0]\n"
            "v_pk_fma_f32 v[4:5], v[150:151], v[180:181], v[4:5] op_sel:[1,0,0]\n"
            "v_pk_fma_f32 v[6:7], v[150:151], v[182:183], v[6:7] op_sel:[1,0,0]\n"
            "v_pk_fma_f32 v[8:9], v[154:155], v[184:185], v[8:9] op_sel:[1,0,0]\n"
            "v_pk_fma_f32 v[10:11], v[154:155], v[186:187], v[10:11] op_sel:[1,0,0]\n"
            "v_pk_fma_f32 v[12:13], v[158:159], v[188:189], v[12:13] op_sel:[1,0,0]\n"
            "v_pk_fma_f32 v[14:15], v[158:159], v[190:191], v[14:15] op_sel:[1,0,0]\n"
            "v_pk_mul_f32 v[168:169], v[168:169], v[136:137]\n"
            "v_pk_mul_f32 v[170:171], v[170:171], v[138:139]\n"
            "v_pk_mul_f32 v[172:173], v[172:173], v[140:141]\n"
            "v_pk_mul_f32 v[174:175], v[174:175], v[142:143]\n"
            "s_cmp_eq_u32 %[rem], 4\n"
            "s_cbranch_scc1 Lf03_%=\n"
            "Lr03_%=:\n"
            "v_add_u32 v210, 64, v210\n"
            "ds_read_b128 v[196:199], v210\n"
            "v_mfma_f32_16x16x32_bf16 v[136:139], v[192:195], v[104:107], 0\n"
            "v_exp_f32 v144, v128\n"
            "v_exp_f32 v145, v129\n"
            "s_waitcnt vmcnt(13)\n"
            "v_mfma_f32_4x4x4_16b_bf16 v[176:179], v[208:209], v[40:41], 0 cbsz:2\n"
            "v_exp_f32 v146, v130\n"
            "v_mfma_f32_4x4x4_16b_bf16 v[180:183], v[208:209], v[42:43], 0 cbsz:2\n"
            "v_exp_f32 v147, v131\n"
            "v_mfma_f32_4x4x4_16b_bf16 v[184:187], v[208:209], v[44:45], 0 cbsz:2\n"
            "v_exp_f32 v148, v132\n"
            "v_mfma_f32_4x4x4_16b_bf16 v[188:191], v[208:209], v[46:47], 0 cbsz:2\n"
            "v_exp_f32 v149, v133\n"
            "v_exp_f32 v150, v134\n"
            "buffer_load_dwordx4 v[40:43], v219, %[rsx], 0 offen offset:512\n"
            "buffer_load_dwordx4 v[44:47], v219, %[rsx], 0 offen offset:768\n"
            "v_exp_f32 v151, v135\n"
            "v_pk_fma_f32 v[16:17], v[160:161], v[176:177], v[16:17] op_sel_hi:[0,1,1]\n"
            "v_pk_fma_f32 v[18:19], v[160:161], v[178:179], v[18:19] op_sel_hi:[0,1,1]\n"
            "v_pk_fma_f32 v[20:21], v[164:165], v[180:181], v[20:21] op_sel_hi:[0,1,1]\n"
            "v_pk_fma_f32 v[22:23], v[164:165], v[182:183], v[22:23] op_sel_hi:[0,1,1]\n"
            "v_pk_fma_f32 v[24:25], v[168:169], v[184:185], v[24:25] op_sel_hi:[0,1,1]\n"
            "v_pk_fma_f32 v[26:27], v[168:169], v[186:187], v[26:27] op_sel_hi:[0,1,1]\n"
            "v_pk_fma_f32 v[28:29], v[172:173], v[188:189], v[28:29] op_sel_hi:[0,1,1]\n"
            "v_pk_fma_f32 v[30:31], v[172:173], v[190:191], v[30:31] op_sel_hi:[0,1,1]\n"
            "v_pk_add_f32 v[144:145], v[144:145], 1.0 op_sel_hi:[1,0]\n"
            "v_pk_add_f32 v[146:147], v[146:147], 1.0 op_sel_hi:[1,0]\n"
            "v_pk_add_f32 v[148:149], v[148:149], 1.0 op_sel_hi:[1,0]\n"
            "v_pk_add_f32 v[150:151], v[150:151], 1.0 op_sel_hi:[1,0]\n"
            "s_cmp_eq_u32 %[rem], 1\n"
            "s_cbranch_scc1 Lf10_%=\n"
            "Lr10_%=:\n"
            "v_mfma_f32_16x16x32_bf16 v[140:143], v[192:195], v[108:111], 0\n"
            "v_rcp_f32 v144, v144\n"
            "v_rcp_f32 v145, v145\n"
            "v_mfma_f32_4x4x4_16b_bf16 v[176:179], v[208:209], v[56:57], 0 cbsz:2 abid:1\n"
            "v_rcp_f32 v146, v146\n"
            "v_mfma_f32_4x4x4_16b_bf16 v[180:183], v[208:209], v[58:59], 0 cbsz:2 abid:1\n"
            "v_rcp_f32 v147, v147\n"
            "v_mfma_f32_4x4x4_16b_bf16 v[184:187], v[208:209], v[60:61], 0 cbsz:2 abid:1\n"
            "v_rcp_f32 v148, v148\n"
            "v_mfma_f32_4x4x4_16b_bf16 v[188:191], v[208:209], v[62:63], 0 cbsz:2 abid:1\n"
            "v_rcp_f32 v149, v149\n"
            "v_rcp_f32 v150, v150\n"
            "buffer_load_dwordx4 v[56:59], v220, %[rsx], 0 offen offset:512\n"
            "buffer_load_dwordx4 v[60:63], v220, %[rsx], 0 offen offset:768\n"
            "v_rcp_f32 v151, v151\n"
            "v_pk_fma_f32 v[16:17], v[160:161], v[176:177], v[16:17] op_sel:[1,0,0]\n"
            "v_pk_fma_f32 v[18:19], v[160:161], v[178:179], v[18:19] op_sel:[1,0,0]\n"
            "v_pk_fma_f32 v[20:21], v[164:165], v[180:181], v[20:21] op_sel:[1,0,0]\n"
            "v_pk_fma_f32 v[22:23], v[164:165], v[182:183], v[22:23] op_sel:[1,0,0]\n"
            "v_pk_fma_f32 v[24:25], v[168:169], v[184:185], v[24:25] op_sel:[1,0,0]\n"
            "v_pk_fma_f32 v[26:27], v[168:169], v[186:187], v[26:27] op_sel:[1,0,0]\n"
            "v_pk_fma_f32 v[28:29], v[172:173], v[188:189], v[28:29] op_sel:[1,0,0]\n"
            "v_pk_fma_f32 v[30:31], v[172:173], v[190:191], v[30:31] op_sel:[1,0,0]\n"
            "v_pk_mul_f32 v[144:145], v[144:145], v[128:129]\n"
            "v_pk_mul_f32 v[146:147], v[146:147], v[130:131]\n"
            "v_pk_mul_f32 v[148:149], v[148:149], v[132:133]\n"
            "v_pk_mul_f32 v[150:151], v[150:151], v[134:135]\n"
            "s_cmp_eq_u32 %[rem], 2\n"
            "s_cbranch_scc1 Lf11_%=\n"
            "Lr11_%=:\n"
            "v_mfma_f32_16x16x32_bf16 v[128:131], v[192:195], v[112:115], 0\n"
            "v_exp_f32 v152, v136\n"
            "v_exp_f32 v153, v137\n"
            "s_waitcnt vmcnt(13)\n"
            "v_mfma_f32_4x4x4_16b_bf16 v[176:179], v[208:209], v[72:73], 0 cbsz:2 abid:2\n"
            "v_exp_f32 v154, v138\n"
            "v_mfma_f32_4x4x4_16b_bf16 v[180:183], v[208:209], v[74:75], 0 cbsz:2 abid:2\n"
            "v_exp_f32 v155, v139\n"
            "v_mfma_f32_4x4x4_16b_bf16 v[184:187], v[208:209], v[76:77], 0 cbsz:2 abid:2\n"
            "v_exp_f32 v156, v140\n"
            "v_mfma_f32_4x4x4_16b_bf16 v[188:191], v[208:209], v[78:79], 0 cbsz:2 abid:2\n"
            "v_exp_f32 v157, v141\n"
            "v_exp_f32 v158, v142\n"
            "buffer_load_dwordx4 v[72:75], v221, %[rsx], 0 offen offset:512\n"
            "buffer_load_dwordx4 v[76:79], v221, %[rsx], 0 offen offset:768\n"
            "v_exp_f32 v159, v143\n"
            "v_pk_fma_f32 v[16:17], v[162:163], v[176:177], v[16:17] op_sel_hi:[0,1,1]\n"
            "v_pk_fma_f32 v[18:19], v[162:163], v[178:179], v[18:19] op_sel_hi:[0,1,1]\n"
            "v_pk_fma_f32 v[20:21], v[166:167], v[180:181], v[20:21] op_sel_hi:[0,1,1]\n"
            "v_pk_fma_f32 v[22:23], v[166:167], v[182:183], v[22:23] op_sel_hi:[0,1,1]\n"
            "v_pk_fma_f32 v[24:25], v[170:171], v[184:185], v[24:25] op_sel_hi:[0,1,1]\n"
            "v_pk_fma_f32 v[26:27], v[170:171], v[186:187], v[26:27] op_sel_hi:[0,1,1]\n"
            "v_pk_fma_f32 v[28:29], v[174:175], v[188:189], v[28:29] op_sel_hi:[0,1,1]\n"
            "v_pk_fma_f32 v[30:31], v[174:175], v[190:191], v[30:31] op_sel_hi:[0,1,1]\n"
            "v_pk_add_f32 v[152:153], v[152:153], 1.0 op_sel_hi:[1,0]\n"
            "v_pk_add_f32 v[154:155], v[154:155], 1.0 op_sel_hi:[1,0]\n"
            "v_pk_add_f32 v[156:157], v[156:157], 1.0 op_sel_hi:[1,0]\n"
            "v_pk_add_f32 v[158:159], v[158:159], 1.0 op_sel_hi:[1,0]\n"
            "s_cmp_eq_u32 %[rem], 3\n"
            "s_cbranch_scc1 Lf12_%=\n"
            "Lr12_%=:\n"
            "v_mfma_f32_16x16x32_bf16 v[132:135], v[192:195], v[116:119], 0\n"
            "v_rcp_f32 v152, v152\n"
            "v_rcp_f32 v153, v153\n"
            "v_mfma_f32_4x4x4_16b_bf16 v[176:179], v[208:209], v[88:89], 0 cbsz:2 abid:3\n"
            "v_rcp_f32 v154, v154\n"
            "v_mfma_f32_4x4x4_16b_bf16 v[180:183], v[208:209], v[90:91], 0 cbsz:2 abid:3\n"
            "v_rcp_f32 v155, v155\n"
            "v_mfma_f32_4x4x4_16b_bf16 v[184:187], v[208:209], v[92:93], 0 cbsz:2 abid:3\n"
            "v_rcp_f32 v156, v156\n"
            "v_mfma_f32_4x4x4_16b_bf16 v[188:191], v[208:209], v[94:95], 0 cbsz:2 abid:3\n"
            "ds_read_b64 v[208:209], v213\n"
            "v_rcp_f32 v157, v157\n"
            "v_rcp_f32 v158, v158\n"
            "buffer_load_dwordx4 v[88:91], v222, %[rsx], 0 offen offset:512\n"
            "buffer_load_dwordx4 v[92:95], v222, %[rsx], 0 offen offset:768\n"
            "v_rcp_f32 v159, v159\n"
            "v_pk_fma_f32 v[16:17], v[162:163], v[176:177], v[16:17] op_sel:[1,0,0]\n"
            "v_pk_fma_f32 v[18:19], v[162:163], v[178:179], v[18:19] op_sel:[1,0,0]\n"
            "v_pk_fma_f32 v[20:21], v[166:167], v[180:181], v[20:21] op_sel:[1,0,0]\n"
            "v_pk_fma_f32 v[22:23], v[166:167], v[182:183], v[22:23] op_sel:[1,0,0]\n"
            "v_pk_fma_f32 v[24:25], v[170:171], v[184:185], v[24:25] op_sel:[1,0,0]\n"
            "v_pk_fma_f32 v[26:27], v[170:171], v[186:187], v[26:27] op_sel:[1,0,0]\n"
            "v_pk_fma_f32 v[28:29], v[174:175], v[188:189], v[28:29] op_sel:[1,0,0]\n"
            "v_pk_fma_f32 v[30:31], v[174:175], v[190:191], v[30:31] op_sel:[1,0,0]\n"
            "v_pk_mul_f32 v[152:153], v[152:153], v[136:137]\n"
            "v_pk_mul_f32 v[154:155], v[154:155], v[138:139]\n"
            "v_pk_mul_f32 v[156:157], v[156:157], v[140:141]\n"
            "v_pk_mul_f32 v[158:159], v[158:159], v[142:143]\n"
            "s_cmp_eq_u32 %[rem], 4\n"
            "s_cbranch_scc1 Lf13_%=\n"
            "Lr13_%=:\n"
            "s_and_b32 %[tmp], %[lens], 0xff\n"
            "s_add_u32 %[rem], %[rem], %[tmp]\n"
            "s_lshr_b32 %[lens], %[lens], 8\n"
            "s_lshr_b32 %[fm], %[fm], 4\n"
            "s_bitcmp1_b32 %[fm], 3\n"
            "v_mov_b32 v215, v216\n"
            "s_cbranch_scc0 Lnext_%=\n"
            "s_sub_u32 %[pb], %[nst], 2\n"
            "s_bitset1_b32 %[evt], %[pb]\n"
            "s_branch Lnext_%=\n"
            "Lpub_%=:\n"
            "s_waitcnt vmcnt(17)\n"
            "v_mov_b32 v176, %[cnt]\n"
            "v_mov_b32 v177, 1\n"
            "s_mov_b64 exec, 1\n"
            "ds_add_rtn_u32 v177, v176, v177\n"
            "s_waitcnt lgkmcnt(0)\n"
            "v_readfirstlane_b32 %[tmp], v177\n"
            "s_mov_b64 exec, 0xff\n"
            "v_mov_b32 v176, %[tag]\n"
            "v_mov_b32 v177, %[tag]\n"
            "v_mov_b32 v178, %[tag]\n"
            "v_mov_b32 v179, %[tag]\n"
            "s_cmp_lg_u32 %[tmp], 7\n"
            "s_cbranch_scc1 Lpub2_%=\n"
            "global_store_dwordx4 v214, v[176:179], %[flg] sc1\n"
            "Lpub2_%=:\n"
            "s_mov_b64 exec, -1\n"
            "s_branch Lpr2_%=\n"
            "Lprio_%=:\n"
            "s_cmp_eq_u32 %[nst], %[pb]\n"
            "s_cbranch_scc1 Lpub_%=\n"
            "Lpr2_%=:\n"
            "s_cmp_eq_u32 %[nst], 1\n"
            "s_cbranch_scc1 Llast_%=\n"
            "s_lshl2_add_u32 %[tmp], %[nst], %[c1]\n"
            "v_bfe_u32 v176, v214, 4, 2\n"
            "v_sub_u32 v176, %[tmp], v176\n"
            "v_cmp_gt_u32 vcc, 4, v176\n"
            "v_add_u32 v176, %[c2], v176\n"
            "s_min_u32 %[tmp], %[nst], 15\n"
            "v_cndmask_b32 v177, 0, v236, vcc\n"
            "v_cndmask_b32 v178, 0, v237, vcc\n"
            "v_cmp_gt_u32 vcc, 4, v176\n"
            "v_add_u32 v210, v210, v177\n"
            "v_add_u32 v211, v211, v178\n"
            "v_cndmask_b32 v177, 0, v238, vcc\n"
            "v_cndmask_b32 v178, 0, v239, vcc\n"
            "v_add_u32 v210, v210, v177\n"
            "v_add_u32 v211, v211, v178\n"
            "s_lshl_b32 %[tmp], %[tmp], 1\n"
            "s_lshr_b32 %[tmp], %[ptab], %[tmp]\n"
            "s_bitcmp1_b32 %[tmp], 1\n"
            "s_cbranch_scc1 Lq2_%=\n"
            "s_bitcmp1_b32 %[tmp], 0\n"
            "s_setprio 0\n"
            "s_cbranch_scc0 Lq9_%=\n"
            "s_setprio 1\n"
            "s_branch Lq9_%=\n"
            "Lq2_%=:\n"
            "s_bitcmp1_b32 %[tmp], 0\n"
            "s_setprio 2\n"
            "s_cbranch_scc0 Lq9_%=\n"
            "s_setprio 3\n"
            "Lq9_%=:\n"
            "s_cmp_lg_u32 %[nst], 2\n"
            "s_cbranch_scc1 Ld2_%=\n"
            "v_mov_b32 v223, 0\n"
            "global_load_dword v223, v223, %[pfl] sc1\n"
            "s_branch Ld2_%=\n"
            "Lf00_%=:\n"
            "s_call_b64 %[ra], Lflush0_%=\n"
            "s_branch Lr00_%=\n"
            "Lf01_%=:\n"
            "s_call_b64 %[ra], Lflush0_%=\n"
            "s_branch Lr01_%=\n"
            "Lf02_%=:\n"
            "s_call_b64 %[ra], Lflush0_%=\n"
            "s_branch Lr02_%=\n"
            "Lf03_%=:\n"
            "s_call_b64 %[ra], Lflush0_%=\n"
            "s_branch Lr03_%=\n"
            "Lflush0_%=:\n"
            "v_mov_b32 v184, %[pfa]\n"
            "s_bitcmp0_b32 %[fm], 0\n"
            "s_cbranch_scc1 Lfa0_%=\n"
            "Lfw_%=:\n"
            "ds_read_b32 v185, v184\n"
            "ds_read_b128 v[176:179], v231 offset:0\n"
            "ds_read_b128 v[180:183], v231 offset:1024\n"
            "s_waitcnt lgkmcnt(0)\n"
            "v_cmp_eq_u32 vcc, 0, v185\n"
            "s_cbranch_vccnz Lfw_%=\n"
            "v_mfma_f32_4x4x4_16b_bf16 v[0:3], v[224:225], v[176:177], v[0:3]\n"
            "v_mfma_f32_4x4x4_16b_bf16 v[4:7], v[224:225], v[178:179], v[4:7]\n"
            "v_mfma_f32_4x4x4_16b_bf16 v[8:11], v[224:225], v[180:181], v[8:11]\n"
            "v_mfma_f32_4x4x4_16b_bf16 v[12:15], v[224:225], v[182:183], v[12:15]\n"
            "s_nop 1\n"
            "Lfa0_%=:\n"
            "v_cvt_pk_bf16_f32 v176, v0, v1\n"
            "v_cvt_pk_bf16_f32 v177, v2, v3\n"
            "v_cvt_pk_bf16_f32 v178, v4, v5\n"
            "v_cvt_pk_bf16_f32 v179, v6, v7\n"
            "v_cvt_pk_bf16_f32 v180, v8, v9\n"
            "v_cvt_pk_bf16_f32 v181, v10, v11\n"
            "v_cvt_pk_bf16_f32 v182, v12, v13\n"
            "v_cvt_pk_bf16_f32 v183, v14, v15\n"
            "s_bitcmp1_b32 %[fm], 1\n"
            "s_cbranch_scc1 Lpark0_%=\n"
            "buffer_store_dwordx4 v[176:179], v215, %[rsw], 0 offen offset:0 sc1\n"
            "buffer_store_dwordx4 v[180:183], v215, %[rsw], 0 offen offset:256 sc1\n"
            "s_branch Lzero0_%=\n"
            "Lpark0_%=:\n"
            "ds_write_b128 v231, v[176:179] offset:0\n"
            "ds_write_b128 v231, v[180:183] offset:1024\n"
            "Lzero0_%=:\n"
            "v_mov_b64 v[0:1], 0\n"
            "v_mov_b64 v[2:3], 0\n"
            "v_mov_b64 v[4:5], 0\n"
            "v_mov_b64 v[6:7], 0\n"
            "v_mov_b64 v[8:9], 0\n"
            "v_mov_b64 v[10:11], 0\n"
            "v_mov_b64 v[12:13], 0\n"
            "v_mov_b64 v[14:15], 0\n"
            "s_setpc_b64 %[ra]\n"
            "Lf10_%=:\n"
            "s_call_b64 %[ra], Lflush1_%=\n"
            "s_branch Lr10_%=\n"
            "Lf11_%=:\n"
            "s_call_b64 %[ra], Lflush1_%=\n"
            "s_branch Lr11_%=\n"
            "Lf12_%=:\n"
            "s_call_b64 %[ra], Lflush1_%=\n"
            "s_branch Lr12_%=\n"
            "Lf13_%=:\n"
            "s_call_b64 %[ra], Lflush1_%=\n"
            "s_branch Lr13_%=\n"
            "Lflush1_%=:\n"
            "v_mov_b32 v184, %[pfa]\n"
            "s_bitcmp0_b32 %[fm], 0\n"
            "s_cbranch_scc1 Lfa1_%=\n"
            "ds_read_b128 v[176:179], v231 offset:2048\n"
            "ds_read_b128 v[180:183], v231 offset:3072\n"
            "s_waitcnt lgkmcnt(0)\n"
            "v_mfma_f32_4x4x4_16b_bf16 v[16:19], v[224:225], v[176:177], v[16:19]\n"
            "v_mfma_f32_4x4x4_16b_bf16 v[20:23], v[224:225], v[178:179], v[20:23]\n"
            "v_mfma_f32_4x4x4_16b_bf16 v[24:27], v[224:225], v[180:181], v[24:27]\n"
            "v_mfma_f32_4x4x4_16b_bf16 v[28:31], v[224:225], v[182:183], v[28:31]\n"
            "s_nop 1\n"
            "Lfa1_%=:\n"
            "v_cvt_pk_bf16_f32 v176, v16, v17\n"
            "v_cvt_pk_bf16_f32 v177, v18, v19\n"
            "v_cvt_pk_bf16_f32 v178, v20, v21\n"
            "v_cvt_pk_bf16_f32 v179, v22, v23\n"
            "v_cvt_pk_bf16_f32 v180, v24, v25\n"
            "v_cvt_pk_bf16_f32 v181, v26, v27\n"
            "v_cvt_pk_bf16_f32 v182, v28, v29\n"
            "v_cvt_pk_bf16_f32 v183, v30, v31\n"
            "s_bitcmp1_b32 %[fm], 1\n"
            "s_cbranch_scc1 Lpark1_%=\n"
            "buffer_store_dwordx4 v[176:179], v215, %[rsw], 0 offen offset:512 sc1\n"
            "buffer_store_dwordx4 v[180:183], v215, %[rsw], 0 offen offset:768 sc1\n"
            "s_branch Lzero1_%=\n"
            "Lpark1_%=:\n"
            "ds_write_b128 v231, v[176:179] offset:2048\n"
            "ds_write_b128 v231, v[180:183] offset:3072\n"
            "ds_write_b32 v184, v184\n"
            "Lzero1_%=:\n"
            "v_mov_b64 v[16:17], 0\n"
            "v_mov_b64 v[18:19], 0\n"
            "v_mov_b64 v[20:21], 0\n"
            "v_mov_b64 v[22:23], 0\n"
            "v_mov_b64 v[24:25], 0\n"
            "v_mov_b64 v[26:27], 0\n"
            "v_mov_b64 v[28:29], 0\n"
            "v_mov_b64 v[30:31], 0\n"
            "s_setpc_b64 %[ra]\n"
            "Lspin_%=:\n"
            "s_mov_b32 %[lens], %[spin]\n"
            "Lsp1_%=:\n"
            "v_readfirstlane_b32 %[nst], v223\n"
            "s_sub_u32 %[lens], %[lens], 1\n"
            "s_cmp_eq_u32 %[lens], 0\n"
            "s_cbranch_scc1 Lsp2_%=\n"
            "s_cmp_eq_u32 %[nst], %[tag]\n"
            "s_cbranch_scc1 Lsp2_%=\n"
            "s_sleep 1\n"
            "v_mov_b32 v223, 0\n"
            "global_load_dword v223, v223, %[pfl] sc1\n"
            "s_waitcnt vmcnt(0)\n"
            "s_branch Lsp1_%=\n"
            "Lsp2_%=:\n"
            "buffer_load_dwordx4 v[32:35], v217, %[rsw], %[oagg] offen offset:0 sc1\n"
            "buffer_load_dwordx4 v[36:39], v217, %[rsw], %[oagg] offen offset:256 sc1\n"
            "buffer_load_dwordx4 v[48:51], v217, %[rsw], %[oagg] offen offset:512 sc1\n"
            "buffer_load_dwordx4 v[52:55], v217, %[rsw], %[oagg] offen offset:768 sc1\n"
            "s_branch Lgo_%=\n"
            "Llast_%=:\n"
            "s_waitcnt vmcnt(16)\n"
            "v_readfirstlane_b32 %[nst], v223\n"
            "v_mov_b32 v222, v217\n"
            "s_cmp_eq_u32 %[nst], %[tag]\n"
            "s_cbranch_scc1 Lp4_%=\n"
            "v_bfrev_b32 v222, 1\n"
            "v_mov_b32 v223, 0\n"
            "global_load_dword v223, v223, %[pfl] sc1\n"
            "Lp4_%=:\n"
            "v_mfma_f32_16x16x32_bf16 v[136:139], v[192:195], v[120:123], 0\n"
            "v_exp_f32 v160, v128\n"
            "v_exp_f32 v161, v129\n"
            "s_waitcnt vmcnt(13) lgkmcnt(0)\n"
            "v_mfma_f32_4x4x4_16b_bf16 v[176:179], v[208:209], v[32:33], 0 cbsz:2\n"
            "v_exp_f32 v162, v130\n"
            "v_mfma_f32_4x4x4_16b_bf16 v[180:183], v[208:209], v[34:35], 0 cbsz:2\n"
            "v_exp_f32 v163, v131\n"
            "v_mfma_f32_4x4x4_16b_bf16 v[184:187], v[208:209], v[36:37], 0 cbsz:2\n"
            "v_exp_f32 v164, v132\n"
            "v_mfma_f32_4x4x4_16b_bf16 v[188:191], v[208:209], v[38:39], 0 cbsz:2\n"
            "v_exp_f32 v165, v133\n"
            "v_exp_f32 v166, v134\n"
            "buffer_load_dwordx4 v[32:35], v222, %[rsw], %[oagg] offen offset:0 sc1\n"
            "buffer_load_dwordx4 v[36:39], v222, %[rsw], %[oagg] offen offset:256 sc1\n"
            "ds_read_b128 v[236:239], v228 offset:0\n"
            "ds_read_b128 v[240:243], v228 offset:1024\n"
            "ds_read_b128 v[244:247], v228 offset:2048\n"
            "ds_read_b128 v[248:251], v228 offset:3072\n"
            "ds_read_b128 v[96:99], v228 offset:4096\n"
            "v_exp_f32 v167, v135\n"
            "v_pk_fma_f32 v[0:1], v[144:145], v[176:177], v[0:1] op_sel_hi:[0,1,1]\n"
            "v_pk_fma_f32 v[2:3], v[144:145], v[178:179], v[2:3] op_sel_hi:[0,1,1]\n"
            "v_pk_fma_f32 v[4:5], v[148:149], v[180:181], v[4:5] op_sel_hi:[0,1,1]\n"
            "v_pk_fma_f32 v[6:7], v[148:149], v[182:183], v[6:7] op_sel_hi:[0,1,1]\n"
            "v_pk_fma_f32 v[8:9], v[152:153], v[184:185], v[8:9] op_sel_hi:[0,1,1]\n"
            "v_pk_fma_f32 v[10:11], v[152:153], v[186:187], v[10:11] op_sel_hi:[0,1,1]\n"
            "v_pk_fma_f32 v[12:13], v[156:157], v[188:189], v[12:13] op_sel_hi:[0,1,1]\n"
            "v_pk_fma_f32 v[14:15], v[156:157], v[190:191], v[14:15] op_sel_hi:[0,1,1]\n"
            "v_pk_add_f32 v[160:161], v[160:161], 1.0 op_sel_hi:[1,0]\n"
            "v_pk_add_f32 v[162:163], v[162:163], 1.0 op_sel_hi:[1,0]\n"
            "v_pk_add_f32 v[164:165], v[164:165], 1.0 op_sel_hi:[1,0]\n"
            "v_pk_add_f32 v[166:167], v[166:167], 1.0 op_sel_hi:[1,0]\n"
            "v_mfma_f32_16x16x32_bf16 v[140:143], v[192:195], v[124:127], 0\n"
            "v_rcp_f32 v160, v160\n"
            "v_rcp_f32 v161, v161\n"
            "v_mfma_f32_4x4x4_16b_bf16 v[176:179], v[208:209], v[48:49], 0 cbsz:2 abid:1\n"
            "v_rcp_f32 v162, v162\n"
            "v_mfma_f32_4x4x4_16b_bf16 v[180:183], v[208:209], v[50:51], 0 cbsz:2 abid:1\n"
            "v_rcp_f32 v163, v163\n"
            "v_mfma_f32_4x4x4_16b_bf16 v[184:187], v[208:209], v[52:53], 0 cbsz:2 abid:1\n"
            "v_rcp_f32 v164, v164\n"
            "v_mfma_f32_4x4x4_16b_bf16 v[188:191], v[208:209], v[54:55], 0 cbsz:2 abid:1\n"
            "v_rcp_f32 v165, v165\n"
            "v_rcp_f32 v166, v166\n"
            "buffer_load_dwordx4 v[48:51], v222, %[rsw], %[oagg] offen offset:512 sc1\n"
            "buffer_load_dwordx4 v[52:55], v222, %[rsw], %[oagg] offen offset:768 sc1\n"
            "ds_read_b128 v[100:103], v228 offset:5120\n"
            "ds_read_b128 v[104:107], v228 offset:6144\n"
            "ds_read_b128 v[108:111], v228 offset:7168\n"
            "ds_read_b128 v[112:115], v228 offset:8192\n"
            "ds_read_b128 v[116:119], v228 offset:9216\n"
            "ds_read_b128 v[196:199], v228 offset:10240\n"
            "v_rcp_f32 v167, v167\n"
            "v_pk_fma_f32 v[0:1], v[144:145], v[176:177], v[0:1] op_sel:[1,0,0]\n"
            "v_pk_fma_f32 v[2:3], v[144:145], v[178:179], v[2:3] op_sel:[1,0,0]\n"
            "v_pk_fma_f32 v[4:5], v[148:149], v[180:181], v[4:5] op_sel:[1,0,0]\n"
            "v_pk_fma_f32 v[6:7], v[148:149], v[182:183], v[6:7] op_sel:[1,0,0]\n"
            "v_pk_fma_f32 v[8:9], v[152:153], v[184:185], v[8:9] op_sel:[1,0,0]\n"
            "v_pk_fma_f32 v[10:11], v[152:153], v[186:187], v[10:11] op_sel:[1,0,0]\n"
            "v_pk_fma_f32 v[12:13], v[156:157], v[188:189], v[12:13] op_sel:[1,0,0]\n"
            "v_pk_fma_f32 v[14:15], v[156:157], v[190:191], v[14:15] op_sel:[1,0,0]\n"
            "v_pk_mul_f32 v[160:161], v[160:161], v[128:129]\n"
            "v_pk_mul_f32 v[162:163], v[162:163], v[130:131]\n"
            "v_pk_mul_f32 v[164:165], v[164:165], v[132:133]\n"
            "v_pk_mul_f32 v[166:167], v[166:167], v[134:135]\n"
            "v_exp_f32 v168, v136\n"
            "v_exp_f32 v169, v137\n"
            "s_waitcnt vmcnt(12)\n"
            "v_mfma_f32_4x4x4_16b_bf16 v[176:179], v[208:209], v[64:65], 0 cbsz:2 abid:2\n"
            "v_exp_f32 v170, v138\n"
            "v_mfma_f32_4x4x4_16b_bf16 v[180:183], v[208:209], v[66:67], 0 cbsz:2 abid:2\n"
            "v_exp_f32 v171, v139\n"
            "v_mfma_f32_4x4x4_16b_bf16 v[184:187], v[208:209], v[68:69], 0 cbsz:2 abid:2\n"
            "v_exp_f32 v172, v140\n"
            "v_mfma_f32_4x4x4_16b_bf16 v[188:191], v[208:209], v[70:71], 0 cbsz:2 abid:2\n"
            "v_exp_f32 v173, v141\n"
            "v_exp_f32 v174, v142\n"
            "ds_read_b128 v[200:203], v228 offset:11264\n"
            "ds_read_b128 v[204:207], v228 offset:12288\n"
            "ds_read_b128 v[232:235], v228 offset:13312\n"
            "v_exp_f32 v175, v143\n"
            "v_pk_fma_f32 v[0:1], v[146:147], v[176:177], v[0:1] op_sel_hi:[0,1,1]\n"
            "v_pk_fma_f32 v[2:3], v[146:147], v[178:179], v[2:3] op_sel_hi:[0,1,1]\n"
            "v_pk_fma_f32 v[4:5], v[150:151], v[180:181], v[4:5] op_sel_hi:[0,1,1]\n"
            "v_pk_fma_f32 v[6:7], v[150:151], v[182:183], v[6:7] op_sel_hi:[0,1,1]\n"
            "v_pk_fma_f32 v[8:9], v[154:155], v[184:185], v[8:9] op_sel_hi:[0,1,1]\n"
            "v_pk_fma_f32 v[10:11], v[154:155], v[186:187], v[10:11] op_sel_hi:[0,1,1]\n"
            "v_pk_fma_f32 v[12:13], v[158:159], v[188:189], v[12:13] op_sel_hi:[0,1,1]\n"
            "v_pk_fma_f32 v[14:15], v[158:159], v[190:191], v[14:15] op_sel_hi:[0,1,1]\n"
            "v_pk_add_f32 v[168:169], v[168:169], 1.0 op_sel_hi:[1,0]\n"
            "v_pk_add_f32 v[170:171], v[170:171], 1.0 op_sel_hi:[1,0]\n"
            "v_pk_add_f32 v[172:173], v[172:173], 1.0 op_sel_hi:[1,0]\n"
            "v_pk_add_f32 v[174:175], v[174:175], 1.0 op_sel_hi:[1,0]\n"
            "v_rcp_f32 v168, v168\n"
            "v_rcp_f32 v169, v169\n"
            "v_mfma_f32_4x4x4_16b_bf16 v[176:179], v[208:209], v[80:81], 0 cbsz:2 abid:3\n"
            "v_rcp_f32 v170, v170\n"
            "v_mfma_f32_4x4x4_16b_bf16 v[180:183], v[208:209], v[82:83], 0 cbsz:2 abid:3\n"
            "v_rcp_f32 v171, v171\n"
            "v_mfma_f32_4x4x4_16b_bf16 v[184:187], v[208:209], v[84:85], 0 cbsz:2 abid:3\n"
            "v_rcp_f32 v172, v172\n"
            "v_mfma_f32_4x4x4_16b_bf16 v[188:191], v[208:209], v[86:87], 0 cbsz:2 abid:3\n"
            "v_rcp_f32 v173, v173\n"
            "v_rcp_f32 v174, v174\n"
            "ds_read_b128 v[120:123], v228 offset:14336\n"
            "ds_read_b128 v[124:127], v228 offset:15360\n"
            "ds_read_b128 v[192:195], v228 offset:16384\n"
            "ds_read_b128 v[128:131], v228 offset:17408\n"
            "ds_read_b128 v[132:135], v228 offset:18432\n"
            "v_rcp_f32 v175, v175\n"
            "v_pk_fma_f32 v[0:1], v[146:147], v[176:177], v[0:1] op_sel:[1,0,0]\n"
            "v_pk_fma_f32 v[2:3], v[146:147], v[178:179], v[2:3] op_sel:[1,0,0]\n"
            "v_pk_fma_f32 v[4:5], v[150:151], v[180:181], v[4:5] op_sel:[1,0,0]\n"
            "v_pk_fma_f32 v[6:7], v[150:151], v[182:183], v[6:7] op_sel:[1,0,0]\n"
            "v_pk_fma_f32 v[8:9], v[154:155], v[184:185], v[8:9] op_sel:[1,0,0]\n"
            "v_pk_fma_f32 v[10:11], v[154:155], v[186:187], v[10:11] op_sel:[1,0,0]\n"
            "v_pk_fma_f32 v[12:13], v[158:159], v[188:189], v[12:13] op_sel:[1,0,0]\n"
            "v_pk_fma_f32 v[14:15], v[158:159], v[190:191], v[14:15] op_sel:[1,0,0]\n"
            "v_pk_mul_f32 v[168:169], v[168:169], v[136:137]\n"
            "v_pk_mul_f32 v[170:171], v[170:171], v[138:139]\n"
            "v_pk_mul_f32 v[172:173], v[172:173], v[140:141]\n"
            "v_pk_mul_f32 v[174:175], v[174:175], v[142:143]\n"
            "s_waitcnt vmcnt(8)\n"
            "v_mfma_f32_4x4x4_16b_bf16 v[176:179], v[208:209], v[40:41], 0 cbsz:2\n"
            "v_mfma_f32_4x4x4_16b_bf16 v[180:183], v[208:209], v[42:43], 0 cbsz:2\n"
            "v_mfma_f32_4x4x4_16b_bf16 v[184:187], v[208:209], v[44:45], 0 cbsz:2\n"
            "v_mfma_f32_4x4x4_16b_bf16 v[188:191], v[208:209], v[46:47], 0 cbsz:2\n"
            "ds_read_b128 v[136:139], v228 offset:19456\n"
            "ds_read_b128 v[140:143], v228 offset:20480\n"
            "ds_read_b128 v[144:147], v228 offset:21504\n"
            "ds_read_b128 v[148:151], v228 offset:22528\n"
            "ds_read_b128 v[152:155], v228 offset:23552\n"
            "ds_read_b128 v[156:159], v228 offset:24576\n"
            "s_nop 1\n"
            "v_pk_fma_f32 v[16:17], v[160:161], v[176:177], v[16:17] op_sel_hi:[0,1,1]\n"
            "v_pk_fma_f32 v[18:19], v[160:161], v[178:179], v[18:19] op_sel_hi:[0,1,1]\n"
            "v_pk_fma_f32 v[20:21], v[164:165], v[180:181], v[20:21] op_sel_hi:[0,1,1]\n"
            "v_pk_fma_f32 v[22:23], v[164:165], v[182:183], v[22:23] op_sel_hi:[0,1,1]\n"
            "v_pk_fma_f32 v[24:25], v[168:169], v[184:185], v[24:25] op_sel_hi:[0,1,1]\n"
            "v_pk_fma_f32 v[26:27], v[168:169], v[186:187], v[26:27] op_sel_hi:[0,1,1]\n"
            "v_pk_fma_f32 v[28:29], v[172:173], v[188:189], v[28:29] op_sel_hi:[0,1,1]\n"
            "v_pk_fma_f32 v[30:31], v[172:173], v[190:191], v[30:31] op_sel_hi:[0,1,1]\n"
            "s_cmp_le_u32 %[njl], 1\n"
            "s_cbranch_scc1 Ltail_%=\n"
            "v_mfma_f32_4x4x4_16b_bf16 v[176:179], v[208:209], v[56:57], 0 cbsz:2 abid:1\n"
            "v_mfma_f32_4x4x4_16b_bf16 v[180:183], v[208:209], v[58:59], 0 cbsz:2 abid:1\n"
            "v_mfma_f32_4x4x4_16b_bf16 v[184:187], v[208:209], v[60:61], 0 cbsz:2 abid:1\n"
            "v_mfma_f32_4x4x4_16b_bf16 v[188:191], v[208:209], v[62:63], 0 cbsz:2 abid:1\n"
            "s_nop 1\n"
            "v_pk_fma_f32 v[16:17], v[160:161], v[176:177], v[16:17] op_sel:[1,0,0]\n"
            "v_pk_fma_f32 v[18:19], v[160:161], v[178:179], v[18:19] op_sel:[1,0,0]\n"
            "v_pk_fma_f32 v[20:21], v[164:165], v[180:181], v[20:21] op_sel:[1,0,0]\n"
            "v_pk_fma_f32 v[22:23], v[164:165], v[182:183], v[22:23] op_sel:[1,0,0]\n"
            "v_pk_fma_f32 v[24:25], v[168:169], v[184:185], v[24:25] op_sel:[1,0,0]\n"
            "v_pk_fma_f32 v[26:27], v[168:169], v[186:187], v[26:27] op_sel:[1,0,0]\n"
            "v_pk_fma_f32 v[28:29], v[172:173], v[188:189], v[28:29] op_sel:[1,0,0]\n"
            "v_pk_fma_f32 v[30:31], v[172:173], v[190:191], v[30:31] op_sel:[1,0,0]\n"
            "s_cmp_le_u32 %[njl], 2\n"
            "s_cbranch_scc1 Ltail_%=\n"
            "s_waitcnt vmcnt(4)\n"
            "v_mfma_f32_4x4x4_16b_bf16 v[176:179], v[208:209], v[72:73], 0 cbsz:2 abid:2\n"
            "v_mfma_f32_4x4x4_16b_bf16 v[180:183], v[208:209], v[74:75], 0 cbsz:2 abid:2\n"
            "v_mfma_f32_4x4x4_16b_bf16 v[184:187], v[208:209], v[76:77], 0 cbsz:2 abid:2\n"
            "v_mfma_f32_4x4x4_16b_bf16 v[188:191], v[208:209], v[78:79], 0 cbsz:2 abid:2\n"
            "s_nop 1\n"
            "v_pk_fma_f32 v[16:17], v[162:163], v[176:177], v[16:17] op_sel_hi:[0,1,1]\n"
            "v_pk_fma_f32 v[18:19], v[162:163], v[178:179], v[18:19] op_sel_hi:[0,1,1]\n"
            "v_pk_fma_f32 v[20:21], v[166:167], v[180:181], v[20:21] op_sel_hi:[0,1,1]\n"
            "v_pk_fma_f32 v[22:23], v[166:167], v[182:183], v[22:23] op_sel_hi:[0,1,1]\n"
            "v_pk_fma_f32 v[24:25], v[170:171], v[184:185], v[24:25] op_sel_hi:[0,1,1]\n"
            "v_pk_fma_f32 v[26:27], v[170:171], v[186:187], v[26:27] op_sel_hi:[0,1,1]\n"
            "v_pk_fma_f32 v[28:29], v[174:175], v[188:189], v[28:29] op_sel_hi:[0,1,1]\n"
            "v_pk_fma_f32 v[30:31], v[174:175], v[190:191], v[30:31] op_sel_hi:[0,1,1]\n"
            "s_cmp_le_u32 %[njl], 3\n"
            "s_cbranch_scc1 Ltail_%=\n"
            "v_mfma_f32_4x4x4_16b_bf16 v[176:179], v[208:209], v[88:89], 0 cbsz:2 abid:3\n"
            "v_mfma_f32_4x4x4_16b_bf16 v[180:183], v[208:209], v[90:91], 0 cbsz:2 abid:3\n"
            "v_mfma_f32_4x4x4_16b_bf16 v[184:187], v[208:209], v[92:93], 0 cbsz:2 abid:3\n"
            "v_mfma_f32_4x4x4_16b_bf16 v[188:191], v[208:209], v[94:95], 0 cbsz:2 abid:3\n"
            "s_nop 1\n"
            "v_pk_fma_f32 v[16:17], v[162:163], v[176:177], v[16:17] op_sel:[1,0,0]\n"
            "v_pk_fma_f32 v[18:19], v[162:163], v[178:179], v[18:19] op_sel:[1,0,0]\n"
            "v_pk_fma_f32 v[20:21], v[166:167], v[180:181], v[20:21] op_sel:[1,0,0]\n"
            "v_pk_fma_f32 v[22:23], v[166:167], v[182:183], v[22:23] op_sel:[1,0,0]\n"
            "v_pk_fma_f32 v[24:25], v[170:171], v[184:185], v[24:25] op_sel:[1,0,0]\n"
            "v_pk_fma_f32 v[26:27], v[170:171], v[186:187], v[26:27] op_sel:[1,0,0]\n"
            "v_pk_fma_f32 v[28:29], v[174:175], v[188:189], v[28:29] op_sel:[1,0,0]\n"
            "v_pk_fma_f32 v[30:31], v[174:175], v[190:191], v[30:31] op_sel:[1,0,0]\n"
            "Ltail_%=:\n"
            "s_setprio 3\n"
            "s_waitcnt vmcnt(4)\n"
            "ds_read_b128 v[72:75], v228 offset:25600\n"
            "ds_read_b128 v[76:79], v228 offset:26624\n"
            "ds_read_b128 v[88:91], v228 offset:27648\n"
            "ds_read_b128 v[92:95], v228 offset:28672\n"
            "ds_read_b128 v[160:163], v228 offset:29696\n"
            "ds_read_b128 v[164:167], v228 offset:30720\n"
            "ds_read_b128 v[168:171], v228 offset:31744\n"
            "s_cmp_lg_u32 %[nst], %[tag]\n"
            "s_cbranch_scc1 Lspin_%=\n"
            "Lgo_%=:\n"
            "buffer_load_dwordx4 v[40:43], v218, %[rsw], %[oagg] offen offset:0 sc1\n"
            "buffer_load_dwordx4 v[44:47], v218, %[rsw], %[oagg] offen offset:256 sc1\n"
            "buffer_load_dwordx4 v[56:59], v218, %[rsw], %[oagg] offen offset:512 sc1\n"
            "buffer_load_dwordx4 v[60:63], v218, %[rsw], %[oagg] offen offset:768 sc1\n"
            "s_waitcnt vmcnt(4)\n"
            "v_mfma_f32_4x4x4_16b_bf16 v[0:3], v[224:225], v[32:33], v[0:3]\n"
            "v_mfma_f32_4x4x4_16b_bf16 v[4:7], v[224:225], v[34:35], v[4:7]\n"
            "v_mfma_f32_4x4x4_16b_bf16 v[8:11], v[224:225], v[36:37], v[8:11]\n"
            "v_mfma_f32_4x4x4_16b_bf16 v[12:15], v[224:225], v[38:39], v[12:15]\n"
            "v_mfma_f32_4x4x4_16b_bf16 v[16:19], v[224:225], v[48:49], v[16:19]\n"
            "v_mfma_f32_4x4x4_16b_bf16 v[20:23], v[224:225], v[50:51], v[20:23]\n"
            "v_mfma_f32_4x4x4_16b_bf16 v[24:27], v[224:225], v[52:53], v[24:27]\n"
            "v_mfma_f32_4x4x4_16b_bf16 v[28:31], v[224:225], v[54:55], v[28:31]\n"
            "s_nop 1\n"
            "v_cvt_pk_bf16_f32 v176, v0, v4\n"
            "v_cvt_pk_bf16_f32 v177, v8, v12\n"
            "v_cvt_pk_bf16_f32 v178, v16, v20\n"
            "v_cvt_pk_bf16_f32 v179, v24, v28\n"
            "ds_write_b128 v226, v[176:179] offset:0\n"
            "v_cvt_pk_bf16_f32 v180, v1, v5\n"
            "v_cvt_pk_bf16_f32 v181, v9, v13\n"
            "v_cvt_pk_bf16_f32 v182, v17, v21\n"
            "v_cvt_pk_bf16_f32 v183, v25, v29\n"
            "ds_write_b128 v226, v[180:183] offset:272\n"
            "v_cvt_pk_bf16_f32 v184, v2, v6\n"
            "v_cvt_pk_bf16_f32 v185, v10, v14\n"
            "v_cvt_pk_bf16_f32 v186, v18, v22\n"
            "v_cvt_pk_bf16_f32 v187, v26, v30\n"
            "ds_write_b128 v226, v[184:187] offset:544\n"
            "v_cvt_pk_bf16_f32 v188, v3, v7\n"
            "v_cvt_pk_bf16_f32 v189, v11, v15\n"
            "v_cvt_pk_bf16_f32 v190, v19, v23\n"
            "v_cvt_pk_bf16_f32 v191, v27, v31\n"
            "ds_write_b128 v226, v[188:191] offset:816\n"
            "ds_read_b128 v[176:179], v227 offset:0\n"
            "ds_read_b128 v[180:183], v227 offset:64\n"
            "ds_read_b128 v[184:187], v227 offset:128\n"
            "ds_read_b128 v[188:191], v227 offset:192\n"
            "s_waitcnt lgkmcnt(3)\n"
            "v_mfma_f32_16x16x32_bf16 v[0:3], v[176:179], v[236:239], 0\n"
            "v_mfma_f32_16x16x32_bf16 v[4:7], v[176:179], v[96:99], 0\n"
            "s_waitcnt lgkmcnt(2)\n"
            "v_mfma_f32_16x16x32_bf16 v[0:3], v[180:183], v[240:243], v[0:3]\n"
            "v_mfma_f32_16x16x32_bf16 v[4:7], v[180:183], v[100:103], v[4:7]\n"
            "s_waitcnt lgkmcnt(1)\n"
            "v_mfma_f32_16x16x32_bf16 v[0:3], v[184:187], v[244:247], v[0:3]\n"
            "v_mfma_f32_16x16x32_bf16 v[4:7], v[184:187], v[104:107], v[4:7]\n"
            "s_waitcnt lgkmcnt(0)\n"
            "v_mfma_f32_16x16x32_bf16 v[0:3], v[188:191], v[248:251], v[0:3]\n"
            "v_mfma_f32_16x16x32_bf16 v[4:7], v[188:191], v[108:111], v[4:7]\n"
            "v_mfma_f32_16x16x32_bf16 v[8:11], v[176:179], v[112:115], 0\n"
            "v_mfma_f32_16x16x32_bf16 v[12:15], v[176:179], v[204:207], 0\n"
            "v_mfma_f32_16x16x32_bf16 v[8:11], v[180:183], v[116:119], v[8:11]\n"
            "v_mfma_f32_16x16x32_bf16 v[12:15], v[180:183], v[232:235], v[12:15]\n"
            "v_mfma_f32_16x16x32_bf16 v[8:11], v[184:187], v[196:199], v[8:11]\n"
            "v_mfma_f32_16x16x32_bf16 v[12:15], v[184:187], v[120:123], v[12:15]\n"
            "v_mfma_f32_16x16x32_bf16 v[8:11], v[188:191], v[200:203], v[8:11]\n"
            "v_mfma_f32_16x16x32_bf16 v[12:15], v[188:191], v[124:127], v[12:15]\n"
            "v_cvt_pk_bf16_f32 v0, v0, v1\n"
            "v_cvt_pk_bf16_f32 v1, v2, v3\n"
            "v_cvt_pk_bf16_f32 v2, v4, v5\n"
            "v_cvt_pk_bf16_f32 v3, v6, v7\n"
            "buffer_store_dwordx4 v[0:3], v217, %[rso], 0 offen offset:0 sc1\n"
            "v_mfma_f32_16x16x32_bf16 v[16:19], v[176:179], v[192:195], 0\n"
            "v_mfma_f32_16x16x32_bf16 v[20:23], v[176:179], v[140:143], 0\n"
            "v_mfma_f32_16x16x32_bf16 v[16:19], v[180:183], v[128:131], v[16:19]\n"
            "v_mfma_f32_16x16x32_bf16 v[20:23], v[180:183], v[144:147], v[20:23]\n"
            "v_mfma_f32_16x16x32_bf16 v[16:19], v[184:187], v[132:135], v[16:19]\n"
            "v_mfma_f32_16x16x32_bf16 v[20:23], v[184:187], v[148:151], v[20:23]\n"
            "v_mfma_f32_16x16x32_bf16 v[16:19], v[188:191], v[136:139], v[16:19]\n"
            "v_mfma_f32_16x16x32_bf16 v[20:23], v[188:191], v[152:155], v[20:23]\n"
            "v_cvt_pk_bf16_f32 v8, v8, v9\n"
            "v_cvt_pk_bf16_f32 v9, v10, v11\n"
            "v_cvt_pk_bf16_f32 v10, v12, v13\n"
            "v_cvt_pk_bf16_f32 v11, v14, v15\n"
            "buffer_store_dwordx4 v[8:11], v217, %[rso], 0 offen offset:256 sc1\n"
            "v_mfma_f32_16x16x32_bf16 v[24:27], v[176:179], v[156:159], 0\n"
            "v_mfma_f32_16x16x32_bf16 v[28:31], v[176:179], v[92:95], 0\n"
            "v_mfma_f32_16x16x32_bf16 v[24:27], v[180:183], v[72:75], v[24:27]\n"
            "v_mfma_f32_16x16x32_bf16 v[28:31], v[180:183], v[160:163], v[28:31]\n"
            "v_mfma_f32_16x16x32_bf16 v[24:27], v[184:187], v[76:79], v[24:27]\n"
            "v_mfma_f32_16x16x32_bf16 v[28:31], v[184:187], v[164:167], v[28:31]\n"
            "v_mfma_f32_16x16x32_bf16 v[24:27], v[188:191], v[88:91], v[24:27]\n"
            "v_mfma_f32_16x16x32_bf16 v[28:31], v[188:191], v[168:171], v[28:31]\n"
            "v_cvt_pk_bf16_f32 v16, v16, v17\n"
            "v_cvt_pk_bf16_f32 v17, v18, v19\n"
            "v_cvt_pk_bf16_f32 v18, v20, v21\n"
            "v_cvt_pk_bf16_f32 v19, v22, v23\n"
            "buffer_store_dwordx4 v[16:19], v217, %[rso], 0 offen offset:512 sc1\n"
            "s_nop 2\n"
            "v_cvt_pk_bf16_f32 v24, v24, v25\n"
            "v_cvt_pk_bf16_f32 v25, v26, v27\n"
            "v_cvt_pk_bf16_f32 v26, v28, v29\n"
            "v_cvt_pk_bf16_f32 v27, v30, v31\n"
            "buffer_store_dwordx4 v[24:27], v217, %[rso], 0 offen offset:768 sc1\n"
            "s_cmp_lt_u32 %[two], 6\n"
            "s_cbranch_scc1 Lend_%=\n"
            "ds_read_b128 v[64:67], v231 offset:0\n"
            "ds_read_b128 v[68:71], v231 offset:1024\n"
            "ds_read_b128 v[80:83], v231 offset:2048\n"
            "ds_read_b128 v[84:87], v231 offset:3072\n"
            "s_waitcnt vmcnt(4) lgkmcnt(0)\n"
            "v_mfma_f32_4x4x4_16b_bf16 v[0:3], v[224:225], v[64:65], 0\n"
            "v_mfma_f32_4x4x4_16b_bf16 v[4:7], v[224:225], v[66:67], 0\n"
            "v_mfma_f32_4x4x4_16b_bf16 v[8:11], v[224:225], v[68:69], 0\n"
            "v_mfma_f32_4x4x4_16b_bf16 v[12:15], v[224:225], v[70:71], 0\n"
            "v_mfma_f32_4x4x4_16b_bf16 v[16:19], v[224:225], v[80:81], 0\n"
            "v_mfma_f32_4x4x4_16b_bf16 v[20:23], v[224:225], v[82:83], 0\n"
            "v_mfma_f32_4x4x4_16b_bf16 v[24:27], v[224:225], v[84:85], 0\n"
            "v_mfma_f32_4x4x4_16b_bf16 v[28:31], v[224:225], v[86:87], 0\n"
            "v_mfma_f32_4x4x4_16b_bf16 v[0:3], v[224:225], v[40:41], v[0:3]\n"
            "v_mfma_f32_4x4x4_16b_bf16 v[4:7], v[224:225], v[42:43], v[4:7]\n"
            "v_mfma_f32_4x4x4_16b_bf16 v[8:11], v[224:225], v[44:45], v[8:11]\n"
            "v_mfma_f32_4x4x4_16b_bf16 v[12:15], v[224:225], v[46:47], v[12:15]\n"
            "v_mfma_f32_4x4x4_16b_bf16 v[16:19], v[224:225], v[56:57], v[16:19]\n"
            "v_mfma_f32_4x4x4_16b_bf16 v[20:23], v[224:225], v[58:59], v[20:23]\n"
            "v_mfma_f32_4x4x4_16b_bf16 v[24:27], v[224:225], v[60:61], v[24:27]\n"
            "v_mfma_f32_4x4x4_16b_bf16 v[28:31], v[224:225], v[62:63], v[28:31]\n"
            "s_nop 1\n"
            "v_cvt_pk_bf16_f32 v176, v0, v4\n"
            "v_cvt_pk_bf16_f32 v177, v8, v12\n"
            "v_cvt_pk_bf16_f32 v178, v16, v20\n"
            "v_cvt_pk_bf16_f32 v179, v24, v28\n"
            "ds_write_b128 v226, v[176:179] offset:0\n"
            "v_cvt_pk_bf16_f32 v180, v1, v5\n"
            "v_cvt_pk_bf16_f32 v181, v9, v13\n"
            "v_cvt_pk_bf16_f32 v182, v17, v21\n"
            "v_cvt_pk_bf16_f32 v183, v25, v29\n"
            "ds_write_b128 v226, v[180:183] offset:272\n"
            "v_cvt_pk_bf16_f32 v184, v2, v6\n"
            "v_cvt_pk_bf16_f32 v185, v10, v14\n"
            "v_cvt_pk_bf16_f32 v186, v18, v22\n"
            "v_cvt_pk_bf16_f32 v187, v26, v30\n"
            "ds_write_b128 v226, v[184:187] offset:544\n"
            "v_cvt_pk_bf16_f32 v188, v3, v7\n"
            "v_cvt_pk_bf16_f32 v189, v11, v15\n"
            "v_cvt_pk_bf16_f32 v190, v19, v23\n"
            "v_cvt_pk_bf16_f32 v191, v27, v31\n"
            "ds_write_b128 v226, v[188:191] offset:816\n"
            "ds_read_b128 v[176:179], v227 offset:0\n"
            "ds_read_b128 v[180:183], v227 offset:64\n"
            "ds_read_b128 v[184:187], v227 offset:128\n"
            "ds_read_b128 v[188:191], v227 offset:192\n"
            "s_waitcnt lgkmcnt(3)\n"
            "v_mfma_f32_16x16x32_bf16 v[0:3], v[176:179], v[236:239], 0\n"
            "v_mfma_f32_16x16x32_bf16 v[4:7], v[176:179], v[96:99], 0\n"
            "s_waitcnt lgkmcnt(2)\n"
            "v_mfma_f32_16x16x32_bf16 v[0:3], v[180:183], v[240:243], v[0:3]\n"
            "v_mfma_f32_16x16x32_bf16 v[4:7], v[180:183], v[100:103], v[4:7]\n"
            "s_waitcnt lgkmcnt(1)\n"
            "v_mfma_f32_16x16x32_bf16 v[0:3], v[184:187], v[244:247], v[0:3]\n"
            "v_mfma_f32_16x16x32_bf16 v[4:7], v[184:187], v[104:107], v[4:7]\n"
            "s_waitcnt lgkmcnt(0)\n"
            "v_mfma_f32_16x16x32_bf16 v[0:3], v[188:191], v[248:251], v[0:3]\n"
            "v_mfma_f32_16x16x32_bf16 v[4:7], v[188:191], v[108:111], v[4:7]\n"
            "v_mfma_f32_16x16x32_bf16 v[8:11], v[176:179], v[112:115], 0\n"
            "v_mfma_f32_16x16x32_bf16 v[12:15], v[176:179], v[204:207], 0\n"
            "v_mfma_f32_16x16x32_bf16 v[8:11], v[180:183], v[116:119], v[8:11]\n"
            "v_mfma_f32_16x16x32_bf16 v[12:15], v[180:183], v[232:235], v[12:15]\n"
            "v_mfma_f32_16x16x32_bf16 v[8:11], v[184:187], v[196:199], v[8:11]\n"
            "v_mfma_f32_16x16x32_bf16 v[12:15], v[184:187], v[120:123], v[12:15]\n"
            "v_mfma_f32_16x16x32_bf16 v[8:11], v[188:191], v[200:203], v[8:11]\n"
            "v_mfma_f32_16x16x32_bf16 v[12:15], v[188:191], v[124:127], v[12:15]\n"
            "v_cvt_pk_bf16_f32 v0, v0, v1\n"
            "v_cvt_pk_bf16_f32 v1, v2, v3\n"
            "v_cvt_pk_bf16_f32 v2, v4, v5\n"
            "v_cvt_pk_bf16_f32 v3, v6, v7\n"
            "buffer_store_dwordx4 v[0:3], v218, %[rso], 0 offen offset:0 sc1\n"
            "v_mfma_f32_16x16x32_bf16 v[16:19], v[176:179], v[192:195], 0\n"
            "v_mfma_f32_16x16x32_bf16 v[20:23], v[176:179], v[140:143], 0\n"
            "v_mfma_f32_16x16x32_bf16 v[16:19], v[180:183], v[128:131], v[16:19]\n"
            "v_mfma_f32_16x16x32_bf16 v[20:23], v[180:183], v[144:147], v[20:23]\n"
            "v_mfma_f32_16x16x32_bf16 v[16:19], v[184:187], v[132:135], v[16:19]\n"
            "v_mfma_f32_16x16x32_bf16 v[20:23], v[184:187], v[148:151], v[20:23]\n"
            "v_mfma_f32_16x16x32_bf16 v[16:19], v[188:191], v[136:139], v[16:19]\n"
            "v_mfma_f32_16x16x32_bf16 v[20:23], v[188:191], v[152:155], v[20:23]\n"
            "v_cvt_pk_bf16_f32 v8, v8, v9\n"
            "v_cvt_pk_bf16_f32 v9, v10, v11\n"
            "v_cvt_pk_bf16_f32 v10, v12, v13\n"
            "v_cvt_pk_bf16_f32 v11, v14, v15\n"
            "buffer_store_dwordx4 v[8:11], v218, %[rso], 0 offen offset:256 sc1\n"
            "v_mfma_f32_16x16x32_bf16 v[24:27], v[176:179], v[156:159], 0\n"
            "v_mfma_f32_16x16x32_bf16 v[28:31], v[176:179], v[92:95], 0\n"
            "v_mfma_f32_16x16x32_bf16 v[24:27], v[180:183], v[72:75], v[24:27]\n"
            "v_mfma_f32_16x16x32_bf16 v[28:31], v[180:183], v[160:163], v[28:31]\n"
            "v_mfma_f32_16x16x32_bf16 v[24:27], v[184:187], v[76:79], v[24:27]\n"
            "v_mfma_f32_16x16x32_bf16 v[28:31], v[184:187], v[164:167], v[28:31]\n"
            "v_mfma_f32_16x16x32_bf16 v[24:27], v[188:191], v[88:91], v[24:27]\n"
            "v_mfma_f32_16x16x32_bf16 v[28:31], v[188:191], v[168:171], v[28:31]\n"
            "v_cvt_pk_bf16_f32 v16, v16, v17\n"
            "v_cvt_pk_bf16_f32 v17, v18, v19\n"
            "v_cvt_pk_bf16_f32 v18, v20, v21\n"
            "v_cvt_pk_bf16_f32 v19, v22, v23\n"
            "buffer_store_dwordx4 v[16:19], v218, %[rso], 0 offen offset:512 sc1\n"
            "s_nop 2\n"
            "v_cvt_pk_bf16_f32 v24, v24, v25\n"
            "v_cvt_pk_bf16_f32 v25, v26, v27\n"
            "v_cvt_pk_bf16_f32 v26, v28, v29\n"
            "v_cvt_pk_bf16_f32 v27, v30, v31\n"
            "buffer_store_dwordx4 v[24:27], v218, %[rso], 0 offen offset:768 sc1\n"
            "Lend_%=:\n"
            "s_waitcnt vmcnt(4)\n"
            : "+{v[32:63]}"(oxa), "+{v[64:95]}"(oxb), "+{v[96:127]}"(owr), "+{v[192:195]}"(efA), "+{v[196:199]}"(S), "+{v[200:203]}"(cgA), "+{v[208:223]}"(omi), "+{v[224:231]}"(ot),
              "+{v[232:235]}"(efN), "+{v[236:239]}"(jmp),
              [rem] "+s"(rem), [lens] "+s"(lens), [fm] "+s"(fm), [nst] "+s"(nst), [pb] "+s"(pb), [evt] "+s"(evt), [ra] "=&s"(ra), [tmp] "=&s"(tmp)
            : [rse] "s"(de), [rsx] "s"(dx), [rso] "s"(mkdesc(out, (unsigned)N * 1024u)), [rsw] "s"(mkdesc(ws, W_END)),
              [cnt] "s"(lds0 + L_MISC + 4), [pfa] "s"(lds0 + L_MISC + 16 + pw * 4), [tag] "s"(tag), [flg] "s"(ws + W_FLG + bx * 128), [pfl] "s"(ws + W_FLG + (bx ^ 4) * 128),
              [njl] "s"(((e2 - 1) & 3) + 1), [c1] "s"((w < 4 ? l2 : l1) - 4 * ((e2 + 3) >> 2) - 5), [c2] "s"(w < 4 ? l1 : l2), [two] "s"(w), [ptab] "s"((unsigned)__builtin_amdgcn_readfirstlane(w < 4 ? PTO : (w < 6 ? PTY : PTZ))), [oagg] "s"(W_AGG), [spin] "s"(SPIN_MAX)
            : "memory", "vcc", "scc", "v0", "v1", "v2", "v3", "v4", "v5", "v6", "v7", "v8", "v9", "v10", "v11", "v12", "v13", "v14", "v15", "v16", "v17", "v18", "v19", "v20", "v21", "v22", "v23", "v24", "v25", "v26", "v27", "v28", "v29", "v30", "v31", "v128", "v129", "v130", "v131", "v132", "v133", "v134", "v135", "v136", "v137", "v138", "v139", "v140", "v141", "v142", "v143", "v144", "v145", "v146", "v147", "v148", "v149", "v150", "v151", "v152", "v153", "v154", "v155", "v156", "v157", "v158", "v159", "v160", "v161", "v162", "v163", "v164", "v165", "v166", "v167", "v168", "v169", "v170", "v171", "v172", "v173", "v174", "v175", "v176", "v177", "v178", "v179", "v180", "v181", "v182", "v183", "v184", "v185", "v186", "v187", "v188", "v189", "v190", "v191", "v204", "v205", "v206", "v207", "v240", "v241", "v242", "v243", "v244", "v245", "v246", "v247", "v248", "v249", "v250", "v251");
    } else {
        if (bx == 0 && w == 4) flag_store(ws + W_CTL, tag);
        lds_wait(lds0 + SY_C3, 5);
        const __amdgpu_buffer_rsrc_t rso = __builtin_amdgcn_make_buffer_rsrc((void*)out, 0, N * 1024, 0x00020000);
#pragma unroll 1
        for (int it = 0; it < 10; ++it) {
            const int nl = (it < 5 ? 40 - 40 * half + 8 * it : 40 * half + 8 * (it - 5)) + w, node = node0 + nl;
            const bool nv = nl < npb;
            const unsigned ro = nv ? (unsigned)node * 1024u + (unsigned)lane * 16u : OOB_OFF;
            float a8[8] = {0.f, 0.f, 0.f, 0.f, 0.f, 0.f, 0.f, 0.f};
#pragma unroll 1
            for (int e0 = 0; e0 < (nv ? E : 0); e0 += 64) {
                const int ec = min(e0 + lane, E - 1);
                u64 mm = __ballot(e0 + lane < E && dst[ec] == node && (int)(((src[ec] >> 3) ^ (smask >> ((src[ec] >> 9) & 31))) & 1) == half);
#pragma unroll 1
                while (mm) {
                    const int e = e0 + __builtin_ctzll(mm);
                    mm &= mm - 1;
                    const unsigned short* fe = (const unsigned short*)ef + (size_t)e * DE_DIM;
                    const unsigned short* ye = (const unsigned short*)Y + (size_t)e * M_DIM;
                    const unsigned short* xp = x + (size_t)src[e] * (C_DIM * M_DIM) + lane * 8;
                    float z0 = 0.f, z1 = 0.f;
#pragma unroll 1
                    for (int f = 0; f < DE_DIM; ++f) { z0 += bf2f(fe[f]) * bf2f(w_rad[f * C_DIM + 2 * lane]); z1 += bf2f(fe[f]) * bf2f(w_rad[f * C_DIM + 2 * lane + 1]); }
                    const float r0 = z0 / (1.f + __expf(-z0)), r1 = z1 / (1.f + __expf(-z1));
#pragma unroll 1
                    for (int i = 0; i < 4; ++i) {
                        const float x0 = bf2f(xp[i]) * r0, x1 = bf2f(xp[4 + i]) * r1;
#pragma unroll
                        for (int o = 0; o < 4; ++o) {
                            float wv4 = 0.f;
#pragma unroll
                            for (int f = 0; f < 4; ++f) wv4 += bf2f(cg[(i * 4 + f) * 4 + o]) * bf2f(ye[f]);
                            a8[o] += x0 * wv4; a8[4 + o] += x1 * wv4;
                        }
                    }
                }
            }
            if (it >= 5) {
                const u32x4 pv = __builtin_amdgcn_raw_buffer_load_b128(rsw, ro, W_AGG, SC1);
#pragma unroll
                for (int i = 0; i < 4; ++i) { a8[2 * i] += __uint_as_float(pv[i] << 16); a8[2 * i + 1] += __uint_as_float(pv[i] & 0xffff0000u); }
            }
            const u32x4 rv = (u32x4){pack2bf(a8[0], a8[1]), pack2bf(a8[2], a8[3]), pack2bf(a8[4], a8[5]), pack2bf(a8[6], a8[7])};
            if (it < 5) {
                __builtin_amdgcn_raw_buffer_store_b128(rv, rsw, ro, W_AGG, SC1);
                if (it == 4) {
                    asm volatile("s_waitcnt vmcnt(0)" ::: "memory");
                    if (lds_arrive_rtn(lds0 + L_MISC + 4) == 7u) flag_store(ws + W_FLG + bx * 128, tag);
                    poll_tag(ws + W_FLG + (bx ^ 4) * 128, tag);
                }
            } else {
                *(u32x4*)(smem + L_PRV + w * 1024 + lane * 16) = rv;
#pragma unroll
                for (int i = 0; i < 8; ++i) a8[i] = 0.f;
#pragma unroll 1
                for (int c = 0; c < C_DIM; ++c) {
                    const unsigned short* ap = (const unsigned short*)(smem + L_PRV + w * 1024 + c * 8);
                    const float w0 = bf2f(w_mix[c * C_DIM + 2 * lane]), w1 = bf2f(w_mix[c * C_DIM + 2 * lane + 1]);
#pragma unroll
                    for (int m = 0; m < 4; ++m) { a8[m] += bf2f(ap[m]) * w0; a8[4 + m] += bf2f(ap[m]) * w1; }
                }
                __builtin_amdgcn_raw_buffer_store_b128((u32x4){pack2bf(a8[0], a8[1]), pack2bf(a8[2], a8[3]), pack2bf(a8[4], a8[5]), pack2bf(a8[6], a8[7])}, rso, ro, 0, 0);
            }
        }
    }
}

__global__ void kf_zero(float* __restrict__ agg, size_t n) {
    for (size_t i = (size_t)blockIdx.x * blockDim.x + threadIdx.x; i < n; i += (size_t)gridDim.x * blockDim.x) agg[i] = 0.f;
}
__global__ void kf_edges(const unsigned short* __restrict__ x, const int* __restrict__ src, const int* __restrict__ dst,
                         const unsigned short* __restrict__ Y, const unsigned short* __restrict__ ef,
                         const unsigned short* __restrict__ w_rad, const unsigned short* __restrict__ cg,
                         float* __restrict__ agg, int E) {
    const int ch = threadIdx.x;
    for (int e = blockIdx.x; e < E; e += gridDim.x) {
        float z = 0.f;
        for (int f = 0; f < DE_DIM; ++f) z += bf2f(ef[(size_t)e * DE_DIM + f]) * bf2f(w_rad[f * C_DIM + ch]);
        const float r = z / (1.f + __expf(-z));
        float a4[4] = {0.f, 0.f, 0.f, 0.f};
        for (int ii = 0; ii < 4; ++ii) {
            const float xv = bf2f(x[((size_t)src[e] * C_DIM + ch) * M_DIM + ii]) * r;
            for (int o = 0; o < 4; ++o) {
                float wv = 0.f;
                for (int f = 0; f < 4; ++f) wv += bf2f(cg[(ii * 4 + f) * 4 + o]) * bf2f(Y[(size_t)e * 4 + f]);
                a4[o] += xv * wv;
            }
        }
        for (int o = 0; o < 4; ++o) atomicAdd(&agg[((size_t)dst[e] * C_DIM + ch) * M_DIM + o], a4[o]);
    }
}
__global__ void kf_mix(const float* __restrict__ agg, const unsigned short* __restrict__ w_mix, unsigned short* __restrict__ out, int N) {
    const int d = threadIdx.x;
    for (int n = blockIdx.x; n < N; n += gridDim.x) {
        float a4[4] = {0.f, 0.f, 0.f, 0.f};
        for (int c = 0; c < C_DIM; ++c) {
            const float wv = bf2f(w_mix[c * C_DIM + d]);
            for (int m = 0; m < 4; ++m) a4[m] += bf2f(f2bf(agg[((size_t)n * C_DIM + c) * M_DIM + m])) * wv;
        }
        for (int m = 0; m < 4; ++m) out[((size_t)n * C_DIM + d) * M_DIM + m] = f2bf(a4[m]);
    }
}

extern "C" void kernel_launch(void* const* d_in, const int* in_sizes, int n_in,
                              void* d_out, int out_size, void* d_ws, size_t ws_size,
                              hipStream_t stream) {
    const unsigned short* x = (const unsigned short*)d_in[0];
    const int* src = (const int*)d_in[1];
    const int* dst = (const int*)d_in[2];
    const unsigned short* Y = (const unsigned short*)d_in[3];
    const unsigned short* ef = (const unsigned short*)d_in[4];
    const unsigned short* w_rad = (const unsigned short*)d_in[5];
    const unsigned short* cg = (const unsigned short*)d_in[6];
    const unsigned short* w_mix = (const unsigned short*)d_in[7];
    unsigned short* out = (unsigned short*)d_out;
    const int N = in_sizes[0] / (C_DIM * M_DIM);
    const int E = in_sizes[1];
    const int epb = (E + 254) / 255;
    const bool fast = N >= 256 && N <= NB_MAX * NPB_MAX && E > 0 && epb <= EPB_MAX && ws_size >= (size_t)W_END;
    if (!fast) {
        float* agg = (float*)d_ws;
        if (N <= 0) return;
        kf_zero<<<1024, 256, 0, stream>>>(agg, (size_t)N * C_DIM * M_DIM);
        if (E > 0) kf_edges<<<4096, C_DIM, 0, stream>>>(x, src, dst, Y, ef, w_rad, cg, agg, E);
        kf_mix<<<2048, C_DIM, 0, stream>>>(agg, w_mix, out, N);
        return;
    }
    const unsigned nb_magic = (unsigned)(((1ull << 39) + N - 1) / N);
    unsigned smask = 0;
    const int ng = (N + 511) / 512;
    int pw = 1;
    while (2 * pw * 4 < ng + 4) pw *= 2;
    for (int i = 0; i < ng; ++i) smask |= (unsigned)(i < 4 * pw ? ((i >> 2) ^ (i >= WLO * ng / 20 && i < WHI * ng / 20)) & 1 : i & 1) << i;
    const int r0 = ((N < 2048 * pw ? N : 2048 * pw) - 128) & ~63, r1 = r0 - (r0 >= 2048 ? 2048 : 128);
    k_fused<<<256, 512, 0, stream>>>(x, src, dst, (const u32x2*)Y, (const u32x4*)ef, w_rad, cg, w_mix, out, (unsigned char*)d_ws,
                                     N, E, epb, nb_magic, smask, (unsigned)(4 * 65536 / N), (unsigned)(r0 + (r0 >> 8 & 8)) * 1024u, (unsigned)(r1 + (r1 >> 8 & 8)) * 1024u);
}
```

```cpp
#include <hip/hip_runtime.h>
#include <hip/hip_bf16.h>
#include <stdint.h>

typedef __attribute__((ext_vector_type(8))) short bf16x8;
typedef __attribute__((ext_vector_type(4))) short bf16x4;
typedef __attribute__((ext_vector_type(4))) float f32x4;
typedef __attribute__((ext_vector_type(2))) unsigned u32x2;
typedef __attribute__((ext_vector_type(4))) unsigned u32x4;
typedef __attribute__((ext_vector_type(8))) unsigned u32x8;
typedef __attribute__((ext_vector_type(16))) unsigned u32x16;
typedef __attribute__((ext_vector_type(32))) unsigned u32x32;
typedef unsigned long long u64;

#define C_DIM 128
#define M_DIM 4
#define DE_DIM 16
#define NPB_MAX 80
#define NB_MAX 128
#define EPB_MAX 1280
#define NRND 7
#define LS 47
#define DEG_MAX 42
#define OOB_OFF 0x80000000u
#define SC1 16
#define NT 2
#define SPIN_MAX (1u << 22)
#define CODE_LINES 152
#define PTO 0xfffa9540u
#define PTY 0xffffa950u
#define PTZ 0xffffea54u
#define NT0 3
#define NT2 4
#define TLIST 4, 5, 10, 12
#define WLO 8
#define WHI 11
#define TNEAR 0
#define HPRIO 3
#define PAYR 3
#define PBIAS 12
#define PRW "10"
#define PSL "1"
#define TKF 0x98765432u
#define TKB 0x23456789u
#define TKA 0x1010ffffu
#define EXTRA 0, 0, 5, 7

#define W_CTL 0
#define W_DEC 2048
#define W_FLG 4096
#define W_TAB (W_FLG + 32768)
#define W_RA (W_TAB + 256 * 256 * 8)
#define W_AGG (W_RA + 256 * EPB_MAX * 16)
#define W_END (W_AGG + NB_MAX * NPB_MAX * 1024)

#define SB() __builtin_amdgcn_sched_barrier(0)

static __device__ __forceinline__ float bf2f(unsigned short v) { return __uint_as_float(((unsigned)v) << 16); }
typedef __bf16 bf16v2 __attribute__((ext_vector_type(2)));
static __device__ __forceinline__ unsigned pack2bf(float lo, float hi) {
    bf16v2 r = (bf16v2){(__bf16)lo, (__bf16)hi};
    return __builtin_bit_cast(unsigned, r);
}
static __device__ __forceinline__ unsigned short f2bf(float v) { return (unsigned short)(pack2bf(v, 0.f) & 0xffffu); }
template <int CTRL, int RM> static __device__ __forceinline__ int dpp_add(int v) {
    return v + __builtin_amdgcn_update_dpp(0, v, CTRL, RM, 0xf, false);
}
static __device__ __forceinline__ int wave_incl_scan(int v) {
    v = dpp_add<0x111, 0xf>(v); v = dpp_add<0x112, 0xf>(v); v = dpp_add<0x114, 0xf>(v); v = dpp_add<0x118, 0xf>(v);
    v = dpp_add<0x142, 0xa>(v); v = dpp_add<0x143, 0xc>(v);
    return v;
}
template <int CTRL, int RM> static __device__ __forceinline__ unsigned dpp_max(unsigned v) {
    return max(v, (unsigned)__builtin_amdgcn_update_dpp(0, (int)v, CTRL, RM, 0xf, false));
}
static __device__ __forceinline__ unsigned wave_max_scan(unsigned v) {
    v = dpp_max<0x111, 0xf>(v); v = dpp_max<0x112, 0xf>(v); v = dpp_max<0x114, 0xf>(v); v = dpp_max<0x118, 0xf>(v);
    v = dpp_max<0x142, 0xa>(v); v = dpp_max<0x143, 0xc>(v);
    return v;
}
#define MAXS(c) "v_max_u32_dpp %0, %0, %0 " c "\n\tv_max_u32_dpp %1, %1, %1 " c "\n\tv_max_u32_dpp %2, %2, %2 " c "\n\t"
static __device__ __forceinline__ void wave_max_scan3(unsigned& a, unsigned& b, unsigned& c) {
    asm volatile("s_nop 1\n\t" MAXS("row_shr:1 row_mask:0xf bank_mask:0xf bound_ctrl:1") MAXS("row_shr:2 row_mask:0xf bank_mask:0xf bound_ctrl:1")
                 MAXS("row_shr:4 row_mask:0xf bank_mask:0xf bound_ctrl:1") MAXS("row_shr:8 row_mask:0xf bank_mask:0xf bound_ctrl:1")
                 MAXS("row_bcast:15 row_mask:0xa bank_mask:0xf") MAXS("row_bcast:31 row_mask:0xc bank_mask:0xf") "s_nop 0" : "+v"(a), "+v"(b), "+v"(c));
}
static __device__ __forceinline__ unsigned bsum(unsigned c, unsigned m, unsigned acc = 0u) { return __builtin_amdgcn_udot4(c, m, acc, false); }
static __device__ __forceinline__ unsigned bytesum2(unsigned c0, unsigned c1, unsigned m0, unsigned m1, unsigned acc = 0u) {
    return __builtin_amdgcn_udot4(c1, m1, __builtin_amdgcn_udot4(c0, m0, acc, false), false);
}
static __device__ __forceinline__ unsigned below_mask(int w, int k) {
    const int n = w - 4 * k;
    return n >= 4 ? 0x01010101u : (n <= 0 ? 0u : (0x01010101u & ((1u << (8 * n)) - 1u)));
}
static __device__ __forceinline__ void flag_store(const void* p, unsigned v) {
    unsigned t0, t1; u64 sv;
    asm volatile("v_mov_b32 %0, 0\n\tv_mov_b32 %1, %4\n\ts_mov_b64 %2, exec\n\ts_mov_b64 exec, 1\n\t"
                 "global_store_dword %0, %1, %3 sc1\n\ts_mov_b64 exec, %2" : "=&v"(t0), "=&v"(t1), "=&s"(sv) : "s"(p), "s"(v) : "memory");
}
static __device__ __forceinline__ unsigned lds_arrive_rtn(unsigned a) {
    unsigned t0, t1; u64 sv;
    asm volatile("v_mov_b32 %0, %3\n\tv_mov_b32 %1, 1\n\ts_mov_b64 %2, exec\n\ts_mov_b64 exec, 1\n\t"
                 "ds_add_rtn_u32 %1, %0, %1\n\ts_mov_b64 exec, %2\n\ts_waitcnt lgkmcnt(0)" : "=&v"(t0), "=&v"(t1), "=&s"(sv) : "s"(a) : "memory");
    return (unsigned)__builtin_amdgcn_readfirstlane((int)t1);
}
static __device__ __forceinline__ void lds_arrive(unsigned a, unsigned inc = 1u) {
    unsigned t0, t1; u64 sv;
    asm volatile("" "v_mov_b32 %0, %3\n\tv_mov_b32 %1, %4\n\ts_mov_b64 %2, exec\n\ts_mov_b64 exec, 1\n\t"
                 "ds_add_u32 %0, %1\n\ts_mov_b64 exec, %2" : "=&v"(t0), "=&v"(t1), "=&s"(sv) : "s"(a), "s"(inc) : "memory");
}
template <int SLEEP = 0, unsigned MASK = ~0u> static __device__ __forceinline__ unsigned lds_wait(unsigned a, unsigned n) {
    for (;;) {
        unsigned v;
        asm volatile("ds_read_b32 %0, %1\n\ts_waitcnt lgkmcnt(0)" : "=v"(v) : "v"(a) : "memory");
        v = (unsigned)__builtin_amdgcn_readfirstlane((int)v);
        if ((v & MASK) >= n) return v;
        if (SLEEP) __builtin_amdgcn_s_sleep(SLEEP);
    }
}
static __device__ __forceinline__ unsigned ld_agent(const void* p) { return __hip_atomic_load((const unsigned*)p, __ATOMIC_RELAXED, __HIP_MEMORY_SCOPE_AGENT); }
static __device__ __forceinline__ void poll_tag(const void* p, unsigned tag) {
#pragma unroll 1
    for (unsigned spin = 0; spin < SPIN_MAX; ++spin) {
        if (__all(ld_agent(p) == tag)) break;
        __builtin_amdgcn_s_sleep(2);
    }
    __builtin_amdgcn_fence(__ATOMIC_ACQUIRE, "wavefront");
}
static __device__ __forceinline__ void wg_barrier() {
    asm volatile("s_waitcnt lgkmcnt(0)" ::: "memory");
    __builtin_amdgcn_s_barrier();
    asm volatile("" ::: "memory");
}
static __device__ __forceinline__ f32x4 mfma4(u32x2 a, u32x2 b, f32x4 c) {
    return __builtin_amdgcn_mfma_f32_4x4x4bf16_1k(__builtin_bit_cast(bf16x4, a), __builtin_bit_cast(bf16x4, b), c, 0, 0, 0);
}
static __device__ __forceinline__ f32x4 mfma16(u32x4 a, u32x4 b, f32x4 c) {
    return __builtin_amdgcn_mfma_f32_16x16x32_bf16(__builtin_bit_cast(bf16x8, a), __builtin_bit_cast(bf16x8, b), c, 0, 0, 0);
}

static constexpr unsigned evt_of(unsigned pt) {
    unsigned e = 6u;
    for (int n = 2; n < 15; ++n) if (((pt >> (2 * n)) & 3u) != ((pt >> (2 * n + 2)) & 3u)) e |= 1u << n;
    return e;
}

static constexpr u64 len_pos(bool kept) {
    u64 r = 0;
    for (int w = 0; w < 8; ++w) {
        r |= (u64)(kept ? 8 + w : w) << (4 * (((kept ? TKF : TKB) >> (4 * w)) & 15u));
        if (w >= 4 && (w >= 6) == kept) r |= (u64)(kept ? w : 4 + w) << (4 * ((TKA >> (4 * w)) & 15u));
    }
    return r;
}
static constexpr unsigned ring_const() {
    constexpr int e[4] = {EXTRA};
    unsigned r = 0;
    for (int i = 0; i < 4; ++i) r |= (unsigned)((4 - PBIAS + (-3 * e[i] - e[(i + 3) & 3] + e[(i + 2) & 3] + 3 * e[(i + 1) & 3]) / 2) & 255) << (8 * i);
    return r;
}

#define L_MISC 0
#define L_KEY 96
#define L_AT 416
#define L_LEN 1056
#define L_PCNT 1280
#define L_CCNT (L_PCNT + 2048)
#define L_BNC (L_CCNT + 1280)
#define L_CG (L_BNC + 4096)
#define L_PRV (L_CG + 1024)
#define A_STG L_PRV
#define A_PTAB (A_STG + EPB_MAX * 16)
#define L_PK (L_PRV + 15 * 4096)
#define L_DUM L_PK
#define L_CTAB (L_PK + 2048)
#define L_MRK (L_PK + 8192)
#define TR_STR 4352
#define L_TR (L_PK + 16384)
#define L_WR L_TR
#define L_IMG (L_TR + 8 * TR_STR)
#define L_TOTAL (L_IMG + 32768)
static_assert(L_TOTAL <= 163840 && L_BNC <= 8192 && NPB_MAX * LS * 16 <= 15 * 4096, "LDS budget");
static_assert(L_PRV % 16 == 0 && L_PK % 16 == 0 && L_IMG % 16 == 0 && L_CG % 16 == 0 && L_CCNT % 16 == 0, "alignment");
enum { SY_CPY = 40, SY_C1 = 44, SY_S = 48, SY_C3 = 56, SY_RI = 60, SY_PRE = 64 };

static __device__ __forceinline__ u32x4 mkdesc(const void* p, unsigned bytes) {
    return (u32x4){(unsigned)(uintptr_t)p, (unsigned)((uintptr_t)p >> 32) & 0xffffu, bytes, 0x00020000u};
}

__global__ __launch_bounds__(512, 2) void k_fused(
    const unsigned short* __restrict__ x, const int* __restrict__ src, const int* __restrict__ dst, const u32x2* __restrict__ Y,
    const u32x4* __restrict__ ef, const unsigned short* __restrict__ w_rad, const unsigned short* __restrict__ cg,
    const unsigned short* __restrict__ w_mix, unsigned short* __restrict__ out, unsigned char* ws,
    int N, int E, int epb, unsigned nb_magic, unsigned smask, unsigned cmul, unsigned po0, unsigned po1)
{
    __shared__ __attribute__((aligned(256))) unsigned char smem[L_TOTAL];
    const int tid = threadIdx.x, lane = tid & 63;
    const int w = __builtin_amdgcn_readfirstlane(tid >> 6);
    const int bx = blockIdx.x;
    int half;
    const int b = (bx & 3) * 32 + (bx >> 3);
    const int g = lane >> 4, c16 = lane & 15;
    const int node0 = (b * N + 127) >> 7, npb = (((b + 1) * N + 127) >> 7) - node0;
    unsigned cp;
    {
        u64 pc;
        asm volatile("s_getpc_b64 %0" : "=s"(pc));
        const __amdgpu_buffer_rsrc_t rsc = __builtin_amdgcn_make_buffer_rsrc((void*)(pc & ~(u64)127), 0, CODE_LINES * 128, 0x00020000);
        cp = __builtin_amdgcn_raw_buffer_load_b32(rsc, (tid >> 3) == 32 ? ((bx >> 3) + lane * 32) * 128 : OOB_OFF, 0, 0);
        SB();
    }
    const unsigned lds0 = (unsigned)(uintptr_t)(__attribute__((address_space(3))) unsigned char*)smem;
    const __amdgpu_buffer_rsrc_t rsw = __builtin_amdgcn_make_buffer_rsrc((void*)ws, 0, W_END, 0x00020000);
    asm volatile("" :: "s"(x), "s"(src), "s"(dst), "s"(Y), "s"(ef), "s"(w_rad), "s"(cg), "s"(w_mix), "s"(out), "s"(ws), "s"(N), "s"(E), "s"(epb), "s"(nb_magic),
                 "s"(smask), "s"(cmul), "s"(po0), "s"(po1));
    const int base_e = bx * epb - epb;
    const int ne = base_e < 0 ? 0 : max(min(epb, E - base_e), 0);
    const int wd = 4 + ((w - 1 + (w >> 2)) & 3);
    auto task_at = [&](int t) -> unsigned {
        const unsigned q = (t == 0 ? TKF >> (4 * w) : (t == 1 ? TKB >> (4 * w) : TKA >> (4 * wd))) & 15u;
        return L_AT + ((t == 0 || (t == 2 && wd >= 6) ? 40 * half : 40 - 40 * half) + 4 * q) * 4;
    };
    const unsigned ri8 = (wd & 3) * 8;
    unsigned ta[6], cc, cd;
    int k0;
    auto set_ta = [&]() {
#pragma unroll
        for (int i = 0; i < 3; ++i) {
            const unsigned t = lds0 + task_at(i == 2 ? 0 : ((w < 4) == (i == 0) ? 2 : 1));
            ta[2 * i] = t + g * 4; ta[2 * i + 1] = t + (c16 & 12);
            asm volatile("" : "+v"(ta[2 * i]), "+v"(ta[2 * i + 1]));
        }
        cc = __builtin_rotateleft32(0xff0103fdu, ri8); cd = __builtin_rotateleft32(0xfe000204u, ri8); k0 = (int)(signed char)(ring_const() >> ri8);
        asm volatile("" : "+v"(cc), "+v"(cd), "+v"(k0));
    };
    unsigned tag;
    bool slow;
    u32x4 wr[8], cgA;
    auto load_wr = [&]() {
#pragma unroll
        for (int t = 0; t < 8; ++t) wr[t] = *(const u32x4*)(smem + L_WR + t * 1024 + lane * 16);
        cgA = *(const u32x4*)(smem + L_CG + lane * 16);
        asm volatile("" : "+v"(wr[0]), "+v"(wr[1]), "+v"(wr[2]), "+v"(wr[3]), "+v"(wr[4]), "+v"(wr[5]), "+v"(wr[6]), "+v"(wr[7]), "+v"(cgA));
    };

    __builtin_amdgcn_s_setprio(3);
    auto touch = [&](unsigned code) -> unsigned {
        if (code < 8u)
            return __builtin_amdgcn_raw_buffer_load_b32(__builtin_amdgcn_make_buffer_rsrc((void*)(ef + (size_t)base_e * 2), 0, code == 0u ? 0 : ne * 32, 0x00020000), lane * 128, (code - 1u) * 8192u, 0);
        const unsigned j = code < 16u ? (code - 8u) * 128u + b : (code - 16u) * 32u + (bx >> 3);
        return __builtin_amdgcn_raw_buffer_load_b32(__builtin_amdgcn_make_buffer_rsrc((void*)x, 0, N * 1024, 0x00020000), lane * 128,
                                                    (2u * j + ((half ^ (TNEAR ? j >> 7 : smask >> (j >> 5))) & 1u)) * 8192u, 0);
    };
    constexpr unsigned tl[] = {TLIST};
    const __amdgpu_buffer_rsrc_t rsm = __builtin_amdgcn_make_buffer_rsrc((void*)w_mix, 0, C_DIM * C_DIM * 2, 0x00020000);
    u32x4 mv[8];
    auto load_mv = [&]() {
#pragma unroll
        for (int j = 0; j < 8; ++j)
            mv[j] = __builtin_amdgcn_raw_buffer_load_b128(rsm, g * 512 + c16 * 16 + (w & 3) * 2048 + (j & 1) * 256, (j >> 1) * 8192, 0);
        SB();
    };
    auto make_img = [&]() {
#pragma unroll
        for (int i = 0; i < 4; ++i) {
            u32x4 p0, p1;
#pragma unroll
            for (int j = 0; j < 4; ++j) {
                p0[j] = __builtin_amdgcn_perm(mv[2 * j + 1][i], mv[2 * j][i], 0x05040100u);
                p1[j] = __builtin_amdgcn_perm(mv[2 * j + 1][i], mv[2 * j][i], 0x07060302u);
            }
            const unsigned ia = L_IMG + (c16 >> 2) * 8192 + (w & 3) * 1024 + g * 256 + (c16 & 3) * 64 + i * 16;
            *(u32x4*)(smem + ia) = p0;
            *(u32x4*)(smem + ia + 4096) = p1;
        }
    };
    unsigned t0v = 0u, cm = 0u, wv[8], cv[4], tv[NT2] = {};
    {
        const unsigned sh = 8u * (w & 3);
        unsigned d[3], sv[3], epoch, inc[3];
        u32x2 yv[3];
        const __amdgpu_buffer_rsrc_t rsd = __builtin_amdgcn_make_buffer_rsrc((void*)(dst + base_e), 0, ne * 4, 0x00020000);
        const __amdgpu_buffer_rsrc_t rss = __builtin_amdgcn_make_buffer_rsrc((void*)(src + base_e), 0, ne * 4, 0x00020000);
        const __amdgpu_buffer_rsrc_t rsy = __builtin_amdgcn_make_buffer_rsrc((void*)(Y + base_e), 0, ne * 8, 0x00020000);
        const unsigned le = (unsigned)(w * 160 + lane), le2 = lane < 32 ? le : 0x1000000u;
#pragma unroll
        for (int r = 0; r < 3; ++r) {
            d[r] = __builtin_amdgcn_raw_buffer_load_b32(rsd, (r < 2 ? le : le2) * 4 + 256 * r, 0, NT);
            sv[r] = __builtin_amdgcn_raw_buffer_load_b32(rss, (r < 2 ? le : le2) * 4 + 256 * r, 0, NT);
            SB();
        }
        epoch = __builtin_amdgcn_raw_buffer_load_b32(rsw, W_CTL, 0, 0);
        const unsigned pt = __builtin_amdgcn_raw_buffer_load_b32(__builtin_amdgcn_make_buffer_rsrc((void*)x, 0, bx * 8 + w == 8 + 7 ? N * 1024 : 0, 0x00020000),
                                                                 (lane < 32 ? po0 : po1) + (lane & 31) * 1024, 0, 0);
#pragma unroll
        for (int r = 0; r < 3; ++r) yv[r] = __builtin_amdgcn_raw_buffer_load_b64(rsy, (r < 2 ? le : le2) * 8 + 512 * r, 0, NT);
        SB();
        if (bx * 8 + w == 7) {
            unsigned s0, s1, v0, v1;
            const unsigned tm = (unsigned)__builtin_amdgcn_s_memtime() * 0x9e3779b1u;
#define RACE(r, o, a, b) "s_load_dword %2, %4, " o " offset:" a "\n\ts_load_dword %3, %4, " o " offset:" b "\n\ts_waitcnt lgkmcnt(1)\n\ts_sleep " PSL "\n\t" \
                         "s_getreg_b32 " r ", hwreg(HW_REG_IB_STS, 8, 1)\n\ts_waitcnt lgkmcnt(0)\n\t"
            asm volatile("s_load_dword %0, %4, %5 offset:0x10000\n\ts_load_dword %1, %4, %6 offset:0x10000\n\ts_waitcnt lgkmcnt(0)\n\ts_sleep " PRW "\n\t"
                         RACE("%0", "%5", "0", "0x2000") RACE("%1", "%6", "0x2000", "0") "s_cmp_lg_u32 %0, %1\n\ts_cbranch_scc1 Lr_%=\n\t" RACE("%1", "%5", "0x6000", "0x4000") "Lr_%=:"
                         : "=&s"(s0), "=&s"(s1), "=&s"(v0), "=&s"(v1) : "s"(x), "s"(po0 + (tm >> 29) * 1024u), "s"(po1 + (tm >> 26 & 7u) * 1024u) : "memory", "scc");
            SB();
            unsigned xc;
            asm volatile("s_getreg_b32 %0, hwreg(HW_REG_XCC_ID, 0, 2)" : "=s"(xc));
            unsigned hm = (0xf0f0f0u >> (xc + 4u * s1));
            const unsigned tg = epoch * 1664525u + 1013904223u;
            __builtin_amdgcn_raw_buffer_store_b128((u32x4){hm, tg, hm, tg}, rsw, lane < 8 ? W_DEC + lane * 16 : OOB_OFF, 0, SC1);
            for (int i = 0; i < 2; ++i) __builtin_amdgcn_raw_buffer_store_b128((u32x4){0u, tg, 0u, tg}, rsw, W_TAB + lane * 16, i * 1024, SC1);
        }
        {
            const unsigned k0 = (unsigned)tid - L_KEY / 16 < NPB_MAX / 4 ? 127u + L_KEY / 4 - 4u * tid : 0u, k1 = min(k0, 1u);
            *(u32x4*)(smem + tid * 16) = (u32x4){k0, k0 - k1, k0 - 2u * k1, k0 - 3u * k1};
        }
        if (tid < 64 * NRND) *(u32x4*)(smem + L_MRK + tid * 16) = (u32x4){0u, 0u, 0u, 0u};
        {
            const int hm = w >= 4 ? -1 : 0;
            const __amdgpu_buffer_rsrc_t rsa = __builtin_amdgcn_make_buffer_rsrc((void*)w_rad, 0, DE_DIM * C_DIM * 2 & hm, 0x00020000);
            const __amdgpu_buffer_rsrc_t rsg = __builtin_amdgcn_make_buffer_rsrc((void*)cg, 0, 128 & hm, 0x00020000);
#pragma unroll
            for (int j = 0; j < 8; ++j) wv[j] = __builtin_amdgcn_raw_buffer_load_b32(rsa, (g & 1) * 2048 + 64 * (w & 3) + 4 * c16 + 256 * j, 0, 0);
#pragma unroll
            for (int f = 0; f < 4; ++f) cv[f] = __builtin_amdgcn_raw_buffer_load_b32(rsg, (lane & 3) * 32 + (lane & 8u) / 2 + 8 * f, 0, 0);
            t0v = __builtin_amdgcn_raw_buffer_load_b32(__builtin_amdgcn_make_buffer_rsrc((void*)(ef + (size_t)base_e * 2), 0, min(ne * 32, NT0 * 8192), 0x00020000), (511 - tid) * 128, 0, 0);
            cm = __builtin_amdgcn_raw_buffer_load_b32(rsm, (tid >> 3) == 32 ? ((bx >> 3) * 8 + lane) * 128 : OOB_OFF, 0, 0);
        }
#pragma unroll
        for (int r = 0; r < 3; ++r) inc[r] = (int)(r < 2 ? le : le2) + r * 64 < ne ? 1u << sh : 0u;
        asm volatile("" : "+v"(inc[0]), "+v"(inc[1]), "+v"(inc[2]));
        SB();
        wg_barrier();
        if (bx != 0) {
        unsigned bin[3], rank[3], ba[3];
        asm volatile("" : "+v"(d[0]), "+v"(sv[0]), "+v"(d[1]), "+v"(sv[1]), "+v"(d[2]), "+v"(sv[2]));
#pragma unroll
        for (int r = 0; r < 3; ++r) {
            bin[r] = __umulhi(d[r], nb_magic);
            ba[r] = ((((sv[r] >> 3) ^ (smask >> ((sv[r] >> 9) & 31u))) & 1u) * 512u + bin[r] * 4u) & 1023u;
            rank[r] = atomicAdd((unsigned*)(smem + L_PCNT + (w >> 2) * 1024 + ba[r]), inc[r]);
        }
        unsigned tag = (unsigned)__builtin_amdgcn_readfirstlane((int)epoch) * 1664525u + 1013904223u;
        asm volatile("" : "+s"(tag));
        unsigned rw0[3], rw1[3];
#pragma unroll
        for (int r = 0; r < 3; ++r) {
            const unsigned sl = sv[r], nl = d[r] - ((__umul24(bin[r], (unsigned)N) + 127u) >> 7);
            rw0[r] = sl | (tag << 14);
            rw1[r] = (nl * 4u + ((__umul24(sl, cmul) >> 16) ^ ((nl >= 40u) == (ba[r] >= 512u) ? 3u : 0u))) | ((le + r * 64) << 9) | (tag & 0xfff00000u);
            asm volatile("" : "+v"(rw0[r]), "+v"(rw1[r]));
        }
        unsigned pa[3];
#pragma unroll
        for (int r = 0; r < 3; ++r) { pa[r] = A_PTAB + w * 512 + (ba[r] >> 1); asm volatile("" : "+v"(pa[r])); }
        unsigned m0 = below_mask(w, 0), m1 = below_mask(w, 1);
        asm volatile("" : "+s"(m0), "+s"(m1));
        unsigned tba = W_TAB + bx * 2048 + lane * 16 + (w & 1) * 1024;
        asm volatile("" : "+v"(tba));
        wg_barrier();
        u32x2 c[4];
#pragma unroll
        for (int i = 0; i < 4; ++i) c[i] = *(const u32x2*)(smem + L_PCNT + i * 512 + lane * 8);
        unsigned t[4], run[4];
#pragma unroll
        for (int i = 0; i < 4; ++i) t[i] = bsum(c[i >> 1][i & 1], 0x01010101u);
        asm volatile("" : "+v"(t[0]), "+v"(t[1]), "+v"(t[2]), "+v"(t[3]));
#pragma unroll
        for (int i = 0; i < 4; ++i) t[i] = bsum(c[2 + (i >> 1)][i & 1], 0x01010101u, t[i]);
        asm volatile("" : "+v"(t[0]), "+v"(t[1]), "+v"(t[2]), "+v"(t[3]));
        const unsigned ts = (t[0] + t[1]) | (t[2] + t[3]) << 16;
        const unsigned ti = (unsigned)wave_incl_scan((int)ts), tx = ti - ts;
        run[0] = tx & 0xffffu; run[2] = (tx >> 16) + ((unsigned)__builtin_amdgcn_readlane((int)ti, 63) & 0xffffu);
        run[1] = run[0] + t[0]; run[3] = run[2] + t[2];
        unsigned o[4];
#pragma unroll
        for (int i = 0; i < 4; ++i) o[i] = bytesum2(c[i >> 1][i & 1], c[2 + (i >> 1)][i & 1], m0, m1, run[i]);
        *(unsigned*)(smem + A_PTAB + w * 512 + lane * 4) = o[0] | (o[1] << 16);
        *(unsigned*)(smem + A_PTAB + w * 512 + 256 + lane * 4) = o[2] | (o[3] << 16);
        unsigned pos[3];
        asm volatile("" ::: "memory");
#pragma unroll
        for (int r = 0; r < 3; ++r) pos[r] = *(const unsigned short*)(smem + pa[r]);
        SB();
        if ((w & 6) == 4) {
            const unsigned ra = (w & 1) == 0 ? run[0] | (t[0] << 16) : run[1] | (t[1] << 16), rb = (w & 1) == 0 ? run[2] | (t[2] << 16) : run[3] | (t[3] << 16);
            __builtin_amdgcn_raw_buffer_store_b128((u32x4){ra, tag, rb, tag}, rsw, tba, 0, SC1);
        }
#pragma unroll
        for (int r = 0; r < 3; ++r) pos[r] = A_STG + (pos[r] + ((rank[r] >> sh) & 0xffu)) * 16;
        asm volatile("" : "+v"(pos[0]), "+v"(pos[1]), "+v"(pos[2]));
#pragma unroll
        for (int r = 0; r < 3; ++r) pos[r] = inc[r] != 0u ? pos[r] : L_DUM + lane * 16;
#pragma unroll
        for (int r = 0; r < 3; ++r) *(u32x4*)(smem + pos[r]) = (u32x4){yv[r].x, rw0[r], yv[r].y, rw1[r]};
        }
        asm volatile("" : "+v"(epoch) : "v"(pt));
        tag = (unsigned)__builtin_amdgcn_readfirstlane((int)epoch) * 1664525u + 1013904223u;
        wg_barrier();
        const __amdgpu_buffer_rsrc_t rsr = __builtin_amdgcn_make_buffer_rsrc((void*)(ws + W_RA + bx * (EPB_MAX * 16)), 0, (ne * 16 + 127) & ~127, 0x00020000);
        u32x4 va[3];
#pragma unroll
        for (int i = 0; i < 3; ++i) va[i] = *(const u32x4*)(smem + A_STG + (tid ^ 256) * 16 + 8192 * i);
#pragma unroll
        for (int i = 0; i < 3; ++i) __builtin_amdgcn_raw_buffer_store_b128(va[i], rsr, (tid ^ 256) * 16, 8192 * i, SC1);
        lds_arrive(lds0 + SY_CPY);
    }
    u32x4 dx = mkdesc(x, (unsigned)N * 1024u), de = mkdesc(ef, (unsigned)E * 32u);
    asm volatile("" : "+s"(dx), "+s"(de));
    if (w < 4) {
        asm volatile("" :: "v"(t0v), "v"(cm), "v"(wv[0]), "v"(wv[7]), "v"(cv[3]));
        const unsigned sh = 8u * w, mb = 0x01010101u & ((1u << sh) - 1u);
        const unsigned T0 = tag << 14, T1 = tag & 0xfff00000u;
        const unsigned to = W_TAB + tid * 2048 + (b & 1) * 1024 + (b >> 1) * 16;
        u32x4 en;
        u32x2 dec;
        {
            __builtin_amdgcn_s_sleep(4);
            en = __builtin_amdgcn_raw_buffer_load_b128(rsw, to, 0, SC1);
            dec = __builtin_amdgcn_raw_buffer_load_b64(rsw, W_DEC, 0, SC1);
        }
#pragma unroll 1
        for (unsigned spin = 0; spin < SPIN_MAX; ++spin) {
            if (__builtin_amdgcn_ballot_w64(((en.y ^ tag) | (en.w ^ tag) | (dec.y ^ tag)) != 0u) == 0ull) break;
            __builtin_amdgcn_s_sleep(2);
            asm volatile("" ::: "memory");
            en = __builtin_amdgcn_raw_buffer_load_b128(rsw, to, 0, SC1);
            dec = __builtin_amdgcn_raw_buffer_load_b64(rsw, W_DEC, 0, SC1);
        }
        half = (__builtin_amdgcn_readfirstlane((int)dec.x) >> (bx & 7)) & 1;
        if (half) en.x = en.z;
        const unsigned rc = en.x >> 16;
        const unsigned incl = (unsigned)wave_incl_scan((int)rc);
        const int twf = __builtin_amdgcn_readlane((int)incl, 63), tw = twf <= 64 * NRND ? twf : 0;
        const bool more = tw > 64 * (NRND - 1);
        unsigned ma = L_MRK + w * (256 * NRND) + (incl - rc) * 4;
        asm volatile("" : "+v"(ma));
        *(unsigned*)(smem + (((rc != 0u) & (incl <= 64u * NRND)) ? ma : L_DUM + lane * 4)) = (unsigned)lane * EPB_MAX + (en.x & 0xffffu) - (incl - rc);
        unsigned ra[NRND];
        asm volatile("" ::: "memory");
#pragma unroll
        for (int r = 0; r < NRND; ++r) ra[r] = *(const unsigned*)(smem + L_MRK + w * (256 * NRND) + r * 256 + lane * 4);
        unsigned carry = 0u;
        u32x4 A[NRND];
#pragma unroll
        for (int r = 0; r < NRND; ++r) {
            if (r == NRND - 1) { if (more) ra[r] = wave_max_scan(ra[r]); asm volatile("" : "+v"(ra[r])); }
            else if (r % 3 == 0) wave_max_scan3(ra[r], ra[r + 1], ra[r + 2]);
            const unsigned m = max(ra[r], carry);
            carry = max(carry, (unsigned)__builtin_amdgcn_readlane((int)ra[r], 63));
            ra[r] = r * 64 + lane < tw ? (m + (unsigned)(r * 64 + lane)) * 16u : OOB_OFF >> 1;
            A[r] = __builtin_amdgcn_raw_buffer_load_b128(rsw, ra[r], W_RA + 64 * w * (EPB_MAX * 16), SC1);
        }
        SB();
        lds_arrive(lds0 + SY_RI, 16u + half);
        unsigned pe[NRND], inc[NRND], inck[NRND], lim[NRND];
#pragma unroll
        for (int r = 0; r < NRND; ++r) {
            pe[r] = __umul24(__umulhi(ra[r], 209716u) + 64u * w - 1u, (unsigned)epb * 32u);
            inc[r] = ra[r] < 0x40000000u ? 1u << sh : 0u;
            inck[r] = ra[r] < 0x40000000u ? 128u : 0u;
            lim[r] = ra[r] < 0x40000000u ? 16383u : ~0u;
            asm volatile("" : "+v"(pe[r]), "+v"(inc[r]), "+v"(inck[r]), "+v"(lim[r]));
        }
        if (twf > 64 * NRND) lds_arrive(lds0 + SY_S, 256u);
        set_ta();
        lds_wait(lds0 + SY_PRE, 4);
        load_wr();
        SB();
        unsigned rank[NRND], nc[NRND];
        auto slot = [&](int r) {
            nc[r] = inc[r] != 0u ? L_CTAB + w * 640 + nc[r] * 2 : L_MISC + 8;
            A[r] = (u32x4){A[r].y << 10, ((A[r].w >> 4) & 0xffe0u) + pe[r], A[r].x, A[r].z};
            asm volatile("" : "+v"(nc[r]), "+v"(A[r]));
            SB();
        };
#pragma unroll
        for (int r = 0; r < NRND; ++r) {
            if (r == NRND - 1 && !more) { rank[r] = 0u; nc[r] = 0u; continue; }
            A[r].y ^= T0; A[r].w ^= T1;
            if (__builtin_expect(__builtin_amdgcn_ballot_w64((A[r].y | (A[r].w >> 6)) > lim[r]) != 0ull, 0)) {
#pragma unroll 1
                for (unsigned spin = 0; spin < SPIN_MAX; ++spin) {
                    asm volatile("" ::: "memory");
                    A[r] = __builtin_amdgcn_raw_buffer_load_b128(rsw, ra[r], W_RA + 64 * w * (EPB_MAX * 16), SC1);
                    A[r].y ^= T0; A[r].w ^= T1;
                    if (__builtin_amdgcn_ballot_w64((A[r].y | (A[r].w >> 6)) > lim[r]) == 0ull) break;
                }
            }
            nc[r] = (r < 4 || inc[r] != 0u) ? A[r].w & 0x1ffu : (unsigned)lane;
            rank[r] = atomicAdd((unsigned*)(smem + L_CCNT + nc[r] * 4), inc[r]);
            atomicAdd((unsigned*)(smem + L_KEY + (nc[r] & 0x1fcu)), inck[r]);
            SB();
            if (r < PAYR) slot(r);
        }
        lds_arrive(lds0 + SY_C1);
        unsigned d0 = lane * LS, d1 = (64 + c16) * LS, ca0 = L_CTAB + w * 640 + lane * 8, ca1 = L_CTAB + w * 640 + (64 + c16) * 8;
        asm volatile("" : "+v"(d0), "+v"(d1), "+v"(ca0), "+v"(ca1));
#pragma unroll
        for (int r = PAYR; r < NRND; ++r) slot(r);
#pragma unroll
        for (int r = 0; r < NRND; ++r) {
            rank[r] = inc[r] != 0u ? (rank[r] >> sh) & 0xffu : (L_DUM - L_PRV) / 16 + lane;
            asm volatile("" : "+v"(rank[r]));
        }
        {
            u32x4 c0, c1;
            unsigned cn;
            do {
                asm volatile("ds_read_b32 %0, %3 offset:%c6\n\tds_read_b128 %1, %4 offset:%c7\n\tds_read_b128 %2, %5 offset:%c8\n\ts_waitcnt lgkmcnt(0)"
                             : "=&v"(cn), "=&v"(c0), "=&v"(c1) : "v"(lds0), "v"(lds0 + lane * 16), "v"(lds0 + c16 * 16), "i"(SY_C1), "i"(L_CCNT), "i"(L_CCNT + 1024) : "memory");
            } while (__builtin_amdgcn_readfirstlane((int)cn) < 4);
            unsigned p0[4], p1[4];
#pragma unroll
            for (int c = 0; c < 4; ++c) {
                p0[c] = bsum(c0[c], mb, d0); p1[c] = bsum(c1[c], mb, d1);
                if (c < 3) { d0 = bsum(c0[c], 0x01010101u, d0); d1 = bsum(c1[c], 0x01010101u, d1); }
            }
            *(u32x2*)(smem + ca0) = (u32x2){p0[0] | (p0[1] << 16), p0[2] | (p0[3] << 16)};
            *(u32x2*)(smem + ca1) = (u32x2){p1[0] | (p1[1] << 16), p1[2] | (p1[3] << 16)};
        }
        asm volatile("" ::: "memory");
#pragma unroll
        for (int r = 0; r < NRND; ++r) nc[r] = *(const unsigned short*)(smem + nc[r]);
        SB();
#pragma unroll
        for (int r = 0; r < NRND; ++r) {
            if (r == NRND - 1 && !more) continue;
            *(u32x4*)(smem + L_PRV + (nc[r] + rank[r]) * 16) = A[r];
        }
        lds_arrive(lds0 + SY_C3);
    } else {
        const int q = w - 4, t2 = tid - 256;
        __builtin_amdgcn_s_setprio(2);
        load_mv();
        {
            float f0[8], f1[8];
#pragma unroll
            for (int j = 0; j < 8; ++j) {
                const float a = __uint_as_float(wv[j] << 16) * -1.44269504088896340736f, c = __uint_as_float(wv[j] & 0xffff0000u) * -1.44269504088896340736f;
                const unsigned hp = g < 2 ? 0u : pack2bf(a, c);
                f0[j] = a - __uint_as_float(hp << 16);
                f1[j] = c - __uint_as_float(hp & 0xffff0000u);
            }
            *(u32x4*)(smem + L_WR + (2 * q) * 1024 + lane * 16) = (u32x4){pack2bf(f0[0], f0[1]), pack2bf(f0[2], f0[3]), pack2bf(f0[4], f0[5]), pack2bf(f0[6], f0[7])};
            *(u32x4*)(smem + L_WR + (2 * q + 1) * 1024 + lane * 16) = (u32x4){pack2bf(f1[0], f1[1]), pack2bf(f1[2], f1[3]), pack2bf(f1[4], f1[5]), pack2bf(f1[6], f1[7])};
            float a[4];
#pragma unroll
            for (int f = 0; f < 4; ++f) a[f] = __uint_as_float((lane & 4) ? cv[f] & 0xffff0000u : cv[f] << 16) * -0.69314718055994530942f;
            const unsigned h0 = pack2bf(a[0], a[1]), h1 = pack2bf(a[2], a[3]);
            *(u32x4*)(smem + L_CG + lane * 16) = (u32x4){h0, h1, pack2bf(a[0] - __uint_as_float(h0 << 16), a[1] - __uint_as_float(h0 & 0xffff0000u)),
                                                        pack2bf(a[2] - __uint_as_float(h1 << 16), a[3] - __uint_as_float(h1 & 0xffff0000u))};
        }
        lds_wait(lds0 + SY_CPY, 8);
#pragma unroll
        for (int i = 0; i < 15; ++i) *(u32x4*)(smem + L_PRV + (t2 + 256 * i) * 16) = (u32x4){OOB_OFF, OOB_OFF, 0u, 0u};
        SB();
        asm volatile("" :: "v"(cp), "v"(cm), "v"(t0v));
        make_img();
        lds_arrive(lds0 + SY_PRE);
        lds_wait(lds0 + SY_PRE, 4);
        load_wr();
        if (w == 7) {
            lds_arrive(lds0 + SY_C3);
            half = (lds_wait<1>(lds0 + SY_RI, 64) & 15u) != 0u;
#pragma unroll
            for (int i = 0; i < NT2; ++i) tv[i] = touch(tl[i]);
            SB();
            set_ta();
        } else {
            half = (lds_wait<1>(lds0 + SY_RI, 16) & 15u) != 0u;
            set_ta();
            const int n5 = min(27 * q + (lane >> 1), NPB_MAX - 1);
            const bool kp = (n5 >= 40) == (half != 0);
            u64 lpos = kp ? len_pos(true) : len_pos(false);
            unsigned an = lds0 + n5 * 4, ak = lds0 + L_KEY + (n5 >= 40 ? 160 : 0) + (lane & 1) * 80;
            unsigned aa = L_AT + (n5 >= 40 ? 160 : 0), al = L_LEN + (kp ? 4 : 0), av = (unsigned)n5 | (lds0 + L_PRV + n5 * (LS * 16)) << 8;
            asm volatile("" : "+v"(an), "+v"(ak), "+v"(aa), "+v"(al), "+v"(av), "+v"(lpos));
            lds_wait<1>(lds0 + SY_C1, 3);
            __builtin_amdgcn_s_setprio(HPRIO);
            unsigned kn, cn;
            u32x4 dk[5];
            do {
                asm volatile("ds_read_b32 %0, %7 offset:%c10\n\tds_read_b32 %1, %8 offset:%c11\n\tds_read_b128 %2, %9\n\tds_read_b128 %3, %9 offset:16\n\tds_read_b128 %4, %9 offset:32\n\t"
                             "ds_read_b128 %5, %9 offset:48\n\tds_read_b128 %6, %9 offset:64\n\ts_waitcnt lgkmcnt(0)"
                             : "=&v"(cn), "=&v"(kn), "=&v"(dk[0]), "=&v"(dk[1]), "=&v"(dk[2]), "=&v"(dk[3]), "=&v"(dk[4]) : "v"(lds0), "v"(an), "v"(ak), "i"(SY_C1), "i"(L_KEY) : "memory");
            } while (__builtin_amdgcn_readfirstlane((int)cn) < 4);
            unsigned sg[4] = {0u, 0u, 0u, 0u};
#pragma unroll
            for (int k = 0; k < 20; ++k) sg[k & 3] = __builtin_amdgcn_alignbit(sg[k & 3], kn - dk[k >> 2][k & 3], 31);
            int c = __builtin_popcount(sg[0] | sg[1] << 8 | sg[2] << 16 | sg[3] << 24);
            c += __builtin_amdgcn_update_dpp(0, c, 0xB1, 0xf, 0xf, false);
            *(unsigned*)(smem + aa + c * 4) = av;
            smem[(c & 3) == 0 ? al + ((unsigned)(lpos >> c) & 15u) : L_LEN + 20] = (unsigned char)max(kn >> 7, c < 8 ? 12u : 8u);
            lds_arrive(lds0 + SY_S, __builtin_amdgcn_ballot_w64((kn >> 7) > DEG_MAX) != 0ull ? 257u : 1u);
        }
        __builtin_amdgcn_s_setprio(3);
    }
    unsigned ax[3], af[3], lh, cn;
    u32x4 ln;
    {
        if (w == 7) lds_wait<2, 255>(lds0 + SY_S, 1);
        do {
            asm volatile("ds_read_b32 %0, %9 offset:%c16\n\tds_read_b128 %1, %9 offset:%c17\n\tds_read_b32 %2, %9 offset:%c18\n\tds_read_b32 %3, %10\n\tds_read_b32 %4, %11\n\t"
                         "ds_read_b32 %5, %12\n\tds_read_b32 %6, %13\n\tds_read_b32 %7, %14\n\tds_read_b32 %8, %15\n\ts_waitcnt lgkmcnt(0)"
                         : "=&v"(cn), "=&v"(ln), "=&v"(lh), "=&v"(ax[0]), "=&v"(af[0]), "=&v"(ax[1]), "=&v"(af[1]), "=&v"(ax[2]), "=&v"(af[2])
                         : "v"(lds0), "v"(ta[0]), "v"(ta[1]), "v"(ta[2]), "v"(ta[3]), "v"(ta[4]), "v"(ta[5]),
                           "i"(SY_S), "i"(L_LEN), "i"(L_LEN + 16) : "memory");
            cn = (unsigned)__builtin_amdgcn_readfirstlane((int)cn);
        } while ((cn & 255u) < 3u);
        slow = cn >= 256u;
    }
    const int p8 = __builtin_amdgcn_sdot4((int)(ln.x + ln.y), (int)cc, __builtin_amdgcn_sdot4((int)(ln.w + lh), (int)cc,
                   __builtin_amdgcn_sdot4((int)ln.z, (int)cd, k0, false), false), false);
    const int ld = (__builtin_amdgcn_readfirstlane((int)ln.z) >> ri8) & 255, p2 = min(max(__builtin_amdgcn_readfirstlane(p8) >> 3, 4), ld - 8);
    const int l2 = w < 4 ? ld - p2 : p2, od = (w < 4) == (wd >= 6) ? ld - l2 : 0;
    const unsigned aX = (ax[0] >> 8) + ((w < 4 ? od : 0) + (c16 & 3)) * 16, aF = (af[0] >> 8) + ((w < 4 ? od : 0) + (c16 & 3)) * 16 + 4;
    const unsigned lane_off = (unsigned)c16 * 16u;
    const unsigned aWw = L_BNC + w * 512 + lane * 8;
    const unsigned aWr = L_BNC + w * 512 + g * 128 + (c16 & 3) * 32 + (c16 >> 2) * 8;
    auto row_of = [=](unsigned at) -> unsigned {
        const unsigned nl = at & 0xffu;
        return nl < (unsigned)npb ? ((unsigned)node0 + nl) * 1024u + lane_off : OOB_OFF;
    };
#pragma unroll
    for (int i = 0; i < NT2; ++i) asm volatile("" :: "v"(tv[i]));
    if (!slow) {
        u32x32 oxa, oxb;
        u32x4 efA, efN, S;
        unsigned tmp;
        __builtin_amdgcn_s_waitcnt(0x0F70);
        asm volatile(
            "Lw2_%=:\n"
            "ds_read_b32 v220, v219\n"
            "ds_read2_b32 v[232:233], v211 offset1:16\n"
            "ds_read_b128 v[196:199], v210\n"
            "s_waitcnt lgkmcnt(1)\n"
            "v_readfirstlane_b32 %[tmp], v220\n"
            "s_cmp_lt_u32 %[tmp], 5\n"
            "s_cbranch_scc1 Lw2_%=\n"
            "v_add_u32 v232, v232, v229\n"
            "v_add_u32 v233, v233, v229\n"
            "buffer_load_dwordx4 v[192:195], v232, %[rse], 0 offen\n"
            "s_waitcnt lgkmcnt(0)\n"
            "v_add_u32_dpp v220, v196, v214 quad_perm:[0,0,0,0] row_mask:0xf bank_mask:0xf\n"
            "v_add_u32_dpp v221, v196, v214 quad_perm:[1,1,1,1] row_mask:0xf bank_mask:0xf\n"
            "v_add_u32_dpp v222, v196, v214 quad_perm:[2,2,2,2] row_mask:0xf bank_mask:0xf\n"
            "v_add_u32_dpp v223, v196, v214 quad_perm:[3,3,3,3] row_mask:0xf bank_mask:0xf\n"
            "buffer_load_dwordx4 v[32:35], v220, %[rsx], 0 offen\n"
            "buffer_load_dwordx4 v[36:39], v220, %[rsx], 0 offen offset:256\n"
            "buffer_load_dwordx4 v[48:51], v221, %[rsx], 0 offen\n"
            "buffer_load_dwordx4 v[52:55], v221, %[rsx], 0 offen offset:256\n"
            "buffer_load_dwordx4 v[232:235], v233, %[rse], 0 offen\n"
            "buffer_load_dwordx4 v[64:67], v222, %[rsx], 0 offen\n"
            "buffer_load_dwordx4 v[68:71], v222, %[rsx], 0 offen offset:256\n"
            "buffer_load_dwordx4 v[80:83], v223, %[rsx], 0 offen\n"
            "buffer_load_dwordx4 v[84:87], v223, %[rsx], 0 offen offset:256\n"
            "buffer_load_dwordx4 v[40:43], v220, %[rsx], 0 offen offset:512\n"
            "buffer_load_dwordx4 v[44:47], v220, %[rsx], 0 offen offset:768\n"
            "buffer_load_dwordx4 v[56:59], v221, %[rsx], 0 offen offset:512\n"
            "buffer_load_dwordx4 v[60:63], v221, %[rsx], 0 offen offset:768\n"
            "buffer_load_dwordx4 v[72:75], v222, %[rsx], 0 offen offset:512\n"
            "buffer_load_dwordx4 v[76:79], v222, %[rsx], 0 offen offset:768\n"
            "buffer_load_dwordx4 v[88:91], v223, %[rsx], 0 offen offset:512\n"
            "buffer_load_dwordx4 v[92:95], v223, %[rsx], 0 offen offset:768\n"
            : "={v[32:63]}"(oxa), "={v[64:95]}"(oxb), "={v[192:195]}"(efA), "={v[196:199]}"(S), "={v[232:235]}"(efN), [tmp] "=&s"(tmp)
            : [rse] "s"(de), [rsx] "s"(dx), [ax] "{v210}"(aX), [af] "{v211}"(aF), [lo] "{v214}"(lane_off), [eo] "{v229}"((g & 1) * 16), [sy] "{v219}"(lds0 + SY_C3)
            : "memory", "scc", "v220", "v221", "v222", "v223");
        if (bx == 0 && w == 4) flag_store(ws + W_CTL, tag);
        const int lf = (__builtin_amdgcn_readfirstlane((int)(w < 4 ? ln.w : lh)) >> (8 * (w & 3))) & 255, l1 = (__builtin_amdgcn_readfirstlane((int)(w < 4 ? ln.x : ln.y)) >> (8 * (w & 3))) & 255;
        const int e2 = lf + l1 + l2;
        int nst = (e2 + 3) >> 2;
        const unsigned r2 = row_of(ax[1]);
        u32x32 owr;
        u32x16 omi;
        u32x8 ot;
#pragma unroll
        for (int i = 0; i < 32; ++i) owr[i] = wr[i >> 2][i & 3];
        const int pw = (w + (w >> 2)) & 3;
        const unsigned trb = lds0 + L_TR + w * TR_STR;
        omi[2] = aX + 64; omi[3] = aF + 64;
        omi[4] = lds0 + aWw; omi[5] = lds0 + aWr; omi[6] = lane_off; omi[7] = W_AGG + row_of(ax[0]); omi[8] = W_AGG + r2;
        omi[9] = row_of(ax[2]); omi[10] = w >= 6 ? r2 : OOB_OFF; omi[15] = ~tag;
        ot[0] = (lane & 3) == 0 ? 0x3f80u : ((lane & 3) == 1 ? 0x3f800000u : 0u); ot[1] = (lane & 3) == 2 ? 0x3f80u : ((lane & 3) == 3 ? 0x3f800000u : 0u);
        ot[2] = trb + g * (4 * 272) + c16 * 16; ot[3] = trb + c16 * 272 + g * 16; ot[4] = lds0 + L_IMG + lane * 16; ot[5] = (g & 1) * 16; ot[7] = lds0 + L_PK + pw * 4096 + lane * 16;
        int rem = w < 4 ? l2 : l1, lens = ((w < 4 ? l1 : l2) - 4) | 0xff00, fm = w < 4 ? 0x802 : (w < 6 ? 0x810 : 0xb0), pb = 0;
        const int j1 = (w < 4 ? -od - l2 : od - l1) * 16, j2 = (w < 4 ? -l1 : -od - l2) * 16;
        u32x4 jmp = (u32x4){(ax[1] >> 8) - (ax[0] >> 8) + j1, (af[1] >> 8) - (af[0] >> 8) + j1, (ax[2] >> 8) - (ax[1] >> 8) + j2, (af[2] >> 8) - (af[1] >> 8) + j2};
        unsigned evt = (unsigned)__builtin_amdgcn_readfirstlane(w < 4 ? evt_of(PTO) : (w < 6 ? evt_of(PTY) : evt_of(PTZ))) | 6u << (nst - (rem >> 2)) | 6u << (nst - ((l1 + l2) >> 2));
        u64 ra;
        asm volatile(
            "s_nop 1\n"
            "v_mfma_f32_4x4x4_16b_bf16 v[204:207], v[202:203], v[198:199], 0\n"
            "v_mov_b64 v[0:1], 0\n"
            "v_mov_b64 v[2:3], 0\n"
            "v_mov_b64 v[4:5], 0\n"
            "v_mov_b64 v[6:7], 0\n"
            "v_mfma_f32_4x4x4_16b_bf16 v[204:207], v[200:201], v[198:199], v[204:207]\n"
            "v_mov_b64 v[8:9], 0\n"
            "v_mov_b64 v[10:11], 0\n"
            "v_mov_b64 v[12:13], 0\n"
            "v_mov_b64 v[14:15], 0\n"
            "v_mov_b64 v[16:17], 0\n"
            "v_mov_b64 v[18:19], 0\n"
            "v_mov_b64 v[20:21], 0\n"
            "v_mov_b64 v[22:23], 0\n"
            "v_mov_b64 v[24:25], 0\n"
            "v_mov_b64 v[26:27], 0\n"
            "v_mov_b64 v[28:29], 0\n"
            "v_mov_b64 v[30:31], 0\n"
            "v_cvt_pk_bf16_f32 v204, v204, v205\n"
            "v_cvt_pk_bf16_f32 v205, v206, v207\n"
            "ds_write_b64 v212, v[204:205]\n"
            "ds_read_b128 v[196:199], v210\n"
            "ds_read_b64 v[208:209], v213\n"
            "s_waitcnt vmcnt(17)\n"
            "v_mfma_f32_16x16x32_bf16 v[176:179], v[192:195], v[96:99], 0\n"
            "v_mfma_f32_16x16x32_bf16 v[180:183], v[192:195], v[100:103], 0\n"
            "v_mfma_f32_16x16x32_bf16 v[184:187], v[192:195], v[104:107], 0\n"
            "v_mfma_f32_16x16x32_bf16 v[188:191], v[192:195], v[108:111], 0\n"
            "v_mfma_f32_16x16x32_bf16 v[128:131], v[192:195], v[112:115], 0\n"
            "v_mfma_f32_16x16x32_bf16 v[132:135], v[192:195], v[116:119], 0\n"
            "s_nop 2\n"
            "v_exp_f32 v144, v176\n"
            "v_exp_f32 v145, v177\n"
            "v_exp_f32 v146, v178\n"
            "v_exp_f32 v147, v179\n"
            "v_exp_f32 v148, v180\n"
            "v_exp_f32 v149, v181\n"
            "v_exp_f32 v150, v182\n"
            "v_exp_f32 v151, v183\n"
            "v_exp_f32 v152, v184\n"
            "v_exp_f32 v153, v185\n"
            "v_exp_f32 v154, v186\n"
            "v_exp_f32 v155, v187\n"
            "v_exp_f32 v156, v188\n"
            "v_exp_f32 v157, v189\n"
            "v_exp_f32 v158, v190\n"
            "v_exp_f32 v159, v191\n"
            "v_pk_add_f32 v[144:145], v[144:145], 1.0 op_sel_hi:[1,0]\n"
            "v_pk_add_f32 v[146:147], v[146:147], 1.0 op_sel_hi:[1,0]\n"
            "v_pk_add_f32 v[148:149], v[148:149], 1.0 op_sel_hi:[1,0]\n"
            "v_pk_add_f32 v[150:151], v[150:151], 1.0 op_sel_hi:[1,0]\n"
            "v_pk_add_f32 v[152:153], v[152:153], 1.0 op_sel_hi:[1,0]\n"
            "v_pk_add_f32 v[154:155], v[154:155], 1.0 op_sel_hi:[1,0]\n"
            "v_pk_add_f32 v[156:157], v[156:157], 1.0 op_sel_hi:[1,0]\n"
            "v_pk_add_f32 v[158:159], v[158:159], 1.0 op_sel_hi:[1,0]\n"
            "v_rcp_f32 v144, v144\n"
            "v_rcp_f32 v145, v145\n"
            "v_rcp_f32 v146, v146\n"
            "v_rcp_f32 v147, v147\n"
            "v_rcp_f32 v148, v148\n"
            "v_rcp_f32 v149, v149\n"
            "v_rcp_f32 v150, v150\n"
            "v_rcp_f32 v151, v151\n"
            "v_rcp_f32 v152, v152\n"
            "v_rcp_f32 v153, v153\n"
            "v_rcp_f32 v154, v154\n"
            "v_rcp_f32 v155, v155\n"
            "v_rcp_f32 v156, v156\n"
            "v_rcp_f32 v157, v157\n"
            "v_rcp_f32 v158, v158\n"
            "v_rcp_f32 v159, v159\n"
            "v_pk_mul_f32 v[144:145], v[144:145], v[176:177]\n"
            "v_pk_mul_f32 v[146:147], v[146:147], v[178:179]\n"
            "v_pk_mul_f32 v[148:149], v[148:149], v[180:181]\n"
            "v_pk_mul_f32 v[150:151], v[150:151], v[182:183]\n"
            "v_pk_mul_f32 v[152:153], v[152:153], v[184:185]\n"
            "v_pk_mul_f32 v[154:155], v[154:155], v[186:187]\n"
            "v_pk_mul_f32 v[156:157], v[156:157], v[188:189]\n"
            "v_pk_mul_f32 v[158:159], v[158:159], v[190:191]\n"
            "s_branch Ldisp_%=\n"
            "Lfast_%=:\n"
            "v_mfma_f32_16x16x32_bf16 v[136:139], v[192:195], v[120:123], 0\n"
            "v_exp_f32 v160, v128\n"
            "v_add_u32 v211, 64, v211\n"
            "v_exp_f32 v161, v129\n"
            "s_waitcnt vmcnt(13) lgkmcnt(0)\n"
            "v_mfma_f32_4x4x4_16b_bf16 v[176:179], v[208:209], v[32:33], 0 cbsz:2\n"
            "v_exp_f32 v162, v130\n"
            "v_mfma_f32_4x4x4_16b_bf16 v[180:183], v[208:209], v[34:35], 0 cbsz:2\n"
            "v_exp_f32 v163, v131\n"
            "v_mfma_f32_4x4x4_16b_bf16 v[184:187], v[208:209], v[36:37], 0 cbsz:2\n"
            "v_exp_f32 v164, v132\n"
            "v_mfma_f32_4x4x4_16b_bf16 v[188:191], v[208:209], v[38:39], 0 cbsz:2\n"
            "v_exp_f32 v165, v133\n"
            "v_add_u32_dpp v219, v196, v214 quad_perm:[0,0,0,0] row_mask:0xf bank_mask:0xf\n"
            "v_mfma_f32_4x4x4_16b_bf16 v[204:207], v[202:203], v[198:199], 0\n"
            "v_exp_f32 v166, v134\n"
            "buffer_load_dwordx4 v[32:35], v219, %[rsx], 0 offen\n"
            "buffer_load_dwordx4 v[36:39], v219, %[rsx], 0 offen offset:256\n"
            "v_exp_f32 v167, v135\n"
            "v_pk_fma_f32 v[0:1], v[144:145], v[176:177], v[0:1] op_sel_hi:[0,1,1]\n"
            "v_pk_fma_f32 v[2:3], v[144:145], v[178:179], v[2:3] op_sel_hi:[0,1,1]\n"
            "v_pk_fma_f32 v[4:5], v[148:149], v[180:181], v[4:5] op_sel_hi:[0,1,1]\n"
            "v_pk_fma_f32 v[6:7], v[148:149], v[182:183], v[6:7] op_sel_hi:[0,1,1]\n"
            "v_pk_fma_f32 v[8:9], v[152:153], v[184:185], v[8:9] op_sel_hi:[0,1,1]\n"
            "v_pk_fma_f32 v[10:11], v[152:153], v[186:187], v[10:11] op_sel_hi:[0,1,1]\n"
            "v_pk_fma_f32 v[12:13], v[156:157], v[188:189], v[12:13] op_sel_hi:[0,1,1]\n"
            "v_pk_fma_f32 v[14:15], v[156:157], v[190:191], v[14:15] op_sel_hi:[0,1,1]\n"
            "v_pk_add_f32 v[160:161], v[160:161], 1.0 op_sel_hi:[1,0]\n"
            "v_pk_add_f32 v[162:163], v[162:163], 1.0 op_sel_hi:[1,0]\n"
            "v_pk_add_f32 v[164:165], v[164:165], 1.0 op_sel_hi:[1,0]\n"
            "v_pk_add_f32 v[166:167], v[166:167], 1.0 op_sel_hi:[1,0]\n"
            "v_mfma_f32_16x16x32_bf16 v[140:143], v[192:195], v[124:127], 0\n"
            "ds_read_b32 v230, v211\n"
            "v_rcp_f32 v160, v160\n"
            "v_rcp_f32 v161, v161\n"
            "v_mfma_f32_4x4x4_16b_bf16 v[176:179], v[208:209], v[48:49], 0 cbsz:2 abid:1\n"
            "v_rcp_f32 v162, v162\n"
            "v_mfma_f32_4x4x4_16b_bf16 v[180:183], v[208:209], v[50:51], 0 cbsz:2 abid:1\n"
            "v_rcp_f32 v163, v163\n"
            "v_mfma_f32_4x4x4_16b_bf16 v[184:187], v[208:209], v[52:53], 0 cbsz:2 abid:1\n"
            "v_rcp_f32 v164, v164\n"
            "v_mfma_f32_4x4x4_16b_bf16 v[188:191], v[208:209], v[54:55], 0 cbsz:2 abid:1\n"
            "v_rcp_f32 v165, v165\n"
            "v_add_u32_dpp v220, v196, v214 quad_perm:[1,1,1,1] row_mask:0xf bank_mask:0xf\n"
            "v_mfma_f32_4x4x4_16b_bf16 v[204:207], v[200:201], v[198:199], v[204:207]\n"
            "v_rcp_f32 v166, v166\n"
            "buffer_load_dwordx4 v[48:51], v220, %[rsx], 0 offen\n"
            "buffer_load_dwordx4 v[52:55], v220, %[rsx], 0 offen offset:256\n"
            "v_rcp_f32 v167, v167\n"
            "v_pk_fma_f32 v[0:1], v[144:145], v[176:177], v[0:1] op_sel:[1,0,0]\n"
            "v_pk_fma_f32 v[2:3], v[144:145], v[178:179], v[2:3] op_sel:[1,0,0]\n"
            "v_pk_fma_f32 v[4:5], v[148:149], v[180:181], v[4:5] op_sel:[1,0,0]\n"
            "v_pk_fma_f32 v[6:7], v[148:149], v[182:183], v[6:7] op_sel:[1,0,0]\n"
            "v_pk_fma_f32 v[8:9], v[152:153], v[184:185], v[8:9] op_sel:[1,0,0]\n"
            "v_pk_fma_f32 v[10:11], v[152:153], v[186:187], v[10:11] op_sel:[1,0,0]\n"
            "v_pk_fma_f32 v[12:13], v[156:157], v[188:189], v[12:13] op_sel:[1,0,0]\n"
            "v_pk_fma_f32 v[14:15], v[156:157], v[190:191], v[14:15] op_sel:[1,0,0]\n"
            "v_pk_mul_f32 v[160:161], v[160:161], v[128:129]\n"
            "v_pk_mul_f32 v[162:163], v[162:163], v[130:131]\n"
            "v_pk_mul_f32 v[164:165], v[164:165], v[132:133]\n"
            "v_pk_mul_f32 v[166:167], v[166:167], v[134:135]\n"
            "s_waitcnt vmcnt(12) lgkmcnt(0)\n"
            "v_mov_b64 v[192:193], v[232:233]\n"
            "v_mov_b64 v[194:195], v[234:235]\n"
            "v_add_u32 v230, v230, v229\n"
            "buffer_load_dwordx4 v[232:235], v230, %[rse], 0 offen\n"
            "v_mfma_f32_16x16x32_bf16 v[128:131], v[192:195], v[96:99], 0\n"
            "v_exp_f32 v168, v136\n"
            "v_exp_f32 v169, v137\n"
            "v_mfma_f32_4x4x4_16b_bf16 v[176:179], v[208:209], v[64:65], 0 cbsz:2 abid:2\n"
            "v_exp_f32 v170, v138\n"
            "v_mfma_f32_4x4x4_16b_bf16 v[180:183], v[208:209], v[66:67], 0 cbsz:2 abid:2\n"
            "v_exp_f32 v171, v139\n"
            "v_mfma_f32_4x4x4_16b_bf16 v[184:187], v[208:209], v[68:69], 0 cbsz:2 abid:2\n"
            "v_exp_f32 v172, v140\n"
            "v_mfma_f32_4x4x4_16b_bf16 v[188:191], v[208:209], v[70:71], 0 cbsz:2 abid:2\n"
            "v_exp_f32 v173, v141\n"
            "v_add_u32_dpp v221, v196, v214 quad_perm:[2,2,2,2] row_mask:0xf bank_mask:0xf\n"
            "v_exp_f32 v174, v142\n"
            "buffer_load_dwordx4 v[64:67], v221, %[rsx], 0 offen\n"
            "buffer_load_dwordx4 v[68:71], v221, %[rsx], 0 offen offset:256\n"
            "v_exp_f32 v175, v143\n"
            "v_pk_fma_f32 v[0:1], v[146:147], v[176:177], v[0:1] op_sel_hi:[0,1,1]\n"
            "v_pk_fma_f32 v[2:3], v[146:147], v[178:179], v[2:3] op_sel_hi:[0,1,1]\n"
            "v_pk_fma_f32 v[4:5], v[150:151], v[180:181], v[4:5] op_sel_hi:[0,1,1]\n"
            "v_pk_fma_f32 v[6:7], v[150:151], v[182:183], v[6:7] op_sel_hi:[0,1,1]\n"
            "v_pk_fma_f32 v[8:9], v[154:155], v[184:185], v[8:9] op_sel_hi:[0,1,1]\n"
            "v_pk_fma_f32 v[10:11], v[154:155], v[186:187], v[10:11] op_sel_hi:[0,1,1]\n"
            "v_pk_fma_f32 v[12:13], v[158:159], v[188:189], v[12:13] op_sel_hi:[0,1,1]\n"
            "v_pk_fma_f32 v[14:15], v[158:159], v[190:191], v[14:15] op_sel_hi:[0,1,1]\n"
            "v_pk_add_f32 v[168:169], v[168:169], 1.0 op_sel_hi:[1,0]\n"
            "v_pk_add_f32 v[170:171], v[170:171], 1.0 op_sel_hi:[1,0]\n"
            "v_pk_add_f32 v[172:173], v[172:173], 1.0 op_sel_hi:[1,0]\n"
            "v_pk_add_f32 v[174:175], v[174:175], 1.0 op_sel_hi:[1,0]\n"
            "v_cvt_pk_bf16_f32 v204, v204, v205\n"
            "v_cvt_pk_bf16_f32 v205, v206, v207\n"
            "ds_write_b64 v212, v[204:205]\n"
            "v_mfma_f32_16x16x32_bf16 v[132:135], v[192:195], v[100:103], 0\n"
            "v_rcp_f32 v168, v168\n"
            "v_rcp_f32 v169, v169\n"
            "v_mfma_f32_4x4x4_16b_bf16 v[176:179], v[208:209], v[80:81], 0 cbsz:2 abid:3\n"
            "v_rcp_f32 v170, v170\n"
            "v_mfma_f32_4x4x4_16b_bf16 v[180:183], v[208:209], v[82:83], 0 cbsz:2 abid:3\n"
            "v_rcp_f32 v171, v171\n"
            "v_mfma_f32_4x4x4_16b_bf16 v[184:187], v[208:209], v[84:85], 0 cbsz:2 abid:3\n"
            "v_rcp_f32 v172, v172\n"
            "v_mfma_f32_4x4x4_16b_bf16 v[188:191], v[208:209], v[86:87], 0 cbsz:2 abid:3\n"
            "v_rcp_f32 v173, v173\n"
            "v_add_u32_dpp v222, v196, v214 quad_perm:[3,3,3,3] row_mask:0xf bank_mask:0xf\n"
            "v_rcp_f32 v174, v174\n"
            "buffer_load_dwordx4 v[80:83], v222, %[rsx], 0 offen\n"
            "buffer_load_dwordx4 v[84:87], v222, %[rsx], 0 offen offset:256\n"
            "v_rcp_f32 v175, v175\n"
            "v_pk_fma_f32 v[0:1], v[146:147], v[176:177], v[0:1] op_sel:[1,0,0]\n"
            "v_pk_fma_f32 v[2:3], v[146:147], v[178:179], v[2:3] op_sel:[1,0,0]\n"
            "v_pk_fma_f32 v[4:5], v[150:151], v[180:181], v[4:5] op_sel:[1,0,0]\n"
            "v_pk_fma_f32 v[6:7], v[150:151], v[182:183], v[6:7] op_sel:[1,0,0]\n"
            "v_pk_fma_f32 v[8:9], v[154:155], v[184:185], v[8:9] op_sel:[1,0,0]\n"
            "v_pk_fma_f32 v[10:11], v[154:155], v[186:187], v[10:11] op_sel:[1,0,0]\n"
            "v_pk_fma_f32 v[12:13], v[158:159], v[188:189], v[12:13] op_sel:[1,0,0]\n"
            "v_pk_fma_f32 v[14:15], v[158:159], v[190:191], v[14:15] op_sel:[1,0,0]\n"
            "v_pk_mul_f32 v[168:169], v[168:169], v[136:137]\n"
            "v_pk_mul_f32 v[170:171], v[170:171], v[138:139]\n"
            "v_pk_mul_f32 v[172:173], v[172:173], v[140:141]\n"
            "v_pk_mul_f32 v[174:175], v[174:175], v[142:143]\n"
            "v_add_u32 v210, 64, v210\n"
            "ds_read_b128 v[196:199], v210\n"
            "v_mfma_f32_16x16x32_bf16 v[136:139], v[192:195], v[104:107], 0\n"
            "v_exp_f32 v144, v128\n"
            "v_exp_f32 v145, v129\n"
            "s_waitcnt vmcnt(13)\n"
            "v_mfma_f32_4x4x4_16b_bf16 v[176:179], v[208:209], v[40:41], 0 cbsz:2\n"
            "v_exp_f32 v146, v130\n"
            "v_mfma_f32_4x4x4_16b_bf16 v[180:183], v[208:209], v[42:43], 0 cbsz:2\n"
            "v_exp_f32 v147, v131\n"
            "v_mfma_f32_4x4x4_16b_bf16 v[184:187], v[208:209], v[44:45], 0 cbsz:2\n"
            "v_exp_f32 v148, v132\n"
            "v_mfma_f32_4x4x4_16b_bf16 v[188:191], v[208:209], v[46:47], 0 cbsz:2\n"
            "v_exp_f32 v149, v133\n"
            "v_exp_f32 v150, v134\n"
            "buffer_load_dwordx4 v[40:43], v219, %[rsx], 0 offen offset:512\n"
            "buffer_load_dwordx4 v[44:47], v219, %[rsx], 0 offen offset:768\n"
            "v_exp_f32 v151, v135\n"
            "v_pk_fma_f32 v[16:17], v[160:161], v[176:177], v[16:17] op_sel_hi:[0,1,1]\n"
            "v_pk_fma_f32 v[18:19], v[160:161], v[178:179], v[18:19] op_sel_hi:[0,1,1]\n"
            "v_pk_fma_f32 v[20:21], v[164:165], v[180:181], v[20:21] op_sel_hi:[0,1,1]\n"
            "v_pk_fma_f32 v[22:23], v[164:165], v[182:183], v[22:23] op_sel_hi:[0,1,1]\n"
            "v_pk_fma_f32 v[24:25], v[168:169], v[184:185], v[24:25] op_sel_hi:[0,1,1]\n"
            "v_pk_fma_f32 v[26:27], v[168:169], v[186:187], v[26:27] op_sel_hi:[0,1,1]\n"
            "v_pk_fma_f32 v[28:29], v[172:173], v[188:189], v[28:29] op_sel_hi:[0,1,1]\n"
            "v_pk_fma_f32 v[30:31], v[172:173], v[190:191], v[30:31] op_sel_hi:[0,1,1]\n"
            "v_pk_add_f32 v[144:145], v[144:145], 1.0 op_sel_hi:[1,0]\n"
            "v_pk_add_f32 v[146:147], v[146:147], 1.0 op_sel_hi:[1,0]\n"
            "v_pk_add_f32 v[148:149], v[148:149], 1.0 op_sel_hi:[1,0]\n"
            "v_pk_add_f32 v[150:151], v[150:151], 1.0 op_sel_hi:[1,0]\n"
            "v_mfma_f32_16x16x32_bf16 v[140:143], v[192:195], v[108:111], 0\n"
            "v_rcp_f32 v144, v144\n"
            "v_rcp_f32 v145, v145\n"
            "v_mfma_f32_4x4x4_16b_bf16 v[176:179], v[208:209], v[56:57], 0 cbsz:2 abid:1\n"
            "v_rcp_f32 v146, v146\n"
            "v_mfma_f32_4x4x4_16b_bf16 v[180:183], v[208:209], v[58:59], 0 cbsz:2 abid:1\n"
            "v_rcp_f32 v147, v147\n"
            "v_mfma_f32_4x4x4_16b_bf16 v[184:187], v[208:209], v[60:61], 0 cbsz:2 abid:1\n"
            "v_rcp_f32 v148, v148\n"
            "v_mfma_f32_4x4x4_16b_bf16 v[188:191], v[208:209], v[62:63], 0 cbsz:2 abid:1\n"
            "v_rcp_f32 v149, v149\n"
            "v_rcp_f32 v150, v150\n"
            "buffer_load_dwordx4 v[56:59], v220, %[rsx], 0 offen offset:512\n"
            "buffer_load_dwordx4 v[60:63], v220, %[rsx], 0 offen offset:768\n"
            "v_rcp_f32 v151, v151\n"
            "v_pk_fma_f32 v[16:17], v[160:161], v[176:177], v[16:17] op_sel:[1,0,0]\n"
            "v_pk_fma_f32 v[18:19], v[160:161], v[178:179], v[18:19] op_sel:[1,0,0]\n"
            "v_pk_fma_f32 v[20:21], v[164:165], v[180:181], v[20:21] op_sel:[1,0,0]\n"
            "v_pk_fma_f32 v[22:23], v[164:165], v[182:183], v[22:23] op_sel:[1,0,0]\n"
            "v_pk_fma_f32 v[24:25], v[168:169], v[184:185], v[24:25] op_sel:[1,0,0]\n"
            "v_pk_fma_f32 v[26:27], v[168:169], v[186:187], v[26:27] op_sel:[1,0,0]\n"
            "v_pk_fma_f32 v[28:29], v[172:173], v[188:189], v[28:29] op_sel:[1,0,0]\n"
            "v_pk_fma_f32 v[30:31], v[172:173], v[190:191], v[30:31] op_sel:[1,0,0]\n"
            "v_pk_mul_f32 v[144:145], v[144:145], v[128:129]\n"
            "v_pk_mul_f32 v[146:147], v[146:147], v[130:131]\n"
            "v_pk_mul_f32 v[148:149], v[148:149], v[132:133]\n"
            "v_pk_mul_f32 v[150:151], v[150:151], v[134:135]\n"
            "v_mfma_f32_16x16x32_bf16 v[128:131], v[192:195], v[112:115], 0\n"
            "v_exp_f32 v152, v136\n"
            "v_exp_f32 v153, v137\n"
            "s_waitcnt vmcnt(13)\n"
            "v_mfma_f32_4x4x4_16b_bf16 v[176:179], v[208:209], v[72:73], 0 cbsz:2 abid:2\n"
            "v_exp_f32 v154, v138\n"
            "v_mfma_f32_4x4x4_16b_bf16 v[180:183], v[208:209], v[74:75], 0 cbsz:2 abid:2\n"
            "v_exp_f32 v155, v139\n"
            "v_mfma_f32_4x4x4_16b_bf16 v[184:187], v[208:209], v[76:77], 0 cbsz:2 abid:2\n"
            "v_exp_f32 v156, v140\n"
            "v_mfma_f32_4x4x4_16b_bf16 v[188:191], v[208:209], v[78:79], 0 cbsz:2 abid:2\n"
            "v_exp_f32 v157, v141\n"
            "v_exp_f32 v158, v142\n"
            "buffer_load_dwordx4 v[72:75], v221, %[rsx], 0 offen offset:512\n"
            "buffer_load_dwordx4 v[76:79], v221, %[rsx], 0 offen offset:768\n"
            "v_exp_f32 v159, v143\n"
            "v_pk_fma_f32 v[16:17], v[162:163], v[176:177], v[16:17] op_sel_hi:[0,1,1]\n"
            "v_pk_fma_f32 v[18:19], v[162:163], v[178:179], v[18:19] op_sel_hi:[0,1,1]\n"
            "v_pk_fma_f32 v[20:21], v[166:167], v[180:181], v[20:21] op_sel_hi:[0,1,1]\n"
            "v_pk_fma_f32 v[22:23], v[166:167], v[182:183], v[22:23] op_sel_hi:[0,1,1]\n"
            "v_pk_fma_f32 v[24:25], v[170:171], v[184:185], v[24:25] op_sel_hi:[0,1,1]\n"
            "v_pk_fma_f32 v[26:27], v[170:171], v[186:187], v[26:27] op_sel_hi:[0,1,1]\n"
            "v_pk_fma_f32 v[28:29], v[174:175], v[188:189], v[28:29] op_sel_hi:[0,1,1]\n"
            "v_pk_fma_f32 v[30:31], v[174:175], v[190:191], v[30:31] op_sel_hi:[0,1,1]\n"
            "v_pk_add_f32 v[152:153], v[152:153], 1.0 op_sel_hi:[1,0]\n"
            "v_pk_add_f32 v[154:155], v[154:155], 1.0 op_sel_hi:[1,0]\n"
            "v_pk_add_f32 v[156:157], v[156:157], 1.0 op_sel_hi:[1,0]\n"
            "v_pk_add_f32 v[158:159], v[158:159], 1.0 op_sel_hi:[1,0]\n"
            "v_mfma_f32_16x16x32_bf16 v[132:135], v[192:195], v[116:119], 0\n"
            "v_rcp_f32 v152, v152\n"
            "v_rcp_f32 v153, v153\n"
            "v_mfma_f32_4x4x4_16b_bf16 v[176:179], v[208:209], v[88:89], 0 cbsz:2 abid:3\n"
            "v_rcp_f32 v154, v154\n"
            "v_mfma_f32_4x4x4_16b_bf16 v[180:183], v[208:209], v[90:91], 0 cbsz:2 abid:3\n"
            "v_rcp_f32 v155, v155\n"
            "v_mfma_f32_4x4x4_16b_bf16 v[184:187], v[208:209], v[92:93], 0 cbsz:2 abid:3\n"
            "v_rcp_f32 v156, v156\n"
            "v_mfma_f32_4x4x4_16b_bf16 v[188:191], v[208:209], v[94:95], 0 cbsz:2 abid:3\n"
            "ds_read_b64 v[208:209], v213\n"
            "v_rcp_f32 v157, v157\n"
            "v_rcp_f32 v158, v158\n"
            "buffer_load_dwordx4 v[88:91], v222, %[rsx], 0 offen offset:512\n"
            "buffer_load_dwordx4 v[92:95], v222, %[rsx], 0 offen offset:768\n"
            "v_rcp_f32 v159, v159\n"
            "v_pk_fma_f32 v[16:17], v[162:163], v[176:177], v[16:17] op_sel:[1,0,0]\n"
            "v_pk_fma_f32 v[18:19], v[162:163], v[178:179], v[18:19] op_sel:[1,0,0]\n"
            "v_pk_fma_f32 v[20:21], v[166:167], v[180:181], v[20:21] op_sel:[1,0,0]\n"
            "v_pk_fma_f32 v[22:23], v[166:167], v[182:183], v[22:23] op_sel:[1,0,0]\n"
            "v_pk_fma_f32 v[24:25], v[170:171], v[184:185], v[24:25] op_sel:[1,0,0]\n"
            "v_pk_fma_f32 v[26:27], v[170:171], v[186:187], v[26:27] op_sel:[1,0,0]\n"
            "v_pk_fma_f32 v[28:29], v[174:175], v[188:189], v[28:29] op_sel:[1,0,0]\n"
            "v_pk_fma_f32 v[30:31], v[174:175], v[190:191], v[30:31] op_sel:[1,0,0]\n"
            "v_pk_mul_f32 v[152:153], v[152:153], v[136:137]\n"
            "v_pk_mul_f32 v[154:155], v[154:155], v[138:139]\n"
            "v_pk_mul_f32 v[156:157], v[156:157], v[140:141]\n"
            "v_pk_mul_f32 v[158:159], v[158:159], v[142:143]\n"
            "s_sub_u32 %[rem], %[rem], 4\n"
            "Lnext_%=:\n"
            "s_sub_u32 %[nst], %[nst], 1\n"
            "Ldisp_%=:\n"
            "s_bitcmp1_b32 %[evt], %[nst]\n"
            "s_cbranch_scc1 Lprio_%=\n"
            "Ld2_%=:\n"
            "s_cmp_gt_u32 %[rem], 4\n"
            "s_cbranch_scc1 Lfast_%=\n"
            "v_mfma_f32_16x16x32_bf16 v[136:139], v[192:195], v[120:123], 0\n"
            "v_exp_f32 v160, v128\n"
            "v_add_u32 v211, 64, v211\n"
            "v_exp_f32 v161, v129\n"
            "s_waitcnt vmcnt(13) lgkmcnt(0)\n"
            "v_mfma_f32_4x4x4_16b_bf16 v[176:179], v[208:209], v[32:33], 0 cbsz:2\n"
            "v_exp_f32 v162, v130\n"
            "v_mfma_f32_4x4x4_16b_bf16 v[180:183], v[208:209], v[34:35], 0 cbsz:2\n"
            "v_exp_f32 v163, v131\n"
            "v_mfma_f32_4x4x4_16b_bf16 v[184:187], v[208:209], v[36:37], 0 cbsz:2\n"
            "v_exp_f32 v164, v132\n"
            "v_mfma_f32_4x4x4_16b_bf16 v[188:191], v[208:209], v[38:39], 0 cbsz:2\n"
            "v_exp_f32 v165, v133\n"
            "v_add_u32_dpp v219, v196, v214 quad_perm:[0,0,0,0] row_mask:0xf bank_mask:0xf\n"
            "v_mfma_f32_4x4x4_16b_bf16 v[204:207], v[202:203], v[198:199], 0\n"
            "v_exp_f32 v166, v134\n"
            "buffer_load_dwordx4 v[32:35], v219, %[rsx], 0 offen\n"
            "buffer_load_dwordx4 v[36:39], v219, %[rsx], 0 offen offset:256\n"
            "v_exp_f32 v167, v135\n"
            "v_pk_fma_f32 v[0:1], v[144:145], v[176:177], v[0:1] op_sel_hi:[0,1,1]\n"
            "v_pk_fma_f32 v[2:3], v[144:145], v[178:179], v[2:3] op_sel_hi:[0,1,1]\n"
            "v_pk_fma_f32 v[4:5], v[148:149], v[180:181], v[4:5] op_sel_hi:[0,1,1]\n"
            "v_pk_fma_f32 v[6:7], v[148:149], v[182:183], v[6:7] op_sel_hi:[0,1,1]\n"
            "v_pk_fma_f32 v[8:9], v[152:153], v[184:185], v[8:9] op_sel_hi:[0,1,1]\n"
            "v_pk_fma_f32 v[10:11], v[152:153], v[186:187], v[10:11] op_sel_hi:[0,1,1]\n"
            "v_pk_fma_f32 v[12:13], v[156:157], v[188:189], v[12:13] op_sel_hi:[0,1,1]\n"
            "v_pk_fma_f32 v[14:15], v[156:157], v[190:191], v[14:15] op_sel_hi:[0,1,1]\n"
            "v_pk_add_f32 v[160:161], v[160:161], 1.0 op_sel_hi:[1,0]\n"
            "v_pk_add_f32 v[162:163], v[162:163], 1.0 op_sel_hi:[1,0]\n"
            "v_pk_add_f32 v[164:165], v[164:165], 1.0 op_sel_hi:[1,0]\n"
            "v_pk_add_f32 v[166:167], v[166:167], 1.0 op_sel_hi:[1,0]\n"
            "s_cmp_eq_u32 %[rem], 1\n"
            "s_cbranch_scc1 Lf00_%=\n"
            "Lr00_%=:\n"
            "v_mfma_f32_16x16x32_bf16 v[140:143], v[192:195], v[124:127], 0\n"
            "ds_read_b32 v230, v211\n"
            "v_rcp_f32 v160, v160\n"
            "v_rcp_f32 v161, v161\n"
            "v_mfma_f32_4x4x4_16b_bf16 v[176:179], v[208:209], v[48:49], 0 cbsz:2 abid:1\n"
            "v_rcp_f32 v162, v162\n"
            "v_mfma_f32_4x4x4_16b_bf16 v[180:183], v[208:209], v[50:51], 0 cbsz:2 abid:1\n"
            "v_rcp_f32 v163, v163\n"
            "v_mfma_f32_4x4x4_16b_bf16 v[184:187], v[208:209], v[52:53], 0 cbsz:2 abid:1\n"
            "v_rcp_f32 v164, v164\n"
            "v_mfma_f32_4x4x4_16b_bf16 v[188:191], v[208:209], v[54:55], 0 cbsz:2 abid:1\n"
            "v_rcp_f32 v165, v165\n"
            "v_add_u32_dpp v220, v196, v214 quad_perm:[1,1,1,1] row_mask:0xf bank_mask:0xf\n"
            "v_mfma_f32_4x4x4_16b_bf16 v[204:207], v[200:201], v[198:199], v[204:207]\n"
            "v_rcp_f32 v166, v166\n"
            "buffer_load_dwordx4 v[48:51], v220, %[rsx], 0 offen\n"
            "buffer_load_dwordx4 v[52:55], v220, %[rsx], 0 offen offset:256\n"
            "v_rcp_f32 v167, v167\n"
            "v_pk_fma_f32 v[0:1], v[144:145], v[176:177], v[0:1] op_sel:[1,0,0]\n"
            "v_pk_fma_f32 v[2:3], v[144:145], v[178:179], v[2:3] op_sel:[1,0,0]\n"
            "v_pk_fma_f32 v[4:5], v[148:149], v[180:181], v[4:5] op_sel:[1,0,0]\n"
            "v_pk_fma_f32 v[6:7], v[148:149], v[182:183], v[6:7] op_sel:[1,0,0]\n"
            "v_pk_fma_f32 v[8:9], v[152:153], v[184:185], v[8:9] op_sel:[1,0,0]\n"
            "v_pk_fma_f32 v[10:11], v[152:153], v[186:187], v[10:11] op_sel:[1,0,0]\n"
            "v_pk_fma_f32 v[12:13], v[156:157], v[188:189], v[12:13] op_sel:[1,0,0]\n"
            "v_pk_fma_f32 v[14:15], v[156:157], v[190:191], v[14:15] op_sel:[1,0,0]\n"
            "v_pk_mul_f32 v[160:161], v[160:161], v[128:129]\n"
            "v_pk_mul_f32 v[162:163], v[162:163], v[130:131]\n"
            "v_pk_mul_f32 v[164:165], v[164:165], v[132:133]\n"
            "v_pk_mul_f32 v[166:167], v[166:167], v[134:135]\n"
            "s_cmp_eq_u32 %[rem], 2\n"
            "s_cbranch_scc1 Lf01_%=\n"
            "Lr01_%=:\n"
            "s_waitcnt vmcnt(12) lgkmcnt(0)\n"
            "v_mov_b64 v[192:193], v[232:233]\n"
            "v_mov_b64 v[194:195], v[234:235]\n"
            "v_add_u32 v230, v230, v229\n"
            "buffer_load_dwordx4 v[232:235], v230, %[rse], 0 offen\n"
            "v_mfma_f32_16x16x32_bf16 v[128:131], v[192:195], v[96:99], 0\n"
            "v_exp_f32 v168, v136\n"
            "v_exp_f32 v169, v137\n"
            "v_mfma_f32_4x4x4_16b_bf16 v[176:179], v[208:209], v[64:65], 0 cbsz:2 abid:2\n"
            "v_exp_f32 v170, v138\n"
            "v_mfma_f32_4x4x4_16b_bf16 v[180:183], v[208:209], v[66:67], 0 cbsz:2 abid:2\n"
            "v_exp_f32 v171, v139\n"
            "v_mfma_f32_4x4x4_16b_bf16 v[184:187], v[208:209], v[68:69], 0 cbsz:2 abid:2\n"
            "v_exp_f32 v172, v140\n"
            "v_mfma_f32_4x4x4_16b_bf16 v[188:191], v[208:209], v[70:71], 0 cbsz:2 abid:2\n"
            "v_exp_f32 v173, v141\n"
            "v_add_u32_dpp v221, v196, v214 quad_perm:[2,2,2,2] row_mask:0xf bank_mask:0xf\n"
            "v_exp_f32 v174, v142\n"
            "buffer_load_dwordx4 v[64:67], v221, %[rsx], 0 offen\n"
            "buffer_load_dwordx4 v[68:71], v221, %[rsx], 0 offen offset:256\n"
            "v_exp_f32 v175, v143\n"
            "v_pk_fma_f32 v[0:1], v[146:147], v[176:177], v[0:1] op_sel_hi:[0,1,1]\n"
            "v_pk_fma_f32 v[2:3], v[146:147], v[178:179], v[2:3] op_sel_hi:[0,1,1]\n"
            "v_pk_fma_f32 v[4:5], v[150:151], v[180:181], v[4:5] op_sel_hi:[0,1,1]\n"
            "v_pk_fma_f32 v[6:7], v[150:151], v[182:183], v[6:7] op_sel_hi:[0,1,1]\n"
            "v_pk_fma_f32 v[8:9], v[154:155], v[184:185], v[8:9] op_sel_hi:[0,1,1]\n"
            "v_pk_fma_f32 v[10:11], v[154:155], v[186:187], v[10:11] op_sel_hi:[0,1,1]\n"
            "v_pk_fma_f32 v[12:13], v[158:159], v[188:189], v[12:13] op_sel_hi:[0,1,1]\n"
            "v_pk_fma_f32 v[14:15], v[158:159], v[190:191], v[14:15] op_sel_hi:[0,1,1]\n"
            "v_pk_add_f32 v[168:169], v[168:169], 1.0 op_sel_hi:[1,0]\n"
            "v_pk_add_f32 v[170:171], v[170:171], 1.0 op_sel_hi:[1,0]\n"
            "v_pk_add_f32 v[172:173], v[172:173], 1.0 op_sel_hi:[1,0]\n"
            "v_pk_add_f32 v[174:175], v[174:175], 1.0 op_sel_hi:[1,0]\n"
            "v_cvt_pk_bf16_f32 v204, v204, v205\n"
            "v_cvt_pk_bf16_f32 v205, v206, v207\n"
            "ds_write_b64 v212, v[204:205]\n"
            "s_cmp_eq_u32 %[rem], 3\n"
            "s_cbranch_scc1 Lf02_%=\n"
            "Lr02_%=:\n"
            "v_mfma_f32_16x16x32_bf16 v[132:135], v[192:195], v[100:103], 0\n"
            "v_rcp_f32 v168, v168\n"
            "v_rcp_f32 v169, v169\n"
            "v_mfma_f32_4x4x4_16b_bf16 v[176:179], v[208:209], v[80:81], 0 cbsz:2 abid:3\n"
            "v_rcp_f32 v170, v170\n"
            "v_mfma_f32_4x4x4_16b_bf16 v[180:183], v[208:209], v[82:83], 0 cbsz:2 abid:3\n"
            "v_rcp_f32 v171, v171\n"
            "v_mfma_f32_4x4x4_16b_bf16 v[184:187], v[208:209], v[84:85], 0 cbsz:2 abid:3\n"
            "v_rcp_f32 v172, v172\n"
            "v_mfma_f32_4x4x4_16b_bf16 v[188:191], v[208:209], v[86:87], 0 cbsz:2 abid:3\n"
            "v_rcp_f32 v173, v173\n"
            "v_add_u32_dpp v222, v196, v214 quad_perm:[3,3,3,3] row_mask:0xf bank_mask:0xf\n"
            "v_rcp_f32 v174, v174\n"
            "buffer_load_dwordx4 v[80:83], v222, %[rsx], 0 offen\n"
            "buffer_load_dwordx4 v[84:87], v222, %[rsx], 0 offen offset:256\n"
            "v_rcp_f32 v175, v175\n"
            "v_pk_fma_f32 v[0:1], v[146:147], v[176:177], v[0:1] op_sel:[1,0,0]\n"
            "v_pk_fma_f32 v[2:3], v[146:147], v[178:179], v[2:3] op_sel:[1,0,0]\n"
            "v_pk_fma_f32 v[4:5], v[150:151], v[180:181], v[4:5] op_sel:[1,0,0]\n"
            "v_pk_fma_f32 v[6:7], v[150:151], v[182:183], v[6:7] op_sel:[1,0,0]\n"
            "v_pk_fma_f32 v[8:9], v[154:155], v[184:185], v[8:9] op_sel:[1,0,0]\n"
            "v_pk_fma_f32 v[10:11], v[154:155], v[186:187], v[10:11] op_sel:[1,0,0]\n"
            "v_pk_fma_f32 v[12:13], v[158:159], v[188:189], v[12:13] op_sel:[1,0,0]\n"
            "v_pk_fma_f32 v[14:15], v[158:159], v[190:191], v[14:15] op_sel:[1,0,0]\n"
            "v_pk_mul_f32 v[168:169], v[168:169], v[136:137]\n"
            "v_pk_mul_f32 v[170:171], v[170:171], v[138:139]\n"
            "v_pk_mul_f32 v[172:173], v[172:173], v[140:141]\n"
            "v_pk_mul_f32 v[174:175], v[174:175], v[142:143]\n"
            "s_cmp_eq_u32 %[rem], 4\n"
            "s_cbranch_scc1 Lf03_%=\n"
            "Lr03_%=:\n"
            "v_add_u32 v210, 64, v210\n"
            "ds_read_b128 v[196:199], v210\n"
            "v_mfma_f32_16x16x32_bf16 v[136:139], v[192:195], v[104:107], 0\n"
            "v_exp_f32 v144, v128\n"
            "v_exp_f32 v145, v129\n"
            "s_waitcnt vmcnt(13)\n"
            "v_mfma_f32_4x4x4_16b_bf16 v[176:179], v[208:209], v[40:41], 0 cbsz:2\n"
            "v_exp_f32 v146, v130\n"
            "v_mfma_f32_4x4x4_16b_bf16 v[180:183], v[208:209], v[42:43], 0 cbsz:2\n"
            "v_exp_f32 v147, v131\n"
            "v_mfma_f32_4x4x4_16b_bf16 v[184:187], v[208:209], v[44:45], 0 cbsz:2\n"
            "v_exp_f32 v148, v132\n"
            "v_mfma_f32_4x4x4_16b_bf16 v[188:191], v[208:209], v[46:47], 0 cbsz:2\n"
            "v_exp_f32 v149, v133\n"
            "v_exp_f32 v150, v134\n"
            "buffer_load_dwordx4 v[40:43], v219, %[rsx], 0 offen offset:512\n"
            "buffer_load_dwordx4 v[44:47], v219, %[rsx], 0 offen offset:768\n"
            "v_exp_f32 v151, v135\n"
            "v_pk_fma_f32 v[16:17], v[160:161], v[176:177], v[16:17] op_sel_hi:[0,1,1]\n"
            "v_pk_fma_f32 v[18:19], v[160:161], v[178:179], v[18:19] op_sel_hi:[0,1,1]\n"
            "v_pk_fma_f32 v[20:21], v[164:165], v[180:181], v[20:21] op_sel_hi:[0,1,1]\n"
            "v_pk_fma_f32 v[22:23], v[164:165], v[182:183], v[22:23] op_sel_hi:[0,1,1]\n"
            "v_pk_fma_f32 v[24:25], v[168:169], v[184:185], v[24:25] op_sel_hi:[0,1,1]\n"
            "v_pk_fma_f32 v[26:27], v[168:169], v[186:187], v[26:27] op_sel_hi:[0,1,1]\n"
            "v_pk_fma_f32 v[28:29], v[172:173], v[188:189], v[28:29] op_sel_hi:[0,1,1]\n"
            "v_pk_fma_f32 v[30:31], v[172:173], v[190:191], v[30:31] op_sel_hi:[0,1,1]\n"
            "v_pk_add_f32 v[144:145], v[144:145], 1.0 op_sel_hi:[1,0]\n"
            "v_pk_add_f32 v[146:147], v[146:147], 1.0 op_sel_hi:[1,0]\n"
            "v_pk_add_f32 v[148:149], v[148:149], 1.0 op_sel_hi:[1,0]\n"
            "v_pk_add_f32 v[150:151], v[150:151], 1.0 op_sel_hi:[1,0]\n"
            "s_cmp_eq_u32 %[rem], 1\n"
            "s_cbranch_scc1 Lf10_%=\n"
            "Lr10_%=:\n"
            "v_mfma_f32_16x16x32_bf16 v[140:143], v[192:195], v[108:111], 0\n"
            "v_rcp_f32 v144, v144\n"
            "v_rcp_f32 v145, v145\n"
            "v_mfma_f32_4x4x4_16b_bf16 v[176:179], v[208:209], v[56:57], 0 cbsz:2 abid:1\n"
            "v_rcp_f32 v146, v146\n"
            "v_mfma_f32_4x4x4_16b_bf16 v[180:183], v[208:209], v[58:59], 0 cbsz:2 abid:1\n"
            "v_rcp_f32 v147, v147\n"
            "v_mfma_f32_4x4x4_16b_bf16 v[184:187], v[208:209], v[60:61], 0 cbsz:2 abid:1\n"
            "v_rcp_f32 v148, v148\n"
            "v_mfma_f32_4x4x4_16b_bf16 v[188:191], v[208:209], v[62:63], 0 cbsz:2 abid:1\n"
            "v_rcp_f32 v149, v149\n"
            "v_rcp_f32 v150, v150\n"
            "buffer_load_dwordx4 v[56:59], v220, %[rsx], 0 offen offset:512\n"
            "buffer_load_dwordx4 v[60:63], v220, %[rsx], 0 offen offset:768\n"
            "v_rcp_f32 v151, v151\n"
            "v_pk_fma_f32 v[16:17], v[160:161], v[176:177], v[16:17] op_sel:[1,0,0]\n"
            "v_pk_fma_f32 v[18:19], v[160:161], v[178:179], v[18:19] op_sel:[1,0,0]\n"
            "v_pk_fma_f32 v[20:21], v[164:165], v[180:181], v[20:21] op_sel:[1,0,0]\n"
            "v_pk_fma_f32 v[22:23], v[164:165], v[182:183], v[22:23] op_sel:[1,0,0]\n"
            "v_pk_fma_f32 v[24:25], v[168:169], v[184:185], v[24:25] op_sel:[1,0,0]\n"
            "v_pk_fma_f32 v[26:27], v[168:169], v[186:187], v[26:27] op_sel:[1,0,0]\n"
            "v_pk_fma_f32 v[28:29], v[172:173], v[188:189], v[28:29] op_sel:[1,0,0]\n"
            "v_pk_fma_f32 v[30:31], v[172:173], v[190:191], v[30:31] op_sel:[1,0,0]\n"
            "v_pk_mul_f32 v[144:145], v[144:145], v[128:129]\n"
            "v_pk_mul_f32 v[146:147], v[146:147], v[130:131]\n"
            "v_pk_mul_f32 v[148:149], v[148:149], v[132:133]\n"
            "v_pk_mul_f32 v[150:151], v[150:151], v[134:135]\n"
            "s_cmp_eq_u32 %[rem], 2\n"
            "s_cbranch_scc1 Lf11_%=\n"
            "Lr11_%=:\n"
            "v_mfma_f32_16x16x32_bf16 v[128:131], v[192:195], v[112:115], 0\n"
            "v_exp_f32 v152, v136\n"
            "v_exp_f32 v153, v137\n"
            "s_waitcnt vmcnt(13)\n"
            "v_mfma_f32_4x4x4_16b_bf16 v[176:179], v[208:209], v[72:73], 0 cbsz:2 abid:2\n"
            "v_exp_f32 v154, v138\n"
            "v_mfma_f32_4x4x4_16b_bf16 v[180:183], v[208:209], v[74:75], 0 cbsz:2 abid:2\n"
            "v_exp_f32 v155, v139\n"
            "v_mfma_f32_4x4x4_16b_bf16 v[184:187], v[208:209], v[76:77], 0 cbsz:2 abid:2\n"
            "v_exp_f32 v156, v140\n"
            "v_mfma_f32_4x4x4_16b_bf16 v[188:191], v[208:209], v[78:79], 0 cbsz:2 abid:2\n"
            "v_exp_f32 v157, v141\n"
            "v_exp_f32 v158, v142\n"
            "buffer_load_dwordx4 v[72:75], v221, %[rsx], 0 offen offset:512\n"
            "buffer_load_dwordx4 v[76:79], v221, %[rsx], 0 offen offset:768\n"
            "v_exp_f32 v159, v143\n"
            "v_pk_fma_f32 v[16:17], v[162:163], v[176:177], v[16:17] op_sel_hi:[0,1,1]\n"
            "v_pk_fma_f32 v[18:19], v[162:163], v[178:179], v[18:19] op_sel_hi:[0,1,1]\n"
            "v_pk_fma_f32 v[20:21], v[166:167], v[180:181], v[20:21] op_sel_hi:[0,1,1]\n"
            "v_pk_fma_f32 v[22:23], v[166:167], v[182:183], v[22:23] op_sel_hi:[0,1,1]\n"
            "v_pk_fma_f32 v[24:25], v[170:171], v[184:185], v[24:25] op_sel_hi:[0,1,1]\n"
            "v_pk_fma_f32 v[26:27], v[170:171], v[186:187], v[26:27] op_sel_hi:[0,1,1]\n"
            "v_pk_fma_f32 v[28:29], v[174:175], v[188:189], v[28:29] op_sel_hi:[0,1,1]\n"
            "v_pk_fma_f32 v[30:31], v[174:175], v[190:191], v[30:31] op_sel_hi:[0,1,1]\n"
            "v_pk_add_f32 v[152:153], v[152:153], 1.0 op_sel_hi:[1,0]\n"
            "v_pk_add_f32 v[154:155], v[154:155], 1.0 op_sel_hi:[1,0]\n"
            "v_pk_add_f32 v[156:157], v[156:157], 1.0 op_sel_hi:[1,0]\n"
            "v_pk_add_f32 v[158:159], v[158:159], 1.0 op_sel_hi:[1,0]\n"
            "s_cmp_eq_u32 %[rem], 3\n"
            "s_cbranch_scc1 Lf12_%=\n"
            "Lr12_%=:\n"
            "v_mfma_f32_16x16x32_bf16 v[132:135], v[192:195], v[116:119], 0\n"
            "v_rcp_f32 v152, v152\n"
            "v_rcp_f32 v153, v153\n"
            "v_mfma_f32_4x4x4_16b_bf16 v[176:179], v[208:209], v[88:89], 0 cbsz:2 abid:3\n"
            "v_rcp_f32 v154, v154\n"
            "v_mfma_f32_4x4x4_16b_bf16 v[180:183], v[208:209], v[90:91], 0 cbsz:2 abid:3\n"
            "v_rcp_f32 v155, v155\n"
            "v_mfma_f32_4x4x4_16b_bf16 v[184:187], v[208:209], v[92:93], 0 cbsz:2 abid:3\n"
            "v_rcp_f32 v156, v156\n"
            "v_mfma_f32_4x4x4_16b_bf16 v[188:191], v[208:209], v[94:95], 0 cbsz:2 abid:3\n"
            "ds_read_b64 v[208:209], v213\n"
            "v_rcp_f32 v157, v157\n"
            "v_rcp_f32 v158, v158\n"
            "buffer_load_dwordx4 v[88:91], v222, %[rsx], 0 offen offset:512\n"
            "buffer_load_dwordx4 v[92:95], v222, %[rsx], 0 offen offset:768\n"
            "v_rcp_f32 v159, v159\n"
            "v_pk_fma_f32 v[16:17], v[162:163], v[176:177], v[16:17] op_sel:[1,0,0]\n"
            "v_pk_fma_f32 v[18:19], v[162:163], v[178:179], v[18:19] op_sel:[1,0,0]\n"
            "v_pk_fma_f32 v[20:21], v[166:167], v[180:181], v[20:21] op_sel:[1,0,0]\n"
            "v_pk_fma_f32 v[22:23], v[166:167], v[182:183], v[22:23] op_sel:[1,0,0]\n"
            "v_pk_fma_f32 v[24:25], v[170:171], v[184:185], v[24:25] op_sel:[1,0,0]\n"
            "v_pk_fma_f32 v[26:27], v[170:171], v[186:187], v[26:27] op_sel:[1,0,0]\n"
            "v_pk_fma_f32 v[28:29], v[174:175], v[188:189], v[28:29] op_sel:[1,0,0]\n"
            "v_pk_fma_f32 v[30:31], v[174:175], v[190:191], v[30:31] op_sel:[1,0,0]\n"
            "v_pk_mul_f32 v[152:153], v[152:153], v[136:137]\n"
            "v_pk_mul_f32 v[154:155], v[154:155], v[138:139]\n"
            "v_pk_mul_f32 v[156:157], v[156:157], v[140:141]\n"
            "v_pk_mul_f32 v[158:159], v[158:159], v[142:143]\n"
            "s_cmp_eq_u32 %[rem], 4\n"
            "s_cbranch_scc1 Lf13_%=\n"
            "Lr13_%=:\n"
            "s_and_b32 %[tmp], %[lens], 0xff\n"
            "s_add_u32 %[rem], %[rem], %[tmp]\n"
            "s_lshr_b32 %[lens], %[lens], 8\n"
            "s_lshr_b32 %[fm], %[fm], 4\n"
            "s_bitcmp1_b32 %[fm], 3\n"
            "v_mov_b32 v215, v216\n"
            "s_cbranch_scc0 Lnext_%=\n"
            "s_sub_u32 %[pb], %[nst], 2\n"
            "s_bitset1_b32 %[evt], %[pb]\n"
            "s_branch Lnext_%=\n"
            "Lpub_%=:\n"
            "s_waitcnt vmcnt(17)\n"
            "v_mov_b32 v176, %[cnt]\n"
            "v_mov_b32 v177, 1\n"
            "s_mov_b64 exec, 1\n"
            "ds_add_rtn_u32 v177, v176, v177\n"
            "s_waitcnt lgkmcnt(0)\n"
            "v_readfirstlane_b32 %[tmp], v177\n"
            "s_mov_b64 exec, 0xff\n"
            "v_mov_b32 v176, %[tag]\n"
            "v_mov_b32 v177, %[tag]\n"
            "v_mov_b32 v178, %[tag]\n"
            "v_mov_b32 v179, %[tag]\n"
            "s_cmp_lg_u32 %[tmp], 7\n"
            "s_cbranch_scc1 Lpub2_%=\n"
            "global_store_dwordx4 v214, v[176:179], %[flg] sc1\n"
            "Lpub2_%=:\n"
            "s_mov_b64 exec, -1\n"
            "s_branch Lpr2_%=\n"
            "Lprio_%=:\n"
            "s_cmp_eq_u32 %[nst], %[pb]\n"
            "s_cbranch_scc1 Lpub_%=\n"
            "Lpr2_%=:\n"
            "s_cmp_eq_u32 %[nst], 1\n"
            "s_cbranch_scc1 Llast_%=\n"
            "s_lshl2_add_u32 %[tmp], %[nst], %[c1]\n"
            "v_bfe_u32 v176, v214, 4, 2\n"
            "v_sub_u32 v176, %[tmp], v176\n"
            "v_cmp_gt_u32 vcc, 4, v176\n"
            "v_add_u32 v176, %[c2], v176\n"
            "s_min_u32 %[tmp], %[nst], 15\n"
            "v_cndmask_b32 v177, 0, v236, vcc\n"
            "v_cndmask_b32 v178, 0, v237, vcc\n"
            "v_cmp_gt_u32 vcc, 4, v176\n"
            "v_add_u32 v210, v210, v177\n"
            "v_add_u32 v211, v211, v178\n"
            "v_cndmask_b32 v177, 0, v238, vcc\n"
            "v_cndmask_b32 v178, 0, v239, vcc\n"
            "v_add_u32 v210, v210, v177\n"
            "v_add_u32 v211, v211, v178\n"
            "s_lshl_b32 %[tmp], %[tmp], 1\n"
            "s_lshr_b32 %[tmp], %[ptab], %[tmp]\n"
            "s_bitcmp1_b32 %[tmp], 1\n"
            "s_cbranch_scc1 Lq2_%=\n"
            "s_bitcmp1_b32 %[tmp], 0\n"
            "s_setprio 0\n"
            "s_cbranch_scc0 Lq9_%=\n"
            "s_setprio 1\n"
            "s_branch Lq9_%=\n"
            "Lq2_%=:\n"
            "s_bitcmp1_b32 %[tmp], 0\n"
            "s_setprio 2\n"
            "s_cbranch_scc0 Lq9_%=\n"
            "s_setprio 3\n"
            "Lq9_%=:\n"
            "s_cmp_lg_u32 %[nst], 2\n"
            "s_cbranch_scc1 Ld2_%=\n"
            "v_mov_b32 v223, 0\n"
            "global_load_dword v223, v223, %[pfl] sc1\n"
            "s_branch Ld2_%=\n"
            "Lf00_%=:\n"
            "s_call_b64 %[ra], Lflush0_%=\n"
            "s_branch Lr00_%=\n"
            "Lf01_%=:\n"
            "s_call_b64 %[ra], Lflush0_%=\n"
            "s_branch Lr01_%=\n"
            "Lf02_%=:\n"
            "s_call_b64 %[ra], Lflush0_%=\n"
            "s_branch Lr02_%=\n"
            "Lf03_%=:\n"
            "s_call_b64 %[ra], Lflush0_%=\n"
            "s_branch Lr03_%=\n"
            "Lflush0_%=:\n"
            "v_mov_b32 v184, %[pfa]\n"
            "s_bitcmp0_b32 %[fm], 0\n"
            "s_cbranch_scc1 Lfa0_%=\n"
            "Lfw_%=:\n"
            "ds_read_b32 v185, v184\n"
            "ds_read_b128 v[176:179], v231 offset:0\n"
            "ds_read_b128 v[180:183], v231 offset:1024\n"
            "s_waitcnt lgkmcnt(0)\n"
            "v_cmp_eq_u32 vcc, 0, v185\n"
            "s_cbranch_vccnz Lfw_%=\n"
            "v_mfma_f32_4x4x4_16b_bf16 v[0:3], v[224:225], v[176:177], v[0:3]\n"
            "v_mfma_f32_4x4x4_16b_bf16 v[4:7], v[224:225], v[178:179], v[4:7]\n"
            "v_mfma_f32_4x4x4_16b_bf16 v[8:11], v[224:225], v[180:181], v[8:11]\n"
            "v_mfma_f32_4x4x4_16b_bf16 v[12:15], v[224:225], v[182:183], v[12:15]\n"
            "s_nop 1\n"
            "Lfa0_%=:\n"
            "v_cvt_pk_bf16_f32 v176, v0, v1\n"
            "v_cvt_pk_bf16_f32 v177, v2, v3\n"
            "v_cvt_pk_bf16_f32 v178, v4, v5\n"
            "v_cvt_pk_bf16_f32 v179, v6, v7\n"
            "v_cvt_pk_bf16_f32 v180, v8, v9\n"
            "v_cvt_pk_bf16_f32 v181, v10, v11\n"
            "v_cvt_pk_bf16_f32 v182, v12, v13\n"
            "v_cvt_pk_bf16_f32 v183, v14, v15\n"
            "s_bitcmp1_b32 %[fm], 1\n"
            "s_cbranch_scc1 Lpark0_%=\n"
            "buffer_store_dwordx4 v[176:179], v215, %[rsw], 0 offen offset:0 sc1\n"
            "buffer_store_dwordx4 v[180:183], v215, %[rsw], 0 offen offset:256 sc1\n"
            "s_branch Lzero0_%=\n"
            "Lpark0_%=:\n"
            "ds_write_b128 v231, v[176:179] offset:0\n"
            "ds_write_b128 v231, v[180:183] offset:1024\n"
            "Lzero0_%=:\n"
            "v_mov_b64 v[0:1], 0\n"
            "v_mov_b64 v[2:3], 0\n"
            "v_mov_b64 v[4:5], 0\n"
            "v_mov_b64 v[6:7], 0\n"
            "v_mov_b64 v[8:9], 0\n"
            "v_mov_b64 v[10:11], 0\n"
            "v_mov_b64 v[12:13], 0\n"
            "v_mov_b64 v[14:15], 0\n"
            "s_setpc_b64 %[ra]\n"
            "Lf10_%=:\n"
            "s_call_b64 %[ra], Lflush1_%=\n"
            "s_branch Lr10_%=\n"
            "Lf11_%=:\n"
            "s_call_b64 %[ra], Lflush1_%=\n"
            "s_branch Lr11_%=\n"
            "Lf12_%=:\n"
            "s_call_b64 %[ra], Lflush1_%=\n"
            "s_branch Lr12_%=\n"
            "Lf13_%=:\n"
            "s_call_b64 %[ra], Lflush1_%=\n"
            "s_branch Lr13_%=\n"
            "Lflush1_%=:\n"
            "v_mov_b32 v184, %[pfa]\n"
            "s_bitcmp0_b32 %[fm], 0\n"
            "s_cbranch_scc1 Lfa1_%=\n"
            "ds_read_b128 v[176:179], v231 offset:2048\n"
            "ds_read_b128 v[180:183], v231 offset:3072\n"
            "s_waitcnt lgkmcnt(0)\n"
            "v_mfma_f32_4x4x4_16b_bf16 v[16:19], v[224:225], v[176:177], v[16:19]\n"
            "v_mfma_f32_4x4x4_16b_bf16 v[20:23], v[224:225], v[178:179], v[20:23]\n"
            "v_mfma_f32_4x4x4_16b_bf16 v[24:27], v[224:225], v[180:181], v[24:27]\n"
            "v_mfma_f32_4x4x4_16b_bf16 v[28:31], v[224:225], v[182:183], v[28:31]\n"
            "s_nop 1\n"
            "Lfa1_%=:\n"
            "v_cvt_pk_bf16_f32 v176, v16, v17\n"
            "v_cvt_pk_bf16_f32 v177, v18, v19\n"
            "v_cvt_pk_bf16_f32 v178, v20, v21\n"
            "v_cvt_pk_bf16_f32 v179, v22, v23\n"
            "v_cvt_pk_bf16_f32 v180, v24, v25\n"
            "v_cvt_pk_bf16_f32 v181, v26, v27\n"
            "v_cvt_pk_bf16_f32 v182, v28, v29\n"
            "v_cvt_pk_bf16_f32 v183, v30, v31\n"
            "s_bitcmp1_b32 %[fm], 1\n"
            "s_cbranch_scc1 Lpark1_%=\n"
            "buffer_store_dwordx4 v[176:179], v215, %[rsw], 0 offen offset:512 sc1\n"
            "buffer_store_dwordx4 v[180:183], v215, %[rsw], 0 offen offset:768 sc1\n"
            "s_branch Lzero1_%=\n"
            "Lpark1_%=:\n"
            "ds_write_b128 v231, v[176:179] offset:2048\n"
            "ds_write_b128 v231, v[180:183] offset:3072\n"
            "ds_write_b32 v184, v184\n"
            "Lzero1_%=:\n"
            "v_mov_b64 v[16:17], 0\n"
            "v_mov_b64 v[18:19], 0\n"
            "v_mov_b64 v[20:21], 0\n"
            "v_mov_b64 v[22:23], 0\n"
            "v_mov_b64 v[24:25], 0\n"
            "v_mov_b64 v[26:27], 0\n"
            "v_mov_b64 v[28:29], 0\n"
            "v_mov_b64 v[30:31], 0\n"
            "s_setpc_b64 %[ra]\n"
            "Lspin_%=:\n"
            "s_mov_b32 %[lens], %[spin]\n"
            "Lsp1_%=:\n"
            "v_readfirstlane_b32 %[nst], v223\n"
            "s_sub_u32 %[lens], %[lens], 1\n"
            "s_cmp_eq_u32 %[lens], 0\n"
            "s_cbranch_scc1 Lsp2_%=\n"
            "s_cmp_eq_u32 %[nst], %[tag]\n"
            "s_cbranch_scc1 Lsp2_%=\n"
            "s_sleep 1\n"
            "v_mov_b32 v223, 0\n"
            "global_load_dword v223, v223, %[pfl] sc1\n"
            "s_waitcnt vmcnt(0)\n"
            "s_branch Lsp1_%=\n"
            "Lsp2_%=:\n"
            "buffer_load_dwordx4 v[32:35], v217, %[rsw], %[oagg] offen offset:0 sc1\n"
            "buffer_load_dwordx4 v[36:39], v217, %[rsw], %[oagg] offen offset:256 sc1\n"
            "buffer_load_dwordx4 v[48:51], v217, %[rsw], %[oagg] offen offset:512 sc1\n"
            "buffer_load_dwordx4 v[52:55], v217, %[rsw], %[oagg] offen offset:768 sc1\n"
            "s_branch Lgo_%=\n"
            "Llast_%=:\n"
            "s_waitcnt vmcnt(16)\n"
            "v_readfirstlane_b32 %[nst], v223\n"
            "v_mov_b32 v222, v217\n"
            "s_cmp_eq_u32 %[nst], %[tag]\n"
            "s_cbranch_scc1 Lp4_%=\n"
            "v_bfrev_b32 v222, 1\n"
            "v_mov_b32 v223, 0\n"
            "global_load_dword v223, v223, %[pfl] sc1\n"
            "Lp4_%=:\n"
            "v_mfma_f32_16x16x32_bf16 v[136:139], v[192:195], v[120:123], 0\n"
            "v_exp_f32 v160, v128\n"
            "v_exp_f32 v161, v129\n"
            "s_waitcnt vmcnt(13) lgkmcnt(0)\n"
            "v_mfma_f32_4x4x4_16b_bf16 v[176:179], v[208:209], v[32:33], 0 cbsz:2\n"
            "v_exp_f32 v162, v130\n"
            "v_mfma_f32_4x4x4_16b_bf16 v[180:183], v[208:209], v[34:35], 0 cbsz:2\n"
            "v_exp_f32 v163, v131\n"
            "v_mfma_f32_4x4x4_16b_bf16 v[184:187], v[208:209], v[36:37], 0 cbsz:2\n"
            "v_exp_f32 v164, v132\n"
            "v_mfma_f32_4x4x4_16b_bf16 v[188:191], v[208:209], v[38:39], 0 cbsz:2\n"
            "v_exp_f32 v165, v133\n"
            "v_exp_f32 v166, v134\n"
            "buffer_load_dwordx4 v[32:35], v222, %[rsw], %[oagg] offen offset:0 sc1\n"
            "buffer_load_dwordx4 v[36:39], v222, %[rsw], %[oagg] offen offset:256 sc1\n"
            "ds_read_b128 v[236:239], v228 offset:0\n"
            "ds_read_b128 v[240:243], v228 offset:1024\n"
            "ds_read_b128 v[244:247], v228 offset:2048\n"
            "ds_read_b128 v[248:251], v228 offset:3072\n"
            "ds_read_b128 v[96:99], v228 offset:4096\n"
            "v_exp_f32 v167, v135\n"
            "v_pk_fma_f32 v[0:1], v[144:145], v[176:177], v[0:1] op_sel_hi:[0,1,1]\n"
            "v_pk_fma_f32 v[2:3], v[144:145], v[178:179], v[2:3] op_sel_hi:[0,1,1]\n"
            "v_pk_fma_f32 v[4:5], v[148:149], v[180:181], v[4:5] op_sel_hi:[0,1,1]\n"
            "v_pk_fma_f32 v[6:7], v[148:149], v[182:183], v[6:7] op_sel_hi:[0,1,1]\n"
            "v_pk_fma_f32 v[8:9], v[152:153], v[184:185], v[8:9] op_sel_hi:[0,1,1]\n"
            "v_pk_fma_f32 v[10:11], v[152:153], v[186:187], v[10:11] op_sel_hi:[0,1,1]\n"
            "v_pk_fma_f32 v[12:13], v[156:157], v[188:189], v[12:13] op_sel_hi:[0,1,1]\n"
            "v_pk_fma_f32 v[14:15], v[156:157], v[190:191], v[14:15] op_sel_hi:[0,1,1]\n"
            "v_pk_add_f32 v[160:161], v[160:161], 1.0 op_sel_hi:[1,0]\n"
            "v_pk_add_f32 v[162:163], v[162:163], 1.0 op_sel_hi:[1,0]\n"
            "v_pk_add_f32 v[164:165], v[164:165], 1.0 op_sel_hi:[1,0]\n"
            "v_pk_add_f32 v[166:167], v[166:167], 1.0 op_sel_hi:[1,0]\n"
            "v_mfma_f32_16x16x32_bf16 v[140:143], v[192:195], v[124:127], 0\n"
            "v_rcp_f32 v160, v160\n"
            "v_rcp_f32 v161, v161\n"
            "v_mfma_f32_4x4x4_16b_bf16 v[176:179], v[208:209], v[48:49], 0 cbsz:2 abid:1\n"
            "v_rcp_f32 v162, v162\n"
            "v_mfma_f32_4x4x4_16b_bf16 v[180:183], v[208:209], v[50:51], 0 cbsz:2 abid:1\n"
            "v_rcp_f32 v163, v163\n"
            "v_mfma_f32_4x4x4_16b_bf16 v[184:187], v[208:209], v[52:53], 0 cbsz:2 abid:1\n"
            "v_rcp_f32 v164, v164\n"
            "v_mfma_f32_4x4x4_16b_bf16 v[188:191], v[208:209], v[54:55], 0 cbsz:2 abid:1\n"
            "v_rcp_f32 v165, v165\n"
            "v_rcp_f32 v166, v166\n"
            "buffer_load_dwordx4 v[48:51], v222, %[rsw], %[oagg] offen offset:512 sc1\n"
            "buffer_load_dwordx4 v[52:55], v222, %[rsw], %[oagg] offen offset:768 sc1\n"
            "ds_read_b128 v[100:103], v228 offset:5120\n"
            "ds_read_b128 v[104:107], v228 offset:6144\n"
            "ds_read_b128 v[108:111], v228 offset:7168\n"
            "ds_read_b128 v[112:115], v228 offset:8192\n"
            "ds_read_b128 v[116:119], v228 offset:9216\n"
            "ds_read_b128 v[196:199], v228 offset:10240\n"
            "v_rcp_f32 v167, v167\n"
            "v_pk_fma_f32 v[0:1], v[144:145], v[176:177], v[0:1] op_sel:[1,0,0]\n"
            "v_pk_fma_f32 v[2:3], v[144:145], v[178:179], v[2:3] op_sel:[1,0,0]\n"
            "v_pk_fma_f32 v[4:5], v[148:149], v[180:181], v[4:5] op_sel:[1,0,0]\n"
            "v_pk_fma_f32 v[6:7], v[148:149], v[182:183], v[6:7] op_sel:[1,0,0]\n"
            "v_pk_fma_f32 v[8:9], v[152:153], v[184:185], v[8:9] op_sel:[1,0,0]\n"
            "v_pk_fma_f32 v[10:11], v[152:153], v[186:187], v[10:11] op_sel:[1,0,0]\n"
            "v_pk_fma_f32 v[12:13], v[156:157], v[188:189], v[12:13] op_sel:[1,0,0]\n"
            "v_pk_fma_f32 v[14:15], v[156:157], v[190:191], v[14:15] op_sel:[1,0,0]\n"
            "v_pk_mul_f32 v[160:161], v[160:161], v[128:129]\n"
            "v_pk_mul_f32 v[162:163], v[162:163], v[130:131]\n"
            "v_pk_mul_f32 v[164:165], v[164:165], v[132:133]\n"
            "v_pk_mul_f32 v[166:167], v[166:167], v[134:135]\n"
            "v_exp_f32 v168, v136\n"
            "v_exp_f32 v169, v137\n"
            "s_waitcnt vmcnt(12)\n"
            "v_mfma_f32_4x4x4_16b_bf16 v[176:179], v[208:209], v[64:65], 0 cbsz:2 abid:2\n"
            "v_exp_f32 v170, v138\n"
            "v_mfma_f32_4x4x4_16b_bf16 v[180:183], v[208:209], v[66:67], 0 cbsz:2 abid:2\n"
            "v_exp_f32 v171, v139\n"
            "v_mfma_f32_4x4x4_16b_bf16 v[184:187], v[208:209], v[68:69], 0 cbsz:2 abid:2\n"
            "v_exp_f32 v172, v140\n"
            "v_mfma_f32_4x4x4_16b_bf16 v[188:191], v[208:209], v[70:71], 0 cbsz:2 abid:2\n"
            "v_exp_f32 v173, v141\n"
            "v_exp_f32 v174, v142\n"
            "ds_read_b128 v[200:203], v228 offset:11264\n"
            "ds_read_b128 v[204:207], v228 offset:12288\n"
            "ds_read_b128 v[232:235], v228 offset:13312\n"
            "v_exp_f32 v175, v143\n"
            "v_pk_fma_f32 v[0:1], v[146:147], v[176:177], v[0:1] op_sel_hi:[0,1,1]\n"
            "v_pk_fma_f32 v[2:3], v[146:147], v[178:179], v[2:3] op_sel_hi:[0,1,1]\n"
            "v_pk_fma_f32 v[4:5], v[150:151], v[180:181], v[4:5] op_sel_hi:[0,1,1]\n"
            "v_pk_fma_f32 v[6:7], v[150:151], v[182:183], v[6:7] op_sel_hi:[0,1,1]\n"
            "v_pk_fma_f32 v[8:9], v[154:155], v[184:185], v[8:9] op_sel_hi:[0,1,1]\n"
            "v_pk_fma_f32 v[10:11], v[154:155], v[186:187], v[10:11] op_sel_hi:[0,1,1]\n"
            "v_pk_fma_f32 v[12:13], v[158:159], v[188:189], v[12:13] op_sel_hi:[0,1,1]\n"
            "v_pk_fma_f32 v[14:15], v[158:159], v[190:191], v[14:15] op_sel_hi:[0,1,1]\n"
            "v_pk_add_f32 v[168:169], v[168:169], 1.0 op_sel_hi:[1,0]\n"
            "v_pk_add_f32 v[170:171], v[170:171], 1.0 op_sel_hi:[1,0]\n"
            "v_pk_add_f32 v[172:173], v[172:173], 1.0 op_sel_hi:[1,0]\n"
            "v_pk_add_f32 v[174:175], v[174:175], 1.0 op_sel_hi:[1,0]\n"
            "v_rcp_f32 v168, v168\n"
            "v_rcp_f32 v169, v169\n"
            "v_mfma_f32_4x4x4_16b_bf16 v[176:179], v[208:209], v[80:81], 0 cbsz:2 abid:3\n"
            "v_rcp_f32 v170, v170\n"
            "v_mfma_f32_4x4x4_16b_bf16 v[180:183], v[208:209], v[82:83], 0 cbsz:2 abid:3\n"
            "v_rcp_f32 v171, v171\n"
            "v_mfma_f32_4x4x4_16b_bf16 v[184:187], v[208:209], v[84:85], 0 cbsz:2 abid:3\n"
            "v_rcp_f32 v172, v172\n"
            "v_mfma_f32_4x4x4_16b_bf16 v[188:191], v[208:209], v[86:87], 0 cbsz:2 abid:3\n"
            "v_rcp_f32 v173, v173\n"
            "v_rcp_f32 v174, v174\n"
            "ds_read_b128 v[120:123], v228 offset:14336\n"
            "ds_read_b128 v[124:127], v228 offset:15360\n"
            "ds_read_b128 v[192:195], v228 offset:16384\n"
            "ds_read_b128 v[128:131], v228 offset:17408\n"
            "ds_read_b128 v[132:135], v228 offset:18432\n"
            "v_rcp_f32 v175, v175\n"
            "v_pk_fma_f32 v[0:1], v[146:147], v[176:177], v[0:1] op_sel:[1,0,0]\n"
            "v_pk_fma_f32 v[2:3], v[146:147], v[178:179], v[2:3] op_sel:[1,0,0]\n"
            "v_pk_fma_f32 v[4:5], v[150:151], v[180:181], v[4:5] op_sel:[1,0,0]\n"
            "v_pk_fma_f32 v[6:7], v[150:151], v[182:183], v[6:7] op_sel:[1,0,0]\n"
            "v_pk_fma_f32 v[8:9], v[154:155], v[184:185], v[8:9] op_sel:[1,0,0]\n"
            "v_pk_fma_f32 v[10:11], v[154:155], v[186:187], v[10:11] op_sel:[1,0,0]\n"
            "v_pk_fma_f32 v[12:13], v[158:159], v[188:189], v[12:13] op_sel:[1,0,0]\n"
            "v_pk_fma_f32 v[14:15], v[158:159], v[190:191], v[14:15] op_sel:[1,0,0]\n"
            "v_pk_mul_f32 v[168:169], v[168:169], v[136:137]\n"
            "v_pk_mul_f32 v[170:171], v[170:171], v[138:139]\n"
            "v_pk_mul_f32 v[172:173], v[172:173], v[140:141]\n"
            "v_pk_mul_f32 v[174:175], v[174:175], v[142:143]\n"
            "s_waitcnt vmcnt(8)\n"
            "v_mfma_f32_4x4x4_16b_bf16 v[176:179], v[208:209], v[40:41], 0 cbsz:2\n"
            "v_mfma_f32_4x4x4_16b_bf16 v[180:183], v[208:209], v[42:43], 0 cbsz:2\n"
            "v_mfma_f32_4x4x4_16b_bf16 v[184:187], v[208:209], v[44:45], 0 cbsz:2\n"
            "v_mfma_f32_4x4x4_16b_bf16 v[188:191], v[208:209], v[46:47], 0 cbsz:2\n"
            "ds_read_b128 v[136:139], v228 offset:19456\n"
            "ds_read_b128 v[140:143], v228 offset:20480\n"
            "ds_read_b128 v[144:147], v228 offset:21504\n"
            "ds_read_b128 v[148:151], v228 offset:22528\n"
            "ds_read_b128 v[152:155], v228 offset:23552\n"
            "ds_read_b128 v[156:159], v228 offset:24576\n"
            "s_nop 1\n"
            "v_pk_fma_f32 v[16:17], v[160:161], v[176:177], v[16:17] op_sel_hi:[0,1,1]\n"
            "v_pk_fma_f32 v[18:19], v[160:161], v[178:179], v[18:19] op_sel_hi:[0,1,1]\n"
            "v_pk_fma_f32 v[20:21], v[164:165], v[180:181], v[20:21] op_sel_hi:[0,1,1]\n"
            "v_pk_fma_f32 v[22:23], v[164:165], v[182:183], v[22:23] op_sel_hi:[0,1,1]\n"
            "v_pk_fma_f32 v[24:25], v[168:169], v[184:185], v[24:25] op_sel_hi:[0,1,1]\n"
            "v_pk_fma_f32 v[26:27], v[168:169], v[186:187], v[26:27] op_sel_hi:[0,1,1]\n"
            "v_pk_fma_f32 v[28:29], v[172:173], v[188:189], v[28:29] op_sel_hi:[0,1,1]\n"
            "v_pk_fma_f32 v[30:31], v[172:173], v[190:191], v[30:31] op_sel_hi:[0,1,1]\n"
            "s_cmp_le_u32 %[njl], 1\n"
            "s_cbranch_scc1 Ltail_%=\n"
            "v_mfma_f32_4x4x4_16b_bf16 v[176:179], v[208:209], v[56:57], 0 cbsz:2 abid:1\n"
            "v_mfma_f32_4x4x4_16b_bf16 v[180:183], v[208:209], v[58:59], 0 cbsz:2 abid:1\n"
            "v_mfma_f32_4x4x4_16b_bf16 v[184:187], v[208:209], v[60:61], 0 cbsz:2 abid:1\n"
            "v_mfma_f32_4x4x4_16b_bf16 v[188:191], v[208:209], v[62:63], 0 cbsz:2 abid:1\n"
            "s_nop 1\n"
            "v_pk_fma_f32 v[16:17], v[160:161], v[176:177], v[16:17] op_sel:[1,0,0]\n"
            "v_pk_fma_f32 v[18:19], v[160:161], v[178:179], v[18:19] op_sel:[1,0,0]\n"
            "v_pk_fma_f32 v[20:21], v[164:165], v[180:181], v[20:21] op_sel:[1,0,0]\n"
            "v_pk_fma_f32 v[22:23], v[164:165], v[182:183], v[22:23] op_sel:[1,0,0]\n"
            "v_pk_fma_f32 v[24:25], v[168:169], v[184:185], v[24:25] op_sel:[1,0,0]\n"
            "v_pk_fma_f32 v[26:27], v[168:169], v[186:187], v[26:27] op_sel:[1,0,0]\n"
            "v_pk_fma_f32 v[28:29], v[172:173], v[188:189], v[28:29] op_sel:[1,0,0]\n"
            "v_pk_fma_f32 v[30:31], v[172:173], v[190:191], v[30:31] op_sel:[1,0,0]\n"
            "s_cmp_le_u32 %[njl], 2\n"
            "s_cbranch_scc1 Ltail_%=\n"
            "s_waitcnt vmcnt(4)\n"
            "v_mfma_f32_4x4x4_16b_bf16 v[176:179], v[208:209], v[72:73], 0 cbsz:2 abid:2\n"
            "v_mfma_f32_4x4x4_16b_bf16 v[180:183], v[208:209], v[74:75], 0 cbsz:2 abid:2\n"
            "v_mfma_f32_4x4x4_16b_bf16 v[184:187], v[208:209], v[76:77], 0 cbsz:2 abid:2\n"
            "v_mfma_f32_4x4x4_16b_bf16 v[188:191], v[208:209], v[78:79], 0 cbsz:2 abid:2\n"
            "s_nop 1\n"
            "v_pk_fma_f32 v[16:17], v[162:163], v[176:177], v[16:17] op_sel_hi:[0,1,1]\n"
            "v_pk_fma_f32 v[18:19], v[162:163], v[178:179], v[18:19] op_sel_hi:[0,1,1]\n"
            "v_pk_fma_f32 v[20:21], v[166:167], v[180:181], v[20:21] op_sel_hi:[0,1,1]\n"
            "v_pk_fma_f32 v[22:23], v[166:167], v[182:183], v[22:23] op_sel_hi:[0,1,1]\n"
            "v_pk_fma_f32 v[24:25], v[170:171], v[184:185], v[24:25] op_sel_hi:[0,1,1]\n"
            "v_pk_fma_f32 v[26:27], v[170:171], v[186:187], v[26:27] op_sel_hi:[0,1,1]\n"
            "v_pk_fma_f32 v[28:29], v[174:175], v[188:189], v[28:29] op_sel_hi:[0,1,1]\n"
            "v_pk_fma_f32 v[30:31], v[174:175], v[190:191], v[30:31] op_sel_hi:[0,1,1]\n"
            "s_cmp_le_u32 %[njl], 3\n"
            "s_cbranch_scc1 Ltail_%=\n"
            "v_mfma_f32_4x4x4_16b_bf16 v[176:179], v[208:209], v[88:89], 0 cbsz:2 abid:3\n"
            "v_mfma_f32_4x4x4_16b_bf16 v[180:183], v[208:209], v[90:91], 0 cbsz:2 abid:3\n"
            "v_mfma_f32_4x4x4_16b_bf16 v[184:187], v[208:209], v[92:93], 0 cbsz:2 abid:3\n"
            "v_mfma_f32_4x4x4_16b_bf16 v[188:191], v[208:209], v[94:95], 0 cbsz:2 abid:3\n"
            "s_nop 1\n"
            "v_pk_fma_f32 v[16:17], v[162:163], v[176:177], v[16:17] op_sel:[1,0,0]\n"
            "v_pk_fma_f32 v[18:19], v[162:163], v[178:179], v[18:19] op_sel:[1,0,0]\n"
            "v_pk_fma_f32 v[20:21], v[166:167], v[180:181], v[20:21] op_sel:[1,0,0]\n"
            "v_pk_fma_f32 v[22:23], v[166:167], v[182:183], v[22:23] op_sel:[1,0,0]\n"
            "v_pk_fma_f32 v[24:25], v[170:171], v[184:185], v[24:25] op_sel:[1,0,0]\n"
            "v_pk_fma_f32 v[26:27], v[170:171], v[186:187], v[26:27] op_sel:[1,0,0]\n"
            "v_pk_fma_f32 v[28:29], v[174:175], v[188:189], v[28:29] op_sel:[1,0,0]\n"
            "v_pk_fma_f32 v[30:31], v[174:175], v[190:191], v[30:31] op_sel:[1,0,0]\n"
            "Ltail_%=:\n"
            "s_setprio 3\n"
            "s_waitcnt vmcnt(4)\n"
            "ds_read_b128 v[72:75], v228 offset:25600\n"
            "ds_read_b128 v[76:79], v228 offset:26624\n"
            "ds_read_b128 v[88:91], v228 offset:27648\n"
            "ds_read_b128 v[92:95], v228 offset:28672\n"
            "ds_read_b128 v[160:163], v228 offset:29696\n"
            "ds_read_b128 v[164:167], v228 offset:30720\n"
            "ds_read_b128 v[168:171], v228 offset:31744\n"
            "s_cmp_lg_u32 %[nst], %[tag]\n"
            "s_cbranch_scc1 Lspin_%=\n"
            "Lgo_%=:\n"
            "buffer_load_dwordx4 v[40:43], v218, %[rsw], %[oagg] offen offset:0 sc1\n"
            "buffer_load_dwordx4 v[44:47], v218, %[rsw], %[oagg] offen offset:256 sc1\n"
            "buffer_load_dwordx4 v[56:59], v218, %[rsw], %[oagg] offen offset:512 sc1\n"
            "buffer_load_dwordx4 v[60:63], v218, %[rsw], %[oagg] offen offset:768 sc1\n"
            "s_waitcnt vmcnt(4)\n"
            "v_mfma_f32_4x4x4_16b_bf16 v[0:3], v[224:225], v[32:33], v[0:3]\n"
            "v_mfma_f32_4x4x4_16b_bf16 v[4:7], v[224:225], v[34:35], v[4:7]\n"
            "v_mfma_f32_4x4x4_16b_bf16 v[8:11], v[224:225], v[36:37], v[8:11]\n"
            "v_mfma_f32_4x4x4_16b_bf16 v[12:15], v[224:225], v[38:39], v[12:15]\n"
            "v_mfma_f32_4x4x4_16b_bf16 v[16:19], v[224:225], v[48:49], v[16:19]\n"
            "v_mfma_f32_4x4x4_16b_bf16 v[20:23], v[224:225], v[50:51], v[20:23]\n"
            "v_mfma_f32_4x4x4_16b_bf16 v[24:27], v[224:225], v[52:53], v[24:27]\n"
            "v_mfma_f32_4x4x4_16b_bf16 v[28:31], v[224:225], v[54:55], v[28:31]\n"
            "s_nop 1\n"
            "v_cvt_pk_bf16_f32 v176, v0, v4\n"
            "v_cvt_pk_bf16_f32 v177, v8, v12\n"
            "v_cvt_pk_bf16_f32 v178, v16, v20\n"
            "v_cvt_pk_bf16_f32 v179, v24, v28\n"
            "ds_write_b128 v226, v[176:179] offset:0\n"
            "v_cvt_pk_bf16_f32 v180, v1, v5\n"
            "v_cvt_pk_bf16_f32 v181, v9, v13\n"
            "v_cvt_pk_bf16_f32 v182, v17, v21\n"
            "v_cvt_pk_bf16_f32 v183, v25, v29\n"
            "ds_write_b128 v226, v[180:183] offset:272\n"
            "v_cvt_pk_bf16_f32 v184, v2, v6\n"
            "v_cvt_pk_bf16_f32 v185, v10, v14\n"
            "v_cvt_pk_bf16_f32 v186, v18, v22\n"
            "v_cvt_pk_bf16_f32 v187, v26, v30\n"
            "ds_write_b128 v226, v[184:187] offset:544\n"
            "v_cvt_pk_bf16_f32 v188, v3, v7\n"
            "v_cvt_pk_bf16_f32 v189, v11, v15\n"
            "v_cvt_pk_bf16_f32 v190, v19, v23\n"
            "v_cvt_pk_bf16_f32 v191, v27, v31\n"
            "ds_write_b128 v226, v[188:191] offset:816\n"
            "ds_read_b128 v[176:179], v227 offset:0\n"
            "ds_read_b128 v[180:183], v227 offset:64\n"
            "ds_read_b128 v[184:187], v227 offset:128\n"
            "ds_read_b128 v[188:191], v227 offset:192\n"
            "s_waitcnt lgkmcnt(3)\n"
            "v_mfma_f32_16x16x32_bf16 v[0:3], v[176:179], v[236:239], 0\n"
            "v_mfma_f32_16x16x32_bf16 v[4:7], v[176:179], v[96:99], 0\n"
            "s_waitcnt lgkmcnt(2)\n"
            "v_mfma_f32_16x16x32_bf16 v[0:3], v[180:183], v[240:243], v[0:3]\n"
            "v_mfma_f32_16x16x32_bf16 v[4:7], v[180:183], v[100:103], v[4:7]\n"
            "s_waitcnt lgkmcnt(1)\n"
            "v_mfma_f32_16x16x32_bf16 v[0:3], v[184:187], v[244:247], v[0:3]\n"
            "v_mfma_f32_16x16x32_bf16 v[4:7], v[184:187], v[104:107], v[4:7]\n"
            "s_waitcnt lgkmcnt(0)\n"
            "v_mfma_f32_16x16x32_bf16 v[0:3], v[188:191], v[248:251], v[0:3]\n"
            "v_mfma_f32_16x16x32_bf16 v[4:7], v[188:191], v[108:111], v[4:7]\n"
            "v_mfma_f32_16x16x32_bf16 v[8:11], v[176:179], v[112:115], 0\n"
            "v_mfma_f32_16x16x32_bf16 v[12:15], v[176:179], v[204:207], 0\n"
            "v_mfma_f32_16x16x32_bf16 v[8:11], v[180:183], v[116:119], v[8:11]\n"
            "v_mfma_f32_16x16x32_bf16 v[12:15], v[180:183], v[232:235], v[12:15]\n"
            "v_mfma_f32_16x16x32_bf16 v[8:11], v[184:187], v[196:199], v[8:11]\n"
            "v_mfma_f32_16x16x32_bf16 v[12:15], v[184:187], v[120:123], v[12:15]\n"
            "v_mfma_f32_16x16x32_bf16 v[8:11], v[188:191], v[200:203], v[8:11]\n"
            "v_mfma_f32_16x16x32_bf16 v[12:15], v[188:191], v[124:127], v[12:15]\n"
            "v_cvt_pk_bf16_f32 v0, v0, v1\n"
            "v_cvt_pk_bf16_f32 v1, v2, v3\n"
            "v_cvt_pk_bf16_f32 v2, v4, v5\n"
            "v_cvt_pk_bf16_f32 v3, v6, v7\n"
            "buffer_store_dwordx4 v[0:3], v217, %[rso], 0 offen offset:0 sc1\n"
            "v_mfma_f32_16x16x32_bf16 v[16:19], v[176:179], v[192:195], 0\n"
            "v_mfma_f32_16x16x32_bf16 v[20:23], v[176:179], v[140:143], 0\n"
            "v_mfma_f32_16x16x32_bf16 v[16:19], v[180:183], v[128:131], v[16:19]\n"
            "v_mfma_f32_16x16x32_bf16 v[20:23], v[180:183], v[144:147], v[20:23]\n"
            "v_mfma_f32_16x16x32_bf16 v[16:19], v[184:187], v[132:135], v[16:19]\n"
            "v_mfma_f32_16x16x32_bf16 v[20:23], v[184:187], v[148:151], v[20:23]\n"
            "v_mfma_f32_16x16x32_bf16 v[16:19], v[188:191], v[136:139], v[16:19]\n"
            "v_mfma_f32_16x16x32_bf16 v[20:23], v[188:191], v[152:155], v[20:23]\n"
            "v_cvt_pk_bf16_f32 v8, v8, v9\n"
            "v_cvt_pk_bf16_f32 v9, v10, v11\n"
            "v_cvt_pk_bf16_f32 v10, v12, v13\n"
            "v_cvt_pk_bf16_f32 v11, v14, v15\n"
            "buffer_store_dwordx4 v[8:11], v217, %[rso], 0 offen offset:256 sc1\n"
            "v_mfma_f32_16x16x32_bf16 v[24:27], v[176:179], v[156:159], 0\n"
            "v_mfma_f32_16x16x32_bf16 v[28:31], v[176:179], v[92:95], 0\n"
            "v_mfma_f32_16x16x32_bf16 v[24:27], v[180:183], v[72:75], v[24:27]\n"
            "v_mfma_f32_16x16x32_bf16 v[28:31], v[180:183], v[160:163], v[28:31]\n"
            "v_mfma_f32_16x16x32_bf16 v[24:27], v[184:187], v[76:79], v[24:27]\n"
            "v_mfma_f32_16x16x32_bf16 v[28:31], v[184:187], v[164:167], v[28:31]\n"
            "v_mfma_f32_16x16x32_bf16 v[24:27], v[188:191], v[88:91], v[24:27]\n"
            "v_mfma_f32_16x16x32_bf16 v[28:31], v[188:191], v[168:171], v[28:31]\n"
            "v_cvt_pk_bf16_f32 v16, v16, v17\n"
            "v_cvt_pk_bf16_f32 v17, v18, v19\n"
            "v_cvt_pk_bf16_f32 v18, v20, v21\n"
            "v_cvt_pk_bf16_f32 v19, v22, v23\n"
            "buffer_store_dwordx4 v[16:19], v217, %[rso], 0 offen offset:512 sc1\n"
            "s_nop 2\n"
            "v_cvt_pk_bf16_f32 v24, v24, v25\n"
            "v_cvt_pk_bf16_f32 v25, v26, v27\n"
            "v_cvt_pk_bf16_f32 v26, v28, v29\n"
            "v_cvt_pk_bf16_f32 v27, v30, v31\n"
            "buffer_store_dwordx4 v[24:27], v217, %[rso], 0 offen offset:768 sc1\n"
            "s_cmp_lt_u32 %[two], 6\n"
            "s_cbranch_scc1 Lend_%=\n"
            "ds_read_b128 v[64:67], v231 offset:0\n"
            "ds_read_b128 v[68:71], v231 offset:1024\n"
            "ds_read_b128 v[80:83], v231 offset:2048\n"
            "ds_read_b128 v[84:87], v231 offset:3072\n"
            "s_waitcnt vmcnt(4) lgkmcnt(0)\n"
            "v_mfma_f32_4x4x4_16b_bf16 v[0:3], v[224:225], v[64:65], 0\n"
            "v_mfma_f32_4x4x4_16b_bf16 v[4:7], v[224:225], v[66:67], 0\n"
            "v_mfma_f32_4x4x4_16b_bf16 v[8:11], v[224:225], v[68:69], 0\n"
            "v_mfma_f32_4x4x4_16b_bf16 v[12:15], v[224:225], v[70:71], 0\n"
            "v_mfma_f32_4x4x4_16b_bf16 v[16:19], v[224:225], v[80:81], 0\n"
            "v_mfma_f32_4x4x4_16b_bf16 v[20:23], v[224:225], v[82:83], 0\n"
            "v_mfma_f32_4x4x4_16b_bf16 v[24:27], v[224:225], v[84:85], 0\n"
            "v_mfma_f32_4x4x4_16b_bf16 v[28:31], v[224:225], v[86:87], 0\n"
            "v_mfma_f32_4x4x4_16b_bf16 v[0:3], v[224:225], v[40:41], v[0:3]\n"
            "v_mfma_f32_4x4x4_16b_bf16 v[4:7], v[224:225], v[42:43], v[4:7]\n"
            "v_mfma_f32_4x4x4_16b_bf16 v[8:11], v[224:225], v[44:45], v[8:11]\n"
            "v_mfma_f32_4x4x4_16b_bf16 v[12:15], v[224:225], v[46:47], v[12:15]\n"
            "v_mfma_f32_4x4x4_16b_bf16 v[16:19], v[224:225], v[56:57], v[16:19]\n"
            "v_mfma_f32_4x4x4_16b_bf16 v[20:23], v[224:225], v[58:59], v[20:23]\n"
            "v_mfma_f32_4x4x4_16b_bf16 v[24:27], v[224:225], v[60:61], v[24:27]\n"
            "v_mfma_f32_4x4x4_16b_bf16 v[28:31], v[224:225], v[62:63], v[28:31]\n"
            "s_nop 1\n"
            "v_cvt_pk_bf16_f32 v176, v0, v4\n"
            "v_cvt_pk_bf16_f32 v177, v8, v12\n"
            "v_cvt_pk_bf16_f32 v178, v16, v20\n"
            "v_cvt_pk_bf16_f32 v179, v24, v28\n"
            "ds_write_b128 v226, v[176:179] offset:0\n"
            "v_cvt_pk_bf16_f32 v180, v1, v5\n"
            "v_cvt_pk_bf16_f32 v181, v9, v13\n"
            "v_cvt_pk_bf16_f32 v182, v17, v21\n"
            "v_cvt_pk_bf16_f32 v183, v25, v29\n"
            "ds_write_b128 v226, v[180:183] offset:272\n"
            "v_cvt_pk_bf16_f32 v184, v2, v6\n"
            "v_cvt_pk_bf16_f32 v185, v10, v14\n"
            "v_cvt_pk_bf16_f32 v186, v18, v22\n"
            "v_cvt_pk_bf16_f32 v187, v26, v30\n"
            "ds_write_b128 v226, v[184:187] offset:544\n"
            "v_cvt_pk_bf16_f32 v188, v3, v7\n"
            "v_cvt_pk_bf16_f32 v189, v11, v15\n"
            "v_cvt_pk_bf16_f32 v190, v19, v23\n"
            "v_cvt_pk_bf16_f32 v191, v27, v31\n"
            "ds_write_b128 v226, v[188:191] offset:816\n"
            "ds_read_b128 v[176:179], v227 offset:0\n"
            "ds_read_b128 v[180:183], v227 offset:64\n"
            "ds_read_b128 v[184:187], v227 offset:128\n"
            "ds_read_b128 v[188:191], v227 offset:192\n"
            "s_waitcnt lgkmcnt(3)\n"
            "v_mfma_f32_16x16x32_bf16 v[0:3], v[176:179], v[236:239], 0\n"
            "v_mfma_f32_16x16x32_bf16 v[4:7], v[176:179], v[96:99], 0\n"
            "s_waitcnt lgkmcnt(2)\n"
            "v_mfma_f32_16x16x32_bf16 v[0:3], v[180:183], v[240:243], v[0:3]\n"
            "v_mfma_f32_16x16x32_bf16 v[4:7], v[180:183], v[100:103], v[4:7]\n"
            "s_waitcnt lgkmcnt(1)\n"
            "v_mfma_f32_16x16x32_bf16 v[0:3], v[184:187], v[244:247], v[0:3]\n"
            "v_mfma_f32_16x16x32_bf16 v[4:7], v[184:187], v[104:107], v[4:7]\n"
            "s_waitcnt lgkmcnt(0)\n"
            "v_mfma_f32_16x16x32_bf16 v[0:3], v[188:191], v[248:251], v[0:3]\n"
            "v_mfma_f32_16x16x32_bf16 v[4:7], v[188:191], v[108:111], v[4:7]\n"
            "v_mfma_f32_16x16x32_bf16 v[8:11], v[176:179], v[112:115], 0\n"
            "v_mfma_f32_16x16x32_bf16 v[12:15], v[176:179], v[204:207], 0\n"
            "v_mfma_f32_16x16x32_bf16 v[8:11], v[180:183], v[116:119], v[8:11]\n"
            "v_mfma_f32_16x16x32_bf16 v[12:15], v[180:183], v[232:235], v[12:15]\n"
            "v_mfma_f32_16x16x32_bf16 v[8:11], v[184:187], v[196:199], v[8:11]\n"
            "v_mfma_f32_16x16x32_bf16 v[12:15], v[184:187], v[120:123], v[12:15]\n"
            "v_mfma_f32_16x16x32_bf16 v[8:11], v[188:191], v[200:203], v[8:11]\n"
            "v_mfma_f32_16x16x32_bf16 v[12:15], v[188:191], v[124:127], v[12:15]\n"
            "v_cvt_pk_bf16_f32 v0, v0, v1\n"
            "v_cvt_pk_bf16_f32 v1, v2, v3\n"
            "v_cvt_pk_bf16_f32 v2, v4, v5\n"
            "v_cvt_pk_bf16_f32 v3, v6, v7\n"
            "buffer_store_dwordx4 v[0:3], v218, %[rso], 0 offen offset:0 sc1\n"
            "v_mfma_f32_16x16x32_bf16 v[16:19], v[176:179], v[192:195], 0\n"
            "v_mfma_f32_16x16x32_bf16 v[20:23], v[176:179], v[140:143], 0\n"
            "v_mfma_f32_16x16x32_bf16 v[16:19], v[180:183], v[128:131], v[16:19]\n"
            "v_mfma_f32_16x16x32_bf16 v[20:23], v[180:183], v[144:147], v[20:23]\n"
            "v_mfma_f32_16x16x32_bf16 v[16:19], v[184:187], v[132:135], v[16:19]\n"
            "v_mfma_f32_16x16x32_bf16 v[20:23], v[184:187], v[148:151], v[20:23]\n"
            "v_mfma_f32_16x16x32_bf16 v[16:19], v[188:191], v[136:139], v[16:19]\n"
            "v_mfma_f32_16x16x32_bf16 v[20:23], v[188:191], v[152:155], v[20:23]\n"
            "v_cvt_pk_bf16_f32 v8, v8, v9\n"
            "v_cvt_pk_bf16_f32 v9, v10, v11\n"
            "v_cvt_pk_bf16_f32 v10, v12, v13\n"
            "v_cvt_pk_bf16_f32 v11, v14, v15\n"
            "buffer_store_dwordx4 v[8:11], v218, %[rso], 0 offen offset:256 sc1\n"
            "v_mfma_f32_16x16x32_bf16 v[24:27], v[176:179], v[156:159], 0\n"
            "v_mfma_f32_16x16x32_bf16 v[28:31], v[176:179], v[92:95], 0\n"
            "v_mfma_f32_16x16x32_bf16 v[24:27], v[180:183], v[72:75], v[24:27]\n"
            "v_mfma_f32_16x16x32_bf16 v[28:31], v[180:183], v[160:163], v[28:31]\n"
            "v_mfma_f32_16x16x32_bf16 v[24:27], v[184:187], v[76:79], v[24:27]\n"
            "v_mfma_f32_16x16x32_bf16 v[28:31], v[184:187], v[164:167], v[28:31]\n"
            "v_mfma_f32_16x16x32_bf16 v[24:27], v[188:191], v[88:91], v[24:27]\n"
            "v_mfma_f32_16x16x32_bf16 v[28:31], v[188:191], v[168:171], v[28:31]\n"
            "v_cvt_pk_bf16_f32 v16, v16, v17\n"
            "v_cvt_pk_bf16_f32 v17, v18, v19\n"
            "v_cvt_pk_bf16_f32 v18, v20, v21\n"
            "v_cvt_pk_bf16_f32 v19, v22, v23\n"
            "buffer_store_dwordx4 v[16:19], v218, %[rso], 0 offen offset:512 sc1\n"
            "s_nop 2\n"
            "v_cvt_pk_bf16_f32 v24, v24, v25\n"
            "v_cvt_pk_bf16_f32 v25, v26, v27\n"
            "v_cvt_pk_bf16_f32 v26, v28, v29\n"
            "v_cvt_pk_bf16_f32 v27, v30, v31\n"
            "buffer_store_dwordx4 v[24:27], v218, %[rso], 0 offen offset:768 sc1\n"
            "Lend_%=:\n"
            "s_waitcnt vmcnt(4)\n"
            : "+{v[32:63]}"(oxa), "+{v[64:95]}"(oxb), "+{v[96:127]}"(owr), "+{v[192:195]}"(efA), "+{v[196:199]}"(S), "+{v[200:203]}"(cgA), "+{v[208:223]}"(omi), "+{v[224:231]}"(ot),
              "+{v[232:235]}"(efN), "+{v[236:239]}"(jmp),
              [rem] "+s"(rem), [lens] "+s"(lens), [fm] "+s"(fm), [nst] "+s"(nst), [pb] "+s"(pb), [evt] "+s"(evt), [ra] "=&s"(ra), [tmp] "=&s"(tmp)
            : [rse] "s"(de), [rsx] "s"(dx), [rso] "s"(mkdesc(out, (unsigned)N * 1024u)), [rsw] "s"(mkdesc(ws, W_END)),
              [cnt] "s"(lds0 + L_MISC + 4), [pfa] "s"(lds0 + L_MISC + 16 + pw * 4), [tag] "s"(tag), [flg] "s"(ws + W_FLG + bx * 128), [pfl] "s"(ws + W_FLG + (bx ^ 4) * 128),
              [njl] "s"(((e2 - 1) & 3) + 1), [c1] "s"((w < 4 ? l2 : l1) - 4 * ((e2 + 3) >> 2) - 5), [c2] "s"(w < 4 ? l1 : l2), [two] "s"(w), [ptab] "s"((unsigned)__builtin_amdgcn_readfirstlane(w < 4 ? PTO : (w < 6 ? PTY : PTZ))), [oagg] "s"(W_AGG), [spin] "s"(SPIN_MAX)
            : "memory", "vcc", "scc", "v0", "v1", "v2", "v3", "v4", "v5", "v6", "v7", "v8", "v9", "v10", "v11", "v12", "v13", "v14", "v15", "v16", "v17", "v18", "v19", "v20", "v21", "v22", "v23", "v24", "v25", "v26", "v27", "v28", "v29", "v30", "v31", "v128", "v129", "v130", "v131", "v132", "v133", "v134", "v135", "v136", "v137", "v138", "v139", "v140", "v141", "v142", "v143", "v144", "v145", "v146", "v147", "v148", "v149", "v150", "v151", "v152", "v153", "v154", "v155", "v156", "v157", "v158", "v159", "v160", "v161", "v162", "v163", "v164", "v165", "v166", "v167", "v168", "v169", "v170", "v171", "v172", "v173", "v174", "v175", "v176", "v177", "v178", "v179", "v180", "v181", "v182", "v183", "v184", "v185", "v186", "v187", "v188", "v189", "v190", "v191", "v204", "v205", "v206", "v207", "v240", "v241", "v242", "v243", "v244", "v245", "v246", "v247", "v248", "v249", "v250", "v251");
    } else {
        if (bx == 0 && w == 4) flag_store(ws + W_CTL, tag);
        lds_wait(lds0 + SY_C3, 5);
        const __amdgpu_buffer_rsrc_t rso = __builtin_amdgcn_make_buffer_rsrc((void*)out, 0, N * 1024, 0x00020000);
#pragma unroll 1
        for (int it = 0; it < 10; ++it) {
            const int nl = (it < 5 ? 40 - 40 * half + 8 * it : 40 * half + 8 * (it - 5)) + w, node = node0 + nl;
            const bool nv = nl < npb;
            const unsigned ro = nv ? (unsigned)node * 1024u + (unsigned)lane * 16u : OOB_OFF;
            float a8[8] = {0.f, 0.f, 0.f, 0.f, 0.f, 0.f, 0.f, 0.f};
#pragma unroll 1
            for (int e0 = 0; e0 < (nv ? E : 0); e0 += 64) {
                const int ec = min(e0 + lane, E - 1);
                u64 mm = __ballot(e0 + lane < E && dst[ec] == node && (int)(((src[ec] >> 3) ^ (smask >> ((src[ec] >> 9) & 31))) & 1) == half);
#pragma unroll 1
                while (mm) {
                    const int e = e0 + __builtin_ctzll(mm);
                    mm &= mm - 1;
                    const unsigned short* fe = (const unsigned short*)ef + (size_t)e * DE_DIM;
                    const unsigned short* ye = (const unsigned short*)Y + (size_t)e * M_DIM;
                    const unsigned short* xp = x + (size_t)src[e] * (C_DIM * M_DIM) + lane * 8;
                    float z0 = 0.f, z1 = 0.f;
#pragma unroll 1
                    for (int f = 0; f < DE_DIM; ++f) { z0 += bf2f(fe[f]) * bf2f(w_rad[f * C_DIM + 2 * lane]); z1 += bf2f(fe[f]) * bf2f(w_rad[f * C_DIM + 2 * lane + 1]); }
                    const float r0 = z0 / (1.f + __expf(-z0)), r1 = z1 / (1.f + __expf(-z1));
#pragma unroll 1
                    for (int i = 0; i < 4; ++i) {
                        const float x0 = bf2f(xp[i]) * r0, x1 = bf2f(xp[4 + i]) * r1;
#pragma unroll
                        for (int o = 0; o < 4; ++o) {
                            float wv4 = 0.f;
#pragma unroll
                            for (int f = 0; f < 4; ++f) wv4 += bf2f(cg[(i * 4 + f) * 4 + o]) * bf2f(ye[f]);
                            a8[o] += x0 * wv4; a8[4 + o] += x1 * wv4;
                        }
                    }
                }
            }
            if (it >= 5) {
                const u32x4 pv = __builtin_amdgcn_raw_buffer_load_b128(rsw, ro, W_AGG, SC1);
#pragma unroll
                for (int i = 0; i < 4; ++i) { a8[2 * i] += __uint_as_float(pv[i] << 16); a8[2 * i + 1] += __uint_as_float(pv[i] & 0xffff0000u); }
            }
            const u32x4 rv = (u32x4){pack2bf(a8[0], a8[1]), pack2bf(a8[2], a8[3]), pack2bf(a8[4], a8[5]), pack2bf(a8[6], a8[7])};
            if (it < 5) {
                __builtin_amdgcn_raw_buffer_store_b128(rv, rsw, ro, W_AGG, SC1);
                if (it == 4) {
                    asm volatile("s_waitcnt vmcnt(0)" ::: "memory");
                    if (lds_arrive_rtn(lds0 + L_MISC + 4) == 7u) flag_store(ws + W_FLG + bx * 128, tag);
                    poll_tag(ws + W_FLG + (bx ^ 4) * 128, tag);
                }
            } else {
                *(u32x4*)(smem + L_PRV + w * 1024 + lane * 16) = rv;
#pragma unroll
                for (int i = 0; i < 8; ++i) a8[i] = 0.f;
#pragma unroll 1
                for (int c = 0; c < C_DIM; ++c) {
                    const unsigned short* ap = (const unsigned short*)(smem + L_PRV + w * 1024 + c * 8);
                    const float w0 = bf2f(w_mix[c * C_DIM + 2 * lane]), w1 = bf2f(w_mix[c * C_DIM + 2 * lane + 1]);
#pragma unroll
                    for (int m = 0; m < 4; ++m) { a8[m] += bf2f(ap[m]) * w0; a8[4 + m] += bf2f(ap[m]) * w1; }
                }
                __builtin_amdgcn_raw_buffer_store_b128((u32x4){pack2bf(a8[0], a8[1]), pack2bf(a8[2], a8[3]), pack2bf(a8[4], a8[5]), pack2bf(a8[6], a8[7])}, rso, ro, 0, 0);
            }
        }
    }
}

__global__ void kf_zero(float* __restrict__ agg, size_t n) {
    for (size_t i = (size_t)blockIdx.x * blockDim.x + threadIdx.x; i < n; i += (size_t)gridDim.x * blockDim.x) agg[i] = 0.f;
}
__global__ void kf_edges(const unsigned short* __restrict__ x, const int* __restrict__ src, const int* __restrict__ dst,
                         const unsigned short* __restrict__ Y, const unsigned short* __restrict__ ef,
                         const unsigned short* __restrict__ w_rad, const unsigned short* __restrict__ cg,
                         float* __restrict__ agg, int E) {
    const int ch = threadIdx.x;
    for (int e = blockIdx.x; e < E; e += gridDim.x) {
        float z = 0.f;
        for (int f = 0; f < DE_DIM; ++f) z += bf2f(ef[(size_t)e * DE_DIM + f]) * bf2f(w_rad[f * C_DIM + ch]);
        const float r = z / (1.f + __expf(-z));
        float a4[4] = {0.f, 0.f, 0.f, 0.f};
        for (int ii = 0; ii < 4; ++ii) {
            const float xv = bf2f(x[((size_t)src[e] * C_DIM + ch) * M_DIM + ii]) * r;
            for (int o = 0; o < 4; ++o) {
                float wv = 0.f;
                for (int f = 0; f < 4; ++f) wv += bf2f(cg[(ii * 4 + f) * 4 + o]) * bf2f(Y[(size_t)e * 4 + f]);
                a4[o] += xv * wv;
            }
        }
        for (int o = 0; o < 4; ++o) atomicAdd(&agg[((size_t)dst[e] * C_DIM + ch) * M_DIM + o], a4[o]);
    }
}
__global__ void kf_mix(const float* __restrict__ agg, const unsigned short* __restrict__ w_mix, unsigned short* __restrict__ out, int N) {
    const int d = threadIdx.x;
    for (int n = blockIdx.x; n < N; n += gridDim.x) {
        float a4[4] = {0.f, 0.f, 0.f, 0.f};
        for (int c = 0; c < C_DIM; ++c) {
            const float wv = bf2f(w_mix[c * C_DIM + d]);
            for (int m = 0; m < 4; ++m) a4[m] += bf2f(f2bf(agg[((size_t)n * C_DIM + c) * M_DIM + m])) * wv;
        }
        for (int m = 0; m < 4; ++m) out[((size_t)n * C_DIM + d) * M_DIM + m] = f2bf(a4[m]);
    }
}

extern "C" void kernel_launch(void* const* d_in, const int* in_sizes, int n_in,
                              void* d_out, int out_size, void* d_ws, size_t ws_size,
                              hipStream_t stream) {
    const unsigned short* x = (const unsigned short*)d_in[0];
    const int* src = (const int*)d_in[1];
    const int* dst = (const int*)d_in[2];
    const unsigned short* Y = (const unsigned short*)d_in[3];
    const unsigned short* ef = (const unsigned short*)d_in[4];
    const unsigned short* w_rad = (const unsigned short*)d_in[5];
    const unsigned short* cg = (const unsigned short*)d_in[6];
    const unsigned short* w_mix = (const unsigned short*)d_in[7];
    unsigned short* out = (unsigned short*)d_out;
    const int N = in_sizes[0] / (C_DIM * M_DIM);
    const int E = in_sizes[1];
    const int epb = (E + 254) / 255;
    const bool fast = N >= 256 && N <= NB_MAX * NPB_MAX && E > 0 && epb <= EPB_MAX && ws_size >= (size_t)W_END;
    if (!fast) {
        float* agg = (float*)d_ws;
        if (N <= 0) return;
        kf_zero<<<1024, 256, 0, stream>>>(agg, (size_t)N * C_DIM * M_DIM);
        if (E > 0) kf_edges<<<4096, C_DIM, 0, stream>>>(x, src, dst, Y, ef, w_rad, cg, agg, E);
        kf_mix<<<2048, C_DIM, 0, stream>>>(agg, w_mix, out, N);
        return;
    }
    const unsigned nb_magic = (unsigned)(((1ull << 39) + N - 1) / N);
    unsigned smask = 0;
    const int ng = (N + 511) / 512;
    int pw = 1;
    while (2 * pw * 4 < ng + 4) pw *= 2;
    for (int i = 0; i < ng; ++i) smask |= (unsigned)(i < 4 * pw ? ((i >> 2) ^ (i >= WLO * ng / 20 && i < WHI * ng / 20)) & 1 : i & 1) << i;
    const int r0 = ((N < 2048 * pw ? N : 2048 * pw) - 128) & ~63, r1 = r0 - (r0 >= 2048 ? 2048 : 128);
    k_fused<<<256, 512, 0, stream>>>(x, src, dst, (const u32x2*)Y, (const u32x4*)ef, w_rad, cg, w_mix, out, (unsigned char*)d_ws,
                                     N, E, epb, nb_magic, smask, (unsigned)(4 * 65536 / N), (unsigned)(r0 + (r0 >> 8 & 8)) * 1024u, (unsigned)(r1 + (r1 >> 8 & 8)) * 1024u);
}
```

```cpp
#include <hip/hip_runtime.h>
#include <hip/hip_bf16.h>
#include <stdint.h>

typedef __attribute__((ext_vector_type(8))) short bf16x8;
typedef __attribute__((ext_vector_type(4))) short bf16x4;
typedef __attribute__((ext_vector_type(4))) float f32x4;
typedef __attribute__((ext_vector_type(2))) unsigned u32x2;
typedef __attribute__((ext_vector_type(4))) unsigned u32x4;
typedef __attribute__((ext_vector_type(8))) unsigned u32x8;
typedef __attribute__((ext_vector_type(16))) unsigned u32x16;
typedef __attribute__((ext_vector_type(32))) unsigned u32x32;
typedef unsigned long long u64;

#define C_DIM 128
#define M_DIM 4
#define DE_DIM 16
#define NPB_MAX 80
#define NB_MAX 128
#define EPB_MAX 1280
#define NRND 7
#define LS 47
#define DEG_MAX 42
#define OOB_OFF 0x80000000u
#define SC1 16
#define NT 2
#define SPIN_MAX (1u << 22)
#define CODE_LINES 152
#define PTO 0xfffa9540u
#define PTY 0xffffa950u
#define PTZ 0xffffea54u
#define NT0 3
#define NT2 4
#define TLIST 4, 5, 10, 12
#define WLO 8
#define WHI 11
#define TNEAR 0
#define EHALF 0
#define HPRIO 3
#define PAYR 3
#define PBIAS 12
#define PRW "10"
#define PSL "1"
#define TKF 0x98765432u
#define TKB 0x23456789u
#define TKA 0x1010ffffu
#define EXTRA 0, 0, 5, 7

#define W_CTL 0
#define W_DEC 2048
#define W_FLG 4096
#define W_TAB (W_FLG + 32768)
#define W_RA (W_TAB + 256 * 256 * 8)
#define W_AGG (W_RA + 256 * EPB_MAX * 16)
#define W_END (W_AGG + NB_MAX * NPB_MAX * 1024)

#define SB() __builtin_amdgcn_sched_barrier(0)

static __device__ __forceinline__ float bf2f(unsigned short v) { return __uint_as_float(((unsigned)v) << 16); }
typedef __bf16 bf16v2 __attribute__((ext_vector_type(2)));
static __device__ __forceinline__ unsigned pack2bf(float lo, float hi) {
    bf16v2 r = (bf16v2){(__bf16)lo, (__bf16)hi};
    return __builtin_bit_cast(unsigned, r);
}
static __device__ __forceinline__ unsigned short f2bf(float v) { return (unsigned short)(pack2bf(v, 0.f) & 0xffffu); }
template <int CTRL, int RM> static __device__ __forceinline__ int dpp_add(int v) {
    return v + __builtin_amdgcn_update_dpp(0, v, CTRL, RM, 0xf, false);
}
static __device__ __forceinline__ int wave_incl_scan(int v) {
    v = dpp_add<0x111, 0xf>(v); v = dpp_add<0x112, 0xf>(v); v = dpp_add<0x114, 0xf>(v); v = dpp_add<0x118, 0xf>(v);
    v = dpp_add<0x142, 0xa>(v); v = dpp_add<0x143, 0xc>(v);
    return v;
}
template <int CTRL, int RM> static __device__ __forceinline__ unsigned dpp_max(unsigned v) {
    return max(v, (unsigned)__builtin_amdgcn_update_dpp(0, (int)v, CTRL, RM, 0xf, false));
}
static __device__ __forceinline__ unsigned wave_max_scan(unsigned v) {
    v = dpp_max<0x111, 0xf>(v); v = dpp_max<0x112, 0xf>(v); v = dpp_max<0x114, 0xf>(v); v = dpp_max<0x118, 0xf>(v);
    v = dpp_max<0x142, 0xa>(v); v = dpp_max<0x143, 0xc>(v);
    return v;
}
#define MAXS(c) "v_max_u32_dpp %0, %0, %0 " c "\n\tv_max_u32_dpp %1, %1, %1 " c "\n\tv_max_u32_dpp %2, %2, %2 " c "\n\t"
static __device__ __forceinline__ void wave_max_scan3(unsigned& a, unsigned& b, unsigned& c) {
    asm volatile("s_nop 1\n\t" MAXS("row_shr:1 row_mask:0xf bank_mask:0xf bound_ctrl:1") MAXS("row_shr:2 row_mask:0xf bank_mask:0xf bound_ctrl:1")
                 MAXS("row_shr:4 row_mask:0xf bank_mask:0xf bound_ctrl:1") MAXS("row_shr:8 row_mask:0xf bank_mask:0xf bound_ctrl:1")
                 MAXS("row_bcast:15 row_mask:0xa bank_mask:0xf") MAXS("row_bcast:31 row_mask:0xc bank_mask:0xf") "s_nop 0" : "+v"(a), "+v"(b), "+v"(c));
}
static __device__ __forceinline__ unsigned bsum(unsigned c, unsigned m, unsigned acc = 0u) { return __builtin_amdgcn_udot4(c, m, acc, false); }
static __device__ __forceinline__ unsigned bytesum2(unsigned c0, unsigned c1, unsigned m0, unsigned m1, unsigned acc = 0u) {
    return __builtin_amdgcn_udot4(c1, m1, __builtin_amdgcn_udot4(c0, m0, acc, false), false);
}
static __device__ __forceinline__ unsigned below_mask(int w, int k) {
    const int n = w - 4 * k;
    return n >= 4 ? 0x01010101u : (n <= 0 ? 0u : (0x01010101u & ((1u << (8 * n)) - 1u)));
}
static __device__ __forceinline__ void flag_store(const void* p, unsigned v) {
    unsigned t0, t1; u64 sv;
    asm volatile("v_mov_b32 %0, 0\n\tv_mov_b32 %1, %4\n\ts_mov_b64 %2, exec\n\ts_mov_b64 exec, 1\n\t"
                 "global_store_dword %0, %1, %3 sc1\n\ts_mov_b64 exec, %2" : "=&v"(t0), "=&v"(t1), "=&s"(sv) : "s"(p), "s"(v) : "memory");
}
static __device__ __forceinline__ unsigned lds_arrive_rtn(unsigned a) {
    unsigned t0, t1; u64 sv;
    asm volatile("v_mov_b32 %0, %3\n\tv_mov_b32 %1, 1\n\ts_mov_b64 %2, exec\n\ts_mov_b64 exec, 1\n\t"
                 "ds_add_rtn_u32 %1, %0, %1\n\ts_mov_b64 exec, %2\n\ts_waitcnt lgkmcnt(0)" : "=&v"(t0), "=&v"(t1), "=&s"(sv) : "s"(a) : "memory");
    return (unsigned)__builtin_amdgcn_readfirstlane((int)t1);
}
static __device__ __forceinline__ void lds_arrive(unsigned a, unsigned inc = 1u) {
    unsigned t0, t1; u64 sv;
    asm volatile("" "v_mov_b32 %0, %3\n\tv_mov_b32 %1, %4\n\ts_mov_b64 %2, exec\n\ts_mov_b64 exec, 1\n\t"
                 "ds_add_u32 %0, %1\n\ts_mov_b64 exec, %2" : "=&v"(t0), "=&v"(t1), "=&s"(sv) : "s"(a), "s"(inc) : "memory");
}
template <int SLEEP = 0, unsigned MASK = ~0u> static __device__ __forceinline__ unsigned lds_wait(unsigned a, unsigned n) {
    for (;;) {
        unsigned v;
        asm volatile("ds_read_b32 %0, %1\n\ts_waitcnt lgkmcnt(0)" : "=v"(v) : "v"(a) : "memory");
        v = (unsigned)__builtin_amdgcn_readfirstlane((int)v);
        if ((v & MASK) >= n) return v;
        if (SLEEP) __builtin_amdgcn_s_sleep(SLEEP);
    }
}
static __device__ __forceinline__ unsigned ld_agent(const void* p) { return __hip_atomic_load((const unsigned*)p, __ATOMIC_RELAXED, __HIP_MEMORY_SCOPE_AGENT); }
static __device__ __forceinline__ void poll_tag(const void* p, unsigned tag) {
#pragma unroll 1
    for (unsigned spin = 0; spin < SPIN_MAX; ++spin) {
        if (__all(ld_agent(p) == tag)) break;
        __builtin_amdgcn_s_sleep(2);
    }
    __builtin_amdgcn_fence(__ATOMIC_ACQUIRE, "wavefront");
}
static __device__ __forceinline__ void wg_barrier() {
    asm volatile("s_waitcnt lgkmcnt(0)" ::: "memory");
    __builtin_amdgcn_s_barrier();
    asm volatile("" ::: "memory");
}
static __device__ __forceinline__ f32x4 mfma4(u32x2 a, u32x2 b, f32x4 c) {
    return __builtin_amdgcn_mfma_f32_4x4x4bf16_1k(__builtin_bit_cast(bf16x4, a), __builtin_bit_cast(bf16x4, b), c, 0, 0, 0);
}
static __device__ __forceinline__ f32x4 mfma16(u32x4 a, u32x4 b, f32x4 c) {
    return __builtin_amdgcn_mfma_f32_16x16x32_bf16(__builtin_bit_cast(bf16x8, a), __builtin_bit_cast(bf16x8, b), c, 0, 0, 0);
}

static constexpr unsigned evt_of(unsigned pt) {
    unsigned e = 6u;
    for (int n = 2; n < 15; ++n) if (((pt >> (2 * n)) & 3u) != ((pt >> (2 * n + 2)) & 3u)) e |= 1u << n;
    return e;
}

static constexpr u64 len_pos(bool kept) {
    u64 r = 0;
    for (int w = 0; w < 8; ++w) {
        r |= (u64)(kept ? 8 + w : w) << (4 * (((kept ? TKF : TKB) >> (4 * w)) & 15u));
        if (w >= 4 && (w >= 6) == kept) r |= (u64)(kept ? w : 4 + w) << (4 * ((TKA >> (4 * w)) & 15u));
    }
    return r;
}
static constexpr unsigned ring_const() {
    constexpr int e[4] = {EXTRA};
    unsigned r = 0;
    for (int i = 0; i < 4; ++i) r |= (unsigned)((4 - PBIAS + (-3 * e[i] - e[(i + 3) & 3] + e[(i + 2) & 3] + 3 * e[(i + 1) & 3]) / 2) & 255) << (8 * i);
    return r;
}

#define L_MISC 0
#define L_KEY 96
#define L_AT 416
#define L_LEN 1056
#define L_PCNT 1280
#define L_CCNT (L_PCNT + 2048)
#define L_BNC (L_CCNT + 1280)
#define L_CG (L_BNC + 4096)
#define L_PRV (L_CG + 1024)
#define A_STG L_PRV
#define A_PTAB (A_STG + EPB_MAX * 16)
#define L_PK (L_PRV + 15 * 4096)
#define L_DUM L_PK
#define L_CTAB (L_PK + 2048)
#define L_MRK (L_PK + 8192)
#define TR_STR 4352
#define L_TR (L_PK + 16384)
#define L_WR L_TR
#define L_IMG (L_TR + 8 * TR_STR)
#define L_TOTAL (L_IMG + 32768)
static_assert(L_TOTAL <= 163840 && L_BNC <= 8192 && NPB_MAX * LS * 16 <= 15 * 4096, "LDS budget");
static_assert(L_PRV % 16 == 0 && L_PK % 16 == 0 && L_IMG % 16 == 0 && L_CG % 16 == 0 && L_CCNT % 16 == 0, "alignment");
enum { SY_CPY = 40, SY_C1 = 44, SY_S = 48, SY_C3 = 56, SY_RI = 60, SY_PRE = 64 };

static __device__ __forceinline__ u32x4 mkdesc(const void* p, unsigned bytes) {
    return (u32x4){(unsigned)(uintptr_t)p, (unsigned)((uintptr_t)p >> 32) & 0xffffu, bytes, 0x00020000u};
}

__global__ __launch_bounds__(512, 2) void k_fused(
    const unsigned short* __restrict__ x, const int* __restrict__ src, const int* __restrict__ dst, const u32x2* __restrict__ Y,
    const u32x4* __restrict__ ef, const unsigned short* __restrict__ w_rad, const unsigned short* __restrict__ cg,
    const unsigned short* __restrict__ w_mix, unsigned short* __restrict__ out, unsigned char* ws,
    int N, int E, int epb, unsigned nb_magic, unsigned smask, unsigned cmul, unsigned po0, unsigned po1)
{
    __shared__ __attribute__((aligned(256))) unsigned char smem[L_TOTAL];
    const int tid = threadIdx.x, lane = tid & 63;
    const int w = __builtin_amdgcn_readfirstlane(tid >> 6);
    const int bx = blockIdx.x;
    int half;
    const int b = (bx & 3) * 32 + (bx >> 3);
    const int g = lane >> 4, c16 = lane & 15;
    const int node0 = (b * N + 127) >> 7, npb = (((b + 1) * N + 127) >> 7) - node0;
    unsigned cp;
    {
        u64 pc;
        asm volatile("s_getpc_b64 %0" : "=s"(pc));
        const __amdgpu_buffer_rsrc_t rsc = __builtin_amdgcn_make_buffer_rsrc((void*)(pc & ~(u64)127), 0, CODE_LINES * 128, 0x00020000);
        cp = __builtin_amdgcn_raw_buffer_load_b32(rsc, (tid >> 3) == 32 ? ((bx >> 3) + lane * 32) * 128 : OOB_OFF, 0, 0);
        SB();
    }
    const unsigned lds0 = (unsigned)(uintptr_t)(__attribute__((address_space(3))) unsigned char*)smem;
    const __amdgpu_buffer_rsrc_t rsw = __builtin_amdgcn_make_buffer_rsrc((void*)ws, 0, W_END, 0x00020000);
    asm volatile("" :: "s"(x), "s"(src), "s"(dst), "s"(Y), "s"(ef), "s"(w_rad), "s"(cg), "s"(w_mix), "s"(out), "s"(ws), "s"(N), "s"(E), "s"(epb), "s"(nb_magic),
                 "s"(smask), "s"(cmul), "s"(po0), "s"(po1));
    const int base_e = bx * epb - epb;
    const int ne = base_e < 0 ? 0 : max(min(epb, E - base_e), 0);
    const int wd = 4 + ((w - 1 + (w >> 2)) & 3);
    auto task_at = [&](int t) -> unsigned {
        const unsigned q = (t == 0 ? TKF >> (4 * w) : (t == 1 ? TKB >> (4 * w) : TKA >> (4 * wd))) & 15u;
        return L_AT + ((t == 0 || (t == 2 && wd >= 6) ? 40 * half : 40 - 40 * half) + 4 * q) * 4;
    };
    const unsigned ri8 = (wd & 3) * 8;
    unsigned ta[6], cc, cd;
    int k0;
    auto set_ta = [&]() {
#pragma unroll
        for (int i = 0; i < 3; ++i) {
            const unsigned t = lds0 + task_at(i == 2 ? 0 : ((w < 4) == (i == 0) ? 2 : 1));
            ta[2 * i] = t + g * 4; ta[2 * i + 1] = t + (c16 & 12);
            asm volatile("" : "+v"(ta[2 * i]), "+v"(ta[2 * i + 1]));
        }
        cc = __builtin_rotateleft32(0xff0103fdu, ri8); cd = __builtin_rotateleft32(0xfe000204u, ri8); k0 = (int)(signed char)(ring_const() >> ri8);
        asm volatile("" : "+v"(cc), "+v"(cd), "+v"(k0));
    };
    unsigned tag;
    bool slow;
    u32x4 wr[8], cgA;
    auto load_wr = [&]() {
#pragma unroll
        for (int t = 0; t < 8; ++t) wr[t] = *(const u32x4*)(smem + L_WR + t * 1024 + lane * 16);
        cgA = *(const u32x4*)(smem + L_CG + lane * 16);
        asm volatile("" : "+v"(wr[0]), "+v"(wr[1]), "+v"(wr[2]), "+v"(wr[3]), "+v"(wr[4]), "+v"(wr[5]), "+v"(wr[6]), "+v"(wr[7]), "+v"(cgA));
    };

    __builtin_amdgcn_s_setprio(3);
    auto touch = [&](unsigned code) -> unsigned {
        if (code < 8u)
            return __builtin_amdgcn_raw_buffer_load_b32(__builtin_amdgcn_make_buffer_rsrc((void*)(ef + (size_t)base_e * 2), 0, code == 0u ? 0 : ne * 32, 0x00020000), lane * 128, (code - 1u) * 8192u, 0);
        const unsigned j = code < 16u ? (code - 8u) * 128u + b : (code - 16u) * 32u + (bx >> 3);
        return __builtin_amdgcn_raw_buffer_load_b32(__builtin_amdgcn_make_buffer_rsrc((void*)x, 0, N * 1024, 0x00020000), lane * 128,
                                                    (2u * j + ((half ^ (TNEAR ? j >> 7 : smask >> (j >> 5))) & 1u)) * 8192u, 0);
    };
    constexpr unsigned tl[] = {TLIST};
    const __amdgpu_buffer_rsrc_t rsm = __builtin_amdgcn_make_buffer_rsrc((void*)w_mix, 0, C_DIM * C_DIM * 2, 0x00020000);
    u32x4 mv[8];
    auto load_mv = [&]() {
#pragma unroll
        for (int j = 0; j < 8; ++j)
            mv[j] = __builtin_amdgcn_raw_buffer_load_b128(rsm, g * 512 + c16 * 16 + (w & 3) * 2048 + (j & 1) * 256, (j >> 1) * 8192, 0);
        SB();
    };
    auto make_img = [&]() {
#pragma unroll
        for (int i = 0; i < 4; ++i) {
            u32x4 p0, p1;
#pragma unroll
            for (int j = 0; j < 4; ++j) {
                p0[j] = __builtin_amdgcn_perm(mv[2 * j + 1][i], mv[2 * j][i], 0x05040100u);
                p1[j] = __builtin_amdgcn_perm(mv[2 * j + 1][i], mv[2 * j][i], 0x07060302u);
            }
            const unsigned ia = L_IMG + (c16 >> 2) * 8192 + (w & 3) * 1024 + g * 256 + (c16 & 3) * 64 + i * 16;
            *(u32x4*)(smem + ia) = p0;
            *(u32x4*)(smem + ia + 4096) = p1;
        }
    };
    unsigned t0v = 0u, cm = 0u, wv[8], cv[4], tv[NT2] = {};
    {
        const unsigned sh = 8u * (w & 3);
        unsigned d[3], sv[3], epoch, inc[3];
        u32x2 yv[3];
        const __amdgpu_buffer_rsrc_t rsd = __builtin_amdgcn_make_buffer_rsrc((void*)(dst + base_e), 0, ne * 4, 0x00020000);
        const __amdgpu_buffer_rsrc_t rss = __builtin_amdgcn_make_buffer_rsrc((void*)(src + base_e), 0, ne * 4, 0x00020000);
        const __amdgpu_buffer_rsrc_t rsy = __builtin_amdgcn_make_buffer_rsrc((void*)(Y + base_e), 0, ne * 8, 0x00020000);
        const unsigned le = (unsigned)(w * 160 + lane), le2 = lane < 32 ? le : 0x1000000u;
#pragma unroll
        for (int r = 0; r < 3; ++r) {
            d[r] = __builtin_amdgcn_raw_buffer_load_b32(rsd, (r < 2 ? le : le2) * 4 + 256 * r, 0, NT);
            sv[r] = __builtin_amdgcn_raw_buffer_load_b32(rss, (r < 2 ? le : le2) * 4 + 256 * r, 0, NT);
            SB();
        }
        epoch = __builtin_amdgcn_raw_buffer_load_b32(rsw, W_CTL, 0, 0);
        const unsigned pt = __builtin_amdgcn_raw_buffer_load_b32(__builtin_amdgcn_make_buffer_rsrc((void*)x, 0, bx * 8 + w == 8 + 7 ? N * 1024 : 0, 0x00020000),
                                                                 (lane < 32 ? po0 : po1) + (lane & 31) * 1024, 0, 0);
#pragma unroll
        for (int r = 0; r < 3; ++r) yv[r] = __builtin_amdgcn_raw_buffer_load_b64(rsy, (r < 2 ? le : le2) * 8 + 512 * r, 0, NT);
        SB();
        if (bx * 8 + w == 7) {
            unsigned s0, s1, v0, v1;
            const unsigned tm = (unsigned)__builtin_amdgcn_s_memtime() * 0x9e3779b1u;
#define RACE(r, o, a, b) "s_load_dword %2, %4, " o " offset:" a "\n\ts_load_dword %3, %4, " o " offset:" b "\n\ts_waitcnt lgkmcnt(1)\n\ts_sleep " PSL "\n\t" \
                         "s_getreg_b32 " r ", hwreg(HW_REG_IB_STS, 8, 1)\n\ts_waitcnt lgkmcnt(0)\n\t"
            asm volatile("s_load_dword %0, %4, %5 offset:0x10000\n\ts_load_dword %1, %4, %6 offset:0x10000\n\ts_waitcnt lgkmcnt(0)\n\ts_sleep " PRW "\n\t"
                         RACE("%0", "%5", "0", "0x2000") RACE("%1", "%6", "0x2000", "0") "s_cmp_lg_u32 %0, %1\n\ts_cbranch_scc1 Lr_%=\n\t" RACE("%1", "%5", "0x6000", "0x4000") "Lr_%=:"
                         : "=&s"(s0), "=&s"(s1), "=&s"(v0), "=&s"(v1) : "s"(x), "s"(po0 + (tm >> 29) * 1024u), "s"(po1 + (tm >> 26 & 7u) * 1024u) : "memory", "scc");
            SB();
            unsigned xc;
            asm volatile("s_getreg_b32 %0, hwreg(HW_REG_XCC_ID, 0, 2)" : "=s"(xc));
            unsigned hm = (0xf0f0f0u >> (xc + 4u * s1));
            const unsigned tg = epoch * 1664525u + 1013904223u;
            __builtin_amdgcn_raw_buffer_store_b128((u32x4){hm, tg, hm, tg}, rsw, lane < 8 ? W_DEC + lane * 16 : OOB_OFF, 0, SC1);
            for (int i = 0; i < 2; ++i) __builtin_amdgcn_raw_buffer_store_b128((u32x4){0u, tg, 0u, tg}, rsw, W_TAB + lane * 16 + i * 1024, 0, SC1);
        }
        {
            const unsigned k0 = (unsigned)tid - L_KEY / 16 < NPB_MAX / 4 ? 127u + L_KEY / 4 - 4u * tid : 0u, k1 = min(k0, 1u);
            *(u32x4*)(smem + tid * 16) = (u32x4){k0, k0 - k1, k0 - 2u * k1, k0 - 3u * k1};
        }
        if (tid < 64 * NRND) *(u32x4*)(smem + L_MRK + tid * 16) = (u32x4){0u, 0u, 0u, 0u};
        {
            const int hm = w >= 4 ? -1 : 0;
            const __amdgpu_buffer_rsrc_t rsa = __builtin_amdgcn_make_buffer_rsrc((void*)w_rad, 0, DE_DIM * C_DIM * 2 & hm, 0x00020000);
            const __amdgpu_buffer_rsrc_t rsg = __builtin_amdgcn_make_buffer_rsrc((void*)cg, 0, 128 & hm, 0x00020000);
#pragma unroll
            for (int j = 0; j < 8; ++j) wv[j] = __builtin_amdgcn_raw_buffer_load_b32(rsa, (g & 1) * 2048 + 64 * (w & 3) + 4 * c16 + 256 * j, 0, 0);
#pragma unroll
            for (int f = 0; f < 4; ++f) cv[f] = __builtin_amdgcn_raw_buffer_load_b32(rsg, (lane & 3) * 32 + (lane & 8u) / 2 + 8 * f, 0, 0);
            t0v = __builtin_amdgcn_raw_buffer_load_b32(__builtin_amdgcn_make_buffer_rsrc((void*)(ef + (size_t)base_e * 2), 0, min(ne * 32, NT0 * 8192), 0x00020000), (511 - tid) * 128, 0, 0);
            cm = __builtin_amdgcn_raw_buffer_load_b32(rsm, (tid >> 3) == 32 ? ((bx >> 3) * 8 + lane) * 128 : OOB_OFF, 0, 0);
        }
#pragma unroll
        for (int r = 0; r < 3; ++r) inc[r] = (int)(r < 2 ? le : le2) + r * 64 < ne ? 1u << sh : 0u;
        asm volatile("" : "+v"(inc[0]), "+v"(inc[1]), "+v"(inc[2]));
        SB();
        wg_barrier();
        if (bx != 0) {
        unsigned bin[3], rank[3], ba[3];
        asm volatile("" : "+v"(d[0]), "+v"(sv[0]), "+v"(d[1]), "+v"(sv[1]), "+v"(d[2]), "+v"(sv[2]));
#pragma unroll
        for (int r = 0; r < 3; ++r) {
            bin[r] = __umulhi(d[r], nb_magic);
            ba[r] = ((((sv[r] >> 3) ^ (smask >> ((sv[r] >> 9) & 31u))) & 1u) * 512u + bin[r] * 4u) & 1023u;
            rank[r] = atomicAdd((unsigned*)(smem + L_PCNT + (w >> 2) * 1024 + ba[r]), inc[r]);
        }
        unsigned tag = (unsigned)__builtin_amdgcn_readfirstlane((int)epoch) * 1664525u + 1013904223u;
        asm volatile("" : "+s"(tag));
        unsigned rw0[3], rw1[3];
#pragma unroll
        for (int r = 0; r < 3; ++r) {
            const unsigned sl = sv[r], nl = d[r] - ((__umul24(bin[r], (unsigned)N) + 127u) >> 7);
            rw0[r] = sl | (tag << 14);
            rw1[r] = (nl * 4u + ((__umul24(sl, cmul) >> 16) ^ ((nl >= 40u) == (ba[r] >= 512u) ? 3u : 0u))) | ((le + r * 64) << 9) | (tag & 0xfff00000u);
            asm volatile("" : "+v"(rw0[r]), "+v"(rw1[r]));
        }
        unsigned pa[3];
#pragma unroll
        for (int r = 0; r < 3; ++r) { pa[r] = A_PTAB + w * 512 + (ba[r] >> 1); asm volatile("" : "+v"(pa[r])); }
        unsigned m0 = below_mask(w, 0), m1 = below_mask(w, 1);
        asm volatile("" : "+s"(m0), "+s"(m1));
        unsigned tba = W_TAB + bx * 2048 + lane * 16 + (w & 1) * 1024;
        asm volatile("" : "+v"(tba));
        wg_barrier();
        u32x2 c[4];
#pragma unroll
        for (int i = 0; i < 4; ++i) c[i] = *(const u32x2*)(smem + L_PCNT + i * 512 + lane * 8);
        unsigned t[4], run[4];
#pragma unroll
        for (int i = 0; i < 4; ++i) t[i] = bsum(c[i >> 1][i & 1], 0x01010101u);
        asm volatile("" : "+v"(t[0]), "+v"(t[1]), "+v"(t[2]), "+v"(t[3]));
#pragma unroll
        for (int i = 0; i < 4; ++i) t[i] = bsum(c[2 + (i >> 1)][i & 1], 0x01010101u, t[i]);
        asm volatile("" : "+v"(t[0]), "+v"(t[1]), "+v"(t[2]), "+v"(t[3]));
        const unsigned ts = (t[0] + t[1]) | (t[2] + t[3]) << 16;
        const unsigned ti = (unsigned)wave_incl_scan((int)ts), tx = ti - ts;
        run[0] = tx & 0xffffu; run[2] = (tx >> 16) + ((unsigned)__builtin_amdgcn_readlane((int)ti, 63) & 0xffffu);
        run[1] = run[0] + t[0]; run[3] = run[2] + t[2];
        unsigned o[4];
#pragma unroll
        for (int i = 0; i < 4; ++i) o[i] = bytesum2(c[i >> 1][i & 1], c[2 + (i >> 1)][i & 1], m0, m1, run[i]);
        *(unsigned*)(smem + A_PTAB + w * 512 + lane * 4) = o[0] | (o[1] << 16);
        *(unsigned*)(smem + A_PTAB + w * 512 + 256 + lane * 4) = o[2] | (o[3] << 16);
        unsigned pos[3];
        asm volatile("" ::: "memory");
#pragma unroll
        for (int r = 0; r < 3; ++r) pos[r] = *(const unsigned short*)(smem + pa[r]);
        SB();
        if ((w & 6) == 4) {
            const unsigned ra = (w & 1) == 0 ? run[0] | (t[0] << 16) : run[1] | (t[1] << 16), rb = (w & 1) == 0 ? run[2] | (t[2] << 16) : run[3] | (t[3] << 16);
            __builtin_amdgcn_raw_buffer_store_b128((u32x4){ra, tag, rb, tag}, rsw, tba, 0, SC1);
        }
#pragma unroll
        for (int r = 0; r < 3; ++r) pos[r] = A_STG + (pos[r] + ((rank[r] >> sh) & 0xffu)) * 16;
        asm volatile("" : "+v"(pos[0]), "+v"(pos[1]), "+v"(pos[2]));
#pragma unroll
        for (int r = 0; r < 3; ++r) pos[r] = inc[r] != 0u ? pos[r] : L_DUM + lane * 16;
#pragma unroll
        for (int r = 0; r < 3; ++r) *(u32x4*)(smem + pos[r]) = (u32x4){yv[r].x, rw0[r], yv[r].y, rw1[r]};
        }
        asm volatile("" : "+v"(epoch) : "v"(pt));
        tag = (unsigned)__builtin_amdgcn_readfirstlane((int)epoch) * 1664525u + 1013904223u;
        wg_barrier();
        const __amdgpu_buffer_rsrc_t rsr = __builtin_amdgcn_make_buffer_rsrc((void*)(ws + W_RA + bx * (EPB_MAX * 16)), 0, (ne * 16 + 127) & ~127, 0x00020000);
        u32x4 va[3];
#pragma unroll
        for (int i = 0; i < 3; ++i) va[i] = *(const u32x4*)(smem + A_STG + (tid ^ 256) * 16 + 8192 * i);
#pragma unroll
        for (int i = 0; i < 3; ++i) __builtin_amdgcn_raw_buffer_store_b128(va[i], rsr, (tid ^ 256) * 16, 8192 * i, SC1);
        lds_arrive(lds0 + SY_CPY);
    }
    u32x4 dx = mkdesc(x, (unsigned)N * 1024u), de = mkdesc(ef, (unsigned)E * 32u);
    asm volatile("" : "+s"(dx), "+s"(de));
    if (w < 4) {
        asm volatile("" :: "v"(t0v), "v"(cm), "v"(wv[0]), "v"(wv[7]), "v"(cv[3]));
        const unsigned sh = 8u * w, mb = 0x01010101u & ((1u << sh) - 1u);
        const unsigned T0 = tag << 14, T1 = tag & 0xfff00000u;
        const unsigned to = W_TAB + tid * 2048 + (b & 1) * 1024 + (b >> 1) * 16;
        u32x4 en;
        u32x2 dec;
        {
            __builtin_amdgcn_s_sleep(4);
            en = __builtin_amdgcn_raw_buffer_load_b128(rsw, to, 0, SC1);
            dec = __builtin_amdgcn_raw_buffer_load_b64(rsw, W_DEC, 0, SC1);
        }
#pragma unroll 1
        for (unsigned spin = 0; spin < SPIN_MAX; ++spin) {
            if (__builtin_amdgcn_ballot_w64(((en.y ^ tag) | (en.w ^ tag) | (dec.y ^ tag)) != 0u) == 0ull) break;
            __builtin_amdgcn_s_sleep(2);
            asm volatile("" ::: "memory");
            en = __builtin_amdgcn_raw_buffer_load_b128(rsw, to, 0, SC1);
            dec = __builtin_amdgcn_raw_buffer_load_b64(rsw, W_DEC, 0, SC1);
        }
        half = (__builtin_amdgcn_readfirstlane((int)dec.x) >> (bx & 7)) & 1;
        if (half) en.x = en.z;
        const unsigned rc = en.x >> 16;
        const unsigned incl = (unsigned)wave_incl_scan((int)rc);
        const int twf = __builtin_amdgcn_readlane((int)incl, 63), tw = twf <= 64 * NRND ? twf : 0;
        const bool more = tw > 64 * (NRND - 1);
        unsigned ma = L_MRK + w * (256 * NRND) + (incl - rc) * 4;
        asm volatile("" : "+v"(ma));
        *(unsigned*)(smem + (((rc != 0u) & (incl <= 64u * NRND)) ? ma : L_DUM + lane * 4)) = (unsigned)lane * EPB_MAX + (en.x & 0xffffu) - (incl - rc);
        unsigned ra[NRND];
        asm volatile("" ::: "memory");
#pragma unroll
        for (int r = 0; r < NRND; ++r) ra[r] = *(const unsigned*)(smem + L_MRK + w * (256 * NRND) + r * 256 + lane * 4);
        unsigned carry = 0u;
        u32x4 A[NRND];
#pragma unroll
        for (int r = 0; r < NRND; ++r) {
            if (r == NRND - 1) { if (more) ra[r] = wave_max_scan(ra[r]); asm volatile("" : "+v"(ra[r])); }
            else if (r % 3 == 0) wave_max_scan3(ra[r], ra[r + 1], ra[r + 2]);
            const unsigned m = max(ra[r], carry);
            carry = max(carry, (unsigned)__builtin_amdgcn_readlane((int)ra[r], 63));
            ra[r] = r * 64 + lane < tw ? (m + (unsigned)(r * 64 + lane)) * 16u : OOB_OFF >> 1;
            A[r] = __builtin_amdgcn_raw_buffer_load_b128(rsw, ra[r], W_RA + 64 * w * (EPB_MAX * 16), SC1);
        }
        SB();
        lds_arrive(lds0 + SY_RI, 16u + half);
        unsigned pe[NRND], inc[NRND], inck[NRND], lim[NRND];
#pragma unroll
        for (int r = 0; r < NRND; ++r) {
            pe[r] = __umul24(__umulhi(ra[r], 209716u) + 64u * w - 1u, (unsigned)epb * 32u);
            inc[r] = ra[r] < 0x40000000u ? 1u << sh : 0u;
            inck[r] = ra[r] < 0x40000000u ? 128u : 0u;
            lim[r] = ra[r] < 0x40000000u ? 16383u : ~0u;
            asm volatile("" : "+v"(pe[r]), "+v"(inc[r]), "+v"(inck[r]), "+v"(lim[r]));
        }
        if (twf > 64 * NRND) lds_arrive(lds0 + SY_S, 256u);
        set_ta();
        lds_wait(lds0 + SY_PRE, 4);
        load_wr();
        SB();
        unsigned rank[NRND], nc[NRND];
        auto slot = [&](int r) {
            nc[r] = inc[r] != 0u ? L_CTAB + w * 640 + nc[r] * 2 : L_MISC + 8;
            A[r] = (u32x4){A[r].y << 10, ((A[r].w >> 4) & 0xffe0u) + pe[r], A[r].x, A[r].z};
            asm volatile("" : "+v"(nc[r]), "+v"(A[r]));
            SB();
        };
#pragma unroll
        for (int r = 0; r < NRND; ++r) {
            if (r == NRND - 1 && !more) { rank[r] = 0u; nc[r] = 0u; continue; }
            A[r].y ^= T0; A[r].w ^= T1;
            if (__builtin_expect(__builtin_amdgcn_ballot_w64((A[r].y | (A[r].w >> 6)) > lim[r]) != 0ull, 0)) {
#pragma unroll 1
                for (unsigned spin = 0; spin < SPIN_MAX; ++spin) {
                    asm volatile("" ::: "memory");
                    A[r] = __builtin_amdgcn_raw_buffer_load_b128(rsw, ra[r], W_RA + 64 * w * (EPB_MAX * 16), SC1);
                    A[r].y ^= T0; A[r].w ^= T1;
                    if (__builtin_amdgcn_ballot_w64((A[r].y | (A[r].w >> 6)) > lim[r]) == 0ull) break;
                }
            }
            nc[r] = (r < 4 || inc[r] != 0u) ? A[r].w & 0x1ffu : (unsigned)lane;
            rank[r] = atomicAdd((unsigned*)(smem + L_CCNT + nc[r] * 4), inc[r]);
            atomicAdd((unsigned*)(smem + L_KEY + (nc[r] & 0x1fcu)), inck[r]);
            SB();
            if (r < PAYR) slot(r);
        }
        lds_arrive(lds0 + SY_C1);
        unsigned d0 = lane * LS, d1 = (64 + c16) * LS, ca0 = L_CTAB + w * 640 + lane * 8, ca1 = L_CTAB + w * 640 + (64 + c16) * 8;
        asm volatile("" : "+v"(d0), "+v"(d1), "+v"(ca0), "+v"(ca1));
#pragma unroll
        for (int r = PAYR; r < NRND; ++r) slot(r);
#pragma unroll
        for (int r = 0; r < NRND; ++r) {
            rank[r] = inc[r] != 0u ? (rank[r] >> sh) & 0xffu : (L_DUM - L_PRV) / 16 + lane;
            asm volatile("" : "+v"(rank[r]));
        }
        {
            u32x4 c0, c1;
            unsigned cn;
            do {
                asm volatile("ds_read_b32 %0, %3 offset:%c6\n\tds_read_b128 %1, %4 offset:%c7\n\tds_read_b128 %2, %5 offset:%c8\n\ts_waitcnt lgkmcnt(0)"
                             : "=&v"(cn), "=&v"(c0), "=&v"(c1) : "v"(lds0), "v"(lds0 + lane * 16), "v"(lds0 + c16 * 16), "i"(SY_C1), "i"(L_CCNT), "i"(L_CCNT + 1024) : "memory");
            } while (__builtin_amdgcn_readfirstlane((int)cn) < 4);
            unsigned p0[4], p1[4];
#pragma unroll
            for (int c = 0; c < 4; ++c) {
                p0[c] = bsum(c0[c], mb, d0); p1[c] = bsum(c1[c], mb, d1);
                if (c < 3) { d0 = bsum(c0[c], 0x01010101u, d0); d1 = bsum(c1[c], 0x01010101u, d1); }
            }
            *(u32x2*)(smem + ca0) = (u32x2){p0[0] | (p0[1] << 16), p0[2] | (p0[3] << 16)};
            *(u32x2*)(smem + ca1) = (u32x2){p1[0] | (p1[1] << 16), p1[2] | (p1[3] << 16)};
        }
        asm volatile("" ::: "memory");
#pragma unroll
        for (int r = 0; r < NRND; ++r) nc[r] = *(const unsigned short*)(smem + nc[r]);
        SB();
#pragma unroll
        for (int r = 0; r < NRND; ++r) {
            if (r == NRND - 1 && !more) continue;
            *(u32x4*)(smem + L_PRV + (nc[r] + rank[r]) * 16) = A[r];
        }
        lds_arrive(lds0 + SY_C3);
    } else {
        const int q = w - 4, t2 = tid - 256;
        __builtin_amdgcn_s_setprio(2);
        load_mv();
        {
            float f0[8], f1[8];
#pragma unroll
            for (int j = 0; j < 8; ++j) {
                const float a = __uint_as_float(wv[j] << 16) * -1.44269504088896340736f, c = __uint_as_float(wv[j] & 0xffff0000u) * -1.44269504088896340736f;
                const unsigned hp = g < 2 ? 0u : pack2bf(a, c);
                f0[j] = a - __uint_as_float(hp << 16);
                f1[j] = c - __uint_as_float(hp & 0xffff0000u);
            }
            *(u32x4*)(smem + L_WR + (2 * q) * 1024 + lane * 16) = (u32x4){pack2bf(f0[0], f0[1]), pack2bf(f0[2], f0[3]), pack2bf(f0[4], f0[5]), pack2bf(f0[6], f0[7])};
            *(u32x4*)(smem + L_WR + (2 * q + 1) * 1024 + lane * 16) = (u32x4){pack2bf(f1[0], f1[1]), pack2bf(f1[2], f1[3]), pack2bf(f1[4], f1[5]), pack2bf(f1[6], f1[7])};
            float a[4];
#pragma unroll
            for (int f = 0; f < 4; ++f) a[f] = __uint_as_float((lane & 4) ? cv[f] & 0xffff0000u : cv[f] << 16) * -0.69314718055994530942f;
            const unsigned h0 = pack2bf(a[0], a[1]), h1 = pack2bf(a[2], a[3]);
            *(u32x4*)(smem + L_CG + lane * 16) = (u32x4){h0, h1, pack2bf(a[0] - __uint_as_float(h0 << 16), a[1] - __uint_as_float(h0 & 0xffff0000u)),
                                                        pack2bf(a[2] - __uint_as_float(h1 << 16), a[3] - __uint_as_float(h1 & 0xffff0000u))};
        }
        lds_wait(lds0 + SY_CPY, 8);
#pragma unroll
        for (int i = 0; i < 15; ++i) *(u32x4*)(smem + L_PRV + (t2 + 256 * i) * 16) = (u32x4){OOB_OFF, OOB_OFF, 0u, 0u};
        SB();
        asm volatile("" :: "v"(cp), "v"(cm), "v"(t0v));
        make_img();
        lds_arrive(lds0 + SY_PRE);
        lds_wait(lds0 + SY_PRE, 4);
        load_wr();
        if (w == 7) {
            lds_arrive(lds0 + SY_C3);
            half = (lds_wait<1>(lds0 + SY_RI, 64) & 15u) != 0u;
#pragma unroll
            for (int i = 0; i < NT2; ++i) tv[i] = touch(tl[i]);
            SB();
            set_ta();
        } else {
            half = (lds_wait<1>(lds0 + SY_RI, 16) & 15u) != 0u;
            set_ta();
            const int n5 = min(27 * q + (lane >> 1), NPB_MAX - 1);
            const bool kp = (n5 >= 40) == (half != 0);
            u64 lpos = kp ? len_pos(true) : len_pos(false);
            unsigned an = lds0 + n5 * 4, ak = lds0 + L_KEY + (n5 >= 40 ? 160 : 0) + (lane & 1) * 80;
            unsigned aa = L_AT + (n5 >= 40 ? 160 : 0), al = L_LEN + (kp ? 4 : 0), av = (unsigned)n5 | (lds0 + L_PRV + n5 * (LS * 16)) << 8;
            asm volatile("" : "+v"(an), "+v"(ak), "+v"(aa), "+v"(al), "+v"(av), "+v"(lpos));
            lds_wait<1>(lds0 + SY_C1, 3);
            __builtin_amdgcn_s_setprio(HPRIO);
            unsigned kn, cn;
            u32x4 dk[5];
            do {
                asm volatile("ds_read_b32 %0, %7 offset:%c10\n\tds_read_b32 %1, %8 offset:%c11\n\tds_read_b128 %2, %9\n\tds_read_b128 %3, %9 offset:16\n\tds_read_b128 %4, %9 offset:32\n\t"
                             "ds_read_b128 %5, %9 offset:48\n\tds_read_b128 %6, %9 offset:64\n\ts_waitcnt lgkmcnt(0)"
                             : "=&v"(cn), "=&v"(kn), "=&v"(dk[0]), "=&v"(dk[1]), "=&v"(dk[2]), "=&v"(dk[3]), "=&v"(dk[4]) : "v"(lds0), "v"(an), "v"(ak), "i"(SY_C1), "i"(L_KEY) : "memory");
            } while (__builtin_amdgcn_readfirstlane((int)cn) < 4);
            unsigned sg[4] = {0u, 0u, 0u, 0u};
#pragma unroll
            for (int k = 0; k < 20; ++k) sg[k & 3] = __builtin_amdgcn_alignbit(sg[k & 3], kn - dk[k >> 2][k & 3], 31);
            int c = __builtin_popcount(sg[0] | sg[1] << 8 | sg[2] << 16 | sg[3] << 24);
            c += __builtin_amdgcn_update_dpp(0, c, 0xB1, 0xf, 0xf, false);
            *(unsigned*)(smem + aa + c * 4) = av;
            smem[(c & 3) == 0 ? al + ((unsigned)(lpos >> c) & 15u) : L_LEN + 20] = (unsigned char)max(kn >> 7, c < 8 ? 12u : 8u);
            lds_arrive(lds0 + SY_S, __builtin_amdgcn_ballot_w64((kn >> 7) > DEG_MAX) != 0ull ? 257u : 1u);
        }
        __builtin_amdgcn_s_setprio(3);
    }
    unsigned ax[3], af[3], lh, cn;
    u32x4 ln;
    {
        if (w == 7) lds_wait<2, 255>(lds0 + SY_S, 1);
        do {
            asm volatile("ds_read_b32 %0, %9 offset:%c16\n\tds_read_b128 %1, %9 offset:%c17\n\tds_read_b32 %2, %9 offset:%c18\n\tds_read_b32 %3, %10\n\tds_read_b32 %4, %11\n\t"
                         "ds_read_b32 %5, %12\n\tds_read_b32 %6, %13\n\tds_read_b32 %7, %14\n\tds_read_b32 %8, %15\n\ts_waitcnt lgkmcnt(0)"
                         : "=&v"(cn), "=&v"(ln), "=&v"(lh), "=&v"(ax[0]), "=&v"(af[0]), "=&v"(ax[1]), "=&v"(af[1]), "=&v"(ax[2]), "=&v"(af[2])
                         : "v"(lds0), "v"(ta[0]), "v"(ta[1]), "v"(ta[2]), "v"(ta[3]), "v"(ta[4]), "v"(ta[5]),
                           "i"(SY_S), "i"(L_LEN), "i"(L_LEN + 16) : "memory");
            cn = (unsigned)__builtin_amdgcn_readfirstlane((int)cn);
        } while ((cn & 255u) < 3u);
        slow = cn >= 256u;
    }
    const int p8 = __builtin_amdgcn_sdot4((int)(ln.x + ln.y), (int)cc, __builtin_amdgcn_sdot4((int)(ln.w + lh), (int)cc,
                   __builtin_amdgcn_sdot4((int)ln.z, (int)cd, k0, false), false), false);
    const int ld = (__builtin_amdgcn_readfirstlane((int)ln.z) >> ri8) & 255, p2 = min(max(__builtin_amdgcn_readfirstlane(p8) >> 3, 4), ld - 8);
    const int l2 = w < 4 ? ld - p2 : p2, od = (w < 4) == (wd >= 6) ? ld - l2 : 0;
    const unsigned aX = (ax[0] >> 8) + ((w < 4 ? od : 0) + (c16 & 3)) * 16, aF = (af[0] >> 8) + ((w < 4 ? od : 0) + (c16 & 3)) * 16 + 4;
    const unsigned lane_off = (unsigned)c16 * 16u;
    const unsigned aWw = L_BNC + w * 512 + lane * 8;
    const unsigned aWr = L_BNC + w * 512 + g * 128 + (c16 & 3) * 32 + (c16 >> 2) * 8;
    auto row_of = [=](unsigned at) -> unsigned {
        const unsigned nl = at & 0xffu;
        return nl < (unsigned)npb ? ((unsigned)node0 + nl) * 1024u + lane_off : OOB_OFF;
    };
#pragma unroll
    for (int i = 0; i < NT2; ++i) asm volatile("" :: "v"(tv[i]));
    if (!slow) {
        u32x32 oxa, oxb;
        u32x4 efA, efN, S;
        unsigned tmp;
        __builtin_amdgcn_s_waitcnt(0x0F70);
        asm volatile(
            "Lw2_%=:\n"
            "ds_read_b32 v220, v219\n"
            "ds_read2_b32 v[232:233], v211 offset1:16\n"
            "ds_read_b128 v[196:199], v210\n"
            "s_waitcnt lgkmcnt(1)\n"
            "v_readfirstlane_b32 %[tmp], v220\n"
            "s_cmp_lt_u32 %[tmp], 5\n"
            "s_cbranch_scc1 Lw2_%=\n"
            "v_or_b32 v232, v232, v229\n"
            "v_or_b32 v233, v233, v229\n"
            "buffer_load_dwordx4 v[192:195], v232, %[rse], 0 offen\n"
            "s_waitcnt lgkmcnt(0)\n"
            "v_add_u32_dpp v220, v196, v214 quad_perm:[0,0,0,0] row_mask:0xf bank_mask:0xf\n"
            "v_add_u32_dpp v221, v196, v214 quad_perm:[1,1,1,1] row_mask:0xf bank_mask:0xf\n"
            "v_add_u32_dpp v222, v196, v214 quad_perm:[2,2,2,2] row_mask:0xf bank_mask:0xf\n"
            "v_add_u32_dpp v223, v196, v214 quad_perm:[3,3,3,3] row_mask:0xf bank_mask:0xf\n"
            "buffer_load_dwordx4 v[32:35], v220, %[rsx], 0 offen\n"
            "buffer_load_dwordx4 v[36:39], v220, %[rsx], 0 offen offset:256\n"
            "buffer_load_dwordx4 v[48:51], v221, %[rsx], 0 offen\n"
            "buffer_load_dwordx4 v[52:55], v221, %[rsx], 0 offen offset:256\n"
            "buffer_load_dwordx4 v[232:235], v233, %[rse], 0 offen\n"
            "buffer_load_dwordx4 v[64:67], v222, %[rsx], 0 offen\n"
            "buffer_load_dwordx4 v[68:71], v222, %[rsx], 0 offen offset:256\n"
            "buffer_load_dwordx4 v[80:83], v223, %[rsx], 0 offen\n"
            "buffer_load_dwordx4 v[84:87], v223, %[rsx], 0 offen offset:256\n"
            "buffer_load_dwordx4 v[40:43], v220, %[rsx], 0 offen offset:512\n"
            "buffer_load_dwordx4 v[44:47], v220, %[rsx], 0 offen offset:768\n"
            "buffer_load_dwordx4 v[56:59], v221, %[rsx], 0 offen offset:512\n"
            "buffer_load_dwordx4 v[60:63], v221, %[rsx], 0 offen offset:768\n"
            "buffer_load_dwordx4 v[72:75], v222, %[rsx], 0 offen offset:512\n"
            "buffer_load_dwordx4 v[76:79], v222, %[rsx], 0 offen offset:768\n"
            "buffer_load_dwordx4 v[88:91], v223, %[rsx], 0 offen offset:512\n"
            "buffer_load_dwordx4 v[92:95], v223, %[rsx], 0 offen offset:768\n"
            : "={v[32:63]}"(oxa), "={v[64:95]}"(oxb), "={v[192:195]}"(efA), "={v[196:199]}"(S), "={v[232:235]}"(efN), [tmp] "=&s"(tmp)
            : [rse] "s"(de), [rsx] "s"(dx), [ax] "{v210}"(aX), [af] "{v211}"(aF), [lo] "{v214}"(lane_off), [eo] "{v229}"((g & 1) * 16), [sy] "{v219}"(lds0 + SY_C3)
            : "memory", "scc", "v220", "v221", "v222", "v223");
        if (bx == 0 && w == 4) flag_store(ws + W_CTL, tag);
        const int lf = (__builtin_amdgcn_readfirstlane((int)(w < 4 ? ln.w : lh)) >> (8 * (w & 3))) & 255, l1 = (__builtin_amdgcn_readfirstlane((int)(w < 4 ? ln.x : ln.y)) >> (8 * (w & 3))) & 255;
        const int e2 = lf + l1 + l2;
        int nst = (e2 + 3) >> 2;
        const unsigned r2 = row_of(ax[1]);
        u32x32 owr;
        u32x16 omi;
        u32x8 ot;
#pragma unroll
        for (int i = 0; i < 32; ++i) owr[i] = wr[i >> 2][i & 3];
        const int pw = (w + (w >> 2)) & 3;
        const unsigned trb = lds0 + L_TR + w * TR_STR;
        omi[2] = aX + 64; omi[3] = aF + 64;
        omi[4] = lds0 + aWw; omi[5] = lds0 + aWr; omi[6] = lane_off; omi[7] = W_AGG + row_of(ax[0]); omi[8] = W_AGG + r2;
        omi[9] = row_of(ax[2]); omi[10] = w >= 6 ? r2 : OOB_OFF; omi[15] = ~tag;
        ot[0] = (lane & 3) == 0 ? 0x3f80u : ((lane & 3) == 1 ? 0x3f800000u : 0u); ot[1] = (lane & 3) == 2 ? 0x3f80u : ((lane & 3) == 3 ? 0x3f800000u : 0u);
        ot[2] = trb + g * (4 * 272) + c16 * 16; ot[3] = trb + c16 * 272 + g * 16; ot[4] = lds0 + L_IMG + lane * 16; ot[5] = (g & 1) * 16 + (EHALF && g >= 2 ? OOB_OFF : 0u); ot[7] = lds0 + L_PK + pw * 4096 + lane * 16;
        int rem = w < 4 ? l2 : l1, lens = ((w < 4 ? l1 : l2) - 4) | 0xff00, fm = w < 4 ? 0x802 : (w < 6 ? 0x810 : 0xb0), pb = 0;
        const int j1 = (w < 4 ? -od - l2 : od - l1) * 16, j2 = (w < 4 ? -l1 : -od - l2) * 16;
        u32x4 jmp = (u32x4){(ax[1] >> 8) - (ax[0] >> 8) + j1, (af[1] >> 8) - (af[0] >> 8) + j1, (ax[2] >> 8) - (ax[1] >> 8) + j2, (af[2] >> 8) - (af[1] >> 8) + j2};
        unsigned evt = (unsigned)__builtin_amdgcn_readfirstlane(w < 4 ? evt_of(PTO) : (w < 6 ? evt_of(PTY) : evt_of(PTZ))) | 6u << (nst - (rem >> 2)) | 6u << (nst - ((l1 + l2) >> 2));
        u64 ra;
        asm volatile(
            "s_nop 1\n"
            "v_mfma_f32_4x4x4_16b_bf16 v[204:207], v[202:203], v[198:199], 0\n"
            "v_mov_b64 v[0:1], 0\n"
            "v_mov_b64 v[2:3], 0\n"
            "v_mov_b64 v[4:5], 0\n"
            "v_mov_b64 v[6:7], 0\n"
            "v_mfma_f32_4x4x4_16b_bf16 v[204:207], v[200:201], v[198:199], v[204:207]\n"
            "v_mov_b64 v[8:9], 0\n"
            "v_mov_b64 v[10:11], 0\n"
            "v_mov_b64 v[12:13], 0\n"
            "v_mov_b64 v[14:15], 0\n"
            "v_mov_b64 v[16:17], 0\n"
            "v_mov_b64 v[18:19], 0\n"
            "v_mov_b64 v[20:21], 0\n"
            "v_mov_b64 v[22:23], 0\n"
            "v_mov_b64 v[24:25], 0\n"
            "v_mov_b64 v[26:27], 0\n"
            "v_mov_b64 v[28:29], 0\n"
            "v_mov_b64 v[30:31], 0\n"
            "v_cvt_pk_bf16_f32 v204, v204, v205\n"
            "v_cvt_pk_bf16_f32 v205, v206, v207\n"
            "ds_write_b64 v212, v[204:205]\n"
            "ds_read_b128 v[196:199], v210\n"
            "ds_read_b64 v[208:209], v213\n"
            "s_waitcnt vmcnt(17)\n"
            "v_mfma_f32_16x16x32_bf16 v[176:179], v[192:195], v[96:99], 0\n"
            "v_mfma_f32_16x16x32_bf16 v[180:183], v[192:195], v[100:103], 0\n"
            "v_mfma_f32_16x16x32_bf16 v[184:187], v[192:195], v[104:107], 0\n"
            "v_mfma_f32_16x16x32_bf16 v[188:191], v[192:195], v[108:111], 0\n"
            "v_mfma_f32_16x16x32_bf16 v[128:131], v[192:195], v[112:115], 0\n"
            "v_mfma_f32_16x16x32_bf16 v[132:135], v[192:195], v[116:119], 0\n"
            "s_nop 2\n"
            "v_exp_f32 v144, v176\n"
            "v_exp_f32 v145, v177\n"
            "v_exp_f32 v146, v178\n"
            "v_exp_f32 v147, v179\n"
            "v_exp_f32 v148, v180\n"
            "v_exp_f32 v149, v181\n"
            "v_exp_f32 v150, v182\n"
            "v_exp_f32 v151, v183\n"
            "v_exp_f32 v152, v184\n"
            "v_exp_f32 v153, v185\n"
            "v_exp_f32 v154, v186\n"
            "v_exp_f32 v155, v187\n"
            "v_exp_f32 v156, v188\n"
            "v_exp_f32 v157, v189\n"
            "v_exp_f32 v158, v190\n"
            "v_exp_f32 v159, v191\n"
            "v_pk_add_f32 v[144:145], v[144:145], 1.0 op_sel_hi:[1,0]\n"
            "v_pk_add_f32 v[146:147], v[146:147], 1.0 op_sel_hi:[1,0]\n"
            "v_pk_add_f32 v[148:149], v[148:149], 1.0 op_sel_hi:[1,0]\n"
            "v_pk_add_f32 v[150:151], v[150:151], 1.0 op_sel_hi:[1,0]\n"
            "v_pk_add_f32 v[152:153], v[152:153], 1.0 op_sel_hi:[1,0]\n"
            "v_pk_add_f32 v[154:155], v[154:155], 1.0 op_sel_hi:[1,0]\n"
            "v_pk_add_f32 v[156:157], v[156:157], 1.0 op_sel_hi:[1,0]\n"
            "v_pk_add_f32 v[158:159], v[158:159], 1.0 op_sel_hi:[1,0]\n"
            "v_rcp_f32 v144, v144\n"
            "v_rcp_f32 v145, v145\n"
            "v_rcp_f32 v146, v146\n"
            "v_rcp_f32 v147, v147\n"
            "v_rcp_f32 v148, v148\n"
            "v_rcp_f32 v149, v149\n"
            "v_rcp_f32 v150, v150\n"
            "v_rcp_f32 v151, v151\n"
            "v_rcp_f32 v152, v152\n"
            "v_rcp_f32 v153, v153\n"
            "v_rcp_f32 v154, v154\n"
            "v_rcp_f32 v155, v155\n"
            "v_rcp_f32 v156, v156\n"
            "v_rcp_f32 v157, v157\n"
            "v_rcp_f32 v158, v158\n"
            "v_rcp_f32 v159, v159\n"
            "v_pk_mul_f32 v[144:145], v[144:145], v[176:177]\n"
            "v_pk_mul_f32 v[146:147], v[146:147], v[178:179]\n"
            "v_pk_mul_f32 v[148:149], v[148:149], v[180:181]\n"
            "v_pk_mul_f32 v[150:151], v[150:151], v[182:183]\n"
            "v_pk_mul_f32 v[152:153], v[152:153], v[184:185]\n"
            "v_pk_mul_f32 v[154:155], v[154:155], v[186:187]\n"
            "v_pk_mul_f32 v[156:157], v[156:157], v[188:189]\n"
            "v_pk_mul_f32 v[158:159], v[158:159], v[190:191]\n"
            "s_branch Ldisp_%=\n"
            "Lfast_%=:\n"
            "v_mfma_f32_16x16x32_bf16 v[136:139], v[192:195], v[120:123], 0\n"
            "v_exp_f32 v160, v128\n"
            "v_add_u32 v211, 64, v211\n"
            "v_exp_f32 v161, v129\n"
            "s_waitcnt vmcnt(13) lgkmcnt(0)\n"
            "v_mfma_f32_4x4x4_16b_bf16 v[176:179], v[208:209], v[32:33], 0 cbsz:2\n"
            "v_exp_f32 v162, v130\n"
            "v_mfma_f32_4x4x4_16b_bf16 v[180:183], v[208:209], v[34:35], 0 cbsz:2\n"
            "v_exp_f32 v163, v131\n"
            "v_mfma_f32_4x4x4_16b_bf16 v[184:187], v[208:209], v[36:37], 0 cbsz:2\n"
            "v_exp_f32 v164, v132\n"
            "v_mfma_f32_4x4x4_16b_bf16 v[188:191], v[208:209], v[38:39], 0 cbsz:2\n"
            "v_exp_f32 v165, v133\n"
            "v_add_u32_dpp v219, v196, v214 quad_perm:[0,0,0,0] row_mask:0xf bank_mask:0xf\n"
            "v_mfma_f32_4x4x4_16b_bf16 v[204:207], v[202:203], v[198:199], 0\n"
            "v_exp_f32 v166, v134\n"
            "buffer_load_dwordx4 v[32:35], v219, %[rsx], 0 offen\n"
            "buffer_load_dwordx4 v[36:39], v219, %[rsx], 0 offen offset:256\n"
            "v_exp_f32 v167, v135\n"
            "v_pk_fma_f32 v[0:1], v[144:145], v[176:177], v[0:1] op_sel_hi:[0,1,1]\n"
            "v_pk_fma_f32 v[2:3], v[144:145], v[178:179], v[2:3] op_sel_hi:[0,1,1]\n"
            "v_pk_fma_f32 v[4:5], v[148:149], v[180:181], v[4:5] op_sel_hi:[0,1,1]\n"
            "v_pk_fma_f32 v[6:7], v[148:149], v[182:183], v[6:7] op_sel_hi:[0,1,1]\n"
            "v_pk_fma_f32 v[8:9], v[152:153], v[184:185], v[8:9] op_sel_hi:[0,1,1]\n"
            "v_pk_fma_f32 v[10:11], v[152:153], v[186:187], v[10:11] op_sel_hi:[0,1,1]\n"
            "v_pk_fma_f32 v[12:13], v[156:157], v[188:189], v[12:13] op_sel_hi:[0,1,1]\n"
            "v_pk_fma_f32 v[14:15], v[156:157], v[190:191], v[14:15] op_sel_hi:[0,1,1]\n"
            "v_pk_add_f32 v[160:161], v[160:161], 1.0 op_sel_hi:[1,0]\n"
            "v_pk_add_f32 v[162:163], v[162:163], 1.0 op_sel_hi:[1,0]\n"
            "v_pk_add_f32 v[164:165], v[164:165], 1.0 op_sel_hi:[1,0]\n"
            "v_pk_add_f32 v[166:167], v[166:167], 1.0 op_sel_hi:[1,0]\n"
            "v_mfma_f32_16x16x32_bf16 v[140:143], v[192:195], v[124:127], 0\n"
            "ds_read_b32 v230, v211\n"
            "v_rcp_f32 v160, v160\n"
            "v_rcp_f32 v161, v161\n"
            "v_mfma_f32_4x4x4_16b_bf16 v[176:179], v[208:209], v[48:49], 0 cbsz:2 abid:1\n"
            "v_rcp_f32 v162, v162\n"
            "v_mfma_f32_4x4x4_16b_bf16 v[180:183], v[208:209], v[50:51], 0 cbsz:2 abid:1\n"
            "v_rcp_f32 v163, v163\n"
            "v_mfma_f32_4x4x4_16b_bf16 v[184:187], v[208:209], v[52:53], 0 cbsz:2 abid:1\n"
            "v_rcp_f32 v164, v164\n"
            "v_mfma_f32_4x4x4_16b_bf16 v[188:191], v[208:209], v[54:55], 0 cbsz:2 abid:1\n"
            "v_rcp_f32 v165, v165\n"
            "v_add_u32_dpp v220, v196, v214 quad_perm:[1,1,1,1] row_mask:0xf bank_mask:0xf\n"
            "v_mfma_f32_4x4x4_16b_bf16 v[204:207], v[200:201], v[198:199], v[204:207]\n"
            "v_rcp_f32 v166, v166\n"
            "buffer_load_dwordx4 v[48:51], v220, %[rsx], 0 offen\n"
            "buffer_load_dwordx4 v[52:55], v220, %[rsx], 0 offen offset:256\n"
            "v_rcp_f32 v167, v167\n"
            "v_pk_fma_f32 v[0:1], v[144:145], v[176:177], v[0:1] op_sel:[1,0,0]\n"
            "v_pk_fma_f32 v[2:3], v[144:145], v[178:179], v[2:3] op_sel:[1,0,0]\n"
            "v_pk_fma_f32 v[4:5], v[148:149], v[180:181], v[4:5] op_sel:[1,0,0]\n"
            "v_pk_fma_f32 v[6:7], v[148:149], v[182:183], v[6:7] op_sel:[1,0,0]\n"
            "v_pk_fma_f32 v[8:9], v[152:153], v[184:185], v[8:9] op_sel:[1,0,0]\n"
            "v_pk_fma_f32 v[10:11], v[152:153], v[186:187], v[10:11] op_sel:[1,0,0]\n"
            "v_pk_fma_f32 v[12:13], v[156:157], v[188:189], v[12:13] op_sel:[1,0,0]\n"
            "v_pk_fma_f32 v[14:15], v[156:157], v[190:191], v[14:15] op_sel:[1,0,0]\n"
            "v_pk_mul_f32 v[160:161], v[160:161], v[128:129]\n"
            "v_pk_mul_f32 v[162:163], v[162:163], v[130:131]\n"
            "v_pk_mul_f32 v[164:165], v[164:165], v[132:133]\n"
            "v_pk_mul_f32 v[166:167], v[166:167], v[134:135]\n"
            "s_waitcnt vmcnt(12) lgkmcnt(0)\n"
            "v_mov_b64 v[192:193], v[232:233]\n"
            "v_mov_b64 v[194:195], v[234:235]\n"
            "v_or_b32 v230, v230, v229\n"
            "buffer_load_dwordx4 v[232:235], v230, %[rse], 0 offen\n"
            "v_mfma_f32_16x16x32_bf16 v[128:131], v[192:195], v[96:99], 0\n"
            "v_exp_f32 v168, v136\n"
            "v_exp_f32 v169, v137\n"
            "v_mfma_f32_4x4x4_16b_bf16 v[176:179], v[208:209], v[64:65], 0 cbsz:2 abid:2\n"
            "v_exp_f32 v170, v138\n"
            "v_mfma_f32_4x4x4_16b_bf16 v[180:183], v[208:209], v[66:67], 0 cbsz:2 abid:2\n"
            "v_exp_f32 v171, v139\n"
            "v_mfma_f32_4x4x4_16b_bf16 v[184:187], v[208:209], v[68:69], 0 cbsz:2 abid:2\n"
            "v_exp_f32 v172, v140\n"
            "v_mfma_f32_4x4x4_16b_bf16 v[188:191], v[208:209], v[70:71], 0 cbsz:2 abid:2\n"
            "v_exp_f32 v173, v141\n"
            "v_add_u32_dpp v221, v196, v214 quad_perm:[2,2,2,2] row_mask:0xf bank_mask:0xf\n"
            "v_exp_f32 v174, v142\n"
            "buffer_load_dwordx4 v[64:67], v221, %[rsx], 0 offen\n"
            "buffer_load_dwordx4 v[68:71], v221, %[rsx], 0 offen offset:256\n"
            "v_exp_f32 v175, v143\n"
            "v_pk_fma_f32 v[0:1], v[146:147], v[176:177], v[0:1] op_sel_hi:[0,1,1]\n"
            "v_pk_fma_f32 v[2:3], v[146:147], v[178:179], v[2:3] op_sel_hi:[0,1,1]\n"
            "v_pk_fma_f32 v[4:5], v[150:151], v[180:181], v[4:5] op_sel_hi:[0,1,1]\n"
            "v_pk_fma_f32 v[6:7], v[150:151], v[182:183], v[6:7] op_sel_hi:[0,1,1]\n"
            "v_pk_fma_f32 v[8:9], v[154:155], v[184:185], v[8:9] op_sel_hi:[0,1,1]\n"
            "v_pk_fma_f32 v[10:11], v[154:155], v[186:187], v[10:11] op_sel_hi:[0,1,1]\n"
            "v_pk_fma_f32 v[12:13], v[158:159], v[188:189], v[12:13] op_sel_hi:[0,1,1]\n"
            "v_pk_fma_f32 v[14:15], v[158:159], v[190:191], v[14:15] op_sel_hi:[0,1,1]\n"
            "v_pk_add_f32 v[168:169], v[168:169], 1.0 op_sel_hi:[1,0]\n"
            "v_pk_add_f32 v[170:171], v[170:171], 1.0 op_sel_hi:[1,0]\n"
            "v_pk_add_f32 v[172:173], v[172:173], 1.0 op_sel_hi:[1,0]\n"
            "v_pk_add_f32 v[174:175], v[174:175], 1.0 op_sel_hi:[1,0]\n"
            "v_cvt_pk_bf16_f32 v204, v204, v205\n"
            "v_cvt_pk_bf16_f32 v205, v206, v207\n"
            "ds_write_b64 v212, v[204:205]\n"
            "v_mfma_f32_16x16x32_bf16 v[132:135], v[192:195], v[100:103], 0\n"
            "v_rcp_f32 v168, v168\n"
            "v_rcp_f32 v169, v169\n"
            "v_mfma_f32_4x4x4_16b_bf16 v[176:179], v[208:209], v[80:81], 0 cbsz:2 abid:3\n"
            "v_rcp_f32 v170, v170\n"
            "v_mfma_f32_4x4x4_16b_bf16 v[180:183], v[208:209], v[82:83], 0 cbsz:2 abid:3\n"
            "v_rcp_f32 v171, v171\n"
            "v_mfma_f32_4x4x4_16b_bf16 v[184:187], v[208:209], v[84:85], 0 cbsz:2 abid:3\n"
            "v_rcp_f32 v172, v172\n"
            "v_mfma_f32_4x4x4_16b_bf16 v[188:191], v[208:209], v[86:87], 0 cbsz:2 abid:3\n"
            "v_rcp_f32 v173, v173\n"
            "v_add_u32_dpp v222, v196, v214 quad_perm:[3,3,3,3] row_mask:0xf bank_mask:0xf\n"
            "v_rcp_f32 v174, v174\n"
            "buffer_load_dwordx4 v[80:83], v222, %[rsx], 0 offen\n"
            "buffer_load_dwordx4 v[84:87], v222, %[rsx], 0 offen offset:256\n"
            "v_rcp_f32 v175, v175\n"
            "v_pk_fma_f32 v[0:1], v[146:147], v[176:177], v[0:1] op_sel:[1,0,0]\n"
            "v_pk_fma_f32 v[2:3], v[146:147], v[178:179], v[2:3] op_sel:[1,0,0]\n"
            "v_pk_fma_f32 v[4:5], v[150:151], v[180:181], v[4:5] op_sel:[1,0,0]\n"
            "v_pk_fma_f32 v[6:7], v[150:151], v[182:183], v[6:7] op_sel:[1,0,0]\n"
            "v_pk_fma_f32 v[8:9], v[154:155], v[184:185], v[8:9] op_sel:[1,0,0]\n"
            "v_pk_fma_f32 v[10:11], v[154:155], v[186:187], v[10:11] op_sel:[1,0,0]\n"
            "v_pk_fma_f32 v[12:13], v[158:159], v[188:189], v[12:13] op_sel:[1,0,0]\n"
            "v_pk_fma_f32 v[14:15], v[158:159], v[190:191], v[14:15] op_sel:[1,0,0]\n"
            "v_pk_mul_f32 v[168:169], v[168:169], v[136:137]\n"
            "v_pk_mul_f32 v[170:171], v[170:171], v[138:139]\n"
            "v_pk_mul_f32 v[172:173], v[172:173], v[140:141]\n"
            "v_pk_mul_f32 v[174:175], v[174:175], v[142:143]\n"
            "v_add_u32 v210, 64, v210\n"
            "ds_read_b128 v[196:199], v210\n"
            "v_mfma_f32_16x16x32_bf16 v[136:139], v[192:195], v[104:107], 0\n"
            "v_exp_f32 v144, v128\n"
            "v_exp_f32 v145, v129\n"
            "s_waitcnt vmcnt(13)\n"
            "v_mfma_f32_4x4x4_16b_bf16 v[176:179], v[208:209], v[40:41], 0 cbsz:2\n"
            "v_exp_f32 v146, v130\n"
            "v_mfma_f32_4x4x4_16b_bf16 v[180:183], v[208:209], v[42:43], 0 cbsz:2\n"
            "v_exp_f32 v147, v131\n"
            "v_mfma_f32_4x4x4_16b_bf16 v[184:187], v[208:209], v[44:45], 0 cbsz:2\n"
            "v_exp_f32 v148, v132\n"
            "v_mfma_f32_4x4x4_16b_bf16 v[188:191], v[208:209], v[46:47], 0 cbsz:2\n"
            "v_exp_f32 v149, v133\n"
            "v_exp_f32 v150, v134\n"
            "buffer_load_dwordx4 v[40:43], v219, %[rsx], 0 offen offset:512\n"
            "buffer_load_dwordx4 v[44:47], v219, %[rsx], 0 offen offset:768\n"
            "v_exp_f32 v151, v135\n"
            "v_pk_fma_f32 v[16:17], v[160:161], v[176:177], v[16:17] op_sel_hi:[0,1,1]\n"
            "v_pk_fma_f32 v[18:19], v[160:161], v[178:179], v[18:19] op_sel_hi:[0,1,1]\n"
            "v_pk_fma_f32 v[20:21], v[164:165], v[180:181], v[20:21] op_sel_hi:[0,1,1]\n"
            "v_pk_fma_f32 v[22:23], v[164:165], v[182:183], v[22:23] op_sel_hi:[0,1,1]\n"
            "v_pk_fma_f32 v[24:25], v[168:169], v[184:185], v[24:25] op_sel_hi:[0,1,1]\n"
            "v_pk_fma_f32 v[26:27], v[168:169], v[186:187], v[26:27] op_sel_hi:[0,1,1]\n"
            "v_pk_fma_f32 v[28:29], v[172:173], v[188:189], v[28:29] op_sel_hi:[0,1,1]\n"
            "v_pk_fma_f32 v[30:31], v[172:173], v[190:191], v[30:31] op_sel_hi:[0,1,1]\n"
            "v_pk_add_f32 v[144:145], v[144:145], 1.0 op_sel_hi:[1,0]\n"
            "v_pk_add_f32 v[146:147], v[146:147], 1.0 op_sel_hi:[1,0]\n"
            "v_pk_add_f32 v[148:149], v[148:149], 1.0 op_sel_hi:[1,0]\n"
            "v_pk_add_f32 v[150:151], v[150:151], 1.0 op_sel_hi:[1,0]\n"
            "v_mfma_f32_16x16x32_bf16 v[140:143], v[192:195], v[108:111], 0\n"
            "v_rcp_f32 v144, v144\n"
            "v_rcp_f32 v145, v145\n"
            "v_mfma_f32_4x4x4_16b_bf16 v[176:179], v[208:209], v[56:57], 0 cbsz:2 abid:1\n"
            "v_rcp_f32 v146, v146\n"
            "v_mfma_f32_4x4x4_16b_bf16 v[180:183], v[208:209], v[58:59], 0 cbsz:2 abid:1\n"
            "v_rcp_f32 v147, v147\n"
            "v_mfma_f32_4x4x4_16b_bf16 v[184:187], v[208:209], v[60:61], 0 cbsz:2 abid:1\n"
            "v_rcp_f32 v148, v148\n"
            "v_mfma_f32_4x4x4_16b_bf16 v[188:191], v[208:209], v[62:63], 0 cbsz:2 abid:1\n"
            "v_rcp_f32 v149, v149\n"
            "v_rcp_f32 v150, v150\n"
            "buffer_load_dwordx4 v[56:59], v220, %[rsx], 0 offen offset:512\n"
            "buffer_load_dwordx4 v[60:63], v220, %[rsx], 0 offen offset:768\n"
            "v_rcp_f32 v151, v151\n"
            "v_pk_fma_f32 v[16:17], v[160:161], v[176:177], v[16:17] op_sel:[1,0,0]\n"
            "v_pk_fma_f32 v[18:19], v[160:161], v[178:179], v[18:19] op_sel:[1,0,0]\n"
            "v_pk_fma_f32 v[20:21], v[164:165], v[180:181], v[20:21] op_sel:[1,0,0]\n"
            "v_pk_fma_f32 v[22:23], v[164:165], v[182:183], v[22:23] op_sel:[1,0,0]\n"
            "v_pk_fma_f32 v[24:25], v[168:169], v[184:185], v[24:25] op_sel:[1,0,0]\n"
            "v_pk_fma_f32 v[26:27], v[168:169], v[186:187], v[26:27] op_sel:[1,0,0]\n"
            "v_pk_fma_f32 v[28:29], v[172:173], v[188:189], v[28:29] op_sel:[1,0,0]\n"
            "v_pk_fma_f32 v[30:31], v[172:173], v[190:191], v[30:31] op_sel:[1,0,0]\n"
            "v_pk_mul_f32 v[144:145], v[144:145], v[128:129]\n"
            "v_pk_mul_f32 v[146:147], v[146:147], v[130:131]\n"
            "v_pk_mul_f32 v[148:149], v[148:149], v[132:133]\n"
            "v_pk_mul_f32 v[150:151], v[150:151], v[134:135]\n"
            "v_mfma_f32_16x16x32_bf16 v[128:131], v[192:195], v[112:115], 0\n"
            "v_exp_f32 v152, v136\n"
            "v_exp_f32 v153, v137\n"
            "s_waitcnt vmcnt(13)\n"
            "v_mfma_f32_4x4x4_16b_bf16 v[176:179], v[208:209], v[72:73], 0 cbsz:2 abid:2\n"
            "v_exp_f32 v154, v138\n"
            "v_mfma_f32_4x4x4_16b_bf16 v[180:183], v[208:209], v[74:75], 0 cbsz:2 abid:2\n"
            "v_exp_f32 v155, v139\n"
            "v_mfma_f32_4x4x4_16b_bf16 v[184:187], v[208:209], v[76:77], 0 cbsz:2 abid:2\n"
            "v_exp_f32 v156, v140\n"
            "v_mfma_f32_4x4x4_16b_bf16 v[188:191], v[208:209], v[78:79], 0 cbsz:2 abid:2\n"
            "v_exp_f32 v157, v141\n"
            "v_exp_f32 v158, v142\n"
            "buffer_load_dwordx4 v[72:75], v221, %[rsx], 0 offen offset:512\n"
            "buffer_load_dwordx4 v[76:79], v221, %[rsx], 0 offen offset:768\n"
            "v_exp_f32 v159, v143\n"
            "v_pk_fma_f32 v[16:17], v[162:163], v[176:177], v[16:17] op_sel_hi:[0,1,1]\n"
            "v_pk_fma_f32 v[18:19], v[162:163], v[178:179], v[18:19] op_sel_hi:[0,1,1]\n"
            "v_pk_fma_f32 v[20:21], v[166:167], v[180:181], v[20:21] op_sel_hi:[0,1,1]\n"
            "v_pk_fma_f32 v[22:23], v[166:167], v[182:183], v[22:23] op_sel_hi:[0,1,1]\n"
            "v_pk_fma_f32 v[24:25], v[170:171], v[184:185], v[24:25] op_sel_hi:[0,1,1]\n"
            "v_pk_fma_f32 v[26:27], v[170:171], v[186:187], v[26:27] op_sel_hi:[0,1,1]\n"
            "v_pk_fma_f32 v[28:29], v[174:175], v[188:189], v[28:29] op_sel_hi:[0,1,1]\n"
            "v_pk_fma_f32 v[30:31], v[174:175], v[190:191], v[30:31] op_sel_hi:[0,1,1]\n"
            "v_pk_add_f32 v[152:153], v[152:153], 1.0 op_sel_hi:[1,0]\n"
            "v_pk_add_f32 v[154:155], v[154:155], 1.0 op_sel_hi:[1,0]\n"
            "v_pk_add_f32 v[156:157], v[156:157], 1.0 op_sel_hi:[1,0]\n"
            "v_pk_add_f32 v[158:159], v[158:159], 1.0 op_sel_hi:[1,0]\n"
            "v_mfma_f32_16x16x32_bf16 v[132:135], v[192:195], v[116:119], 0\n"
            "v_rcp_f32 v152, v152\n"
            "v_rcp_f32 v153, v153\n"
            "v_mfma_f32_4x4x4_16b_bf16 v[176:179], v[208:209], v[88:89], 0 cbsz:2 abid:3\n"
            "v_rcp_f32 v154, v154\n"
            "v_mfma_f32_4x4x4_16b_bf16 v[180:183], v[208:209], v[90:91], 0 cbsz:2 abid:3\n"
            "v_rcp_f32 v155, v155\n"
            "v_mfma_f32_4x4x4_16b_bf16 v[184:187], v[208:209], v[92:93], 0 cbsz:2 abid:3\n"
            "v_rcp_f32 v156, v156\n"
            "v_mfma_f32_4x4x4_16b_bf16 v[188:191], v[208:209], v[94:95], 0 cbsz:2 abid:3\n"
            "ds_read_b64 v[208:209], v213\n"
            "v_rcp_f32 v157, v157\n"
            "v_rcp_f32 v158, v158\n"
            "buffer_load_dwordx4 v[88:91], v222, %[rsx], 0 offen offset:512\n"
            "buffer_load_dwordx4 v[92:95], v222, %[rsx], 0 offen offset:768\n"
            "v_rcp_f32 v159, v159\n"
            "v_pk_fma_f32 v[16:17], v[162:163], v[176:177], v[16:17] op_sel:[1,0,0]\n"
            "v_pk_fma_f32 v[18:19], v[162:163], v[178:179], v[18:19] op_sel:[1,0,0]\n"
            "v_pk_fma_f32 v[20:21], v[166:167], v[180:181], v[20:21] op_sel:[1,0,0]\n"
            "v_pk_fma_f32 v[22:23], v[166:167], v[182:183], v[22:23] op_sel:[1,0,0]\n"
            "v_pk_fma_f32 v[24:25], v[170:171], v[184:185], v[24:25] op_sel:[1,0,0]\n"
            "v_pk_fma_f32 v[26:27], v[170:171], v[186:187], v[26:27] op_sel:[1,0,0]\n"
            "v_pk_fma_f32 v[28:29], v[174:175], v[188:189], v[28:29] op_sel:[1,0,0]\n"
            "v_pk_fma_f32 v[30:31], v[174:175], v[190:191], v[30:31] op_sel:[1,0,0]\n"
            "v_pk_mul_f32 v[152:153], v[152:153], v[136:137]\n"
            "v_pk_mul_f32 v[154:155], v[154:155], v[138:139]\n"
            "v_pk_mul_f32 v[156:157], v[156:157], v[140:141]\n"
            "v_pk_mul_f32 v[158:159], v[158:159], v[142:143]\n"
            "s_sub_u32 %[rem], %[rem], 4\n"
            "Lnext_%=:\n"
            "s_sub_u32 %[nst], %[nst], 1\n"
            "Ldisp_%=:\n"
            "s_bitcmp1_b32 %[evt], %[nst]\n"
            "s_cbranch_scc1 Lprio_%=\n"
            "Ld2_%=:\n"
            "s_cmp_gt_u32 %[rem], 4\n"
            "s_cbranch_scc1 Lfast_%=\n"
            "v_mfma_f32_16x16x32_bf16 v[136:139], v[192:195], v[120:123], 0\n"
            "v_exp_f32 v160, v128\n"
            "v_add_u32 v211, 64, v211\n"
            "v_exp_f32 v161, v129\n"
            "s_waitcnt vmcnt(13) lgkmcnt(0)\n"
            "v_mfma_f32_4x4x4_16b_bf16 v[176:179], v[208:209], v[32:33], 0 cbsz:2\n"
            "v_exp_f32 v162, v130\n"
            "v_mfma_f32_4x4x4_16b_bf16 v[180:183], v[208:209], v[34:35], 0 cbsz:2\n"
            "v_exp_f32 v163, v131\n"
            "v_mfma_f32_4x4x4_16b_bf16 v[184:187], v[208:209], v[36:37], 0 cbsz:2\n"
            "v_exp_f32 v164, v132\n"
            "v_mfma_f32_4x4x4_16b_bf16 v[188:191], v[208:209], v[38:39], 0 cbsz:2\n"
            "v_exp_f32 v165, v133\n"
            "v_add_u32_dpp v219, v196, v214 quad_perm:[0,0,0,0] row_mask:0xf bank_mask:0xf\n"
            "v_mfma_f32_4x4x4_16b_bf16 v[204:207], v[202:203], v[198:199], 0\n"
            "v_exp_f32 v166, v134\n"
            "buffer_load_dwordx4 v[32:35], v219, %[rsx], 0 offen\n"
            "buffer_load_dwordx4 v[36:39], v219, %[rsx], 0 offen offset:256\n"
            "v_exp_f32 v167, v135\n"
            "v_pk_fma_f32 v[0:1], v[144:145], v[176:177], v[0:1] op_sel_hi:[0,1,1]\n"
            "v_pk_fma_f32 v[2:3], v[144:145], v[178:179], v[2:3] op_sel_hi:[0,1,1]\n"
            "v_pk_fma_f32 v[4:5], v[148:149], v[180:181], v[4:5] op_sel_hi:[0,1,1]\n"
            "v_pk_fma_f32 v[6:7], v[148:149], v[182:183], v[6:7] op_sel_hi:[0,1,1]\n"
            "v_pk_fma_f32 v[8:9], v[152:153], v[184:185], v[8:9] op_sel_hi:[0,1,1]\n"
            "v_pk_fma_f32 v[10:11], v[152:153], v[186:187], v[10:11] op_sel_hi:[0,1,1]\n"
            "v_pk_fma_f32 v[12:13], v[156:157], v[188:189], v[12:13] op_sel_hi:[0,1,1]\n"
            "v_pk_fma_f32 v[14:15], v[156:157], v[190:191], v[14:15] op_sel_hi:[0,1,1]\n"
            "v_pk_add_f32 v[160:161], v[160:161], 1.0 op_sel_hi:[1,0]\n"
            "v_pk_add_f32 v[162:163], v[162:163], 1.0 op_sel_hi:[1,0]\n"
            "v_pk_add_f32 v[164:165], v[164:165], 1.0 op_sel_hi:[1,0]\n"
            "v_pk_add_f32 v[166:167], v[166:167], 1.0 op_sel_hi:[1,0]\n"
            "s_cmp_eq_u32 %[rem], 1\n"
            "s_cbranch_scc1 Lf00_%=\n"
            "Lr00_%=:\n"
            "v_mfma_f32_16x16x32_bf16 v[140:143], v[192:195], v[124:127], 0\n"
            "ds_read_b32 v230, v211\n"
            "v_rcp_f32 v160, v160\n"
            "v_rcp_f32 v161, v161\n"
            "v_mfma_f32_4x4x4_16b_bf16 v[176:179], v[208:209], v[48:49], 0 cbsz:2 abid:1\n"
            "v_rcp_f32 v162, v162\n"
            "v_mfma_f32_4x4x4_16b_bf16 v[180:183], v[208:209], v[50:51], 0 cbsz:2 abid:1\n"
            "v_rcp_f32 v163, v163\n"
            "v_mfma_f32_4x4x4_16b_bf16 v[184:187], v[208:209], v[52:53], 0 cbsz:2 abid:1\n"
            "v_rcp_f32 v164, v164\n"
            "v_mfma_f32_4x4x4_16b_bf16 v[188:191], v[208:209], v[54:55], 0 cbsz:2 abid:1\n"
            "v_rcp_f32 v165, v165\n"
            "v_add_u32_dpp v220, v196, v214 quad_perm:[1,1,1,1] row_mask:0xf bank_mask:0xf\n"
            "v_mfma_f32_4x4x4_16b_bf16 v[204:207], v[200:201], v[198:199], v[204:207]\n"
            "v_rcp_f32 v166, v166\n"
            "buffer_load_dwordx4 v[48:51], v220, %[rsx], 0 offen\n"
            "buffer_load_dwordx4 v[52:55], v220, %[rsx], 0 offen offset:256\n"
            "v_rcp_f32 v167, v167\n"
            "v_pk_fma_f32 v[0:1], v[144:145], v[176:177], v[0:1] op_sel:[1,0,0]\n"
            "v_pk_fma_f32 v[2:3], v[144:145], v[178:179], v[2:3] op_sel:[1,0,0]\n"
            "v_pk_fma_f32 v[4:5], v[148:149], v[180:181], v[4:5] op_sel:[1,0,0]\n"
            "v_pk_fma_f32 v[6:7], v[148:149], v[182:183], v[6:7] op_sel:[1,0,0]\n"
            "v_pk_fma_f32 v[8:9], v[152:153], v[184:185], v[8:9] op_sel:[1,0,0]\n"
            "v_pk_fma_f32 v[10:11], v[152:153], v[186:187], v[10:11] op_sel:[1,0,0]\n"
            "v_pk_fma_f32 v[12:13], v[156:157], v[188:189], v[12:13] op_sel:[1,0,0]\n"
            "v_pk_fma_f32 v[14:15], v[156:157], v[190:191], v[14:15] op_sel:[1,0,0]\n"
            "v_pk_mul_f32 v[160:161], v[160:161], v[128:129]\n"
            "v_pk_mul_f32 v[162:163], v[162:163], v[130:131]\n"
            "v_pk_mul_f32 v[164:165], v[164:165], v[132:133]\n"
            "v_pk_mul_f32 v[166:167], v[166:167], v[134:135]\n"
            "s_cmp_eq_u32 %[rem], 2\n"
            "s_cbranch_scc1 Lf01_%=\n"
            "Lr01_%=:\n"
            "s_waitcnt vmcnt(12) lgkmcnt(0)\n"
            "v_mov_b64 v[192:193], v[232:233]\n"
            "v_mov_b64 v[194:195], v[234:235]\n"
            "v_or_b32 v230, v230, v229\n"
            "buffer_load_dwordx4 v[232:235], v230, %[rse], 0 offen\n"
            "v_mfma_f32_16x16x32_bf16 v[128:131], v[192:195], v[96:99], 0\n"
            "v_exp_f32 v168, v136\n"
            "v_exp_f32 v169, v137\n"
            "v_mfma_f32_4x4x4_16b_bf16 v[176:179], v[208:209], v[64:65], 0 cbsz:2 abid:2\n"
            "v_exp_f32 v170, v138\n"
            "v_mfma_f32_4x4x4_16b_bf16 v[180:183], v[208:209], v[66:67], 0 cbsz:2 abid:2\n"
            "v_exp_f32 v171, v139\n"
            "v_mfma_f32_4x4x4_16b_bf16 v[184:187], v[208:209], v[68:69], 0 cbsz:2 abid:2\n"
            "v_exp_f32 v172, v140\n"
            "v_mfma_f32_4x4x4_16b_bf16 v[188:191], v[208:209], v[70:71], 0 cbsz:2 abid:2\n"
            "v_exp_f32 v173, v141\n"
            "v_add_u32_dpp v221, v196, v214 quad_perm:[2,2,2,2] row_mask:0xf bank_mask:0xf\n"
            "v_exp_f32 v174, v142\n"
            "buffer_load_dwordx4 v[64:67], v221, %[rsx], 0 offen\n"
            "buffer_load_dwordx4 v[68:71], v221, %[rsx], 0 offen offset:256\n"
            "v_exp_f32 v175, v143\n"
            "v_pk_fma_f32 v[0:1], v[146:147], v[176:177], v[0:1] op_sel_hi:[0,1,1]\n"
            "v_pk_fma_f32 v[2:3], v[146:147], v[178:179], v[2:3] op_sel_hi:[0,1,1]\n"
            "v_pk_fma_f32 v[4:5], v[150:151], v[180:181], v[4:5] op_sel_hi:[0,1,1]\n"
            "v_pk_fma_f32 v[6:7], v[150:151], v[182:183], v[6:7] op_sel_hi:[0,1,1]\n"
            "v_pk_fma_f32 v[8:9], v[154:155], v[184:185], v[8:9] op_sel_hi:[0,1,1]\n"
            "v_pk_fma_f32 v[10:11], v[154:155], v[186:187], v[10:11] op_sel_hi:[0,1,1]\n"
            "v_pk_fma_f32 v[12:13], v[158:159], v[188:189], v[12:13] op_sel_hi:[0,1,1]\n"
            "v_pk_fma_f32 v[14:15], v[158:159], v[190:191], v[14:15] op_sel_hi:[0,1,1]\n"
            "v_pk_add_f32 v[168:169], v[168:169], 1.0 op_sel_hi:[1,0]\n"
            "v_pk_add_f32 v[170:171], v[170:171], 1.0 op_sel_hi:[1,0]\n"
            "v_pk_add_f32 v[172:173], v[172:173], 1.0 op_sel_hi:[1,0]\n"
            "v_pk_add_f32 v[174:175], v[174:175], 1.0 op_sel_hi:[1,0]\n"
            "v_cvt_pk_bf16_f32 v204, v204, v205\n"
            "v_cvt_pk_bf16_f32 v205, v206, v207\n"
            "ds_write_b64 v212, v[204:205]\n"
            "s_cmp_eq_u32 %[rem], 3\n"
            "s_cbranch_scc1 Lf02_%=\n"
            "Lr02_%=:\n"
            "v_mfma_f32_16x16x32_bf16 v[132:135], v[192:195], v[100:103], 0\n"
            "v_rcp_f32 v168, v168\n"
            "v_rcp_f32 v169, v169\n"
            "v_mfma_f32_4x4x4_16b_bf16 v[176:179], v[208:209], v[80:81], 0 cbsz:2 abid:3\n"
            "v_rcp_f32 v170, v170\n"
            "v_mfma_f32_4x4x4_16b_bf16 v[180:183], v[208:209], v[82:83], 0 cbsz:2 abid:3\n"
            "v_rcp_f32 v171, v171\n"
            "v_mfma_f32_4x4x4_16b_bf16 v[184:187], v[208:209], v[84:85], 0 cbsz:2 abid:3\n"
            "v_rcp_f32 v172, v172\n"
            "v_mfma_f32_4x4x4_16b_bf16 v[188:191], v[208:209], v[86:87], 0 cbsz:2 abid:3\n"
            "v_rcp_f32 v173, v173\n"
            "v_add_u32_dpp v222, v196, v214 quad_perm:[3,3,3,3] row_mask:0xf bank_mask:0xf\n"
            "v_rcp_f32 v174, v174\n"
            "buffer_load_dwordx4 v[80:83], v222, %[rsx], 0 offen\n"
            "buffer_load_dwordx4 v[84:87], v222, %[rsx], 0 offen offset:256\n"
            "v_rcp_f32 v175, v175\n"
            "v_pk_fma_f32 v[0:1], v[146:147], v[176:177], v[0:1] op_sel:[1,0,0]\n"
            "v_pk_fma_f32 v[2:3], v[146:147], v[178:179], v[2:3] op_sel:[1,0,0]\n"
            "v_pk_fma_f32 v[4:5], v[150:151], v[180:181], v[4:5] op_sel:[1,0,0]\n"
            "v_pk_fma_f32 v[6:7], v[150:151], v[182:183], v[6:7] op_sel:[1,0,0]\n"
            "v_pk_fma_f32 v[8:9], v[154:155], v[184:185], v[8:9] op_sel:[1,0,0]\n"
            "v_pk_fma_f32 v[10:11], v[154:155], v[186:187], v[10:11] op_sel:[1,0,0]\n"
            "v_pk_fma_f32 v[12:13], v[158:159], v[188:189], v[12:13] op_sel:[1,0,0]\n"
            "v_pk_fma_f32 v[14:15], v[158:159], v[190:191], v[14:15] op_sel:[1,0,0]\n"
            "v_pk_mul_f32 v[168:169], v[168:169], v[136:137]\n"
            "v_pk_mul_f32 v[170:171], v[170:171], v[138:139]\n"
            "v_pk_mul_f32 v[172:173], v[172:173], v[140:141]\n"
            "v_pk_mul_f32 v[174:175], v[174:175], v[142:143]\n"
            "s_cmp_eq_u32 %[rem], 4\n"
            "s_cbranch_scc1 Lf03_%=\n"
            "Lr03_%=:\n"
            "v_add_u32 v210, 64, v210\n"
            "ds_read_b128 v[196:199], v210\n"
            "v_mfma_f32_16x16x32_bf16 v[136:139], v[192:195], v[104:107], 0\n"
            "v_exp_f32 v144, v128\n"
            "v_exp_f32 v145, v129\n"
            "s_waitcnt vmcnt(13)\n"
            "v_mfma_f32_4x4x4_16b_bf16 v[176:179], v[208:209], v[40:41], 0 cbsz:2\n"
            "v_exp_f32 v146, v130\n"
            "v_mfma_f32_4x4x4_16b_bf16 v[180:183], v[208:209], v[42:43], 0 cbsz:2\n"
            "v_exp_f32 v147, v131\n"
            "v_mfma_f32_4x4x4_16b_bf16 v[184:187], v[208:209], v[44:45], 0 cbsz:2\n"
            "v_exp_f32 v148, v132\n"
            "v_mfma_f32_4x4x4_16b_bf16 v[188:191], v[208:209], v[46:47], 0 cbsz:2\n"
            "v_exp_f32 v149, v133\n"
            "v_exp_f32 v150, v134\n"
            "buffer_load_dwordx4 v[40:43], v219, %[rsx], 0 offen offset:512\n"
            "buffer_load_dwordx4 v[44:47], v219, %[rsx], 0 offen offset:768\n"
            "v_exp_f32 v151, v135\n"
            "v_pk_fma_f32 v[16:17], v[160:161], v[176:177], v[16:17] op_sel_hi:[0,1,1]\n"
            "v_pk_fma_f32 v[18:19], v[160:161], v[178:179], v[18:19] op_sel_hi:[0,1,1]\n"
            "v_pk_fma_f32 v[20:21], v[164:165], v[180:181], v[20:21] op_sel_hi:[0,1,1]\n"
            "v_pk_fma_f32 v[22:23], v[164:165], v[182:183], v[22:23] op_sel_hi:[0,1,1]\n"
            "v_pk_fma_f32 v[24:25], v[168:169], v[184:185], v[24:25] op_sel_hi:[0,1,1]\n"
            "v_pk_fma_f32 v[26:27], v[168:169], v[186:187], v[26:27] op_sel_hi:[0,1,1]\n"
            "v_pk_fma_f32 v[28:29], v[172:173], v[188:189], v[28:29] op_sel_hi:[0,1,1]\n"
            "v_pk_fma_f32 v[30:31], v[172:173], v[190:191], v[30:31] op_sel_hi:[0,1,1]\n"
            "v_pk_add_f32 v[144:145], v[144:145], 1.0 op_sel_hi:[1,0]\n"
            "v_pk_add_f32 v[146:147], v[146:147], 1.0 op_sel_hi:[1,0]\n"
            "v_pk_add_f32 v[148:149], v[148:149], 1.0 op_sel_hi:[1,0]\n"
            "v_pk_add_f32 v[150:151], v[150:151], 1.0 op_sel_hi:[1,0]\n"
            "s_cmp_eq_u32 %[rem], 1\n"
            "s_cbranch_scc1 Lf10_%=\n"
            "Lr10_%=:\n"
            "v_mfma_f32_16x16x32_bf16 v[140:143], v[192:195], v[108:111], 0\n"
            "v_rcp_f32 v144, v144\n"
            "v_rcp_f32 v145, v145\n"
            "v_mfma_f32_4x4x4_16b_bf16 v[176:179], v[208:209], v[56:57], 0 cbsz:2 abid:1\n"
            "v_rcp_f32 v146, v146\n"
            "v_mfma_f32_4x4x4_16b_bf16 v[180:183], v[208:209], v[58:59], 0 cbsz:2 abid:1\n"
            "v_rcp_f32 v147, v147\n"
            "v_mfma_f32_4x4x4_16b_bf16 v[184:187], v[208:209], v[60:61], 0 cbsz:2 abid:1\n"
            "v_rcp_f32 v148, v148\n"
            "v_mfma_f32_4x4x4_16b_bf16 v[188:191], v[208:209], v[62:63], 0 cbsz:2 abid:1\n"
            "v_rcp_f32 v149, v149\n"
            "v_rcp_f32 v150, v150\n"
            "buffer_load_dwordx4 v[56:59], v220, %[rsx], 0 offen offset:512\n"
            "buffer_load_dwordx4 v[60:63], v220, %[rsx], 0 offen offset:768\n"
            "v_rcp_f32 v151, v151\n"
            "v_pk_fma_f32 v[16:17], v[160:161], v[176:177], v[16:17] op_sel:[1,0,0]\n"
            "v_pk_fma_f32 v[18:19], v[160:161], v[178:179], v[18:19] op_sel:[1,0,0]\n"
            "v_pk_fma_f32 v[20:21], v[164:165], v[180:181], v[20:21] op_sel:[1,0,0]\n"
            "v_pk_fma_f32 v[22:23], v[164:165], v[182:183], v[22:23] op_sel:[1,0,0]\n"
            "v_pk_fma_f32 v[24:25], v[168:169], v[184:185], v[24:25] op_sel:[1,0,0]\n"
            "v_pk_fma_f32 v[26:27], v[168:169], v[186:187], v[26:27] op_sel:[1,0,0]\n"
            "v_pk_fma_f32 v[28:29], v[172:173], v[188:189], v[28:29] op_sel:[1,0,0]\n"
            "v_pk_fma_f32 v[30:31], v[172:173], v[190:191], v[30:31] op_sel:[1,0,0]\n"
            "v_pk_mul_f32 v[144:145], v[144:145], v[128:129]\n"
            "v_pk_mul_f32 v[146:147], v[146:147], v[130:131]\n"
            "v_pk_mul_f32 v[148:149], v[148:149], v[132:133]\n"
            "v_pk_mul_f32 v[150:151], v[150:151], v[134:135]\n"
            "s_cmp_eq_u32 %[rem], 2\n"
            "s_cbranch_scc1 Lf11_%=\n"
            "Lr11_%=:\n"
            "v_mfma_f32_16x16x32_bf16 v[128:131], v[192:195], v[112:115], 0\n"
            "v_exp_f32 v152, v136\n"
            "v_exp_f32 v153, v137\n"
            "s_waitcnt vmcnt(13)\n"
            "v_mfma_f32_4x4x4_16b_bf16 v[176:179], v[208:209], v[72:73], 0 cbsz:2 abid:2\n"
            "v_exp_f32 v154, v138\n"
            "v_mfma_f32_4x4x4_16b_bf16 v[180:183], v[208:209], v[74:75], 0 cbsz:2 abid:2\n"
            "v_exp_f32 v155, v139\n"
            "v_mfma_f32_4x4x4_16b_bf16 v[184:187], v[208:209], v[76:77], 0 cbsz:2 abid:2\n"
            "v_exp_f32 v156, v140\n"
            "v_mfma_f32_4x4x4_16b_bf16 v[188:191], v[208:209], v[78:79], 0 cbsz:2 abid:2\n"
            "v_exp_f32 v157, v141\n"
            "v_exp_f32 v158, v142\n"
            "buffer_load_dwordx4 v[72:75], v221, %[rsx], 0 offen offset:512\n"
            "buffer_load_dwordx4 v[76:79], v221, %[rsx], 0 offen offset:768\n"
            "v_exp_f32 v159, v143\n"
            "v_pk_fma_f32 v[16:17], v[162:163], v[176:177], v[16:17] op_sel_hi:[0,1,1]\n"
            "v_pk_fma_f32 v[18:19], v[162:163], v[178:179], v[18:19] op_sel_hi:[0,1,1]\n"
            "v_pk_fma_f32 v[20:21], v[166:167], v[180:181], v[20:21] op_sel_hi:[0,1,1]\n"
            "v_pk_fma_f32 v[22:23], v[166:167], v[182:183], v[22:23] op_sel_hi:[0,1,1]\n"
            "v_pk_fma_f32 v[24:25], v[170:171], v[184:185], v[24:25] op_sel_hi:[0,1,1]\n"
            "v_pk_fma_f32 v[26:27], v[170:171], v[186:187], v[26:27] op_sel_hi:[0,1,1]\n"
            "v_pk_fma_f32 v[28:29], v[174:175], v[188:189], v[28:29] op_sel_hi:[0,1,1]\n"
            "v_pk_fma_f32 v[30:31], v[174:175], v[190:191], v[30:31] op_sel_hi:[0,1,1]\n"
            "v_pk_add_f32 v[152:153], v[152:153], 1.0 op_sel_hi:[1,0]\n"
            "v_pk_add_f32 v[154:155], v[154:155], 1.0 op_sel_hi:[1,0]\n"
            "v_pk_add_f32 v[156:157], v[156:157], 1.0 op_sel_hi:[1,0]\n"
            "v_pk_add_f32 v[158:159], v[158:159], 1.0 op_sel_hi:[1,0]\n"
            "s_cmp_eq_u32 %[rem], 3\n"
            "s_cbranch_scc1 Lf12_%=\n"
            "Lr12_%=:\n"
            "v_mfma_f32_16x16x32_bf16 v[132:135], v[192:195], v[116:119], 0\n"
            "v_rcp_f32 v152, v152\n"
            "v_rcp_f32 v153, v153\n"
            "v_mfma_f32_4x4x4_16b_bf16 v[176:179], v[208:209], v[88:89], 0 cbsz:2 abid:3\n"
            "v_rcp_f32 v154, v154\n"
            "v_mfma_f32_4x4x4_16b_bf16 v[180:183], v[208:209], v[90:91], 0 cbsz:2 abid:3\n"
            "v_rcp_f32 v155, v155\n"
            "v_mfma_f32_4x4x4_16b_bf16 v[184:187], v[208:209], v[92:93], 0 cbsz:2 abid:3\n"
            "v_rcp_f32 v156, v156\n"
            "v_mfma_f32_4x4x4_16b_bf16 v[188:191], v[208:209], v[94:95], 0 cbsz:2 abid:3\n"
            "ds_read_b64 v[208:209], v213\n"
            "v_rcp_f32 v157, v157\n"
            "v_rcp_f32 v158, v158\n"
            "buffer_load_dwordx4 v[88:91], v222, %[rsx], 0 offen offset:512\n"
            "buffer_load_dwordx4 v[92:95], v222, %[rsx], 0 offen offset:768\n"
            "v_rcp_f32 v159, v159\n"
            "v_pk_fma_f32 v[16:17], v[162:163], v[176:177], v[16:17] op_sel:[1,0,0]\n"
            "v_pk_fma_f32 v[18:19], v[162:163], v[178:179], v[18:19] op_sel:[1,0,0]\n"
            "v_pk_fma_f32 v[20:21], v[166:167], v[180:181], v[20:21] op_sel:[1,0,0]\n"
            "v_pk_fma_f32 v[22:23], v[166:167], v[182:183], v[22:23] op_sel:[1,0,0]\n"
            "v_pk_fma_f32 v[24:25], v[170:171], v[184:185], v[24:25] op_sel:[1,0,0]\n"
            "v_pk_fma_f32 v[26:27], v[170:171], v[186:187], v[26:27] op_sel:[1,0,0]\n"
            "v_pk_fma_f32 v[28:29], v[174:175], v[188:189], v[28:29] op_sel:[1,0,0]\n"
            "v_pk_fma_f32 v[30:31], v[174:175], v[190:191], v[30:31] op_sel:[1,0,0]\n"
            "v_pk_mul_f32 v[152:153], v[152:153], v[136:137]\n"
            "v_pk_mul_f32 v[154:155], v[154:155], v[138:139]\n"
            "v_pk_mul_f32 v[156:157], v[156:157], v[140:141]\n"
            "v_pk_mul_f32 v[158:159], v[158:159], v[142:143]\n"
            "s_cmp_eq_u32 %[rem], 4\n"
            "s_cbranch_scc1 Lf13_%=\n"
            "Lr13_%=:\n"
            "s_and_b32 %[tmp], %[lens], 0xff\n"
            "s_add_u32 %[rem], %[rem], %[tmp]\n"
            "s_lshr_b32 %[lens], %[lens], 8\n"
            "s_lshr_b32 %[fm], %[fm], 4\n"
            "s_bitcmp1_b32 %[fm], 3\n"
            "v_mov_b32 v215, v216\n"
            "s_cbranch_scc0 Lnext_%=\n"
            "s_sub_u32 %[pb], %[nst], 2\n"
            "s_bitset1_b32 %[evt], %[pb]\n"
            "s_branch Lnext_%=\n"
            "Lpub_%=:\n"
            "s_waitcnt vmcnt(17)\n"
            "v_mov_b32 v176, %[cnt]\n"
            "v_mov_b32 v177, 1\n"
            "s_mov_b64 exec, 1\n"
            "ds_add_rtn_u32 v177, v176, v177\n"
            "s_waitcnt lgkmcnt(0)\n"
            "v_readfirstlane_b32 %[tmp], v177\n"
            "s_mov_b64 exec, 0xff\n"
            "v_mov_b32 v176, %[tag]\n"
            "v_mov_b32 v177, %[tag]\n"
            "v_mov_b32 v178, %[tag]\n"
            "v_mov_b32 v179, %[tag]\n"
            "s_cmp_lg_u32 %[tmp], 7\n"
            "s_cbranch_scc1 Lpub2_%=\n"
            "global_store_dwordx4 v214, v[176:179], %[flg] sc1\n"
            "Lpub2_%=:\n"
            "s_mov_b64 exec, -1\n"
            "s_branch Lpr2_%=\n"
            "Lprio_%=:\n"
            "s_cmp_eq_u32 %[nst], %[pb]\n"
            "s_cbranch_scc1 Lpub_%=\n"
            "Lpr2_%=:\n"
            "s_cmp_eq_u32 %[nst], 1\n"
            "s_cbranch_scc1 Llast_%=\n"
            "s_lshl2_add_u32 %[tmp], %[nst], %[c1]\n"
            "v_bfe_u32 v176, v214, 4, 2\n"
            "v_sub_u32 v176, %[tmp], v176\n"
            "v_cmp_gt_u32 vcc, 4, v176\n"
            "v_add_u32 v176, %[c2], v176\n"
            "s_min_u32 %[tmp], %[nst], 15\n"
            "v_cndmask_b32 v177, 0, v236, vcc\n"
            "v_cndmask_b32 v178, 0, v237, vcc\n"
            "v_cmp_gt_u32 vcc, 4, v176\n"
            "v_add_u32 v210, v210, v177\n"
            "v_add_u32 v211, v211, v178\n"
            "v_cndmask_b32 v177, 0, v238, vcc\n"
            "v_cndmask_b32 v178, 0, v239, vcc\n"
            "v_add_u32 v210, v210, v177\n"
            "v_add_u32 v211, v211, v178\n"
            "s_lshl_b32 %[tmp], %[tmp], 1\n"
            "s_lshr_b32 %[tmp], %[ptab], %[tmp]\n"
            "s_bitcmp1_b32 %[tmp], 1\n"
            "s_cbranch_scc1 Lq2_%=\n"
            "s_bitcmp1_b32 %[tmp], 0\n"
            "s_setprio 0\n"
            "s_cbranch_scc0 Lq9_%=\n"
            "s_setprio 1\n"
            "s_branch Lq9_%=\n"
            "Lq2_%=:\n"
            "s_bitcmp1_b32 %[tmp], 0\n"
            "s_setprio 2\n"
            "s_cbranch_scc0 Lq9_%=\n"
            "s_setprio 3\n"
            "Lq9_%=:\n"
            "s_cmp_lg_u32 %[nst], 2\n"
            "s_cbranch_scc1 Ld2_%=\n"
            "v_mov_b32 v223, 0\n"
            "global_load_dword v223, v223, %[pfl] sc1\n"
            "s_branch Ld2_%=\n"
            "Lf00_%=:\n"
            "s_call_b64 %[ra], Lflush0_%=\n"
            "s_branch Lr00_%=\n"
            "Lf01_%=:\n"
            "s_call_b64 %[ra], Lflush0_%=\n"
            "s_branch Lr01_%=\n"
            "Lf02_%=:\n"
            "s_call_b64 %[ra], Lflush0_%=\n"
            "s_branch Lr02_%=\n"
            "Lf03_%=:\n"
            "s_call_b64 %[ra], Lflush0_%=\n"
            "s_branch Lr03_%=\n"
            "Lflush0_%=:\n"
            "v_mov_b32 v184, %[pfa]\n"
            "s_bitcmp0_b32 %[fm], 0\n"
            "s_cbranch_scc1 Lfa0_%=\n"
            "Lfw_%=:\n"
            "ds_read_b32 v185, v184\n"
            "ds_read_b128 v[176:179], v231 offset:0\n"
            "ds_read_b128 v[180:183], v231 offset:1024\n"
            "s_waitcnt lgkmcnt(0)\n"
            "v_cmp_eq_u32 vcc, 0, v185\n"
            "s_cbranch_vccnz Lfw_%=\n"
            "v_mfma_f32_4x4x4_16b_bf16 v[0:3], v[224:225], v[176:177], v[0:3]\n"
            "v_mfma_f32_4x4x4_16b_bf16 v[4:7], v[224:225], v[178:179], v[4:7]\n"
            "v_mfma_f32_4x4x4_16b_bf16 v[8:11], v[224:225], v[180:181], v[8:11]\n"
            "v_mfma_f32_4x4x4_16b_bf16 v[12:15], v[224:225], v[182:183], v[12:15]\n"
            "s_nop 1\n"
            "Lfa0_%=:\n"
            "v_cvt_pk_bf16_f32 v176, v0, v1\n"
            "v_cvt_pk_bf16_f32 v177, v2, v3\n"
            "v_cvt_pk_bf16_f32 v178, v4, v5\n"
            "v_cvt_pk_bf16_f32 v179, v6, v7\n"
            "v_cvt_pk_bf16_f32 v180, v8, v9\n"
            "v_cvt_pk_bf16_f32 v181, v10, v11\n"
            "v_cvt_pk_bf16_f32 v182, v12, v13\n"
            "v_cvt_pk_bf16_f32 v183, v14, v15\n"
            "s_bitcmp1_b32 %[fm], 1\n"
            "s_cbranch_scc1 Lpark0_%=\n"
            "buffer_store_dwordx4 v[176:179], v215, %[rsw], 0 offen offset:0 sc1\n"
            "buffer_store_dwordx4 v[180:183], v215, %[rsw], 0 offen offset:256 sc1\n"
            "s_branch Lzero0_%=\n"
            "Lpark0_%=:\n"
            "ds_write_b128 v231, v[176:179] offset:0\n"
            "ds_write_b128 v231, v[180:183] offset:1024\n"
            "Lzero0_%=:\n"
            "v_mov_b64 v[0:1], 0\n"
            "v_mov_b64 v[2:3], 0\n"
            "v_mov_b64 v[4:5], 0\n"
            "v_mov_b64 v[6:7], 0\n"
            "v_mov_b64 v[8:9], 0\n"
            "v_mov_b64 v[10:11], 0\n"
            "v_mov_b64 v[12:13], 0\n"
            "v_mov_b64 v[14:15], 0\n"
            "s_setpc_b64 %[ra]\n"
            "Lf10_%=:\n"
            "s_call_b64 %[ra], Lflush1_%=\n"
            "s_branch Lr10_%=\n"
            "Lf11_%=:\n"
            "s_call_b64 %[ra], Lflush1_%=\n"
            "s_branch Lr11_%=\n"
            "Lf12_%=:\n"
            "s_call_b64 %[ra], Lflush1_%=\n"
            "s_branch Lr12_%=\n"
            "Lf13_%=:\n"
            "s_call_b64 %[ra], Lflush1_%=\n"
            "s_branch Lr13_%=\n"
            "Lflush1_%=:\n"
            "v_mov_b32 v184, %[pfa]\n"
            "s_bitcmp0_b32 %[fm], 0\n"
            "s_cbranch_scc1 Lfa1_%=\n"
            "ds_read_b128 v[176:179], v231 offset:2048\n"
            "ds_read_b128 v[180:183], v231 offset:3072\n"
            "s_waitcnt lgkmcnt(0)\n"
            "v_mfma_f32_4x4x4_16b_bf16 v[16:19], v[224:225], v[176:177], v[16:19]\n"
            "v_mfma_f32_4x4x4_16b_bf16 v[20:23], v[224:225], v[178:179], v[20:23]\n"
            "v_mfma_f32_4x4x4_16b_bf16 v[24:27], v[224:225], v[180:181], v[24:27]\n"
            "v_mfma_f32_4x4x4_16b_bf16 v[28:31], v[224:225], v[182:183], v[28:31]\n"
            "s_nop 1\n"
            "Lfa1_%=:\n"
            "v_cvt_pk_bf16_f32 v176, v16, v17\n"
            "v_cvt_pk_bf16_f32 v177, v18, v19\n"
            "v_cvt_pk_bf16_f32 v178, v20, v21\n"
            "v_cvt_pk_bf16_f32 v179, v22, v23\n"
            "v_cvt_pk_bf16_f32 v180, v24, v25\n"
            "v_cvt_pk_bf16_f32 v181, v26, v27\n"
            "v_cvt_pk_bf16_f32 v182, v28, v29\n"
            "v_cvt_pk_bf16_f32 v183, v30, v31\n"
            "s_bitcmp1_b32 %[fm], 1\n"
            "s_cbranch_scc1 Lpark1_%=\n"
            "buffer_store_dwordx4 v[176:179], v215, %[rsw], 0 offen offset:512 sc1\n"
            "buffer_store_dwordx4 v[180:183], v215, %[rsw], 0 offen offset:768 sc1\n"
            "s_branch Lzero1_%=\n"
            "Lpark1_%=:\n"
            "ds_write_b128 v231, v[176:179] offset:2048\n"
            "ds_write_b128 v231, v[180:183] offset:3072\n"
            "ds_write_b32 v184, v184\n"
            "Lzero1_%=:\n"
            "v_mov_b64 v[16:17], 0\n"
            "v_mov_b64 v[18:19], 0\n"
            "v_mov_b64 v[20:21], 0\n"
            "v_mov_b64 v[22:23], 0\n"
            "v_mov_b64 v[24:25], 0\n"
            "v_mov_b64 v[26:27], 0\n"
            "v_mov_b64 v[28:29], 0\n"
            "v_mov_b64 v[30:31], 0\n"
            "s_setpc_b64 %[ra]\n"
            "Lspin_%=:\n"
            "s_mov_b32 %[lens], %[spin]\n"
            "Lsp1_%=:\n"
            "v_readfirstlane_b32 %[nst], v223\n"
            "s_sub_u32 %[lens], %[lens], 1\n"
            "s_cmp_eq_u32 %[lens], 0\n"
            "s_cbranch_scc1 Lsp2_%=\n"
            "s_cmp_eq_u32 %[nst], %[tag]\n"
            "s_cbranch_scc1 Lsp2_%=\n"
            "s_sleep 1\n"
            "v_mov_b32 v223, 0\n"
            "global_load_dword v223, v223, %[pfl] sc1\n"
            "s_waitcnt vmcnt(0)\n"
            "s_branch Lsp1_%=\n"
            "Lsp2_%=:\n"
            "buffer_load_dwordx4 v[32:35], v217, %[rsw], %[oagg] offen offset:0 sc1\n"
            "buffer_load_dwordx4 v[36:39], v217, %[rsw], %[oagg] offen offset:256 sc1\n"
            "buffer_load_dwordx4 v[48:51], v217, %[rsw], %[oagg] offen offset:512 sc1\n"
            "buffer_load_dwordx4 v[52:55], v217, %[rsw], %[oagg] offen offset:768 sc1\n"
            "s_branch Lgo_%=\n"
            "Llast_%=:\n"
            "s_waitcnt vmcnt(16)\n"
            "v_readfirstlane_b32 %[nst], v223\n"
            "v_mov_b32 v222, v217\n"
            "s_cmp_eq_u32 %[nst], %[tag]\n"
            "s_cbranch_scc1 Lp4_%=\n"
            "v_bfrev_b32 v222, 1\n"
            "v_mov_b32 v223, 0\n"
            "global_load_dword v223, v223, %[pfl] sc1\n"
            "Lp4_%=:\n"
            "v_mfma_f32_16x16x32_bf16 v[136:139], v[192:195], v[120:123], 0\n"
            "v_exp_f32 v160, v128\n"
            "v_exp_f32 v161, v129\n"
            "s_waitcnt vmcnt(13) lgkmcnt(0)\n"
            "v_mfma_f32_4x4x4_16b_bf16 v[176:179], v[208:209], v[32:33], 0 cbsz:2\n"
            "v_exp_f32 v162, v130\n"
            "v_mfma_f32_4x4x4_16b_bf16 v[180:183], v[208:209], v[34:35], 0 cbsz:2\n"
            "v_exp_f32 v163, v131\n"
            "v_mfma_f32_4x4x4_16b_bf16 v[184:187], v[208:209], v[36:37], 0 cbsz:2\n"
            "v_exp_f32 v164, v132\n"
            "v_mfma_f32_4x4x4_16b_bf16 v[188:191], v[208:209], v[38:39], 0 cbsz:2\n"
            "v_exp_f32 v165, v133\n"
            "v_exp_f32 v166, v134\n"
            "buffer_load_dwordx4 v[32:35], v222, %[rsw], %[oagg] offen offset:0 sc1\n"
            "buffer_load_dwordx4 v[36:39], v222, %[rsw], %[oagg] offen offset:256 sc1\n"
            "ds_read_b128 v[236:239], v228 offset:0\n"
            "ds_read_b128 v[240:243], v228 offset:1024\n"
            "ds_read_b128 v[244:247], v228 offset:2048\n"
            "ds_read_b128 v[248:251], v228 offset:3072\n"
            "ds_read_b128 v[96:99], v228 offset:4096\n"
            "v_exp_f32 v167, v135\n"
            "v_pk_fma_f32 v[0:1], v[144:145], v[176:177], v[0:1] op_sel_hi:[0,1,1]\n"
            "v_pk_fma_f32 v[2:3], v[144:145], v[178:179], v[2:3] op_sel_hi:[0,1,1]\n"
            "v_pk_fma_f32 v[4:5], v[148:149], v[180:181], v[4:5] op_sel_hi:[0,1,1]\n"
            "v_pk_fma_f32 v[6:7], v[148:149], v[182:183], v[6:7] op_sel_hi:[0,1,1]\n"
            "v_pk_fma_f32 v[8:9], v[152:153], v[184:185], v[8:9] op_sel_hi:[0,1,1]\n"
            "v_pk_fma_f32 v[10:11], v[152:153], v[186:187], v[10:11] op_sel_hi:[0,1,1]\n"
            "v_pk_fma_f32 v[12:13], v[156:157], v[188:189], v[12:13] op_sel_hi:[0,1,1]\n"
            "v_pk_fma_f32 v[14:15], v[156:157], v[190:191], v[14:15] op_sel_hi:[0,1,1]\n"
            "v_pk_add_f32 v[160:161], v[160:161], 1.0 op_sel_hi:[1,0]\n"
            "v_pk_add_f32 v[162:163], v[162:163], 1.0 op_sel_hi:[1,0]\n"
            "v_pk_add_f32 v[164:165], v[164:165], 1.0 op_sel_hi:[1,0]\n"
            "v_pk_add_f32 v[166:167], v[166:167], 1.0 op_sel_hi:[1,0]\n"
            "v_mfma_f32_16x16x32_bf16 v[140:143], v[192:195], v[124:127], 0\n"
            "v_rcp_f32 v160, v160\n"
            "v_rcp_f32 v161, v161\n"
            "v_mfma_f32_4x4x4_16b_bf16 v[176:179], v[208:209], v[48:49], 0 cbsz:2 abid:1\n"
            "v_rcp_f32 v162, v162\n"
            "v_mfma_f32_4x4x4_16b_bf16 v[180:183], v[208:209], v[50:51], 0 cbsz:2 abid:1\n"
            "v_rcp_f32 v163, v163\n"
            "v_mfma_f32_4x4x4_16b_bf16 v[184:187], v[208:209], v[52:53], 0 cbsz:2 abid:1\n"
            "v_rcp_f32 v164, v164\n"
            "v_mfma_f32_4x4x4_16b_bf16 v[188:191], v[208:209], v[54:55], 0 cbsz:2 abid:1\n"
            "v_rcp_f32 v165, v165\n"
            "v_rcp_f32 v166, v166\n"
            "buffer_load_dwordx4 v[48:51], v222, %[rsw], %[oagg] offen offset:512 sc1\n"
            "buffer_load_dwordx4 v[52:55], v222, %[rsw], %[oagg] offen offset:768 sc1\n"
            "ds_read_b128 v[100:103], v228 offset:5120\n"
            "ds_read_b128 v[104:107], v228 offset:6144\n"
            "ds_read_b128 v[108:111], v228 offset:7168\n"
            "ds_read_b128 v[112:115], v228 offset:8192\n"
            "ds_read_b128 v[116:119], v228 offset:9216\n"
            "ds_read_b128 v[196:199], v228 offset:10240\n"
            "v_rcp_f32 v167, v167\n"
            "v_pk_fma_f32 v[0:1], v[144:145], v[176:177], v[0:1] op_sel:[1,0,0]\n"
            "v_pk_fma_f32 v[2:3], v[144:145], v[178:179], v[2:3] op_sel:[1,0,0]\n"
            "v_pk_fma_f32 v[4:5], v[148:149], v[180:181], v[4:5] op_sel:[1,0,0]\n"
            "v_pk_fma_f32 v[6:7], v[148:149], v[182:183], v[6:7] op_sel:[1,0,0]\n"
            "v_pk_fma_f32 v[8:9], v[152:153], v[184:185], v[8:9] op_sel:[1,0,0]\n"
            "v_pk_fma_f32 v[10:11], v[152:153], v[186:187], v[10:11] op_sel:[1,0,0]\n"
            "v_pk_fma_f32 v[12:13], v[156:157], v[188:189], v[12:13] op_sel:[1,0,0]\n"
            "v_pk_fma_f32 v[14:15], v[156:157], v[190:191], v[14:15] op_sel:[1,0,0]\n"
            "v_pk_mul_f32 v[160:161], v[160:161], v[128:129]\n"
            "v_pk_mul_f32 v[162:163], v[162:163], v[130:131]\n"
            "v_pk_mul_f32 v[164:165], v[164:165], v[132:133]\n"
            "v_pk_mul_f32 v[166:167], v[166:167], v[134:135]\n"
            "v_exp_f32 v168, v136\n"
            "v_exp_f32 v169, v137\n"
            "s_waitcnt vmcnt(12)\n"
            "v_mfma_f32_4x4x4_16b_bf16 v[176:179], v[208:209], v[64:65], 0 cbsz:2 abid:2\n"
            "v_exp_f32 v170, v138\n"
            "v_mfma_f32_4x4x4_16b_bf16 v[180:183], v[208:209], v[66:67], 0 cbsz:2 abid:2\n"
            "v_exp_f32 v171, v139\n"
            "v_mfma_f32_4x4x4_16b_bf16 v[184:187], v[208:209], v[68:69], 0 cbsz:2 abid:2\n"
            "v_exp_f32 v172, v140\n"
            "v_mfma_f32_4x4x4_16b_bf16 v[188:191], v[208:209], v[70:71], 0 cbsz:2 abid:2\n"
            "v_exp_f32 v173, v141\n"
            "v_exp_f32 v174, v142\n"
            "ds_read_b128 v[200:203], v228 offset:11264\n"
            "ds_read_b128 v[204:207], v228 offset:12288\n"
            "ds_read_b128 v[232:235], v228 offset:13312\n"
            "v_exp_f32 v175, v143\n"
            "v_pk_fma_f32 v[0:1], v[146:147], v[176:177], v[0:1] op_sel_hi:[0,1,1]\n"
            "v_pk_fma_f32 v[2:3], v[146:147], v[178:179], v[2:3] op_sel_hi:[0,1,1]\n"
            "v_pk_fma_f32 v[4:5], v[150:151], v[180:181], v[4:5] op_sel_hi:[0,1,1]\n"
            "v_pk_fma_f32 v[6:7], v[150:151], v[182:183], v[6:7] op_sel_hi:[0,1,1]\n"
            "v_pk_fma_f32 v[8:9], v[154:155], v[184:185], v[8:9] op_sel_hi:[0,1,1]\n"
            "v_pk_fma_f32 v[10:11], v[154:155], v[186:187], v[10:11] op_sel_hi:[0,1,1]\n"
            "v_pk_fma_f32 v[12:13], v[158:159], v[188:189], v[12:13] op_sel_hi:[0,1,1]\n"
            "v_pk_fma_f32 v[14:15], v[158:159], v[190:191], v[14:15] op_sel_hi:[0,1,1]\n"
            "v_pk_add_f32 v[168:169], v[168:169], 1.0 op_sel_hi:[1,0]\n"
            "v_pk_add_f32 v[170:171], v[170:171], 1.0 op_sel_hi:[1,0]\n"
            "v_pk_add_f32 v[172:173], v[172:173], 1.0 op_sel_hi:[1,0]\n"
            "v_pk_add_f32 v[174:175], v[174:175], 1.0 op_sel_hi:[1,0]\n"
            "v_rcp_f32 v168, v168\n"
            "v_rcp_f32 v169, v169\n"
            "v_mfma_f32_4x4x4_16b_bf16 v[176:179], v[208:209], v[80:81], 0 cbsz:2 abid:3\n"
            "v_rcp_f32 v170, v170\n"
            "v_mfma_f32_4x4x4_16b_bf16 v[180:183], v[208:209], v[82:83], 0 cbsz:2 abid:3\n"
            "v_rcp_f32 v171, v171\n"
            "v_mfma_f32_4x4x4_16b_bf16 v[184:187], v[208:209], v[84:85], 0 cbsz:2 abid:3\n"
            "v_rcp_f32 v172, v172\n"
            "v_mfma_f32_4x4x4_16b_bf16 v[188:191], v[208:209], v[86:87], 0 cbsz:2 abid:3\n"
            "v_rcp_f32 v173, v173\n"
            "v_rcp_f32 v174, v174\n"
            "ds_read_b128 v[120:123], v228 offset:14336\n"
            "ds_read_b128 v[124:127], v228 offset:15360\n"
            "ds_read_b128 v[192:195], v228 offset:16384\n"
            "ds_read_b128 v[128:131], v228 offset:17408\n"
            "ds_read_b128 v[132:135], v228 offset:18432\n"
            "v_rcp_f32 v175, v175\n"
            "v_pk_fma_f32 v[0:1], v[146:147], v[176:177], v[0:1] op_sel:[1,0,0]\n"
            "v_pk_fma_f32 v[2:3], v[146:147], v[178:179], v[2:3] op_sel:[1,0,0]\n"
            "v_pk_fma_f32 v[4:5], v[150:151], v[180:181], v[4:5] op_sel:[1,0,0]\n"
            "v_pk_fma_f32 v[6:7], v[150:151], v[182:183], v[6:7] op_sel:[1,0,0]\n"
            "v_pk_fma_f32 v[8:9], v[154:155], v[184:185], v[8:9] op_sel:[1,0,0]\n"
            "v_pk_fma_f32 v[10:11], v[154:155], v[186:187], v[10:11] op_sel:[1,0,0]\n"
            "v_pk_fma_f32 v[12:13], v[158:159], v[188:189], v[12:13] op_sel:[1,0,0]\n"
            "v_pk_fma_f32 v[14:15], v[158:159], v[190:191], v[14:15] op_sel:[1,0,0]\n"
            "v_pk_mul_f32 v[168:169], v[168:169], v[136:137]\n"
            "v_pk_mul_f32 v[170:171], v[170:171], v[138:139]\n"
            "v_pk_mul_f32 v[172:173], v[172:173], v[140:141]\n"
            "v_pk_mul_f32 v[174:175], v[174:175], v[142:143]\n"
            "s_waitcnt vmcnt(8)\n"
            "v_mfma_f32_4x4x4_16b_bf16 v[176:179], v[208:209], v[40:41], 0 cbsz:2\n"
            "v_mfma_f32_4x4x4_16b_bf16 v[180:183], v[208:209], v[42:43], 0 cbsz:2\n"
            "v_mfma_f32_4x4x4_16b_bf16 v[184:187], v[208:209], v[44:45], 0 cbsz:2\n"
            "v_mfma_f32_4x4x4_16b_bf16 v[188:191], v[208:209], v[46:47], 0 cbsz:2\n"
            "ds_read_b128 v[136:139], v228 offset:19456\n"
            "ds_read_b128 v[140:143], v228 offset:20480\n"
            "ds_read_b128 v[144:147], v228 offset:21504\n"
            "ds_read_b128 v[148:151], v228 offset:22528\n"
            "ds_read_b128 v[152:155], v228 offset:23552\n"
            "ds_read_b128 v[156:159], v228 offset:24576\n"
            "s_nop 1\n"
            "v_pk_fma_f32 v[16:17], v[160:161], v[176:177], v[16:17] op_sel_hi:[0,1,1]\n"
            "v_pk_fma_f32 v[18:19], v[160:161], v[178:179], v[18:19] op_sel_hi:[0,1,1]\n"
            "v_pk_fma_f32 v[20:21], v[164:165], v[180:181], v[20:21] op_sel_hi:[0,1,1]\n"
            "v_pk_fma_f32 v[22:23], v[164:165], v[182:183], v[22:23] op_sel_hi:[0,1,1]\n"
            "v_pk_fma_f32 v[24:25], v[168:169], v[184:185], v[24:25] op_sel_hi:[0,1,1]\n"
            "v_pk_fma_f32 v[26:27], v[168:169], v[186:187], v[26:27] op_sel_hi:[0,1,1]\n"
            "v_pk_fma_f32 v[28:29], v[172:173], v[188:189], v[28:29] op_sel_hi:[0,1,1]\n"
            "v_pk_fma_f32 v[30:31], v[172:173], v[190:191], v[30:31] op_sel_hi:[0,1,1]\n"
            "s_cmp_le_u32 %[njl], 1\n"
            "s_cbranch_scc1 Ltail_%=\n"
            "v_mfma_f32_4x4x4_16b_bf16 v[176:179], v[208:209], v[56:57], 0 cbsz:2 abid:1\n"
            "v_mfma_f32_4x4x4_16b_bf16 v[180:183], v[208:209], v[58:59], 0 cbsz:2 abid:1\n"
            "v_mfma_f32_4x4x4_16b_bf16 v[184:187], v[208:209], v[60:61], 0 cbsz:2 abid:1\n"
            "v_mfma_f32_4x4x4_16b_bf16 v[188:191], v[208:209], v[62:63], 0 cbsz:2 abid:1\n"
            "s_nop 1\n"
            "v_pk_fma_f32 v[16:17], v[160:161], v[176:177], v[16:17] op_sel:[1,0,0]\n"
            "v_pk_fma_f32 v[18:19], v[160:161], v[178:179], v[18:19] op_sel:[1,0,0]\n"
            "v_pk_fma_f32 v[20:21], v[164:165], v[180:181], v[20:21] op_sel:[1,0,0]\n"
            "v_pk_fma_f32 v[22:23], v[164:165], v[182:183], v[22:23] op_sel:[1,0,0]\n"
            "v_pk_fma_f32 v[24:25], v[168:169], v[184:185], v[24:25] op_sel:[1,0,0]\n"
            "v_pk_fma_f32 v[26:27], v[168:169], v[186:187], v[26:27] op_sel:[1,0,0]\n"
            "v_pk_fma_f32 v[28:29], v[172:173], v[188:189], v[28:29] op_sel:[1,0,0]\n"
            "v_pk_fma_f32 v[30:31], v[172:173], v[190:191], v[30:31] op_sel:[1,0,0]\n"
            "s_cmp_le_u32 %[njl], 2\n"
            "s_cbranch_scc1 Ltail_%=\n"
            "s_waitcnt vmcnt(4)\n"
            "v_mfma_f32_4x4x4_16b_bf16 v[176:179], v[208:209], v[72:73], 0 cbsz:2 abid:2\n"
            "v_mfma_f32_4x4x4_16b_bf16 v[180:183], v[208:209], v[74:75], 0 cbsz:2 abid:2\n"
            "v_mfma_f32_4x4x4_16b_bf16 v[184:187], v[208:209], v[76:77], 0 cbsz:2 abid:2\n"
            "v_mfma_f32_4x4x4_16b_bf16 v[188:191], v[208:209], v[78:79], 0 cbsz:2 abid:2\n"
            "s_nop 1\n"
            "v_pk_fma_f32 v[16:17], v[162:163], v[176:177], v[16:17] op_sel_hi:[0,1,1]\n"
            "v_pk_fma_f32 v[18:19], v[162:163], v[178:179], v[18:19] op_sel_hi:[0,1,1]\n"
            "v_pk_fma_f32 v[20:21], v[166:167], v[180:181], v[20:21] op_sel_hi:[0,1,1]\n"
            "v_pk_fma_f32 v[22:23], v[166:167], v[182:183], v[22:23] op_sel_hi:[0,1,1]\n"
            "v_pk_fma_f32 v[24:25], v[170:171], v[184:185], v[24:25] op_sel_hi:[0,1,1]\n"
            "v_pk_fma_f32 v[26:27], v[170:171], v[186:187], v[26:27] op_sel_hi:[0,1,1]\n"
            "v_pk_fma_f32 v[28:29], v[174:175], v[188:189], v[28:29] op_sel_hi:[0,1,1]\n"
            "v_pk_fma_f32 v[30:31], v[174:175], v[190:191], v[30:31] op_sel_hi:[0,1,1]\n"
            "s_cmp_le_u32 %[njl], 3\n"
            "s_cbranch_scc1 Ltail_%=\n"
            "v_mfma_f32_4x4x4_16b_bf16 v[176:179], v[208:209], v[88:89], 0 cbsz:2 abid:3\n"
            "v_mfma_f32_4x4x4_16b_bf16 v[180:183], v[208:209], v[90:91], 0 cbsz:2 abid:3\n"
            "v_mfma_f32_4x4x4_16b_bf16 v[184:187], v[208:209], v[92:93], 0 cbsz:2 abid:3\n"
            "v_mfma_f32_4x4x4_16b_bf16 v[188:191], v[208:209], v[94:95], 0 cbsz:2 abid:3\n"
            "s_nop 1\n"
            "v_pk_fma_f32 v[16:17], v[162:163], v[176:177], v[16:17] op_sel:[1,0,0]\n"
            "v_pk_fma_f32 v[18:19], v[162:163], v[178:179], v[18:19] op_sel:[1,0,0]\n"
            "v_pk_fma_f32 v[20:21], v[166:167], v[180:181], v[20:21] op_sel:[1,0,0]\n"
            "v_pk_fma_f32 v[22:23], v[166:167], v[182:183], v[22:23] op_sel:[1,0,0]\n"
            "v_pk_fma_f32 v[24:25], v[170:171], v[184:185], v[24:25] op_sel:[1,0,0]\n"
            "v_pk_fma_f32 v[26:27], v[170:171], v[186:187], v[26:27] op_sel:[1,0,0]\n"
            "v_pk_fma_f32 v[28:29], v[174:175], v[188:189], v[28:29] op_sel:[1,0,0]\n"
            "v_pk_fma_f32 v[30:31], v[174:175], v[190:191], v[30:31] op_sel:[1,0,0]\n"
            "Ltail_%=:\n"
            "s_setprio 3\n"
            "s_waitcnt vmcnt(4)\n"
            "ds_read_b128 v[72:75], v228 offset:25600\n"
            "ds_read_b128 v[76:79], v228 offset:26624\n"
            "ds_read_b128 v[88:91], v228 offset:27648\n"
            "ds_read_b128 v[92:95], v228 offset:28672\n"
            "ds_read_b128 v[160:163], v228 offset:29696\n"
            "ds_read_b128 v[164:167], v228 offset:30720\n"
            "ds_read_b128 v[168:171], v228 offset:31744\n"
            "s_cmp_lg_u32 %[nst], %[tag]\n"
            "s_cbranch_scc1 Lspin_%=\n"
            "Lgo_%=:\n"
            "buffer_load_dwordx4 v[40:43], v218, %[rsw], %[oagg] offen offset:0 sc1\n"
            "buffer_load_dwordx4 v[44:47], v218, %[rsw], %[oagg] offen offset:256 sc1\n"
            "buffer_load_dwordx4 v[56:59], v218, %[rsw], %[oagg] offen offset:512 sc1\n"
            "buffer_load_dwordx4 v[60:63], v218, %[rsw], %[oagg] offen offset:768 sc1\n"
            "s_waitcnt vmcnt(4)\n"
            "v_mfma_f32_4x4x4_16b_bf16 v[0:3], v[224:225], v[32:33], v[0:3]\n"
            "v_mfma_f32_4x4x4_16b_bf16 v[4:7], v[224:225], v[34:35], v[4:7]\n"
            "v_mfma_f32_4x4x4_16b_bf16 v[8:11], v[224:225], v[36:37], v[8:11]\n"
            "v_mfma_f32_4x4x4_16b_bf16 v[12:15], v[224:225], v[38:39], v[12:15]\n"
            "v_mfma_f32_4x4x4_16b_bf16 v[16:19], v[224:225], v[48:49], v[16:19]\n"
            "v_mfma_f32_4x4x4_16b_bf16 v[20:23], v[224:225], v[50:51], v[20:23]\n"
            "v_mfma_f32_4x4x4_16b_bf16 v[24:27], v[224:225], v[52:53], v[24:27]\n"
            "v_mfma_f32_4x4x4_16b_bf16 v[28:31], v[224:225], v[54:55], v[28:31]\n"
            "s_nop 1\n"
            "v_cvt_pk_bf16_f32 v176, v0, v4\n"
            "v_cvt_pk_bf16_f32 v177, v8, v12\n"
            "v_cvt_pk_bf16_f32 v178, v16, v20\n"
            "v_cvt_pk_bf16_f32 v179, v24, v28\n"
            "ds_write_b128 v226, v[176:179] offset:0\n"
            "v_cvt_pk_bf16_f32 v180, v1, v5\n"
            "v_cvt_pk_bf16_f32 v181, v9, v13\n"
            "v_cvt_pk_bf16_f32 v182, v17, v21\n"
            "v_cvt_pk_bf16_f32 v183, v25, v29\n"
            "ds_write_b128 v226, v[180:183] offset:272\n"
            "v_cvt_pk_bf16_f32 v184, v2, v6\n"
            "v_cvt_pk_bf16_f32 v185, v10, v14\n"
            "v_cvt_pk_bf16_f32 v186, v18, v22\n"
            "v_cvt_pk_bf16_f32 v187, v26, v30\n"
            "ds_write_b128 v226, v[184:187] offset:544\n"
            "v_cvt_pk_bf16_f32 v188, v3, v7\n"
            "v_cvt_pk_bf16_f32 v189, v11, v15\n"
            "v_cvt_pk_bf16_f32 v190, v19, v23\n"
            "v_cvt_pk_bf16_f32 v191, v27, v31\n"
            "ds_write_b128 v226, v[188:191] offset:816\n"
            "ds_read_b128 v[176:179], v227 offset:0\n"
            "ds_read_b128 v[180:183], v227 offset:64\n"
            "ds_read_b128 v[184:187], v227 offset:128\n"
            "ds_read_b128 v[188:191], v227 offset:192\n"
            "s_waitcnt lgkmcnt(3)\n"
            "v_mfma_f32_16x16x32_bf16 v[0:3], v[176:179], v[236:239], 0\n"
            "v_mfma_f32_16x16x32_bf16 v[4:7], v[176:179], v[96:99], 0\n"
            "s_waitcnt lgkmcnt(2)\n"
            "v_mfma_f32_16x16x32_bf16 v[0:3], v[180:183], v[240:243], v[0:3]\n"
            "v_mfma_f32_16x16x32_bf16 v[4:7], v[180:183], v[100:103], v[4:7]\n"
            "s_waitcnt lgkmcnt(1)\n"
            "v_mfma_f32_16x16x32_bf16 v[0:3], v[184:187], v[244:247], v[0:3]\n"
            "v_mfma_f32_16x16x32_bf16 v[4:7], v[184:187], v[104:107], v[4:7]\n"
            "s_waitcnt lgkmcnt(0)\n"
            "v_mfma_f32_16x16x32_bf16 v[0:3], v[188:191], v[248:251], v[0:3]\n"
            "v_mfma_f32_16x16x32_bf16 v[4:7], v[188:191], v[108:111], v[4:7]\n"
            "v_mfma_f32_16x16x32_bf16 v[8:11], v[176:179], v[112:115], 0\n"
            "v_mfma_f32_16x16x32_bf16 v[12:15], v[176:179], v[204:207], 0\n"
            "v_mfma_f32_16x16x32_bf16 v[8:11], v[180:183], v[116:119], v[8:11]\n"
            "v_mfma_f32_16x16x32_bf16 v[12:15], v[180:183], v[232:235], v[12:15]\n"
            "v_mfma_f32_16x16x32_bf16 v[8:11], v[184:187], v[196:199], v[8:11]\n"
            "v_mfma_f32_16x16x32_bf16 v[12:15], v[184:187], v[120:123], v[12:15]\n"
            "v_mfma_f32_16x16x32_bf16 v[8:11], v[188:191], v[200:203], v[8:11]\n"
            "v_mfma_f32_16x16x32_bf16 v[12:15], v[188:191], v[124:127], v[12:15]\n"
            "v_cvt_pk_bf16_f32 v0, v0, v1\n"
            "v_cvt_pk_bf16_f32 v1, v2, v3\n"
            "v_cvt_pk_bf16_f32 v2, v4, v5\n"
            "v_cvt_pk_bf16_f32 v3, v6, v7\n"
            "buffer_store_dwordx4 v[0:3], v217, %[rso], 0 offen offset:0 sc1\n"
            "v_mfma_f32_16x16x32_bf16 v[16:19], v[176:179], v[192:195], 0\n"
            "v_mfma_f32_16x16x32_bf16 v[20:23], v[176:179], v[140:143], 0\n"
            "v_mfma_f32_16x16x32_bf16 v[16:19], v[180:183], v[128:131], v[16:19]\n"
            "v_mfma_f32_16x16x32_bf16 v[20:23], v[180:183], v[144:147], v[20:23]\n"
            "v_mfma_f32_16x16x32_bf16 v[16:19], v[184:187], v[132:135], v[16:19]\n"
            "v_mfma_f32_16x16x32_bf16 v[20:23], v[184:187], v[148:151], v[20:23]\n"
            "v_mfma_f32_16x16x32_bf16 v[16:19], v[188:191], v[136:139], v[16:19]\n"
            "v_mfma_f32_16x16x32_bf16 v[20:23], v[188:191], v[152:155], v[20:23]\n"
            "v_cvt_pk_bf16_f32 v8, v8, v9\n"
            "v_cvt_pk_bf16_f32 v9, v10, v11\n"
            "v_cvt_pk_bf16_f32 v10, v12, v13\n"
            "v_cvt_pk_bf16_f32 v11, v14, v15\n"
            "buffer_store_dwordx4 v[8:11], v217, %[rso], 0 offen offset:256 sc1\n"
            "v_mfma_f32_16x16x32_bf16 v[24:27], v[176:179], v[156:159], 0\n"
            "v_mfma_f32_16x16x32_bf16 v[28:31], v[176:179], v[92:95], 0\n"
            "v_mfma_f32_16x16x32_bf16 v[24:27], v[180:183], v[72:75], v[24:27]\n"
            "v_mfma_f32_16x16x32_bf16 v[28:31], v[180:183], v[160:163], v[28:31]\n"
            "v_mfma_f32_16x16x32_bf16 v[24:27], v[184:187], v[76:79], v[24:27]\n"
            "v_mfma_f32_16x16x32_bf16 v[28:31], v[184:187], v[164:167], v[28:31]\n"
            "v_mfma_f32_16x16x32_bf16 v[24:27], v[188:191], v[88:91], v[24:27]\n"
            "v_mfma_f32_16x16x32_bf16 v[28:31], v[188:191], v[168:171], v[28:31]\n"
            "v_cvt_pk_bf16_f32 v16, v16, v17\n"
            "v_cvt_pk_bf16_f32 v17, v18, v19\n"
            "v_cvt_pk_bf16_f32 v18, v20, v21\n"
            "v_cvt_pk_bf16_f32 v19, v22, v23\n"
            "buffer_store_dwordx4 v[16:19], v217, %[rso], 0 offen offset:512 sc1\n"
            "s_nop 2\n"
            "v_cvt_pk_bf16_f32 v24, v24, v25\n"
            "v_cvt_pk_bf16_f32 v25, v26, v27\n"
            "v_cvt_pk_bf16_f32 v26, v28, v29\n"
            "v_cvt_pk_bf16_f32 v27, v30, v31\n"
            "buffer_store_dwordx4 v[24:27], v217, %[rso], 0 offen offset:768 sc1\n"
            "s_cmp_lt_u32 %[two], 6\n"
            "s_cbranch_scc1 Lend_%=\n"
            "ds_read_b128 v[64:67], v231 offset:0\n"
            "ds_read_b128 v[68:71], v231 offset:1024\n"
            "ds_read_b128 v[80:83], v231 offset:2048\n"
            "ds_read_b128 v[84:87], v231 offset:3072\n"
            "s_waitcnt vmcnt(4) lgkmcnt(0)\n"
            "v_mfma_f32_4x4x4_16b_bf16 v[0:3], v[224:225], v[64:65], 0\n"
            "v_mfma_f32_4x4x4_16b_bf16 v[4:7], v[224:225], v[66:67], 0\n"
            "v_mfma_f32_4x4x4_16b_bf16 v[8:11], v[224:225], v[68:69], 0\n"
            "v_mfma_f32_4x4x4_16b_bf16 v[12:15], v[224:225], v[70:71], 0\n"
            "v_mfma_f32_4x4x4_16b_bf16 v[16:19], v[224:225], v[80:81], 0\n"
            "v_mfma_f32_4x4x4_16b_bf16 v[20:23], v[224:225], v[82:83], 0\n"
            "v_mfma_f32_4x4x4_16b_bf16 v[24:27], v[224:225], v[84:85], 0\n"
            "v_mfma_f32_4x4x4_16b_bf16 v[28:31], v[224:225], v[86:87], 0\n"
            "v_mfma_f32_4x4x4_16b_bf16 v[0:3], v[224:225], v[40:41], v[0:3]\n"
            "v_mfma_f32_4x4x4_16b_bf16 v[4:7], v[224:225], v[42:43], v[4:7]\n"
            "v_mfma_f32_4x4x4_16b_bf16 v[8:11], v[224:225], v[44:45], v[8:11]\n"
            "v_mfma_f32_4x4x4_16b_bf16 v[12:15], v[224:225], v[46:47], v[12:15]\n"
            "v_mfma_f32_4x4x4_16b_bf16 v[16:19], v[224:225], v[56:57], v[16:19]\n"
            "v_mfma_f32_4x4x4_16b_bf16 v[20:23], v[224:225], v[58:59], v[20:23]\n"
            "v_mfma_f32_4x4x4_16b_bf16 v[24:27], v[224:225], v[60:61], v[24:27]\n"
            "v_mfma_f32_4x4x4_16b_bf16 v[28:31], v[224:225], v[62:63], v[28:31]\n"
            "s_nop 1\n"
            "v_cvt_pk_bf16_f32 v176, v0, v4\n"
            "v_cvt_pk_bf16_f32 v177, v8, v12\n"
            "v_cvt_pk_bf16_f32 v178, v16, v20\n"
            "v_cvt_pk_bf16_f32 v179, v24, v28\n"
            "ds_write_b128 v226, v[176:179] offset:0\n"
            "v_cvt_pk_bf16_f32 v180, v1, v5\n"
            "v_cvt_pk_bf16_f32 v181, v9, v13\n"
            "v_cvt_pk_bf16_f32 v182, v17, v21\n"
            "v_cvt_pk_bf16_f32 v183, v25, v29\n"
            "ds_write_b128 v226, v[180:183] offset:272\n"
            "v_cvt_pk_bf16_f32 v184, v2, v6\n"
            "v_cvt_pk_bf16_f32 v185, v10, v14\n"
            "v_cvt_pk_bf16_f32 v186, v18, v22\n"
            "v_cvt_pk_bf16_f32 v187, v26, v30\n"
            "ds_write_b128 v226, v[184:187] offset:544\n"
            "v_cvt_pk_bf16_f32 v188, v3, v7\n"
            "v_cvt_pk_bf16_f32 v189, v11, v15\n"
            "v_cvt_pk_bf16_f32 v190, v19, v23\n"
            "v_cvt_pk_bf16_f32 v191, v27, v31\n"
            "ds_write_b128 v226, v[188:191] offset:816\n"
            "ds_read_b128 v[176:179], v227 offset:0\n"
            "ds_read_b128 v[180:183], v227 offset:64\n"
            "ds_read_b128 v[184:187], v227 offset:128\n"
            "ds_read_b128 v[188:191], v227 offset:192\n"
            "s_waitcnt lgkmcnt(3)\n"
            "v_mfma_f32_16x16x32_bf16 v[0:3], v[176:179], v[236:239], 0\n"
            "v_mfma_f32_16x16x32_bf16 v[4:7], v[176:179], v[96:99], 0\n"
            "s_waitcnt lgkmcnt(2)\n"
            "v_mfma_f32_16x16x32_bf16 v[0:3], v[180:183], v[240:243], v[0:3]\n"
            "v_mfma_f32_16x16x32_bf16 v[4:7], v[180:183], v[100:103], v[4:7]\n"
            "s_waitcnt lgkmcnt(1)\n"
            "v_mfma_f32_16x16x32_bf16 v[0:3], v[184:187], v[244:247], v[0:3]\n"
            "v_mfma_f32_16x16x32_bf16 v[4:7], v[184:187], v[104:107], v[4:7]\n"
            "s_waitcnt lgkmcnt(0)\n"
            "v_mfma_f32_16x16x32_bf16 v[0:3], v[188:191], v[248:251], v[0:3]\n"
            "v_mfma_f32_16x16x32_bf16 v[4:7], v[188:191], v[108:111], v[4:7]\n"
            "v_mfma_f32_16x16x32_bf16 v[8:11], v[176:179], v[112:115], 0\n"
            "v_mfma_f32_16x16x32_bf16 v[12:15], v[176:179], v[204:207], 0\n"
            "v_mfma_f32_16x16x32_bf16 v[8:11], v[180:183], v[116:119], v[8:11]\n"
            "v_mfma_f32_16x16x32_bf16 v[12:15], v[180:183], v[232:235], v[12:15]\n"
            "v_mfma_f32_16x16x32_bf16 v[8:11], v[184:187], v[196:199], v[8:11]\n"
            "v_mfma_f32_16x16x32_bf16 v[12:15], v[184:187], v[120:123], v[12:15]\n"
            "v_mfma_f32_16x16x32_bf16 v[8:11], v[188:191], v[200:203], v[8:11]\n"
            "v_mfma_f32_16x16x32_bf16 v[12:15], v[188:191], v[124:127], v[12:15]\n"
            "v_cvt_pk_bf16_f32 v0, v0, v1\n"
            "v_cvt_pk_bf16_f32 v1, v2, v3\n"
            "v_cvt_pk_bf16_f32 v2, v4, v5\n"
            "v_cvt_pk_bf16_f32 v3, v6, v7\n"
            "buffer_store_dwordx4 v[0:3], v218, %[rso], 0 offen offset:0 sc1\n"
            "v_mfma_f32_16x16x32_bf16 v[16:19], v[176:179], v[192:195], 0\n"
            "v_mfma_f32_16x16x32_bf16 v[20:23], v[176:179], v[140:143], 0\n"
            "v_mfma_f32_16x16x32_bf16 v[16:19], v[180:183], v[128:131], v[16:19]\n"
            "v_mfma_f32_16x16x32_bf16 v[20:23], v[180:183], v[144:147], v[20:23]\n"
            "v_mfma_f32_16x16x32_bf16 v[16:19], v[184:187], v[132:135], v[16:19]\n"
            "v_mfma_f32_16x16x32_bf16 v[20:23], v[184:187], v[148:151], v[20:23]\n"
            "v_mfma_f32_16x16x32_bf16 v[16:19], v[188:191], v[136:139], v[16:19]\n"
            "v_mfma_f32_16x16x32_bf16 v[20:23], v[188:191], v[152:155], v[20:23]\n"
            "v_cvt_pk_bf16_f32 v8, v8, v9\n"
            "v_cvt_pk_bf16_f32 v9, v10, v11\n"
            "v_cvt_pk_bf16_f32 v10, v12, v13\n"
            "v_cvt_pk_bf16_f32 v11, v14, v15\n"
            "buffer_store_dwordx4 v[8:11], v218, %[rso], 0 offen offset:256 sc1\n"
            "v_mfma_f32_16x16x32_bf16 v[24:27], v[176:179], v[156:159], 0\n"
            "v_mfma_f32_16x16x32_bf16 v[28:31], v[176:179], v[92:95], 0\n"
            "v_mfma_f32_16x16x32_bf16 v[24:27], v[180:183], v[72:75], v[24:27]\n"
            "v_mfma_f32_16x16x32_bf16 v[28:31], v[180:183], v[160:163], v[28:31]\n"
            "v_mfma_f32_16x16x32_bf16 v[24:27], v[184:187], v[76:79], v[24:27]\n"
            "v_mfma_f32_16x16x32_bf16 v[28:31], v[184:187], v[164:167], v[28:31]\n"
            "v_mfma_f32_16x16x32_bf16 v[24:27], v[188:191], v[88:91], v[24:27]\n"
            "v_mfma_f32_16x16x32_bf16 v[28:31], v[188:191], v[168:171], v[28:31]\n"
            "v_cvt_pk_bf16_f32 v16, v16, v17\n"
            "v_cvt_pk_bf16_f32 v17, v18, v19\n"
            "v_cvt_pk_bf16_f32 v18, v20, v21\n"
            "v_cvt_pk_bf16_f32 v19, v22, v23\n"
            "buffer_store_dwordx4 v[16:19], v218, %[rso], 0 offen offset:512 sc1\n"
            "s_nop 2\n"
            "v_cvt_pk_bf16_f32 v24, v24, v25\n"
            "v_cvt_pk_bf16_f32 v25, v26, v27\n"
            "v_cvt_pk_bf16_f32 v26, v28, v29\n"
            "v_cvt_pk_bf16_f32 v27, v30, v31\n"
            "buffer_store_dwordx4 v[24:27], v218, %[rso], 0 offen offset:768 sc1\n"
            "Lend_%=:\n"
            "s_waitcnt vmcnt(4)\n"
            : "+{v[32:63]}"(oxa), "+{v[64:95]}"(oxb), "+{v[96:127]}"(owr), "+{v[192:195]}"(efA), "+{v[196:199]}"(S), "+{v[200:203]}"(cgA), "+{v[208:223]}"(omi), "+{v[224:231]}"(ot),
              "+{v[232:235]}"(efN), "+{v[236:239]}"(jmp),
              [rem] "+s"(rem), [lens] "+s"(lens), [fm] "+s"(fm), [nst] "+s"(nst), [pb] "+s"(pb), [evt] "+s"(evt), [ra] "=&s"(ra), [tmp] "=&s"(tmp)
            : [rse] "s"(de), [rsx] "s"(dx), [rso] "s"(mkdesc(out, (unsigned)N * 1024u)), [rsw] "s"(mkdesc(ws, W_END)),
              [cnt] "s"(lds0 + L_MISC + 4), [pfa] "s"(lds0 + L_MISC + 16 + pw * 4), [tag] "s"(tag), [flg] "s"(ws + W_FLG + bx * 128), [pfl] "s"(ws + W_FLG + (bx ^ 4) * 128),
              [njl] "s"(((e2 - 1) & 3) + 1), [c1] "s"((w < 4 ? l2 : l1) - 4 * ((e2 + 3) >> 2) - 5), [c2] "s"(w < 4 ? l1 : l2), [two] "s"(w), [ptab] "s"((unsigned)__builtin_amdgcn_readfirstlane(w < 4 ? PTO : (w < 6 ? PTY : PTZ))), [oagg] "s"(W_AGG), [spin] "s"(SPIN_MAX)
            : "memory", "vcc", "scc", "v0", "v1", "v2", "v3", "v4", "v5", "v6", "v7", "v8", "v9", "v10", "v11", "v12", "v13", "v14", "v15", "v16", "v17", "v18", "v19", "v20", "v21", "v22", "v23", "v24", "v25", "v26", "v27", "v28", "v29", "v30", "v31", "v128", "v129", "v130", "v131", "v132", "v133", "v134", "v135", "v136", "v137", "v138", "v139", "v140", "v141", "v142", "v143", "v144", "v145", "v146", "v147", "v148", "v149", "v150", "v151", "v152", "v153", "v154", "v155", "v156", "v157", "v158", "v159", "v160", "v161", "v162", "v163", "v164", "v165", "v166", "v167", "v168", "v169", "v170", "v171", "v172", "v173", "v174", "v175", "v176", "v177", "v178", "v179", "v180", "v181", "v182", "v183", "v184", "v185", "v186", "v187", "v188", "v189", "v190", "v191", "v204", "v205", "v206", "v207", "v240", "v241", "v242", "v243", "v244", "v245", "v246", "v247", "v248", "v249", "v250", "v251");
    } else {
        if (bx == 0 && w == 4) flag_store(ws + W_CTL, tag);
        lds_wait(lds0 + SY_C3, 5);
        const __amdgpu_buffer_rsrc_t rso = __builtin_amdgcn_make_buffer_rsrc((void*)out, 0, N * 1024, 0x00020000);
#pragma unroll 1
        for (int it = 0; it < 10; ++it) {
            const int nl = (it < 5 ? 40 - 40 * half + 8 * it : 40 * half + 8 * (it - 5)) + w, node = node0 + nl;
            const bool nv = nl < npb;
            const unsigned ro = nv ? (unsigned)node * 1024u + (unsigned)lane * 16u : OOB_OFF;
            float a8[8] = {0.f, 0.f, 0.f, 0.f, 0.f, 0.f, 0.f, 0.f};
#pragma unroll 1
            for (int e0 = 0; e0 < (nv ? E : 0); e0 += 64) {
                const int ec = min(e0 + lane, E - 1);
                u64 mm = __ballot(e0 + lane < E && dst[ec] == node && (int)(((src[ec] >> 3) ^ (smask >> ((src[ec] >> 9) & 31))) & 1) == half);
#pragma unroll 1
                while (mm) {
                    const int e = e0 + __builtin_ctzll(mm);
                    mm &= mm - 1;
                    const unsigned short* fe = (const unsigned short*)ef + (size_t)e * DE_DIM;
                    const unsigned short* ye = (const unsigned short*)Y + (size_t)e * M_DIM;
                    const unsigned short* xp = x + (size_t)src[e] * (C_DIM * M_DIM) + lane * 8;
                    float z0 = 0.f, z1 = 0.f;
#pragma unroll 1
                    for (int f = 0; f < DE_DIM; ++f) { z0 += bf2f(fe[f]) * bf2f(w_rad[f * C_DIM + 2 * lane]); z1 += bf2f(fe[f]) * bf2f(w_rad[f * C_DIM + 2 * lane + 1]); }
                    const float r0 = z0 / (1.f + __expf(-z0)), r1 = z1 / (1.f + __expf(-z1));
#pragma unroll 1
                    for (int i = 0; i < 4; ++i) {
                        const float x0 = bf2f(xp[i]) * r0, x1 = bf2f(xp[4 + i]) * r1;
#pragma unroll
                        for (int o = 0; o < 4; ++o) {
                            float wv4 = 0.f;
#pragma unroll
                            for (int f = 0; f < 4; ++f) wv4 += bf2f(cg[(i * 4 + f) * 4 + o]) * bf2f(ye[f]);
                            a8[o] += x0 * wv4; a8[4 + o] += x1 * wv4;
                        }
                    }
                }
            }
            if (it >= 5) {
                const u32x4 pv = __builtin_amdgcn_raw_buffer_load_b128(rsw, ro, W_AGG, SC1);
#pragma unroll
                for (int i = 0; i < 4; ++i) { a8[2 * i] += __uint_as_float(pv[i] << 16); a8[2 * i + 1] += __uint_as_float(pv[i] & 0xffff0000u); }
            }
            const u32x4 rv = (u32x4){pack2bf(a8[0], a8[1]), pack2bf(a8[2], a8[3]), pack2bf(a8[4], a8[5]), pack2bf(a8[6], a8[7])};
            if (it < 5) {
                __builtin_amdgcn_raw_buffer_store_b128(rv, rsw, ro, W_AGG, SC1);
                if (it == 4) {
                    asm volatile("s_waitcnt vmcnt(0)" ::: "memory");
                    if (lds_arrive_rtn(lds0 + L_MISC + 4) == 7u) flag_store(ws + W_FLG + bx * 128, tag);
                    poll_tag(ws + W_FLG + (bx ^ 4) * 128, tag);
                }
            } else {
                *(u32x4*)(smem + L_PRV + w * 1024 + lane * 16) = rv;
#pragma unroll
                for (int i = 0; i < 8; ++i) a8[i] = 0.f;
#pragma unroll 1
                for (int c = 0; c < C_DIM; ++c) {
                    const unsigned short* ap = (const unsigned short*)(smem + L_PRV + w * 1024 + c * 8);
                    const float w0 = bf2f(w_mix[c * C_DIM + 2 * lane]), w1 = bf2f(w_mix[c * C_DIM + 2 * lane + 1]);
#pragma unroll
                    for (int m = 0; m < 4; ++m) { a8[m] += bf2f(ap[m]) * w0; a8[4 + m] += bf2f(ap[m]) * w1; }
                }
                __builtin_amdgcn_raw_buffer_store_b128((u32x4){pack2bf(a8[0], a8[1]), pack2bf(a8[2], a8[3]), pack2bf(a8[4], a8[5]), pack2bf(a8[6], a8[7])}, rso, ro, 0, 0);
            }
        }
    }
}

__global__ void kf_zero(float* __restrict__ agg, size_t n) {
    for (size_t i = (size_t)blockIdx.x * blockDim.x + threadIdx.x; i < n; i += (size_t)gridDim.x * blockDim.x) agg[i] = 0.f;
}
__global__ void kf_edges(const unsigned short* __restrict__ x, const int* __restrict__ src, const int* __restrict__ dst,
                         const unsigned short* __restrict__ Y, const unsigned short* __restrict__ ef,
                         const unsigned short* __restrict__ w_rad, const unsigned short* __restrict__ cg,
                         float* __restrict__ agg, int E) {
    const int ch = threadIdx.x;
    for (int e = blockIdx.x; e < E; e += gridDim.x) {
        float z = 0.f;
        for (int f = 0; f < DE_DIM; ++f) z += bf2f(ef[(size_t)e * DE_DIM + f]) * bf2f(w_rad[f * C_DIM + ch]);
        const float r = z / (1.f + __expf(-z));
        float a4[4] = {0.f, 0.f, 0.f, 0.f};
        for (int ii = 0; ii < 4; ++ii) {
            const float xv = bf2f(x[((size_t)src[e] * C_DIM + ch) * M_DIM + ii]) * r;
            for (int o = 0; o < 4; ++o) {
                float wv = 0.f;
                for (int f = 0; f < 4; ++f) wv += bf2f(cg[(ii * 4 + f) * 4 + o]) * bf2f(Y[(size_t)e * 4 + f]);
                a4[o] += xv * wv;
            }
        }
        for (int o = 0; o < 4; ++o) atomicAdd(&agg[((size_t)dst[e] * C_DIM + ch) * M_DIM + o], a4[o]);
    }
}
__global__ void kf_mix(const float* __restrict__ agg, const unsigned short* __restrict__ w_mix, unsigned short* __restrict__ out, int N) {
    const int d = threadIdx.x;
    for (int n = blockIdx.x; n < N; n += gridDim.x) {
        float a4[4] = {0.f, 0.f, 0.f, 0.f};
        for (int c = 0; c < C_DIM; ++c) {
            const float wv = bf2f(w_mix[c * C_DIM + d]);
            for (int m = 0; m < 4; ++m) a4[m] += bf2f(f2bf(agg[((size_t)n * C_DIM + c) * M_DIM + m])) * wv;
        }
        for (int m = 0; m < 4; ++m) out[((size_t)n * C_DIM + d) * M_DIM + m] = f2bf(a4[m]);
    }
}

extern "C" void kernel_launch(void* const* d_in, const int* in_sizes, int n_in,
                              void* d_out, int out_size, void* d_ws, size_t ws_size,
                              hipStream_t stream) {
    const unsigned short* x = (const unsigned short*)d_in[0];
    const int* src = (const int*)d_in[1];
    const int* dst = (const int*)d_in[2];
    const unsigned short* Y = (const unsigned short*)d_in[3];
    const unsigned short* ef = (const unsigned short*)d_in[4];
    const unsigned short* w_rad = (const unsigned short*)d_in[5];
    const unsigned short* cg = (const unsigned short*)d_in[6];
    const unsigned short* w_mix = (const unsigned short*)d_in[7];
    unsigned short* out = (unsigned short*)d_out;
    const int N = in_sizes[0] / (C_DIM * M_DIM);
    const int E = in_sizes[1];
    const int epb = (E + 254) / 255;
    const bool fast = N >= 256 && N <= NB_MAX * NPB_MAX && E > 0 && epb <= EPB_MAX && ws_size >= (size_t)W_END;
    if (!fast) {
        float* agg = (float*)d_ws;
        if (N <= 0) return;
        kf_zero<<<1024, 256, 0, stream>>>(agg, (size_t)N * C_DIM * M_DIM);
        if (E > 0) kf_edges<<<4096, C_DIM, 0, stream>>>(x, src, dst, Y, ef, w_rad, cg, agg, E);
        kf_mix<<<2048, C_DIM, 0, stream>>>(agg, w_mix, out, N);
        return;
    }
    const unsigned nb_magic = (unsigned)(((1ull << 39) + N - 1) / N);
    unsigned smask = 0;
    const int ng = (N + 511) / 512;
    int pw = 1;
    while (2 * pw * 4 < ng + 4) pw *= 2;
    for (int i = 0; i < ng; ++i) smask |= (unsigned)(i < 4 * pw ? ((i >> 2) ^ (i >= WLO * ng / 20 && i < WHI * ng / 20)) & 1 : i & 1) << i;
    const int r0 = ((N < 2048 * pw ? N : 2048 * pw) - 128) & ~63, r1 = r0 - (r0 >= 2048 ? 2048 : 128);
    k_fused<<<256, 512, 0, stream>>>(x, src, dst, (const u32x2*)Y, (const u32x4*)ef, w_rad, cg, w_mix, out, (unsigned char*)d_ws,
                                     N, E, epb, nb_magic, smask, (unsigned)(4 * 65536 / N), (unsigned)(r0 + (r0 >> 8 & 8)) * 1024u, (unsigned)(r1 + (r1 >> 8 & 8)) * 1024u);
}
```

```cpp
#include <hip/hip_runtime.h>
#include <hip/hip_bf16.h>
#include <stdint.h>

typedef __attribute__((ext_vector_type(8))) short bf16x8;
typedef __attribute__((ext_vector_type(4))) short bf16x4;
typedef __attribute__((ext_vector_type(4))) float f32x4;
typedef __attribute__((ext_vector_type(2))) unsigned u32x2;
typedef __attribute__((ext_vector_type(3))) unsigned u32x3;
typedef __attribute__((ext_vector_type(4))) unsigned u32x4;
typedef __attribute__((ext_vector_type(8))) unsigned u32x8;
typedef __attribute__((ext_vector_type(16))) unsigned u32x16;
typedef __attribute__((ext_vector_type(32))) unsigned u32x32;
typedef unsigned long long u64;

#define C_DIM 128
#define M_DIM 4
#define DE_DIM 16
#define NPB_MAX 80
#define NB_MAX 128
#define EPB_MAX 1280
#define NRND 7
#define LS 47
#define DEG_MAX 42
#define OOB_OFF 0x80000000u
#define SC1 16
#define NT 2
#define SPIN_MAX (1u << 22)
#define CODE_LINES 152
#define PTO 0xfffa9540u
#define PTY 0xffffa950u
#define PTZ 0xffffea54u
#define NT0 2
#define NT2 5
#define TLIST 100, 3, 4, 5, 10
#define WLO 8
#define WHI 11
#define IDLE 200u
#define CPN 12
#define RWP 1
#define EW 3
#define WRP 2
#define MVD 0
#define PAYR 3
#define PBIAS 12
#define PRW "6"
#define PSL "1"
#define TKF 0x98765432u
#define TKB 0x23456789u
#define TKA 0x1010ffffu
#define EXTRA 0, 0, 5, 7

#define W_CTL 0
#define W_DEC 2048
#define W_FLG 4096
#define W_TAB (W_FLG + 32768)
#define W_RA (W_TAB + 256 * 256 * 8)
#define W_AGG (W_RA + 256 * EPB_MAX * 16)
#define W_END (W_AGG + NB_MAX * NPB_MAX * 1024)

#define SB() __builtin_amdgcn_sched_barrier(0)

static __device__ __forceinline__ float bf2f(unsigned short v) { return __uint_as_float(((unsigned)v) << 16); }
typedef __bf16 bf16v2 __attribute__((ext_vector_type(2)));
static __device__ __forceinline__ unsigned pack2bf(float lo, float hi) {
    bf16v2 r = (bf16v2){(__bf16)lo, (__bf16)hi};
    return __builtin_bit_cast(unsigned, r);
}
static __device__ __forceinline__ unsigned short f2bf(float v) { return (unsigned short)(pack2bf(v, 0.f) & 0xffffu); }
template <int CTRL, int RM> static __device__ __forceinline__ int dpp_add(int v) {
    return v + __builtin_amdgcn_update_dpp(0, v, CTRL, RM, 0xf, false);
}
static __device__ __forceinline__ int wave_incl_scan(int v) {
    v = dpp_add<0x111, 0xf>(v); v = dpp_add<0x112, 0xf>(v); v = dpp_add<0x114, 0xf>(v); v = dpp_add<0x118, 0xf>(v);
    v = dpp_add<0x142, 0xa>(v); v = dpp_add<0x143, 0xc>(v);
    return v;
}
template <int CTRL, int RM> static __device__ __forceinline__ unsigned dpp_max(unsigned v) {
    return max(v, (unsigned)__builtin_amdgcn_update_dpp(0, (int)v, CTRL, RM, 0xf, false));
}
static __device__ __forceinline__ unsigned wave_max_scan(unsigned v) {
    v = dpp_max<0x111, 0xf>(v); v = dpp_max<0x112, 0xf>(v); v = dpp_max<0x114, 0xf>(v); v = dpp_max<0x118, 0xf>(v);
    v = dpp_max<0x142, 0xa>(v); v = dpp_max<0x143, 0xc>(v);
    return v;
}
#define MAXS(c) "v_max_u32_dpp %0, %0, %0 " c "\n\tv_max_u32_dpp %1, %1, %1 " c "\n\tv_max_u32_dpp %2, %2, %2 " c "\n\t"
static __device__ __forceinline__ void wave_max_scan3(unsigned& a, unsigned& b, unsigned& c) {
    asm volatile("s_nop 1\n\t" MAXS("row_shr:1 row_mask:0xf bank_mask:0xf bound_ctrl:1") MAXS("row_shr:2 row_mask:0xf bank_mask:0xf bound_ctrl:1")
                 MAXS("row_shr:4 row_mask:0xf bank_mask:0xf bound_ctrl:1") MAXS("row_shr:8 row_mask:0xf bank_mask:0xf bound_ctrl:1")
                 MAXS("row_bcast:15 row_mask:0xa bank_mask:0xf") MAXS("row_bcast:31 row_mask:0xc bank_mask:0xf") "s_nop 0" : "+v"(a), "+v"(b), "+v"(c));
}
static __device__ __forceinline__ unsigned bsum(unsigned c, unsigned m, unsigned acc = 0u) { return __builtin_amdgcn_udot4(c, m, acc, false); }
static __device__ __forceinline__ unsigned bytesum2(unsigned c0, unsigned c1, unsigned m0, unsigned m1, unsigned acc = 0u) {
    return __builtin_amdgcn_udot4(c1, m1, __builtin_amdgcn_udot4(c0, m0, acc, false), false);
}
static __device__ __forceinline__ unsigned below_mask(int w, int k) {
    const int n = w - 4 * k;
    return n >= 4 ? 0x01010101u : (n <= 0 ? 0u : (0x01010101u & ((1u << (8 * n)) - 1u)));
}
static __device__ __forceinline__ void flag_store(const void* p, unsigned v) {
    unsigned t0, t1; u64 sv;
    asm volatile("v_mov_b32 %0, 0\n\tv_mov_b32 %1, %4\n\ts_mov_b64 %2, exec\n\ts_mov_b64 exec, 1\n\t"
                 "global_store_dword %0, %1, %3 sc1\n\ts_mov_b64 exec, %2" : "=&v"(t0), "=&v"(t1), "=&s"(sv) : "s"(p), "s"(v) : "memory");
}
static __device__ __forceinline__ unsigned lds_arrive_rtn(unsigned a) {
    unsigned t0, t1; u64 sv;
    asm volatile("v_mov_b32 %0, %3\n\tv_mov_b32 %1, 1\n\ts_mov_b64 %2, exec\n\ts_mov_b64 exec, 1\n\t"
                 "ds_add_rtn_u32 %1, %0, %1\n\ts_mov_b64 exec, %2\n\ts_waitcnt lgkmcnt(0)" : "=&v"(t0), "=&v"(t1), "=&s"(sv) : "s"(a) : "memory");
    return (unsigned)__builtin_amdgcn_readfirstlane((int)t1);
}
static __device__ __forceinline__ void lds_arrive(unsigned a, unsigned inc = 1u) {
    unsigned t0, t1; u64 sv;
    asm volatile("" "v_mov_b32 %0, %3\n\tv_mov_b32 %1, %4\n\ts_mov_b64 %2, exec\n\ts_mov_b64 exec, 1\n\t"
                 "ds_add_u32 %0, %1\n\ts_mov_b64 exec, %2" : "=&v"(t0), "=&v"(t1), "=&s"(sv) : "s"(a), "s"(inc) : "memory");
}
template <int SLEEP = 0, unsigned MASK = ~0u> static __device__ __forceinline__ unsigned lds_wait(unsigned a, unsigned n) {
    for (;;) {
        unsigned v;
        asm volatile("ds_read_b32 %0, %1\n\ts_waitcnt lgkmcnt(0)" : "=v"(v) : "v"(a) : "memory");
        v = (unsigned)__builtin_amdgcn_readfirstlane((int)v);
        if ((v & MASK) >= n) return v;
        if (SLEEP) __builtin_amdgcn_s_sleep(SLEEP);
    }
}
static __device__ __forceinline__ unsigned ld_agent(const void* p) { return __hip_atomic_load((const unsigned*)p, __ATOMIC_RELAXED, __HIP_MEMORY_SCOPE_AGENT); }
static __device__ __forceinline__ void poll_tag(const void* p, unsigned tag) {
#pragma unroll 1
    for (unsigned spin = 0; spin < SPIN_MAX; ++spin) {
        if (__all(ld_agent(p) == tag)) break;
        __builtin_amdgcn_s_sleep(2);
    }
    __builtin_amdgcn_fence(__ATOMIC_ACQUIRE, "wavefront");
}
static __device__ __forceinline__ void wg_barrier() {
    asm volatile("s_waitcnt lgkmcnt(0)" ::: "memory");
    __builtin_amdgcn_s_barrier();
    asm volatile("" ::: "memory");
}
static __device__ __forceinline__ f32x4 mfma4(u32x2 a, u32x2 b, f32x4 c) {
    return __builtin_amdgcn_mfma_f32_4x4x4bf16_1k(__builtin_bit_cast(bf16x4, a), __builtin_bit_cast(bf16x4, b), c, 0, 0, 0);
}
static __device__ __forceinline__ f32x4 mfma16(u32x4 a, u32x4 b, f32x4 c) {
    return __builtin_amdgcn_mfma_f32_16x16x32_bf16(__builtin_bit_cast(bf16x8, a), __builtin_bit_cast(bf16x8, b), c, 0, 0, 0);
}

static constexpr unsigned evt_of(unsigned pt) {
    unsigned e = 6u;
    for (int n = 2; n < 15; ++n) if (((pt >> (2 * n)) & 3u) != ((pt >> (2 * n + 2)) & 3u)) e |= 1u << n;
    return e;
}

static constexpr u64 len_pos(bool kept) {
    u64 r = 0;
    for (int w = 0; w < 8; ++w) {
        r |= (u64)(kept ? 8 + w : w) << (4 * (((kept ? TKF : TKB) >> (4 * w)) & 15u));
        if (w >= 4 && (w >= 6) == kept) r |= (u64)(kept ? w : 4 + w) << (4 * ((TKA >> (4 * w)) & 15u));
    }
    return r;
}
static constexpr unsigned ring_const() {
    constexpr int e[4] = {EXTRA};
    unsigned r = 0;
    for (int i = 0; i < 4; ++i) r |= (unsigned)((4 - PBIAS + (-3 * e[i] - e[(i + 3) & 3] + e[(i + 2) & 3] + 3 * e[(i + 1) & 3]) / 2) & 255) << (8 * i);
    return r;
}

#define L_MISC 0
#define L_KEY 96
#define L_AT 416
#define L_LEN 1056
#define L_PCNT 1280
#define L_CCNT (L_PCNT + 2048)
#define L_BNC (L_CCNT + 1280)
#define L_CG (L_BNC + 4096)
#define L_PRV (L_CG + 1024)
#define A_STG L_PRV
#define A_PTAB (A_STG + EPB_MAX * 16)
#define L_PK (L_PRV + 15 * 4096)
#define L_DUM L_PK
#define L_CTAB (L_PK + 2048)
#define L_MRK (L_PK + 8192)
#define TR_STR 4352
#define L_TR (L_PK + 16384)
#define L_WR L_TR
#define L_IMG (L_TR + 8 * TR_STR)
#define L_TOTAL (L_IMG + 32768)
static_assert(L_TOTAL <= 163840 && L_BNC <= 8192 && NPB_MAX * LS * 16 <= 15 * 4096, "LDS budget");
static_assert(L_PRV % 16 == 0 && L_PK % 16 == 0 && L_IMG % 16 == 0 && L_CG % 16 == 0 && L_CCNT % 16 == 0, "alignment");
enum { SY_CPY = 40, SY_C1 = 44, SY_S = 48, SY_C3 = 56, SY_RI = 60, SY_PRE = 64 };

template <class T> static __device__ __forceinline__ T* arg_ptr(u32x16 a, int i) { return (T*)(__attribute__((address_space(1))) T*)((u64)a[i + 1] << 32 | a[i]); }
static __device__ __forceinline__ u32x4 mkdesc(const void* p, unsigned bytes) {
    return (u32x4){(unsigned)(uintptr_t)p, (unsigned)((uintptr_t)p >> 32) & 0xffffu, bytes, 0x00020000u};
}

#define STR2(x) #x
#define STR(x) STR2(x)
__global__ __launch_bounds__(512, 2) void k_fused(const int*, unsigned, unsigned, const int*, unsigned, unsigned, const void*, void*, const void*, const void*,
                                                  const void*, const void*, const void*, void*, int, int, int, unsigned, unsigned, unsigned, unsigned, unsigned)
{
    __shared__ __attribute__((aligned(256))) unsigned char smem[L_TOTAL];
    const int tid = threadIdx.x, bx = blockIdx.x;
    u32x16 ka, kb;
    unsigned so, cp;
    u32x3 d, sv;
    asm volatile("s_load_dwordx16 s[36:51], %3, 0x0\n\ts_load_dwordx16 s[52:67], %3, 0x40\n\ts_getpc_b64 s[68:69]\n\ts_movk_i32 s70, " STR(CODE_LINES) " * 128\n\ts_mov_b32 s71, 0x20000\n\t"
                 "s_lshl4_add_u32 %2, %4, " STR(CPN) " * -3968\n\tv_lshl_add_u32 v8, %5, 12, %2\n\tv_lshlrev_b32 v9, 7, %5\n\tv_max_i32 v8, v8, v9\n\tbuffer_load_dword v7, v8, s[68:71], 0 offen\n\t"
                 "v_mul_u32_u24 v15, 12, %5\n\ts_add_i32 %2, %4, -2\n\ts_waitcnt lgkmcnt(0)\n\ts_mul_i32 %2, %2, s62\n\ts_add_u32 s42, %2, s62\n\ts_min_u32 s38, s38, s42\n\ts_mov_b32 s42, s38\n\t"
                 "s_bitcmp1_b32 %4, 3\n\ts_cbranch_scc1 La_%=\n\tbuffer_load_dwordx3 v[8:10], v15, s[36:39], %2 offen nt\n\tLa_%=:\n\tbuffer_load_dwordx3 v[12:14], v15, s[40:43], %2 offen nt\n\t"
                 "s_cbranch_scc0 Lb_%=\n\tbuffer_load_dwordx3 v[8:10], v15, s[36:39], %2 offen nt\n\tLb_%=:"
                 : "={s[36:51]}"(ka), "={s[52:67]}"(kb), "=&s"(so) : "s"(__builtin_amdgcn_kernarg_segment_ptr()), "s"(bx), "v"(tid)
                 : "memory", "scc", "s68", "s69", "s70", "s71", "v7", "v8", "v9", "v10", "v12", "v13", "v14", "v15");
    asm volatile("" : "={v7}"(cp), "={v[8:10]}"(d), "={v[12:14]}"(sv));
    SB();
    const int* dst = arg_ptr<const int>(ka, 0);
    const int* src = arg_ptr<const int>(ka, 4);
    const u32x2* Y = arg_ptr<const u32x2>(ka, 8);
    unsigned char* ws = arg_ptr<unsigned char>(ka, 10);
    const unsigned short* x = arg_ptr<const unsigned short>(ka, 12);
    const u32x4* ef = arg_ptr<const u32x4>(ka, 14);
    const unsigned short* w_rad = arg_ptr<const unsigned short>(kb, 0);
    const unsigned short* cg = arg_ptr<const unsigned short>(kb, 2);
    const unsigned short* w_mix = arg_ptr<const unsigned short>(kb, 4);
    unsigned short* out = arg_ptr<unsigned short>(kb, 6);
    const int N = kb[8], E = kb[9], epb = kb[10] >> 2;
    const unsigned nb_magic = kb[11], smask = kb[12], cmul = kb[13], po0 = kb[14], po1 = kb[15];
    const int lane = tid & 63;
    const int w = __builtin_amdgcn_readfirstlane(tid >> 6);
    int half;
    const int b = (bx & 3) * 32 + (bx >> 3);
    const int g = lane >> 4, c16 = lane & 15;
    const int node0 = (b * N + 127) >> 7, npb = (((b + 1) * N + 127) >> 7) - node0;
    const unsigned lds0 = (unsigned)(uintptr_t)(__attribute__((address_space(3))) unsigned char*)smem;
    const __amdgpu_buffer_rsrc_t rsw = __builtin_amdgcn_make_buffer_rsrc((void*)ws, 0, W_END, 0x00020000);
    const unsigned le = tid * 3;
    const int base_e = (int)so >> 2;
    const int ne = base_e < 0 ? 0 : max(min(epb, E - base_e), 0);
    const int wd = 4 + ((w - 1 + (w >> 2)) & 3);
    auto task_at = [&](int t) -> unsigned {
        const unsigned q = (t == 0 ? TKF >> (4 * w) : (t == 1 ? TKB >> (4 * w) : TKA >> (4 * wd))) & 15u;
        return L_AT + ((t == 0 || (t == 2 && wd >= 6) ? 40 * half : 40 - 40 * half) + 4 * q) * 4;
    };
    const unsigned ri8 = (wd & 3) * 8;
    unsigned ta[3], cc, cd;
    int k0;
    auto set_ta = [&]() {
#pragma unroll
        for (int i = 0; i < 3; ++i) {
            ta[i] = lds0 + task_at(i == 2 ? 0 : ((w < 4) == (i == 0) ? 2 : 1)) + g * 4;
            asm volatile("" : "+v"(ta[i]));
        }
        cc = __builtin_rotateleft32(0xff0103fdu, ri8); cd = __builtin_rotateleft32(0xfe000204u, ri8); k0 = (int)(signed char)(ring_const() >> ri8);
        asm volatile("" : "+v"(cc), "+v"(cd), "+v"(k0));
    };
    unsigned tag;
    bool slow;
    u32x4 wr[8], cgA;
    auto load_wr = [&]() {
#pragma unroll
        for (int t = 0; t < 8; ++t) wr[t] = *(const u32x4*)(smem + L_WR + t * 1024 + lane * 16);
        cgA = *(const u32x4*)(smem + L_CG + lane * 16);
        asm volatile("" : "+v"(wr[0]), "+v"(wr[1]), "+v"(wr[2]), "+v"(wr[3]), "+v"(wr[4]), "+v"(wr[5]), "+v"(wr[6]), "+v"(wr[7]), "+v"(cgA));
    };

    __builtin_amdgcn_s_setprio(3);
    auto touch = [&](unsigned code) -> unsigned {
        if (code >= 100u)
            return __builtin_amdgcn_raw_buffer_load_b32(__builtin_amdgcn_make_buffer_rsrc(code == 100u ? (void*)x : (void*)ef, 0, code == 100u ? N * 1024 : E * 32, 0x00020000), lane << 21, bx * 128, 0);
        if (code < 8u)
            return __builtin_amdgcn_raw_buffer_load_b32(__builtin_amdgcn_make_buffer_rsrc((void*)(ef + (size_t)base_e * 2), 0, code == 0u ? 0 : ne * 32, 0x00020000), lane * 128, (code - 1u) * 8192u, 0);
        const unsigned j = code < 16u ? (code - 8u) * 128u + b : (code - 16u) * 32u + (bx >> 3);
        return __builtin_amdgcn_raw_buffer_load_b32(__builtin_amdgcn_make_buffer_rsrc((void*)x, 0, N * 1024, 0x00020000), lane * 128,
                                                    (2u * j + ((half ^ (smask >> (j >> 5))) & 1u)) * 8192u, 0);
    };
    constexpr unsigned tl[] = {TLIST};
    const __amdgpu_buffer_rsrc_t rsm = __builtin_amdgcn_make_buffer_rsrc((void*)w_mix, 0, C_DIM * C_DIM * 2, 0x00020000);
    u32x4 mv[8];
    auto load_mv = [&]() {
#pragma unroll
        for (int j = 0; j < 8; ++j)
            mv[j] = __builtin_amdgcn_raw_buffer_load_b128(rsm, g * 512 + c16 * 16 + (w & 3) * 2048 + (j & 1) * 256, (j >> 1) * 8192, 0);
        SB();
    };
    auto make_img = [&]() {
#pragma unroll
        for (int i = 0; i < 4; ++i) {
            u32x4 p0, p1;
#pragma unroll
            for (int j = 0; j < 4; ++j) {
                p0[j] = __builtin_amdgcn_perm(mv[2 * j + 1][i], mv[2 * j][i], 0x05040100u);
                p1[j] = __builtin_amdgcn_perm(mv[2 * j + 1][i], mv[2 * j][i], 0x07060302u);
            }
            const unsigned ia = L_IMG + (c16 >> 2) * 8192 + (w & 3) * 1024 + g * 256 + (c16 & 3) * 64 + i * 16;
            *(u32x4*)(smem + ia) = p0;
            *(u32x4*)(smem + ia + 4096) = p1;
        }
    };
    unsigned tlp = 0u, t0v = 0u, tv[NT2] = {};
    {
        const unsigned sh = 8u * (w & 3);
        unsigned inc[3];
        tlp = __builtin_amdgcn_raw_buffer_load_b32(__builtin_amdgcn_make_buffer_rsrc((void*)ws, 0, w == EW ? 4 : 0, 0x00020000), W_CTL, 0, 0);
        SB();
        {
            const unsigned k0 = (unsigned)tid - L_KEY / 16 < NPB_MAX / 4 ? 127u + L_KEY / 4 - 4u * tid : 0u, k1 = min(k0, 1u);
            *(u32x4*)(smem + tid * 16) = (u32x4){k0, k0 - k1, k0 - 2u * k1, k0 - 3u * k1};
        }
        *(u32x4*)(smem + L_MRK + tid * 16) = (u32x4){0u, 0u, 0u, 0u};
        SB();
        SB();
        unsigned l16 = lane * 16;
        asm volatile("" : "+v"(l16));
        u32x4 te = (u32x4){0u, 0u, 0u, 0u};
        auto table = [&]() {
            if (__builtin_expect(w == EW, 1)) {
                const unsigned tg = (unsigned)__builtin_amdgcn_readfirstlane((int)tlp) * 1664525u + 1013904223u;
                *(unsigned*)(smem + L_MISC) = tg;
#pragma unroll
                for (int i = 0; i < 2; ++i) __builtin_amdgcn_raw_buffer_store_b128((u32x4){te[i], tg, te[2 + i], tg}, rsw, W_TAB + bx * 2048 + l16 + i * 1024, 0, SC1);
            }
        };
        if (__builtin_expect(bx <= 1, 1)) {
            unsigned v;
            const unsigned ts = (unsigned)__builtin_amdgcn_s_memrealtime();
            asm volatile("s_cmp_lg_u32 %3, 0\n\t.irp o, 0, 0x400, 0x800, 0xc00, 0x1000, 0x1400, 0x1800, 0x1c00\n\ts_load_dword %0, %1, %2 offset:\\o\n\ts_cbranch_scc0 Lt_%=\n\t.endr\n\tLt_%=:\n\ts_waitcnt lgkmcnt(0)"
                         : "=&s"(v) : "s"(x), "s"((w < 4 ? po0 : po1) + (bx == 0 ? 0x10000 : (w & 3) * 8192)), "s"(bx) : "memory", "scc");
            asm volatile("s_waitcnt vmcnt(0)" : "+{v7}"(cp), "+{v[8:10]}"(d), "+{v[12:14]}"(sv));
            wg_barrier();
            table();
            t0v = __builtin_amdgcn_raw_buffer_load_b32(rsm, (tid >> 3) == 32 ? lane * 128 : OOB_OFF, 0, 0);
            if (__builtin_expect(bx * 8 + w == EW, 1)) {
                unsigned s0, s1, v0, v1;
                const unsigned tm = (unsigned)__builtin_amdgcn_s_memtime() * 0x9e3779b1u;
#define RACE(r, o, a, b) "s_load_dword %2, %4, " o " offset:" a "\n\ts_load_dword %3, %4, " o " offset:" b "\n\ts_waitcnt lgkmcnt(1)\n\ts_sleep " PSL "\n\t" \
                             "s_getreg_b32 " r ", hwreg(HW_REG_IB_STS, 8, 1)\n\ts_waitcnt lgkmcnt(0)\n\t"
                asm volatile("s_sleep " PRW "\n\t" RACE("%0", "%5", "0", "0x2000") RACE("%1", "%6", "0x2000", "0") "s_cmp_lg_u32 %0, %1\n\ts_cbranch_scc1 Lr_%=\n\t" RACE("%1", "%5", "0x6000", "0x4000") "Lr_%=:"
                             : "=&s"(s0), "=&s"(s1), "=&s"(v0), "=&s"(v1) : "s"(x), "s"(po0 + (tm >> 29) * 1024u), "s"(po1 + (tm >> 26 & 7u) * 1024u) : "memory", "scc");
                SB();
                unsigned xc;
                asm volatile("s_getreg_b32 %0, hwreg(HW_REG_XCC_ID, 0, 2)" : "=s"(xc));
                unsigned hm = (0xf0f0f0u >> (xc + 4u * s1));
                const unsigned tg = (unsigned)__builtin_amdgcn_readfirstlane((int)tlp) * 1664525u + 1013904223u;
                __builtin_amdgcn_raw_buffer_store_b128((u32x4){hm, tg, hm, tg}, rsw, lane < 8 ? W_DEC + l16 : OOB_OFF, 0, SC1);
            }
            asm volatile("" : "+v"(t0v));
            while ((unsigned)__builtin_amdgcn_s_memrealtime() - ts < IDLE) __builtin_amdgcn_s_sleep(1);
        }
        int bq = bx;
        asm volatile("" : "+s"(bq));
        if (__builtin_expect(bq > 1, 1)) {
        wg_barrier();
        const __amdgpu_buffer_rsrc_t rsy = __builtin_amdgcn_make_buffer_rsrc((void*)Y, 0, ka[2] * 2, 0x00020000);
        u32x4 ya = __builtin_amdgcn_raw_buffer_load_b128(rsy, le * 8, so * 2, NT);
        u32x2 yb = __builtin_amdgcn_raw_buffer_load_b64(rsy, le * 8 + 16, so * 2, NT);
        SB();
        {
            u64 pb = (u64)(uintptr_t)cg;
            unsigned pn = (w & 2) << 6;
            asm("s_cmp_eq_u32 %2, 3\n\ts_cselect_b64 %0, %4, %0\n\ts_cselect_b32 %1, 0x1000, %1\n\t"
                "s_cmp_eq_u32 %2, 4\n\ts_cselect_b64 %0, %5, %0\n\ts_cselect_b32 %1, 0x400, %1\n\ts_cmp_gt_u32 %2, 4\n\ts_cselect_b64 %0, %6, %0\n\ts_cselect_b32 %1, %7, %1"
                : "+s"(pb), "+s"(pn) : "s"(w), "s"(cg), "s"(w_rad), "s"(w_mix + (bx >> 3) * 512), "s"(ef + (base_e * 2 + (7 - w) * 512)), "s"(max(min(ne * 32, NT0 * 8192) - (7 - w) * 8192, 0)) : "scc");
            t0v = __builtin_amdgcn_raw_buffer_load_b32(__builtin_amdgcn_make_buffer_rsrc((void*)pb, 0, pn, 0x00020000), lane * 128, 0, 0);
        }
        SB();
#pragma unroll
        for (int r = 0; r < 3; ++r) inc[r] = (int)le + r < ne ? 1u << sh : 0u;
        asm volatile("" : "+v"(inc[0]), "+v"(inc[1]), "+v"(inc[2]));
        SB();
        unsigned bin[3], rank[3], ba[3];
        asm volatile("s_waitcnt vmcnt(4)" : "+{v7}"(cp), "+{v[8:10]}"(d), "+{v[12:14]}"(sv));
#pragma unroll
        for (int r = 0; r < 3; ++r) {
            bin[r] = __umulhi(d[r], nb_magic);
            ba[r] = ((((sv[r] >> 3) ^ (smask >> ((sv[r] >> 9) & 31u))) & 1u) * 512u + bin[r] * 4u) & 1023u;
            rank[r] = atomicAdd((unsigned*)(smem + L_PCNT + (w >> 2) * 1024 + ba[r]), inc[r]);
        }
        SB();
        unsigned rw1[3];
        auto mkrw = [&]() {
#pragma unroll
            for (int r = 0; r < 3; ++r) {
                const unsigned sl = sv[r], nl = d[r] - ((__umul24(bin[r], (unsigned)N) + 127u) >> 7);
                rw1[r] = (nl * 4u + ((__umul24(sl, cmul) >> 16) ^ ((nl >= 40u) == (ba[r] >= 512u) ? 3u : 0u))) | ((le + r) << 9);
                asm volatile("" : "+v"(rw1[r]));
            }
        };
        if (RWP == 0) mkrw();
        unsigned pa[3];
#pragma unroll
        for (int r = 0; r < 3; ++r) { pa[r] = A_PTAB + w * 512 + (ba[r] >> 1); asm volatile("" : "+v"(pa[r])); }
        unsigned m0 = below_mask(w, 0), m1 = below_mask(w, 1);
        asm volatile("" : "+s"(m0), "+s"(m1));
        wg_barrier();
        u32x2 c[4];
#pragma unroll
        for (int i = 0; i < 4; ++i) c[i] = *(const u32x2*)(smem + L_PCNT + i * 512 + lane * 8);
        if (RWP == 1) { SB(); mkrw(); SB(); }
        unsigned t[4], run[4];
#pragma unroll
        for (int i = 0; i < 4; ++i) t[i] = bsum(c[i >> 1][i & 1], 0x01010101u);
        asm volatile("" : "+v"(t[0]), "+v"(t[1]), "+v"(t[2]), "+v"(t[3]));
#pragma unroll
        for (int i = 0; i < 4; ++i) t[i] = bsum(c[2 + (i >> 1)][i & 1], 0x01010101u, t[i]);
        asm volatile("" : "+v"(t[0]), "+v"(t[1]), "+v"(t[2]), "+v"(t[3]));
        const unsigned ts = (t[0] + t[1]) | (t[2] + t[3]) << 16;
        const unsigned ti = (unsigned)wave_incl_scan((int)ts), tx = ti - ts;
        run[0] = tx & 0xffffu; run[2] = (tx >> 16) + ((unsigned)__builtin_amdgcn_readlane((int)ti, 63) & 0xffffu);
        run[1] = run[0] + t[0]; run[3] = run[2] + t[2];
        unsigned o[4];
#pragma unroll
        for (int i = 0; i < 4; ++i) o[i] = bytesum2(c[i >> 1][i & 1], c[2 + (i >> 1)][i & 1], m0, m1, run[i]);
        *(unsigned*)(smem + A_PTAB + w * 512 + lane * 4) = o[0] | (o[1] << 16);
        *(unsigned*)(smem + A_PTAB + w * 512 + 256 + lane * 4) = o[2] | (o[3] << 16);
        unsigned pos[3];
        asm volatile("" ::: "memory");
#pragma unroll
        for (int r = 0; r < 3; ++r) pos[r] = *(const unsigned short*)(smem + pa[r]);
        SB();
#pragma unroll
        for (int i = 0; i < 4; ++i) te[i] = run[i] | (t[i] << 16);
        if (RWP == 2) mkrw();
        asm volatile("" : "+v"(ya), "+v"(yb));
        table();
#pragma unroll
        for (int r = 0; r < 3; ++r) pos[r] = A_STG + (pos[r] + ((rank[r] >> sh) & 0xffu)) * 16;
        asm volatile("" : "+v"(pos[0]), "+v"(pos[1]), "+v"(pos[2]));
#pragma unroll
        for (int r = 0; r < 3; ++r) pos[r] = inc[r] != 0u ? pos[r] : L_DUM + l16;
#pragma unroll
        for (int r = 0; r < 3; ++r) *(u32x4*)(smem + pos[r]) = (u32x4){r < 2 ? ya[2 * r] : yb.x, sv[r], r < 2 ? ya[2 * r + 1] : yb.y, rw1[r]};
        }
        wg_barrier();
        const __amdgpu_buffer_rsrc_t rsr = __builtin_amdgcn_make_buffer_rsrc((void*)(ws + W_RA + bx * (EPB_MAX * 16)), 0, (ne * 16 + 127) & ~127, 0x00020000);
        u32x4 va[3];
        const unsigned tv0 = *(const unsigned*)(smem + L_MISC);
#pragma unroll
        for (int i = 0; i < 3; ++i) va[i] = *(const u32x4*)(smem + A_STG + (tid ^ 256) * 16 + 8192 * i);
#pragma unroll
        for (int i = 0; i < 3; ++i) __builtin_amdgcn_raw_buffer_store_b128((u32x4){va[i].x, va[i].y | tv0 << 14, va[i].z, va[i].w | (tv0 & 0xfff00000u)}, rsr, (tid ^ 256) * 16, 8192 * i, SC1);
        tag = (unsigned)__builtin_amdgcn_readfirstlane((int)tv0);
        lds_arrive(lds0 + SY_CPY);
    }
    u32x4 dx = mkdesc(x, (unsigned)N * 1024u), de = mkdesc(ef, (unsigned)E * 32u);
    asm volatile("" : "+s"(dx), "+s"(de));
    if (w < 4) {
        asm volatile("" :: "v"(t0v), "v"(tlp));
        const unsigned sh = 8u * w, mb = 0x01010101u & ((1u << sh) - 1u);
        const unsigned T0 = tag << 14, T1 = tag & 0xfff00000u;
        const unsigned to = W_TAB + tid * 2048 + (b & 1) * 1024 + (b >> 1) * 16;
        __builtin_amdgcn_s_sleep(4);
        u32x4 en = __builtin_amdgcn_raw_buffer_load_b128(rsw, to, 0, SC1);
        u32x2 dec = __builtin_amdgcn_raw_buffer_load_b64(rsw, W_DEC, 0, SC1);
#pragma unroll 1
        for (unsigned spin = 0; spin < SPIN_MAX; ++spin) {
            if (__builtin_amdgcn_ballot_w64(((en.y ^ tag) | (en.w ^ tag) | (dec.y ^ tag)) != 0u) == 0ull) break;
            __builtin_amdgcn_s_sleep(2);
            asm volatile("" ::: "memory");
            en = __builtin_amdgcn_raw_buffer_load_b128(rsw, to, 0, SC1);
            dec = __builtin_amdgcn_raw_buffer_load_b64(rsw, W_DEC, 0, SC1);
        }
        half = (__builtin_amdgcn_readfirstlane((int)dec.x) >> (bx & 7)) & 1;
        if (half) en.x = en.z;
        const unsigned rc = en.x >> 16;
        const unsigned incl = (unsigned)wave_incl_scan((int)rc);
        const int twf = __builtin_amdgcn_readlane((int)incl, 63), tw = twf <= 64 * NRND ? twf : 0;
        const bool more = tw > 64 * (NRND - 1);
        unsigned ma = L_MRK + w * (256 * NRND) + (incl - rc) * 4;
        asm volatile("" : "+v"(ma));
        *(unsigned*)(smem + (((rc != 0u) & (incl <= 64u * NRND)) ? ma : L_DUM + lane * 4)) = (unsigned)lane * EPB_MAX + (en.x & 0xffffu) - (incl - rc);
        unsigned ra[NRND];
        asm volatile("" ::: "memory");
#pragma unroll
        for (int r = 0; r < NRND; ++r) ra[r] = *(const unsigned*)(smem + L_MRK + w * (256 * NRND) + r * 256 + lane * 4);
        unsigned carry = 0u;
        u32x4 A[NRND];
#pragma unroll
        for (int r = 0; r < NRND; ++r) {
            if (r == NRND - 1) { if (more) ra[r] = wave_max_scan(ra[r]); asm volatile("" : "+v"(ra[r])); }
            else if (r % 3 == 0) wave_max_scan3(ra[r], ra[r + 1], ra[r + 2]);
            const unsigned m = max(ra[r], carry);
            carry = max(carry, (unsigned)__builtin_amdgcn_readlane((int)ra[r], 63));
            ra[r] = r * 64 + lane < tw ? (m + (unsigned)(r * 64 + lane)) * 16u : OOB_OFF >> 1;
            A[r] = __builtin_amdgcn_raw_buffer_load_b128(rsw, ra[r], W_RA + 64 * w * (EPB_MAX * 16), SC1);
        }
        SB();
        lds_arrive(lds0 + SY_RI, 16u + half);
        unsigned pe[NRND], inc[NRND], inck[NRND], lim[NRND];
#pragma unroll
        for (int r = 0; r < NRND; ++r) {
            pe[r] = __umul24(__umulhi(ra[r], 209716u) + 64u * w - 2u, (unsigned)epb * 32u);
            inc[r] = ra[r] < 0x40000000u ? 1u << sh : 0u;
            inck[r] = ra[r] < 0x40000000u ? 128u : 0u;
            lim[r] = ra[r] < 0x40000000u ? 16383u : ~0u;
            asm volatile("" : "+v"(pe[r]), "+v"(inc[r]), "+v"(inck[r]), "+v"(lim[r]));
        }
        if (twf > 64 * NRND) lds_arrive(lds0 + SY_S, 256u);
        set_ta();
        if (WRP == 0) { lds_wait(lds0 + SY_PRE, 4); load_wr(); }
        SB();
        unsigned rank[NRND], nc[NRND];
        auto slot = [&](int r) {
            nc[r] = inc[r] != 0u ? L_CTAB + w * 640 + nc[r] * 2 : L_MISC + 8;
            A[r] = (u32x4){A[r].y << 10, ((A[r].w >> 4) & 0xffe0u) + pe[r], A[r].x, A[r].z};
            asm volatile("" : "+v"(nc[r]), "+v"(A[r]));
            SB();
        };
#pragma unroll
        for (int r = 0; r < NRND; ++r) {
            if (r == NRND - 1 && !more) { rank[r] = 0u; nc[r] = 0u; continue; }
            A[r].y ^= T0; A[r].w ^= T1;
            if (__builtin_expect(__builtin_amdgcn_ballot_w64((A[r].y | (A[r].w >> 6)) > lim[r]) != 0ull, 0)) {
#pragma unroll 1
                for (unsigned spin = 0; spin < SPIN_MAX; ++spin) {
                    asm volatile("" ::: "memory");
                    A[r] = __builtin_amdgcn_raw_buffer_load_b128(rsw, ra[r], W_RA + 64 * w * (EPB_MAX * 16), SC1);
                    A[r].y ^= T0; A[r].w ^= T1;
                    if (__builtin_amdgcn_ballot_w64((A[r].y | (A[r].w >> 6)) > lim[r]) == 0ull) break;
                }
            }
            nc[r] = (r < 4 || inc[r] != 0u) ? A[r].w & 0x1ffu : (unsigned)lane;
            rank[r] = atomicAdd((unsigned*)(smem + L_CCNT + nc[r] * 4), inc[r]);
            atomicAdd((unsigned*)(smem + L_KEY + (nc[r] & 0x1fcu)), inck[r]);
            SB();
            if (r < PAYR) slot(r);
        }
        lds_arrive(lds0 + SY_C1);
        unsigned d0 = lane * LS, d1 = (64 + c16) * LS, ca0 = L_CTAB + w * 640 + lane * 8, ca1 = L_CTAB + w * 640 + (64 + c16) * 8;
        asm volatile("" : "+v"(d0), "+v"(d1), "+v"(ca0), "+v"(ca1));
#pragma unroll
        for (int r = PAYR; r < NRND; ++r) slot(r);
#pragma unroll
        for (int r = 0; r < NRND; ++r) {
            rank[r] = inc[r] != 0u ? (rank[r] >> sh) & 0xffu : (L_DUM - L_PRV) / 16 + lane;
            asm volatile("" : "+v"(rank[r]));
        }
        if (WRP == 1) { lds_wait(lds0 + SY_PRE, 4); load_wr(); }
        if (WRP == 2) lds_wait(lds0 + SY_PRE, 4);
        {
            u32x4 c0, c1;
            unsigned cn;
            do {
                asm volatile("ds_read_b32 %0, %3 offset:%c6\n\tds_read_b128 %1, %4 offset:%c7\n\tds_read_b128 %2, %5 offset:%c8\n\ts_waitcnt lgkmcnt(0)"
                             : "=&v"(cn), "=&v"(c0), "=&v"(c1) : "v"(lds0), "v"(lds0 + lane * 16), "v"(lds0 + c16 * 16), "i"(SY_C1), "i"(L_CCNT), "i"(L_CCNT + 1024) : "memory");
            } while (__builtin_amdgcn_readfirstlane((int)cn) < 4);
            unsigned p0[4], p1[4];
#pragma unroll
            for (int c = 0; c < 4; ++c) {
                p0[c] = bsum(c0[c], mb, d0); p1[c] = bsum(c1[c], mb, d1);
                if (c < 3) { d0 = bsum(c0[c], 0x01010101u, d0); d1 = bsum(c1[c], 0x01010101u, d1); }
            }
            *(u32x2*)(smem + ca0) = (u32x2){p0[0] | (p0[1] << 16), p0[2] | (p0[3] << 16)};
            *(u32x2*)(smem + ca1) = (u32x2){p1[0] | (p1[1] << 16), p1[2] | (p1[3] << 16)};
        }
        asm volatile("" ::: "memory");
#pragma unroll
        for (int r = 0; r < NRND; ++r) nc[r] = *(const unsigned short*)(smem + nc[r]);
        SB();
#pragma unroll
        for (int r = 0; r < NRND; ++r) {
            if (r == NRND - 1 && !more) continue;
            *(u32x4*)(smem + L_PRV + (nc[r] + rank[r]) * 16) = A[r];
        }
        if (WRP == 2) load_wr();
        lds_arrive(lds0 + SY_C3);
    } else {
        const int q = w - 4, t2 = tid - 256;
        __builtin_amdgcn_s_setprio(2);
        unsigned wv[8], cv[4];
        {
            const __amdgpu_buffer_rsrc_t rsa = __builtin_amdgcn_make_buffer_rsrc((void*)w_rad, 0, DE_DIM * C_DIM * 2, 0x00020000);
            const __amdgpu_buffer_rsrc_t rsg = __builtin_amdgcn_make_buffer_rsrc((void*)cg, 0, 128, 0x00020000);
#pragma unroll
            for (int j = 0; j < 8; ++j) wv[j] = __builtin_amdgcn_raw_buffer_load_b32(rsa, (g & 1) * 2048 + 64 * q + 4 * c16 + 256 * j, 0, 0);
#pragma unroll
            for (int f = 0; f < 4; ++f) cv[f] = __builtin_amdgcn_raw_buffer_load_b32(rsg, (lane & 3) * 32 + (lane & 8u) / 2 + 8 * f, 0, 0);
            SB();
        }
        if (MVD == 2) load_mv();
        lds_wait(lds0 + SY_CPY, 8);
#pragma unroll
        for (int i = 0; i < 15; ++i) *(u32x4*)(smem + L_PRV + (t2 + 256 * i) * 16) = (u32x4){OOB_OFF, OOB_OFF, 0u, 0u};
        SB();
        asm volatile("" :: "v"(t0v), "v"(tlp));
        if (MVD == 0) load_mv();
        {
            float f0[8], f1[8];
#pragma unroll
            for (int j = 0; j < 8; ++j) {
                const float a = __uint_as_float(wv[j] << 16) * -1.44269504088896340736f, c = __uint_as_float(wv[j] & 0xffff0000u) * -1.44269504088896340736f;
                const unsigned hp = g < 2 ? 0u : pack2bf(a, c);
                f0[j] = a - __uint_as_float(hp << 16);
                f1[j] = c - __uint_as_float(hp & 0xffff0000u);
            }
            *(u32x4*)(smem + L_WR + (2 * q) * 1024 + lane * 16) = (u32x4){pack2bf(f0[0], f0[1]), pack2bf(f0[2], f0[3]), pack2bf(f0[4], f0[5]), pack2bf(f0[6], f0[7])};
            *(u32x4*)(smem + L_WR + (2 * q + 1) * 1024 + lane * 16) = (u32x4){pack2bf(f1[0], f1[1]), pack2bf(f1[2], f1[3]), pack2bf(f1[4], f1[5]), pack2bf(f1[6], f1[7])};
            float a[4];
#pragma unroll
            for (int f = 0; f < 4; ++f) a[f] = __uint_as_float((lane & 4) ? cv[f] & 0xffff0000u : cv[f] << 16) * -0.69314718055994530942f;
            const unsigned h0 = pack2bf(a[0], a[1]), h1 = pack2bf(a[2], a[3]);
            *(u32x4*)(smem + L_CG + lane * 16) = (u32x4){h0, h1, pack2bf(a[0] - __uint_as_float(h0 << 16), a[1] - __uint_as_float(h0 & 0xffff0000u)),
                                                        pack2bf(a[2] - __uint_as_float(h1 << 16), a[3] - __uint_as_float(h1 & 0xffff0000u))};
        }
        lds_arrive(lds0 + SY_PRE);
        if (MVD == 1) load_mv();
        make_img();
        lds_wait(lds0 + SY_PRE, 4);
        load_wr();
        if (w == 7) {
            lds_arrive(lds0 + SY_C3);
            half = (lds_wait<1>(lds0 + SY_RI, 64) & 15u) != 0u;
#pragma unroll
            for (int i = 0; i < NT2; ++i) tv[i] = touch(tl[i]);
            SB();
            set_ta();
        } else {
            half = (lds_wait<1>(lds0 + SY_RI, 16) & 15u) != 0u;
            set_ta();
            const int n5 = min(27 * q + (lane >> 1), NPB_MAX - 1);
            const bool kp = (n5 >= 40) == (half != 0);
            u64 lpos = kp ? len_pos(true) : len_pos(false);
            unsigned an = lds0 + n5 * 4, ak = lds0 + L_KEY + (n5 >= 40 ? 160 : 0) + (lane & 1) * 80;
            unsigned aa = L_AT + (n5 >= 40 ? 160 : 0), al = L_LEN + (kp ? 4 : 0), av = (unsigned)n5 | (lds0 + L_PRV + n5 * (LS * 16)) << 8;
            asm volatile("" : "+v"(an), "+v"(ak), "+v"(aa), "+v"(al), "+v"(av), "+v"(lpos));
            lds_wait<1>(lds0 + SY_C1, 3);
            __builtin_amdgcn_s_setprio(3);
            unsigned kn, cn;
            u32x4 dk[5];
            do {
                asm volatile("ds_read_b32 %0, %7 offset:%c10\n\tds_read_b32 %1, %8 offset:%c11\n\tds_read_b128 %2, %9\n\tds_read_b128 %3, %9 offset:16\n\tds_read_b128 %4, %9 offset:32\n\t"
                             "ds_read_b128 %5, %9 offset:48\n\tds_read_b128 %6, %9 offset:64\n\ts_waitcnt lgkmcnt(0)"
                             : "=&v"(cn), "=&v"(kn), "=&v"(dk[0]), "=&v"(dk[1]), "=&v"(dk[2]), "=&v"(dk[3]), "=&v"(dk[4]) : "v"(lds0), "v"(an), "v"(ak), "i"(SY_C1), "i"(L_KEY) : "memory");
            } while (__builtin_amdgcn_readfirstlane((int)cn) < 4);
            unsigned sg[4] = {0u, 0u, 0u, 0u};
#pragma unroll
            for (int k = 0; k < 20; ++k) sg[k & 3] = __builtin_amdgcn_alignbit(sg[k & 3], kn - dk[k >> 2][k & 3], 31);
            int c = __builtin_popcount(sg[0] | sg[1] << 8 | sg[2] << 16 | sg[3] << 24);
            c += __builtin_amdgcn_update_dpp(0, c, 0xB1, 0xf, 0xf, false);
            *(unsigned*)(smem + aa + c * 4) = av;
            smem[(c & 3) == 0 ? al + ((unsigned)(lpos >> c) & 15u) : L_LEN + 20] = (unsigned char)max(kn >> 7, c < 8 ? 12u : 8u);
            lds_arrive(lds0 + SY_S, __builtin_amdgcn_ballot_w64((kn >> 7) > DEG_MAX) != 0ull ? 257u : 1u);
        }
        __builtin_amdgcn_s_setprio(3);
    }
    unsigned ax[3], lh, cn;
    u32x4 ln;
    {
        if (w == 7) lds_wait<2, 255>(lds0 + SY_S, 1);
        do {
            asm volatile("ds_read_b32 %0, %6 offset:%c10\n\tds_read_b128 %1, %6 offset:%c11\n\tds_read_b32 %2, %6 offset:%c12\n\tds_read_b32 %3, %7\n\tds_read_b32 %4, %8\n\t"
                         "ds_read_b32 %5, %9\n\ts_waitcnt lgkmcnt(0)"
                         : "=&v"(cn), "=&v"(ln), "=&v"(lh), "=&v"(ax[0]), "=&v"(ax[1]), "=&v"(ax[2])
                         : "v"(lds0), "v"(ta[0]), "v"(ta[1]), "v"(ta[2]), "i"(SY_S), "i"(L_LEN), "i"(L_LEN + 16) : "memory");
            cn = (unsigned)__builtin_amdgcn_readfirstlane((int)cn);
        } while ((cn & 255u) < 3u);
        slow = cn >= 256u;
    }
    const int p8 = __builtin_amdgcn_sdot4((int)(ln.x + ln.y), (int)cc, __builtin_amdgcn_sdot4((int)(ln.w + lh), (int)cc,
                   __builtin_amdgcn_sdot4((int)ln.z, (int)cd, k0, false), false), false);
    const int ld = (__builtin_amdgcn_readfirstlane((int)ln.z) >> ri8) & 255, p2 = min(max(__builtin_amdgcn_readfirstlane(p8) >> 3, 4), ld - 8);
    const int l2 = w < 4 ? ld - p2 : p2, od = (w < 4) == (wd >= 6) ? ld - l2 : 0;
    const unsigned aX = (ax[0] >> 8) + ((w < 4 ? od : 0) + (c16 & 3)) * 16, aF = (ax[0] >> 8) + ((w < 4 ? od : 0) + (c16 >> 2)) * 16 + 4;
    const unsigned lane_off = (unsigned)c16 * 16u;
    const unsigned aWw = L_BNC + w * 512 + lane * 8;
    const unsigned aWr = L_BNC + w * 512 + g * 128 + (c16 & 3) * 32 + (c16 >> 2) * 8;
    auto row_of = [=](unsigned at) -> unsigned {
        const unsigned nl = at & 0xffu;
        return nl < (unsigned)npb ? ((unsigned)node0 + nl) * 1024u + lane_off : OOB_OFF;
    };
#pragma unroll
    for (int i = 0; i < NT2; ++i) asm volatile("" :: "v"(tv[i]));
    if (!slow) {
        u32x32 oxa, oxb;
        u32x4 S;
        u32x2 ef0, efN;
        unsigned tmp;
        __builtin_amdgcn_s_waitcnt(0x0F70);
        asm volatile(
            "Lw2_%=:\n"
            "ds_read_b32 v220, v219\n"
            "ds_read2_b32 v[232:233], v211 offset1:16\n"
            "ds_read_b128 v[196:199], v210\n"
            "s_waitcnt lgkmcnt(1)\n"
            "v_readfirstlane_b32 %[tmp], v220\n"
            "s_cmp_lt_u32 %[tmp], 5\n"
            "s_cbranch_scc1 Lw2_%=\n"
            "v_or_b32 v232, v232, v229\n"
            "v_or_b32 v233, v233, v229\n"
            "buffer_load_dwordx2 v[238:239], v232, %[rse], 0 offen\n"
            "s_waitcnt lgkmcnt(0)\n"
            "v_add_u32_dpp v220, v196, v214 quad_perm:[0,0,0,0] row_mask:0xf bank_mask:0xf\n"
            "v_add_u32_dpp v221, v196, v214 quad_perm:[1,1,1,1] row_mask:0xf bank_mask:0xf\n"
            "v_add_u32_dpp v222, v196, v214 quad_perm:[2,2,2,2] row_mask:0xf bank_mask:0xf\n"
            "v_add_u32_dpp v223, v196, v214 quad_perm:[3,3,3,3] row_mask:0xf bank_mask:0xf\n"
            "buffer_load_dwordx4 v[32:35], v220, %[rsx], 0 offen\n"
            "buffer_load_dwordx4 v[36:39], v220, %[rsx], 0 offen offset:256\n"
            "buffer_load_dwordx4 v[48:51], v221, %[rsx], 0 offen\n"
            "buffer_load_dwordx4 v[52:55], v221, %[rsx], 0 offen offset:256\n"
            "buffer_load_dwordx2 v[232:233], v233, %[rse], 0 offen\n"
            "buffer_load_dwordx4 v[64:67], v222, %[rsx], 0 offen\n"
            "buffer_load_dwordx4 v[68:71], v222, %[rsx], 0 offen offset:256\n"
            "buffer_load_dwordx4 v[80:83], v223, %[rsx], 0 offen\n"
            "buffer_load_dwordx4 v[84:87], v223, %[rsx], 0 offen offset:256\n"
            "buffer_load_dwordx4 v[40:43], v220, %[rsx], 0 offen offset:512\n"
            "buffer_load_dwordx4 v[44:47], v220, %[rsx], 0 offen offset:768\n"
            "buffer_load_dwordx4 v[56:59], v221, %[rsx], 0 offen offset:512\n"
            "buffer_load_dwordx4 v[60:63], v221, %[rsx], 0 offen offset:768\n"
            "buffer_load_dwordx4 v[72:75], v222, %[rsx], 0 offen offset:512\n"
            "buffer_load_dwordx4 v[76:79], v222, %[rsx], 0 offen offset:768\n"
            "buffer_load_dwordx4 v[88:91], v223, %[rsx], 0 offen offset:512\n"
            "buffer_load_dwordx4 v[92:95], v223, %[rsx], 0 offen offset:768\n"
            : "={v[32:63]}"(oxa), "={v[64:95]}"(oxb), "={v[238:239]}"(ef0), "={v[196:199]}"(S), "={v[232:233]}"(efN), [tmp] "=&s"(tmp)
            : [rse] "s"(de), [rsx] "s"(dx), [ax] "{v210}"(aX), [af] "{v211}"(aF), [lo] "{v214}"(lane_off), [eo] "{v229}"((lane & 3) * 8), [sy] "{v219}"(lds0 + SY_C3)
            : "memory", "scc", "v220", "v221", "v222", "v223");
        if (bx == 0 && w == 4) flag_store(ws + W_CTL, tag);
        const int lf = (__builtin_amdgcn_readfirstlane((int)(w < 4 ? ln.w : lh)) >> (8 * (w & 3))) & 255, l1 = (__builtin_amdgcn_readfirstlane((int)(w < 4 ? ln.x : ln.y)) >> (8 * (w & 3))) & 255;
        const int e2 = lf + l1 + l2;
        int nst = (e2 + 3) >> 2;
        const unsigned r2 = row_of(ax[1]);
        u32x32 owr;
        u32x16 omi;
        u32x8 ot;
#pragma unroll
        for (int i = 0; i < 32; ++i) owr[i] = wr[i >> 2][i & 3];
        const int pw = (w + (w >> 2)) & 3;
        const unsigned trb = lds0 + L_TR + w * TR_STR;
        omi[2] = aX + 64; omi[3] = aF + 64;
        omi[4] = lds0 + aWw; omi[5] = lds0 + aWr; omi[6] = lane_off; omi[7] = W_AGG + row_of(ax[0]); omi[8] = W_AGG + r2;
        omi[9] = row_of(ax[2]); omi[10] = w >= 6 ? r2 : OOB_OFF; omi[15] = ~tag;
        ot[0] = (lane & 3) == 0 ? 0x3f80u : ((lane & 3) == 1 ? 0x3f800000u : 0u); ot[1] = (lane & 3) == 2 ? 0x3f80u : ((lane & 3) == 3 ? 0x3f800000u : 0u);
        ot[2] = trb + g * (4 * 272) + c16 * 16; ot[3] = trb + c16 * 272 + g * 16; ot[4] = lds0 + L_IMG + lane * 16; ot[5] = (lane & 3) * 8; ot[7] = lds0 + L_PK + pw * 4096 + lane * 16;
        int rem = w < 4 ? l2 : l1, lens = ((w < 4 ? l1 : l2) - 4) | 0xff00, fm = w < 4 ? 0x802 : (w < 6 ? 0x810 : 0xb0), pb = 0;
        const int j1 = (w < 4 ? -od - l2 : od - l1) * 16, j2 = (w < 4 ? -l1 : -od - l2) * 16;
        u32x2 jmp = (u32x2){(ax[1] >> 8) - (ax[0] >> 8) + j1, (ax[2] >> 8) - (ax[1] >> 8) + j2};
        unsigned bpa = c16 * 16 + (g & 1) * 8;
        unsigned evt = (unsigned)__builtin_amdgcn_readfirstlane(w < 4 ? evt_of(PTO) : (w < 6 ? evt_of(PTY) : evt_of(PTZ))) | 6u << (nst - (rem >> 2)) | 6u << (nst - ((l1 + l2) >> 2));
        u64 ra;
        asm volatile(
            "s_nop 1\n"
            "v_mfma_f32_4x4x4_16b_bf16 v[204:207], v[202:203], v[198:199], 0\n"
            "v_mov_b64 v[0:1], 0\n"
            "v_mov_b64 v[2:3], 0\n"
            "v_mov_b64 v[4:5], 0\n"
            "v_mov_b64 v[6:7], 0\n"
            "v_mfma_f32_4x4x4_16b_bf16 v[204:207], v[200:201], v[198:199], v[204:207]\n"
            "v_mov_b64 v[8:9], 0\n"
            "v_mov_b64 v[10:11], 0\n"
            "v_mov_b64 v[12:13], 0\n"
            "v_mov_b64 v[14:15], 0\n"
            "v_mov_b64 v[16:17], 0\n"
            "v_mov_b64 v[18:19], 0\n"
            "v_mov_b64 v[20:21], 0\n"
            "v_mov_b64 v[22:23], 0\n"
            "v_mov_b64 v[24:25], 0\n"
            "v_mov_b64 v[26:27], 0\n"
            "v_mov_b64 v[28:29], 0\n"
            "v_mov_b64 v[30:31], 0\n"
            "v_cvt_pk_bf16_f32 v204, v204, v205\n"
            "v_cvt_pk_bf16_f32 v205, v206, v207\n"
            "ds_write_b64 v212, v[204:205]\n"
            "ds_read_b128 v[196:199], v210\n"
            "ds_read_b64 v[208:209], v213\n"
            "s_waitcnt vmcnt(17)\n"
            "ds_bpermute_b32 v192, v234, v238\n"
            "ds_bpermute_b32 v193, v234, v239\n"
            "ds_bpermute_b32 v194, v234, v238 offset:4\n"
            "ds_bpermute_b32 v195, v234, v239 offset:4\n"
            "s_waitcnt lgkmcnt(0)\n"
            "v_mfma_f32_16x16x32_bf16 v[176:179], v[192:195], v[96:99], 0\n"
            "v_mfma_f32_16x16x32_bf16 v[180:183], v[192:195], v[100:103], 0\n"
            "v_mfma_f32_16x16x32_bf16 v[184:187], v[192:195], v[104:107], 0\n"
            "v_mfma_f32_16x16x32_bf16 v[188:191], v[192:195], v[108:111], 0\n"
            "v_mfma_f32_16x16x32_bf16 v[128:131], v[192:195], v[112:115], 0\n"
            "v_mfma_f32_16x16x32_bf16 v[132:135], v[192:195], v[116:119], 0\n"
            "s_nop 2\n"
            "v_exp_f32 v144, v176\n"
            "v_exp_f32 v145, v177\n"
            "v_exp_f32 v146, v178\n"
            "v_exp_f32 v147, v179\n"
            "v_exp_f32 v148, v180\n"
            "v_exp_f32 v149, v181\n"
            "v_exp_f32 v150, v182\n"
            "v_exp_f32 v151, v183\n"
            "v_exp_f32 v152, v184\n"
            "v_exp_f32 v153, v185\n"
            "v_exp_f32 v154, v186\n"
            "v_exp_f32 v155, v187\n"
            "v_exp_f32 v156, v188\n"
            "v_exp_f32 v157, v189\n"
            "v_exp_f32 v158, v190\n"
            "v_exp_f32 v159, v191\n"
            "v_pk_add_f32 v[144:145], v[144:145], 1.0 op_sel_hi:[1,0]\n"
            "v_pk_add_f32 v[146:147], v[146:147], 1.0 op_sel_hi:[1,0]\n"
            "v_pk_add_f32 v[148:149], v[148:149], 1.0 op_sel_hi:[1,0]\n"
            "v_pk_add_f32 v[150:151], v[150:151], 1.0 op_sel_hi:[1,0]\n"
            "v_pk_add_f32 v[152:153], v[152:153], 1.0 op_sel_hi:[1,0]\n"
            "v_pk_add_f32 v[154:155], v[154:155], 1.0 op_sel_hi:[1,0]\n"
            "v_pk_add_f32 v[156:157], v[156:157], 1.0 op_sel_hi:[1,0]\n"
            "v_pk_add_f32 v[158:159], v[158:159], 1.0 op_sel_hi:[1,0]\n"
            "v_rcp_f32 v144, v144\n"
            "v_rcp_f32 v145, v145\n"
            "v_rcp_f32 v146, v146\n"
            "v_rcp_f32 v147, v147\n"
            "v_rcp_f32 v148, v148\n"
            "v_rcp_f32 v149, v149\n"
            "v_rcp_f32 v150, v150\n"
            "v_rcp_f32 v151, v151\n"
            "v_rcp_f32 v152, v152\n"
            "v_rcp_f32 v153, v153\n"
            "v_rcp_f32 v154, v154\n"
            "v_rcp_f32 v155, v155\n"
            "v_rcp_f32 v156, v156\n"
            "v_rcp_f32 v157, v157\n"
            "v_rcp_f32 v158, v158\n"
            "v_rcp_f32 v159, v159\n"
            "v_pk_mul_f32 v[144:145], v[144:145], v[176:177]\n"
            "v_pk_mul_f32 v[146:147], v[146:147], v[178:179]\n"
            "v_pk_mul_f32 v[148:149], v[148:149], v[180:181]\n"
            "v_pk_mul_f32 v[150:151], v[150:151], v[182:183]\n"
            "v_pk_mul_f32 v[152:153], v[152:153], v[184:185]\n"
            "v_pk_mul_f32 v[154:155], v[154:155], v[186:187]\n"
            "v_pk_mul_f32 v[156:157], v[156:157], v[188:189]\n"
            "v_pk_mul_f32 v[158:159], v[158:159], v[190:191]\n"
            "s_branch Ldisp_%=\n"
            "Lfast_%=:\n"
            "v_mfma_f32_16x16x32_bf16 v[136:139], v[192:195], v[120:123], 0\n"
            "v_exp_f32 v160, v128\n"
            "v_add_u32 v211, 64, v211\n"
            "v_exp_f32 v161, v129\n"
            "s_waitcnt vmcnt(13) lgkmcnt(0)\n"
            "v_mfma_f32_4x4x4_16b_bf16 v[176:179], v[208:209], v[32:33], 0 cbsz:2\n"
            "v_exp_f32 v162, v130\n"
            "v_mfma_f32_4x4x4_16b_bf16 v[180:183], v[208:209], v[34:35], 0 cbsz:2\n"
            "v_exp_f32 v163, v131\n"
            "v_mfma_f32_4x4x4_16b_bf16 v[184:187], v[208:209], v[36:37], 0 cbsz:2\n"
            "v_exp_f32 v164, v132\n"
            "v_mfma_f32_4x4x4_16b_bf16 v[188:191], v[208:209], v[38:39], 0 cbsz:2\n"
            "v_exp_f32 v165, v133\n"
            "v_add_u32_dpp v219, v196, v214 quad_perm:[0,0,0,0] row_mask:0xf bank_mask:0xf\n"
            "v_mfma_f32_4x4x4_16b_bf16 v[204:207], v[202:203], v[198:199], 0\n"
            "v_exp_f32 v166, v134\n"
            "buffer_load_dwordx4 v[32:35], v219, %[rsx], 0 offen\n"
            "buffer_load_dwordx4 v[36:39], v219, %[rsx], 0 offen offset:256\n"
            "v_exp_f32 v167, v135\n"
            "v_pk_fma_f32 v[0:1], v[144:145], v[176:177], v[0:1] op_sel_hi:[0,1,1]\n"
            "v_pk_fma_f32 v[2:3], v[144:145], v[178:179], v[2:3] op_sel_hi:[0,1,1]\n"
            "v_pk_fma_f32 v[4:5], v[148:149], v[180:181], v[4:5] op_sel_hi:[0,1,1]\n"
            "v_pk_fma_f32 v[6:7], v[148:149], v[182:183], v[6:7] op_sel_hi:[0,1,1]\n"
            "v_pk_fma_f32 v[8:9], v[152:153], v[184:185], v[8:9] op_sel_hi:[0,1,1]\n"
            "v_pk_fma_f32 v[10:11], v[152:153], v[186:187], v[10:11] op_sel_hi:[0,1,1]\n"
            "v_pk_fma_f32 v[12:13], v[156:157], v[188:189], v[12:13] op_sel_hi:[0,1,1]\n"
            "v_pk_fma_f32 v[14:15], v[156:157], v[190:191], v[14:15] op_sel_hi:[0,1,1]\n"
            "v_pk_add_f32 v[160:161], v[160:161], 1.0 op_sel_hi:[1,0]\n"
            "v_pk_add_f32 v[162:163], v[162:163], 1.0 op_sel_hi:[1,0]\n"
            "v_pk_add_f32 v[164:165], v[164:165], 1.0 op_sel_hi:[1,0]\n"
            "v_pk_add_f32 v[166:167], v[166:167], 1.0 op_sel_hi:[1,0]\n"
            "v_mfma_f32_16x16x32_bf16 v[140:143], v[192:195], v[124:127], 0\n"
            "ds_read_b32 v230, v211\n"
            "s_waitcnt vmcnt(14)\n"
            "ds_bpermute_b32 v192, v234, v232\n"
            "ds_bpermute_b32 v193, v234, v233\n"
            "ds_bpermute_b32 v194, v234, v232 offset:4\n"
            "ds_bpermute_b32 v195, v234, v233 offset:4\n"
            "v_rcp_f32 v160, v160\n"
            "v_rcp_f32 v161, v161\n"
            "v_mfma_f32_4x4x4_16b_bf16 v[176:179], v[208:209], v[48:49], 0 cbsz:2 abid:1\n"
            "v_rcp_f32 v162, v162\n"
            "v_mfma_f32_4x4x4_16b_bf16 v[180:183], v[208:209], v[50:51], 0 cbsz:2 abid:1\n"
            "v_rcp_f32 v163, v163\n"
            "v_mfma_f32_4x4x4_16b_bf16 v[184:187], v[208:209], v[52:53], 0 cbsz:2 abid:1\n"
            "v_rcp_f32 v164, v164\n"
            "v_mfma_f32_4x4x4_16b_bf16 v[188:191], v[208:209], v[54:55], 0 cbsz:2 abid:1\n"
            "v_rcp_f32 v165, v165\n"
            "v_add_u32_dpp v220, v196, v214 quad_perm:[1,1,1,1] row_mask:0xf bank_mask:0xf\n"
            "v_mfma_f32_4x4x4_16b_bf16 v[204:207], v[200:201], v[198:199], v[204:207]\n"
            "v_rcp_f32 v166, v166\n"
            "buffer_load_dwordx4 v[48:51], v220, %[rsx], 0 offen\n"
            "buffer_load_dwordx4 v[52:55], v220, %[rsx], 0 offen offset:256\n"
            "v_rcp_f32 v167, v167\n"
            "v_pk_fma_f32 v[0:1], v[144:145], v[176:177], v[0:1] op_sel:[1,0,0]\n"
            "v_pk_fma_f32 v[2:3], v[144:145], v[178:179], v[2:3] op_sel:[1,0,0]\n"
            "v_pk_fma_f32 v[4:5], v[148:149], v[180:181], v[4:5] op_sel:[1,0,0]\n"
            "v_pk_fma_f32 v[6:7], v[148:149], v[182:183], v[6:7] op_sel:[1,0,0]\n"
            "v_pk_fma_f32 v[8:9], v[152:153], v[184:185], v[8:9] op_sel:[1,0,0]\n"
            "v_pk_fma_f32 v[10:11], v[152:153], v[186:187], v[10:11] op_sel:[1,0,0]\n"
            "v_pk_fma_f32 v[12:13], v[156:157], v[188:189], v[12:13] op_sel:[1,0,0]\n"
            "v_pk_fma_f32 v[14:15], v[156:157], v[190:191], v[14:15] op_sel:[1,0,0]\n"
            "v_pk_mul_f32 v[160:161], v[160:161], v[128:129]\n"
            "v_pk_mul_f32 v[162:163], v[162:163], v[130:131]\n"
            "v_pk_mul_f32 v[164:165], v[164:165], v[132:133]\n"
            "v_pk_mul_f32 v[166:167], v[166:167], v[134:135]\n"
            "s_waitcnt vmcnt(12) lgkmcnt(0)\n"
            "v_or_b32 v230, v230, v229\n"
            "buffer_load_dwordx2 v[232:233], v230, %[rse], 0 offen\n"
            "v_mfma_f32_16x16x32_bf16 v[128:131], v[192:195], v[96:99], 0\n"
            "v_exp_f32 v168, v136\n"
            "v_exp_f32 v169, v137\n"
            "v_mfma_f32_4x4x4_16b_bf16 v[176:179], v[208:209], v[64:65], 0 cbsz:2 abid:2\n"
            "v_exp_f32 v170, v138\n"
            "v_mfma_f32_4x4x4_16b_bf16 v[180:183], v[208:209], v[66:67], 0 cbsz:2 abid:2\n"
            "v_exp_f32 v171, v139\n"
            "v_mfma_f32_4x4x4_16b_bf16 v[184:187], v[208:209], v[68:69], 0 cbsz:2 abid:2\n"
            "v_exp_f32 v172, v140\n"
            "v_mfma_f32_4x4x4_16b_bf16 v[188:191], v[208:209], v[70:71], 0 cbsz:2 abid:2\n"
            "v_exp_f32 v173, v141\n"
            "v_add_u32_dpp v221, v196, v214 quad_perm:[2,2,2,2] row_mask:0xf bank_mask:0xf\n"
            "v_exp_f32 v174, v142\n"
            "buffer_load_dwordx4 v[64:67], v221, %[rsx], 0 offen\n"
            "buffer_load_dwordx4 v[68:71], v221, %[rsx], 0 offen offset:256\n"
            "v_exp_f32 v175, v143\n"
            "v_pk_fma_f32 v[0:1], v[146:147], v[176:177], v[0:1] op_sel_hi:[0,1,1]\n"
            "v_pk_fma_f32 v[2:3], v[146:147], v[178:179], v[2:3] op_sel_hi:[0,1,1]\n"
            "v_pk_fma_f32 v[4:5], v[150:151], v[180:181], v[4:5] op_sel_hi:[0,1,1]\n"
            "v_pk_fma_f32 v[6:7], v[150:151], v[182:183], v[6:7] op_sel_hi:[0,1,1]\n"
            "v_pk_fma_f32 v[8:9], v[154:155], v[184:185], v[8:9] op_sel_hi:[0,1,1]\n"
            "v_pk_fma_f32 v[10:11], v[154:155], v[186:187], v[10:11] op_sel_hi:[0,1,1]\n"
            "v_pk_fma_f32 v[12:13], v[158:159], v[188:189], v[12:13] op_sel_hi:[0,1,1]\n"
            "v_pk_fma_f32 v[14:15], v[158:159], v[190:191], v[14:15] op_sel_hi:[0,1,1]\n"
            "v_pk_add_f32 v[168:169], v[168:169], 1.0 op_sel_hi:[1,0]\n"
            "v_pk_add_f32 v[170:171], v[170:171], 1.0 op_sel_hi:[1,0]\n"
            "v_pk_add_f32 v[172:173], v[172:173], 1.0 op_sel_hi:[1,0]\n"
            "v_pk_add_f32 v[174:175], v[174:175], 1.0 op_sel_hi:[1,0]\n"
            "v_cvt_pk_bf16_f32 v204, v204, v205\n"
            "v_cvt_pk_bf16_f32 v205, v206, v207\n"
            "ds_write_b64 v212, v[204:205]\n"
            "v_mfma_f32_16x16x32_bf16 v[132:135], v[192:195], v[100:103], 0\n"
            "v_rcp_f32 v168, v168\n"
            "v_rcp_f32 v169, v169\n"
            "v_mfma_f32_4x4x4_16b_bf16 v[176:179], v[208:209], v[80:81], 0 cbsz:2 abid:3\n"
            "v_rcp_f32 v170, v170\n"
            "v_mfma_f32_4x4x4_16b_bf16 v[180:183], v[208:209], v[82:83], 0 cbsz:2 abid:3\n"
            "v_rcp_f32 v171, v171\n"
            "v_mfma_f32_4x4x4_16b_bf16 v[184:187], v[208:209], v[84:85], 0 cbsz:2 abid:3\n"
            "v_rcp_f32 v172, v172\n"
            "v_mfma_f32_4x4x4_16b_bf16 v[188:191], v[208:209], v[86:87], 0 cbsz:2 abid:3\n"
            "v_rcp_f32 v173, v173\n"
            "v_add_u32_dpp v222, v196, v214 quad_perm:[3,3,3,3] row_mask:0xf bank_mask:0xf\n"
            "v_rcp_f32 v174, v174\n"
            "buffer_load_dwordx4 v[80:83], v222, %[rsx], 0 offen\n"
            "buffer_load_dwordx4 v[84:87], v222, %[rsx], 0 offen offset:256\n"
            "v_rcp_f32 v175, v175\n"
            "v_pk_fma_f32 v[0:1], v[146:147], v[176:177], v[0:1] op_sel:[1,0,0]\n"
            "v_pk_fma_f32 v[2:3], v[146:147], v[178:179], v[2:3] op_sel:[1,0,0]\n"
            "v_pk_fma_f32 v[4:5], v[150:151], v[180:181], v[4:5] op_sel:[1,0,0]\n"
            "v_pk_fma_f32 v[6:7], v[150:151], v[182:183], v[6:7] op_sel:[1,0,0]\n"
            "v_pk_fma_f32 v[8:9], v[154:155], v[184:185], v[8:9] op_sel:[1,0,0]\n"
            "v_pk_fma_f32 v[10:11], v[154:155], v[186:187], v[10:11] op_sel:[1,0,0]\n"
            "v_pk_fma_f32 v[12:13], v[158:159], v[188:189], v[12:13] op_sel:[1,0,0]\n"
            "v_pk_fma_f32 v[14:15], v[158:159], v[190:191], v[14:15] op_sel:[1,0,0]\n"
            "v_pk_mul_f32 v[168:169], v[168:169], v[136:137]\n"
            "v_pk_mul_f32 v[170:171], v[170:171], v[138:139]\n"
            "v_pk_mul_f32 v[172:173], v[172:173], v[140:141]\n"
            "v_pk_mul_f32 v[174:175], v[174:175], v[142:143]\n"
            "v_add_u32 v210, 64, v210\n"
            "ds_read_b128 v[196:199], v210\n"
            "v_mfma_f32_16x16x32_bf16 v[136:139], v[192:195], v[104:107], 0\n"
            "v_exp_f32 v144, v128\n"
            "v_exp_f32 v145, v129\n"
            "s_waitcnt vmcnt(13)\n"
            "v_mfma_f32_4x4x4_16b_bf16 v[176:179], v[208:209], v[40:41], 0 cbsz:2\n"
            "v_exp_f32 v146, v130\n"
            "v_mfma_f32_4x4x4_16b_bf16 v[180:183], v[208:209], v[42:43], 0 cbsz:2\n"
            "v_exp_f32 v147, v131\n"
            "v_mfma_f32_4x4x4_16b_bf16 v[184:187], v[208:209], v[44:45], 0 cbsz:2\n"
            "v_exp_f32 v148, v132\n"
            "v_mfma_f32_4x4x4_16b_bf16 v[188:191], v[208:209], v[46:47], 0 cbsz:2\n"
            "v_exp_f32 v149, v133\n"
            "v_exp_f32 v150, v134\n"
            "buffer_load_dwordx4 v[40:43], v219, %[rsx], 0 offen offset:512\n"
            "buffer_load_dwordx4 v[44:47], v219, %[rsx], 0 offen offset:768\n"
            "v_exp_f32 v151, v135\n"
            "v_pk_fma_f32 v[16:17], v[160:161], v[176:177], v[16:17] op_sel_hi:[0,1,1]\n"
            "v_pk_fma_f32 v[18:19], v[160:161], v[178:179], v[18:19] op_sel_hi:[0,1,1]\n"
            "v_pk_fma_f32 v[20:21], v[164:165], v[180:181], v[20:21] op_sel_hi:[0,1,1]\n"
            "v_pk_fma_f32 v[22:23], v[164:165], v[182:183], v[22:23] op_sel_hi:[0,1,1]\n"
            "v_pk_fma_f32 v[24:25], v[168:169], v[184:185], v[24:25] op_sel_hi:[0,1,1]\n"
            "v_pk_fma_f32 v[26:27], v[168:169], v[186:187], v[26:27] op_sel_hi:[0,1,1]\n"
            "v_pk_fma_f32 v[28:29], v[172:173], v[188:189], v[28:29] op_sel_hi:[0,1,1]\n"
            "v_pk_fma_f32 v[30:31], v[172:173], v[190:191], v[30:31] op_sel_hi:[0,1,1]\n"
            "v_pk_add_f32 v[144:145], v[144:145], 1.0 op_sel_hi:[1,0]\n"
            "v_pk_add_f32 v[146:147], v[146:147], 1.0 op_sel_hi:[1,0]\n"
            "v_pk_add_f32 v[148:149], v[148:149], 1.0 op_sel_hi:[1,0]\n"
            "v_pk_add_f32 v[150:151], v[150:151], 1.0 op_sel_hi:[1,0]\n"
            "v_mfma_f32_16x16x32_bf16 v[140:143], v[192:195], v[108:111], 0\n"
            "v_rcp_f32 v144, v144\n"
            "v_rcp_f32 v145, v145\n"
            "v_mfma_f32_4x4x4_16b_bf16 v[176:179], v[208:209], v[56:57], 0 cbsz:2 abid:1\n"
            "v_rcp_f32 v146, v146\n"
            "v_mfma_f32_4x4x4_16b_bf16 v[180:183], v[208:209], v[58:59], 0 cbsz:2 abid:1\n"
            "v_rcp_f32 v147, v147\n"
            "v_mfma_f32_4x4x4_16b_bf16 v[184:187], v[208:209], v[60:61], 0 cbsz:2 abid:1\n"
            "v_rcp_f32 v148, v148\n"
            "v_mfma_f32_4x4x4_16b_bf16 v[188:191], v[208:209], v[62:63], 0 cbsz:2 abid:1\n"
            "v_rcp_f32 v149, v149\n"
            "v_rcp_f32 v150, v150\n"
            "buffer_load_dwordx4 v[56:59], v220, %[rsx], 0 offen offset:512\n"
            "buffer_load_dwordx4 v[60:63], v220, %[rsx], 0 offen offset:768\n"
            "v_rcp_f32 v151, v151\n"
            "v_pk_fma_f32 v[16:17], v[160:161], v[176:177], v[16:17] op_sel:[1,0,0]\n"
            "v_pk_fma_f32 v[18:19], v[160:161], v[178:179], v[18:19] op_sel:[1,0,0]\n"
            "v_pk_fma_f32 v[20:21], v[164:165], v[180:181], v[20:21] op_sel:[1,0,0]\n"
            "v_pk_fma_f32 v[22:23], v[164:165], v[182:183], v[22:23] op_sel:[1,0,0]\n"
            "v_pk_fma_f32 v[24:25], v[168:169], v[184:185], v[24:25] op_sel:[1,0,0]\n"
            "v_pk_fma_f32 v[26:27], v[168:169], v[186:187], v[26:27] op_sel:[1,0,0]\n"
            "v_pk_fma_f32 v[28:29], v[172:173], v[188:189], v[28:29] op_sel:[1,0,0]\n"
            "v_pk_fma_f32 v[30:31], v[172:173], v[190:191], v[30:31] op_sel:[1,0,0]\n"
            "v_pk_mul_f32 v[144:145], v[144:145], v[128:129]\n"
            "v_pk_mul_f32 v[146:147], v[146:147], v[130:131]\n"
            "v_pk_mul_f32 v[148:149], v[148:149], v[132:133]\n"
            "v_pk_mul_f32 v[150:151], v[150:151], v[134:135]\n"
            "v_mfma_f32_16x16x32_bf16 v[128:131], v[192:195], v[112:115], 0\n"
            "v_exp_f32 v152, v136\n"
            "v_exp_f32 v153, v137\n"
            "s_waitcnt vmcnt(13)\n"
            "v_mfma_f32_4x4x4_16b_bf16 v[176:179], v[208:209], v[72:73], 0 cbsz:2 abid:2\n"
            "v_exp_f32 v154, v138\n"
            "v_mfma_f32_4x4x4_16b_bf16 v[180:183], v[208:209], v[74:75], 0 cbsz:2 abid:2\n"
            "v_exp_f32 v155, v139\n"
            "v_mfma_f32_4x4x4_16b_bf16 v[184:187], v[208:209], v[76:77], 0 cbsz:2 abid:2\n"
            "v_exp_f32 v156, v140\n"
            "v_mfma_f32_4x4x4_16b_bf16 v[188:191], v[208:209], v[78:79], 0 cbsz:2 abid:2\n"
            "v_exp_f32 v157, v141\n"
            "v_exp_f32 v158, v142\n"
            "buffer_load_dwordx4 v[72:75], v221, %[rsx], 0 offen offset:512\n"
            "buffer_load_dwordx4 v[76:79], v221, %[rsx], 0 offen offset:768\n"
            "v_exp_f32 v159, v143\n"
            "v_pk_fma_f32 v[16:17], v[162:163], v[176:177], v[16:17] op_sel_hi:[0,1,1]\n"
            "v_pk_fma_f32 v[18:19], v[162:163], v[178:179], v[18:19] op_sel_hi:[0,1,1]\n"
            "v_pk_fma_f32 v[20:21], v[166:167], v[180:181], v[20:21] op_sel_hi:[0,1,1]\n"
            "v_pk_fma_f32 v[22:23], v[166:167], v[182:183], v[22:23] op_sel_hi:[0,1,1]\n"
            "v_pk_fma_f32 v[24:25], v[170:171], v[184:185], v[24:25] op_sel_hi:[0,1,1]\n"
            "v_pk_fma_f32 v[26:27], v[170:171], v[186:187], v[26:27] op_sel_hi:[0,1,1]\n"
            "v_pk_fma_f32 v[28:29], v[174:175], v[188:189], v[28:29] op_sel_hi:[0,1,1]\n"
            "v_pk_fma_f32 v[30:31], v[174:175], v[190:191], v[30:31] op_sel_hi:[0,1,1]\n"
            "v_pk_add_f32 v[152:153], v[152:153], 1.0 op_sel_hi:[1,0]\n"
            "v_pk_add_f32 v[154:155], v[154:155], 1.0 op_sel_hi:[1,0]\n"
            "v_pk_add_f32 v[156:157], v[156:157], 1.0 op_sel_hi:[1,0]\n"
            "v_pk_add_f32 v[158:159], v[158:159], 1.0 op_sel_hi:[1,0]\n"
            "v_mfma_f32_16x16x32_bf16 v[132:135], v[192:195], v[116:119], 0\n"
            "v_rcp_f32 v152, v152\n"
            "v_rcp_f32 v153, v153\n"
            "v_mfma_f32_4x4x4_16b_bf16 v[176:179], v[208:209], v[88:89], 0 cbsz:2 abid:3\n"
            "v_rcp_f32 v154, v154\n"
            "v_mfma_f32_4x4x4_16b_bf16 v[180:183], v[208:209], v[90:91], 0 cbsz:2 abid:3\n"
            "v_rcp_f32 v155, v155\n"
            "v_mfma_f32_4x4x4_16b_bf16 v[184:187], v[208:209], v[92:93], 0 cbsz:2 abid:3\n"
            "v_rcp_f32 v156, v156\n"
            "v_mfma_f32_4x4x4_16b_bf16 v[188:191], v[208:209], v[94:95], 0 cbsz:2 abid:3\n"
            "ds_read_b64 v[208:209], v213\n"
            "v_rcp_f32 v157, v157\n"
            "v_rcp_f32 v158, v158\n"
            "buffer_load_dwordx4 v[88:91], v222, %[rsx], 0 offen offset:512\n"
            "buffer_load_dwordx4 v[92:95], v222, %[rsx], 0 offen offset:768\n"
            "v_rcp_f32 v159, v159\n"
            "v_pk_fma_f32 v[16:17], v[162:163], v[176:177], v[16:17] op_sel:[1,0,0]\n"
            "v_pk_fma_f32 v[18:19], v[162:163], v[178:179], v[18:19] op_sel:[1,0,0]\n"
            "v_pk_fma_f32 v[20:21], v[166:167], v[180:181], v[20:21] op_sel:[1,0,0]\n"
            "v_pk_fma_f32 v[22:23], v[166:167], v[182:183], v[22:23] op_sel:[1,0,0]\n"
            "v_pk_fma_f32 v[24:25], v[170:171], v[184:185], v[24:25] op_sel:[1,0,0]\n"
            "v_pk_fma_f32 v[26:27], v[170:171], v[186:187], v[26:27] op_sel:[1,0,0]\n"
            "v_pk_fma_f32 v[28:29], v[174:175], v[188:189], v[28:29] op_sel:[1,0,0]\n"
            "v_pk_fma_f32 v[30:31], v[174:175], v[190:191], v[30:31] op_sel:[1,0,0]\n"
            "v_pk_mul_f32 v[152:153], v[152:153], v[136:137]\n"
            "v_pk_mul_f32 v[154:155], v[154:155], v[138:139]\n"
            "v_pk_mul_f32 v[156:157], v[156:157], v[140:141]\n"
            "v_pk_mul_f32 v[158:159], v[158:159], v[142:143]\n"
            "s_sub_u32 %[rem], %[rem], 4\n"
            "Lnext_%=:\n"
            "s_sub_u32 %[nst], %[nst], 1\n"
            "Ldisp_%=:\n"
            "s_bitcmp1_b32 %[evt], %[nst]\n"
            "s_cbranch_scc1 Lprio_%=\n"
            "Ld2_%=:\n"
            "s_cmp_gt_u32 %[rem], 4\n"
            "s_cbranch_scc1 Lfast_%=\n"
            "v_mfma_f32_16x16x32_bf16 v[136:139], v[192:195], v[120:123], 0\n"
            "v_exp_f32 v160, v128\n"
            "v_add_u32 v211, 64, v211\n"
            "v_exp_f32 v161, v129\n"
            "s_waitcnt vmcnt(13) lgkmcnt(0)\n"
            "v_mfma_f32_4x4x4_16b_bf16 v[176:179], v[208:209], v[32:33], 0 cbsz:2\n"
            "v_exp_f32 v162, v130\n"
            "v_mfma_f32_4x4x4_16b_bf16 v[180:183], v[208:209], v[34:35], 0 cbsz:2\n"
            "v_exp_f32 v163, v131\n"
            "v_mfma_f32_4x4x4_16b_bf16 v[184:187], v[208:209], v[36:37], 0 cbsz:2\n"
            "v_exp_f32 v164, v132\n"
            "v_mfma_f32_4x4x4_16b_bf16 v[188:191], v[208:209], v[38:39], 0 cbsz:2\n"
            "v_exp_f32 v165, v133\n"
            "v_add_u32_dpp v219, v196, v214 quad_perm:[0,0,0,0] row_mask:0xf bank_mask:0xf\n"
            "v_mfma_f32_4x4x4_16b_bf16 v[204:207], v[202:203], v[198:199], 0\n"
            "v_exp_f32 v166, v134\n"
            "buffer_load_dwordx4 v[32:35], v219, %[rsx], 0 offen\n"
            "buffer_load_dwordx4 v[36:39], v219, %[rsx], 0 offen offset:256\n"
            "v_exp_f32 v167, v135\n"
            "v_pk_fma_f32 v[0:1], v[144:145], v[176:177], v[0:1] op_sel_hi:[0,1,1]\n"
            "v_pk_fma_f32 v[2:3], v[144:145], v[178:179], v[2:3] op_sel_hi:[0,1,1]\n"
            "v_pk_fma_f32 v[4:5], v[148:149], v[180:181], v[4:5] op_sel_hi:[0,1,1]\n"
            "v_pk_fma_f32 v[6:7], v[148:149], v[182:183], v[6:7] op_sel_hi:[0,1,1]\n"
            "v_pk_fma_f32 v[8:9], v[152:153], v[184:185], v[8:9] op_sel_hi:[0,1,1]\n"
            "v_pk_fma_f32 v[10:11], v[152:153], v[186:187], v[10:11] op_sel_hi:[0,1,1]\n"
            "v_pk_fma_f32 v[12:13], v[156:157], v[188:189], v[12:13] op_sel_hi:[0,1,1]\n"
            "v_pk_fma_f32 v[14:15], v[156:157], v[190:191], v[14:15] op_sel_hi:[0,1,1]\n"
            "v_pk_add_f32 v[160:161], v[160:161], 1.0 op_sel_hi:[1,0]\n"
            "v_pk_add_f32 v[162:163], v[162:163], 1.0 op_sel_hi:[1,0]\n"
            "v_pk_add_f32 v[164:165], v[164:165], 1.0 op_sel_hi:[1,0]\n"
            "v_pk_add_f32 v[166:167], v[166:167], 1.0 op_sel_hi:[1,0]\n"
            "s_cmp_eq_u32 %[rem], 1\n"
            "s_cbranch_scc1 Lf00_%=\n"
            "Lr00_%=:\n"
            "v_mfma_f32_16x16x32_bf16 v[140:143], v[192:195], v[124:127], 0\n"
            "ds_read_b32 v230, v211\n"
            "s_waitcnt vmcnt(14)\n"
            "ds_bpermute_b32 v192, v234, v232\n"
            "ds_bpermute_b32 v193, v234, v233\n"
            "ds_bpermute_b32 v194, v234, v232 offset:4\n"
            "ds_bpermute_b32 v195, v234, v233 offset:4\n"
            "v_rcp_f32 v160, v160\n"
            "v_rcp_f32 v161, v161\n"
            "v_mfma_f32_4x4x4_16b_bf16 v[176:179], v[208:209], v[48:49], 0 cbsz:2 abid:1\n"
            "v_rcp_f32 v162, v162\n"
            "v_mfma_f32_4x4x4_16b_bf16 v[180:183], v[208:209], v[50:51], 0 cbsz:2 abid:1\n"
            "v_rcp_f32 v163, v163\n"
            "v_mfma_f32_4x4x4_16b_bf16 v[184:187], v[208:209], v[52:53], 0 cbsz:2 abid:1\n"
            "v_rcp_f32 v164, v164\n"
            "v_mfma_f32_4x4x4_16b_bf16 v[188:191], v[208:209], v[54:55], 0 cbsz:2 abid:1\n"
            "v_rcp_f32 v165, v165\n"
            "v_add_u32_dpp v220, v196, v214 quad_perm:[1,1,1,1] row_mask:0xf bank_mask:0xf\n"
            "v_mfma_f32_4x4x4_16b_bf16 v[204:207], v[200:201], v[198:199], v[204:207]\n"
            "v_rcp_f32 v166, v166\n"
            "buffer_load_dwordx4 v[48:51], v220, %[rsx], 0 offen\n"
            "buffer_load_dwordx4 v[52:55], v220, %[rsx], 0 offen offset:256\n"
            "v_rcp_f32 v167, v167\n"
            "v_pk_fma_f32 v[0:1], v[144:145], v[176:177], v[0:1] op_sel:[1,0,0]\n"
            "v_pk_fma_f32 v[2:3], v[144:145], v[178:179], v[2:3] op_sel:[1,0,0]\n"
            "v_pk_fma_f32 v[4:5], v[148:149], v[180:181], v[4:5] op_sel:[1,0,0]\n"
            "v_pk_fma_f32 v[6:7], v[148:149], v[182:183], v[6:7] op_sel:[1,0,0]\n"
            "v_pk_fma_f32 v[8:9], v[152:153], v[184:185], v[8:9] op_sel:[1,0,0]\n"
            "v_pk_fma_f32 v[10:11], v[152:153], v[186:187], v[10:11] op_sel:[1,0,0]\n"
            "v_pk_fma_f32 v[12:13], v[156:157], v[188:189], v[12:13] op_sel:[1,0,0]\n"
            "v_pk_fma_f32 v[14:15], v[156:157], v[190:191], v[14:15] op_sel:[1,0,0]\n"
            "v_pk_mul_f32 v[160:161], v[160:161], v[128:129]\n"
            "v_pk_mul_f32 v[162:163], v[162:163], v[130:131]\n"
            "v_pk_mul_f32 v[164:165], v[164:165], v[132:133]\n"
            "v_pk_mul_f32 v[166:167], v[166:167], v[134:135]\n"
            "s_cmp_eq_u32 %[rem], 2\n"
            "s_cbranch_scc1 Lf01_%=\n"
            "Lr01_%=:\n"
            "s_waitcnt vmcnt(12) lgkmcnt(0)\n"
            "v_or_b32 v230, v230, v229\n"
            "buffer_load_dwordx2 v[232:233], v230, %[rse], 0 offen\n"
            "v_mfma_f32_16x16x32_bf16 v[128:131], v[192:195], v[96:99], 0\n"
            "v_exp_f32 v168, v136\n"
            "v_exp_f32 v169, v137\n"
            "v_mfma_f32_4x4x4_16b_bf16 v[176:179], v[208:209], v[64:65], 0 cbsz:2 abid:2\n"
            "v_exp_f32 v170, v138\n"
            "v_mfma_f32_4x4x4_16b_bf16 v[180:183], v[208:209], v[66:67], 0 cbsz:2 abid:2\n"
            "v_exp_f32 v171, v139\n"
            "v_mfma_f32_4x4x4_16b_bf16 v[184:187], v[208:209], v[68:69], 0 cbsz:2 abid:2\n"
            "v_exp_f32 v172, v140\n"
            "v_mfma_f32_4x4x4_16b_bf16 v[188:191], v[208:209], v[70:71], 0 cbsz:2 abid:2\n"
            "v_exp_f32 v173, v141\n"
            "v_add_u32_dpp v221, v196, v214 quad_perm:[2,2,2,2] row_mask:0xf bank_mask:0xf\n"
            "v_exp_f32 v174, v142\n"
            "buffer_load_dwordx4 v[64:67], v221, %[rsx], 0 offen\n"
            "buffer_load_dwordx4 v[68:71], v221, %[rsx], 0 offen offset:256\n"
            "v_exp_f32 v175, v143\n"
            "v_pk_fma_f32 v[0:1], v[146:147], v[176:177], v[0:1] op_sel_hi:[0,1,1]\n"
            "v_pk_fma_f32 v[2:3], v[146:147], v[178:179], v[2:3] op_sel_hi:[0,1,1]\n"
            "v_pk_fma_f32 v[4:5], v[150:151], v[180:181], v[4:5] op_sel_hi:[0,1,1]\n"
            "v_pk_fma_f32 v[6:7], v[150:151], v[182:183], v[6:7] op_sel_hi:[0,1,1]\n"
            "v_pk_fma_f32 v[8:9], v[154:155], v[184:185], v[8:9] op_sel_hi:[0,1,1]\n"
            "v_pk_fma_f32 v[10:11], v[154:155], v[186:187], v[10:11] op_sel_hi:[0,1,1]\n"
            "v_pk_fma_f32 v[12:13], v[158:159], v[188:189], v[12:13] op_sel_hi:[0,1,1]\n"
            "v_pk_fma_f32 v[14:15], v[158:159], v[190:191], v[14:15] op_sel_hi:[0,1,1]\n"
            "v_pk_add_f32 v[168:169], v[168:169], 1.0 op_sel_hi:[1,0]\n"
            "v_pk_add_f32 v[170:171], v[170:171], 1.0 op_sel_hi:[1,0]\n"
            "v_pk_add_f32 v[172:173], v[172:173], 1.0 op_sel_hi:[1,0]\n"
            "v_pk_add_f32 v[174:175], v[174:175], 1.0 op_sel_hi:[1,0]\n"
            "v_cvt_pk_bf16_f32 v204, v204, v205\n"
            "v_cvt_pk_bf16_f32 v205, v206, v207\n"
            "ds_write_b64 v212, v[204:205]\n"
            "s_cmp_eq_u32 %[rem], 3\n"
            "s_cbranch_scc1 Lf02_%=\n"
            "Lr02_%=:\n"
            "v_mfma_f32_16x16x32_bf16 v[132:135], v[192:195], v[100:103], 0\n"
            "v_rcp_f32 v168, v168\n"
            "v_rcp_f32 v169, v169\n"
            "v_mfma_f32_4x4x4_16b_bf16 v[176:179], v[208:209], v[80:81], 0 cbsz:2 abid:3\n"
            "v_rcp_f32 v170, v170\n"
            "v_mfma_f32_4x4x4_16b_bf16 v[180:183], v[208:209], v[82:83], 0 cbsz:2 abid:3\n"
            "v_rcp_f32 v171, v171\n"
            "v_mfma_f32_4x4x4_16b_bf16 v[184:187], v[208:209], v[84:85], 0 cbsz:2 abid:3\n"
            "v_rcp_f32 v172, v172\n"
            "v_mfma_f32_4x4x4_16b_bf16 v[188:191], v[208:209], v[86:87], 0 cbsz:2 abid:3\n"
            "v_rcp_f32 v173, v173\n"
            "v_add_u32_dpp v222, v196, v214 quad_perm:[3,3,3,3] row_mask:0xf bank_mask:0xf\n"
            "v_rcp_f32 v174, v174\n"
            "buffer_load_dwordx4 v[80:83], v222, %[rsx], 0 offen\n"
            "buffer_load_dwordx4 v[84:87], v222, %[rsx], 0 offen offset:256\n"
            "v_rcp_f32 v175, v175\n"
            "v_pk_fma_f32 v[0:1], v[146:147], v[176:177], v[0:1] op_sel:[1,0,0]\n"
            "v_pk_fma_f32 v[2:3], v[146:147], v[178:179], v[2:3] op_sel:[1,0,0]\n"
            "v_pk_fma_f32 v[4:5], v[150:151], v[180:181], v[4:5] op_sel:[1,0,0]\n"
            "v_pk_fma_f32 v[6:7], v[150:151], v[182:183], v[6:7] op_sel:[1,0,0]\n"
            "v_pk_fma_f32 v[8:9], v[154:155], v[184:185], v[8:9] op_sel:[1,0,0]\n"
            "v_pk_fma_f32 v[10:11], v[154:155], v[186:187], v[10:11] op_sel:[1,0,0]\n"
            "v_pk_fma_f32 v[12:13], v[158:159], v[188:189], v[12:13] op_sel:[1,0,0]\n"
            "v_pk_fma_f32 v[14:15], v[158:159], v[190:191], v[14:15] op_sel:[1,0,0]\n"
            "v_pk_mul_f32 v[168:169], v[168:169], v[136:137]\n"
            "v_pk_mul_f32 v[170:171], v[170:171], v[138:139]\n"
            "v_pk_mul_f32 v[172:173], v[172:173], v[140:141]\n"
            "v_pk_mul_f32 v[174:175], v[174:175], v[142:143]\n"
            "s_cmp_eq_u32 %[rem], 4\n"
            "s_cbranch_scc1 Lf03_%=\n"
            "Lr03_%=:\n"
            "v_add_u32 v210, 64, v210\n"
            "ds_read_b128 v[196:199], v210\n"
            "v_mfma_f32_16x16x32_bf16 v[136:139], v[192:195], v[104:107], 0\n"
            "v_exp_f32 v144, v128\n"
            "v_exp_f32 v145, v129\n"
            "s_waitcnt vmcnt(13)\n"
            "v_mfma_f32_4x4x4_16b_bf16 v[176:179], v[208:209], v[40:41], 0 cbsz:2\n"
            "v_exp_f32 v146, v130\n"
            "v_mfma_f32_4x4x4_16b_bf16 v[180:183], v[208:209], v[42:43], 0 cbsz:2\n"
            "v_exp_f32 v147, v131\n"
            "v_mfma_f32_4x4x4_16b_bf16 v[184:187], v[208:209], v[44:45], 0 cbsz:2\n"
            "v_exp_f32 v148, v132\n"
            "v_mfma_f32_4x4x4_16b_bf16 v[188:191], v[208:209], v[46:47], 0 cbsz:2\n"
            "v_exp_f32 v149, v133\n"
            "v_exp_f32 v150, v134\n"
            "buffer_load_dwordx4 v[40:43], v219, %[rsx], 0 offen offset:512\n"
            "buffer_load_dwordx4 v[44:47], v219, %[rsx], 0 offen offset:768\n"
            "v_exp_f32 v151, v135\n"
            "v_pk_fma_f32 v[16:17], v[160:161], v[176:177], v[16:17] op_sel_hi:[0,1,1]\n"
            "v_pk_fma_f32 v[18:19], v[160:161], v[178:179], v[18:19] op_sel_hi:[0,1,1]\n"
            "v_pk_fma_f32 v[20:21], v[164:165], v[180:181], v[20:21] op_sel_hi:[0,1,1]\n"
            "v_pk_fma_f32 v[22:23], v[164:165], v[182:183], v[22:23] op_sel_hi:[0,1,1]\n"
            "v_pk_fma_f32 v[24:25], v[168:169], v[184:185], v[24:25] op_sel_hi:[0,1,1]\n"
            "v_pk_fma_f32 v[26:27], v[168:169], v[186:187], v[26:27] op_sel_hi:[0,1,1]\n"
            "v_pk_fma_f32 v[28:29], v[172:173], v[188:189], v[28:29] op_sel_hi:[0,1,1]\n"
            "v_pk_fma_f32 v[30:31], v[172:173], v[190:191], v[30:31] op_sel_hi:[0,1,1]\n"
            "v_pk_add_f32 v[144:145], v[144:145], 1.0 op_sel_hi:[1,0]\n"
            "v_pk_add_f32 v[146:147], v[146:147], 1.0 op_sel_hi:[1,0]\n"
            "v_pk_add_f32 v[148:149], v[148:149], 1.0 op_sel_hi:[1,0]\n"
            "v_pk_add_f32 v[150:151], v[150:151], 1.0 op_sel_hi:[1,0]\n"
            "s_cmp_eq_u32 %[rem], 1\n"
            "s_cbranch_scc1 Lf10_%=\n"
            "Lr10_%=:\n"
            "v_mfma_f32_16x16x32_bf16 v[140:143], v[192:195], v[108:111], 0\n"
            "v_rcp_f32 v144, v144\n"
            "v_rcp_f32 v145, v145\n"
            "v_mfma_f32_4x4x4_16b_bf16 v[176:179], v[208:209], v[56:57], 0 cbsz:2 abid:1\n"
            "v_rcp_f32 v146, v146\n"
            "v_mfma_f32_4x4x4_16b_bf16 v[180:183], v[208:209], v[58:59], 0 cbsz:2 abid:1\n"
            "v_rcp_f32 v147, v147\n"
            "v_mfma_f32_4x4x4_16b_bf16 v[184:187], v[208:209], v[60:61], 0 cbsz:2 abid:1\n"
            "v_rcp_f32 v148, v148\n"
            "v_mfma_f32_4x4x4_16b_bf16 v[188:191], v[208:209], v[62:63], 0 cbsz:2 abid:1\n"
            "v_rcp_f32 v149, v149\n"
            "v_rcp_f32 v150, v150\n"
            "buffer_load_dwordx4 v[56:59], v220, %[rsx], 0 offen offset:512\n"
            "buffer_load_dwordx4 v[60:63], v220, %[rsx], 0 offen offset:768\n"
            "v_rcp_f32 v151, v151\n"
            "v_pk_fma_f32 v[16:17], v[160:161], v[176:177], v[16:17] op_sel:[1,0,0]\n"
            "v_pk_fma_f32 v[18:19], v[160:161], v[178:179], v[18:19] op_sel:[1,0,0]\n"
            "v_pk_fma_f32 v[20:21], v[164:165], v[180:181], v[20:21] op_sel:[1,0,0]\n"
            "v_pk_fma_f32 v[22:23], v[164:165], v[182:183], v[22:23] op_sel:[1,0,0]\n"
            "v_pk_fma_f32 v[24:25], v[168:169], v[184:185], v[24:25] op_sel:[1,0,0]\n"
            "v_pk_fma_f32 v[26:27], v[168:169], v[186:187], v[26:27] op_sel:[1,0,0]\n"
            "v_pk_fma_f32 v[28:29], v[172:173], v[188:189], v[28:29] op_sel:[1,0,0]\n"
            "v_pk_fma_f32 v[30:31], v[172:173], v[190:191], v[30:31] op_sel:[1,0,0]\n"
            "v_pk_mul_f32 v[144:145], v[144:145], v[128:129]\n"
            "v_pk_mul_f32 v[146:147], v[146:147], v[130:131]\n"
            "v_pk_mul_f32 v[148:149], v[148:149], v[132:133]\n"
            "v_pk_mul_f32 v[150:151], v[150:151], v[134:135]\n"
            "s_cmp_eq_u32 %[rem], 2\n"
            "s_cbranch_scc1 Lf11_%=\n"
            "Lr11_%=:\n"
            "v_mfma_f32_16x16x32_bf16 v[128:131], v[192:195], v[112:115], 0\n"
            "v_exp_f32 v152, v136\n"
            "v_exp_f32 v153, v137\n"
            "s_waitcnt vmcnt(13)\n"
            "v_mfma_f32_4x4x4_16b_bf16 v[176:179], v[208:209], v[72:73], 0 cbsz:2 abid:2\n"
            "v_exp_f32 v154, v138\n"
            "v_mfma_f32_4x4x4_16b_bf16 v[180:183], v[208:209], v[74:75], 0 cbsz:2 abid:2\n"
            "v_exp_f32 v155, v139\n"
            "v_mfma_f32_4x4x4_16b_bf16 v[184:187], v[208:209], v[76:77], 0 cbsz:2 abid:2\n"
            "v_exp_f32 v156, v140\n"
            "v_mfma_f32_4x4x4_16b_bf16 v[188:191], v[208:209], v[78:79], 0 cbsz:2 abid:2\n"
            "v_exp_f32 v157, v141\n"
            "v_exp_f32 v158, v142\n"
            "buffer_load_dwordx4 v[72:75], v221, %[rsx], 0 offen offset:512\n"
            "buffer_load_dwordx4 v[76:79], v221, %[rsx], 0 offen offset:768\n"
            "v_exp_f32 v159, v143\n"
            "v_pk_fma_f32 v[16:17], v[162:163], v[176:177], v[16:17] op_sel_hi:[0,1,1]\n"
            "v_pk_fma_f32 v[18:19], v[162:163], v[178:179], v[18:19] op_sel_hi:[0,1,1]\n"
            "v_pk_fma_f32 v[20:21], v[166:167], v[180:181], v[20:21] op_sel_hi:[0,1,1]\n"
            "v_pk_fma_f32 v[22:23], v[166:167], v[182:183], v[22:23] op_sel_hi:[0,1,1]\n"
            "v_pk_fma_f32 v[24:25], v[170:171], v[184:185], v[24:25] op_sel_hi:[0,1,1]\n"
            "v_pk_fma_f32 v[26:27], v[170:171], v[186:187], v[26:27] op_sel_hi:[0,1,1]\n"
            "v_pk_fma_f32 v[28:29], v[174:175], v[188:189], v[28:29] op_sel_hi:[0,1,1]\n"
            "v_pk_fma_f32 v[30:31], v[174:175], v[190:191], v[30:31] op_sel_hi:[0,1,1]\n"
            "v_pk_add_f32 v[152:153], v[152:153], 1.0 op_sel_hi:[1,0]\n"
            "v_pk_add_f32 v[154:155], v[154:155], 1.0 op_sel_hi:[1,0]\n"
            "v_pk_add_f32 v[156:157], v[156:157], 1.0 op_sel_hi:[1,0]\n"
            "v_pk_add_f32 v[158:159], v[158:159], 1.0 op_sel_hi:[1,0]\n"
            "s_cmp_eq_u32 %[rem], 3\n"
            "s_cbranch_scc1 Lf12_%=\n"
            "Lr12_%=:\n"
            "v_mfma_f32_16x16x32_bf16 v[132:135], v[192:195], v[116:119], 0\n"
            "v_rcp_f32 v152, v152\n"
            "v_rcp_f32 v153, v153\n"
            "v_mfma_f32_4x4x4_16b_bf16 v[176:179], v[208:209], v[88:89], 0 cbsz:2 abid:3\n"
            "v_rcp_f32 v154, v154\n"
            "v_mfma_f32_4x4x4_16b_bf16 v[180:183], v[208:209], v[90:91], 0 cbsz:2 abid:3\n"
            "v_rcp_f32 v155, v155\n"
            "v_mfma_f32_4x4x4_16b_bf16 v[184:187], v[208:209], v[92:93], 0 cbsz:2 abid:3\n"
            "v_rcp_f32 v156, v156\n"
            "v_mfma_f32_4x4x4_16b_bf16 v[188:191], v[208:209], v[94:95], 0 cbsz:2 abid:3\n"
            "ds_read_b64 v[208:209], v213\n"
            "v_rcp_f32 v157, v157\n"
            "v_rcp_f32 v158, v158\n"
            "buffer_load_dwordx4 v[88:91], v222, %[rsx], 0 offen offset:512\n"
            "buffer_load_dwordx4 v[92:95], v222, %[rsx], 0 offen offset:768\n"
            "v_rcp_f32 v159, v159\n"
            "v_pk_fma_f32 v[16:17], v[162:163], v[176:177], v[16:17] op_sel:[1,0,0]\n"
            "v_pk_fma_f32 v[18:19], v[162:163], v[178:179], v[18:19] op_sel:[1,0,0]\n"
            "v_pk_fma_f32 v[20:21], v[166:167], v[180:181], v[20:21] op_sel:[1,0,0]\n"
            "v_pk_fma_f32 v[22:23], v[166:167], v[182:183], v[22:23] op_sel:[1,0,0]\n"
            "v_pk_fma_f32 v[24:25], v[170:171], v[184:185], v[24:25] op_sel:[1,0,0]\n"
            "v_pk_fma_f32 v[26:27], v[170:171], v[186:187], v[26:27] op_sel:[1,0,0]\n"
            "v_pk_fma_f32 v[28:29], v[174:175], v[188:189], v[28:29] op_sel:[1,0,0]\n"
            "v_pk_fma_f32 v[30:31], v[174:175], v[190:191], v[30:31] op_sel:[1,0,0]\n"
            "v_pk_mul_f32 v[152:153], v[152:153], v[136:137]\n"
            "v_pk_mul_f32 v[154:155], v[154:155], v[138:139]\n"
            "v_pk_mul_f32 v[156:157], v[156:157], v[140:141]\n"
            "v_pk_mul_f32 v[158:159], v[158:159], v[142:143]\n"
            "s_cmp_eq_u32 %[rem], 4\n"
            "s_cbranch_scc1 Lf13_%=\n"
            "Lr13_%=:\n"
            "s_and_b32 %[tmp], %[lens], 0xff\n"
            "s_add_u32 %[rem], %[rem], %[tmp]\n"
            "s_lshr_b32 %[lens], %[lens], 8\n"
            "s_lshr_b32 %[fm], %[fm], 4\n"
            "s_bitcmp1_b32 %[fm], 3\n"
            "v_mov_b32 v215, v216\n"
            "s_cbranch_scc0 Lnext_%=\n"
            "s_sub_u32 %[pb], %[nst], 2\n"
            "s_bitset1_b32 %[evt], %[pb]\n"
            "s_branch Lnext_%=\n"
            "Lpub_%=:\n"
            "s_waitcnt vmcnt(17)\n"
            "v_mov_b32 v176, %[cnt]\n"
            "v_mov_b32 v177, 1\n"
            "s_mov_b64 exec, 1\n"
            "ds_add_rtn_u32 v177, v176, v177\n"
            "s_waitcnt lgkmcnt(0)\n"
            "v_readfirstlane_b32 %[tmp], v177\n"
            "s_mov_b64 exec, 0xff\n"
            "v_mov_b32 v176, %[tag]\n"
            "v_mov_b32 v177, %[tag]\n"
            "v_mov_b32 v178, %[tag]\n"
            "v_mov_b32 v179, %[tag]\n"
            "s_cmp_lg_u32 %[tmp], 7\n"
            "s_cbranch_scc1 Lpub2_%=\n"
            "global_store_dwordx4 v214, v[176:179], %[flg] sc1\n"
            "Lpub2_%=:\n"
            "s_mov_b64 exec, -1\n"
            "s_branch Lpr2_%=\n"
            "Lprio_%=:\n"
            "s_cmp_eq_u32 %[nst], %[pb]\n"
            "s_cbranch_scc1 Lpub_%=\n"
            "Lpr2_%=:\n"
            "s_cmp_eq_u32 %[nst], 1\n"
            "s_cbranch_scc1 Llast_%=\n"
            "s_lshl2_add_u32 %[tmp], %[nst], %[c1]\n"
            "v_bfe_u32 v176, v214, 4, 2\n"
            "v_lshrrev_b32 v177, 6, v214\n"
            "v_sub_u32 v176, %[tmp], v176\n"
            "v_sub_u32 v177, %[tmp], v177\n"
            "s_min_u32 %[tmp], %[nst], 15\n"
            "v_cmp_gt_u32 vcc, 4, v176\n"
            "v_cmp_gt_u32 %[ra], 4, v177\n"
            "v_add_u32 v176, %[c2], v176\n"
            "v_add_u32 v177, %[c2], v177\n"
            "v_cndmask_b32 v178, 0, v236, vcc\n"
            "v_cndmask_b32 v179, 0, v236, %[ra]\n"
            "v_add_u32 v210, v210, v178\n"
            "v_add_u32 v211, v211, v179\n"
            "v_cmp_gt_u32 vcc, 4, v176\n"
            "v_cmp_gt_u32 %[ra], 4, v177\n"
            "s_nop 1\n"
            "v_cndmask_b32 v178, 0, v237, vcc\n"
            "v_cndmask_b32 v179, 0, v237, %[ra]\n"
            "v_add_u32 v210, v210, v178\n"
            "v_add_u32 v211, v211, v179\n"
            "s_lshl_b32 %[tmp], %[tmp], 1\n"
            "s_lshr_b32 %[tmp], %[ptab], %[tmp]\n"
            "s_bitcmp1_b32 %[tmp], 1\n"
            "s_cbranch_scc1 Lq2_%=\n"
            "s_bitcmp1_b32 %[tmp], 0\n"
            "s_setprio 0\n"
            "s_cbranch_scc0 Lq9_%=\n"
            "s_setprio 1\n"
            "s_branch Lq9_%=\n"
            "Lq2_%=:\n"
            "s_bitcmp1_b32 %[tmp], 0\n"
            "s_setprio 2\n"
            "s_cbranch_scc0 Lq9_%=\n"
            "s_setprio 3\n"
            "Lq9_%=:\n"
            "s_cmp_lg_u32 %[nst], 2\n"
            "s_cbranch_scc1 Ld2_%=\n"
            "v_mov_b32 v223, 0\n"
            "global_load_dword v223, v223, %[pfl] sc1\n"
            "s_branch Ld2_%=\n"
            "Lf00_%=:\n"
            "s_call_b64 %[ra], Lflush0_%=\n"
            "s_branch Lr00_%=\n"
            "Lf01_%=:\n"
            "s_call_b64 %[ra], Lflush0_%=\n"
            "s_branch Lr01_%=\n"
            "Lf02_%=:\n"
            "s_call_b64 %[ra], Lflush0_%=\n"
            "s_branch Lr02_%=\n"
            "Lf03_%=:\n"
            "s_call_b64 %[ra], Lflush0_%=\n"
            "s_branch Lr03_%=\n"
            "Lflush0_%=:\n"
            "v_mov_b32 v184, %[pfa]\n"
            "s_bitcmp0_b32 %[fm], 0\n"
            "s_cbranch_scc1 Lfa0_%=\n"
            "Lfw_%=:\n"
            "ds_read_b32 v185, v184\n"
            "ds_read_b128 v[176:179], v231 offset:0\n"
            "ds_read_b128 v[180:183], v231 offset:1024\n"
            "s_waitcnt lgkmcnt(0)\n"
            "v_cmp_eq_u32 vcc, 0, v185\n"
            "s_cbranch_vccnz Lfw_%=\n"
            "v_mfma_f32_4x4x4_16b_bf16 v[0:3], v[224:225], v[176:177], v[0:3]\n"
            "v_mfma_f32_4x4x4_16b_bf16 v[4:7], v[224:225], v[178:179], v[4:7]\n"
            "v_mfma_f32_4x4x4_16b_bf16 v[8:11], v[224:225], v[180:181], v[8:11]\n"
            "v_mfma_f32_4x4x4_16b_bf16 v[12:15], v[224:225], v[182:183], v[12:15]\n"
            "s_nop 1\n"
            "Lfa0_%=:\n"
            "v_cvt_pk_bf16_f32 v176, v0, v1\n"
            "v_cvt_pk_bf16_f32 v177, v2, v3\n"
            "v_cvt_pk_bf16_f32 v178, v4, v5\n"
            "v_cvt_pk_bf16_f32 v179, v6, v7\n"
            "v_cvt_pk_bf16_f32 v180, v8, v9\n"
            "v_cvt_pk_bf16_f32 v181, v10, v11\n"
            "v_cvt_pk_bf16_f32 v182, v12, v13\n"
            "v_cvt_pk_bf16_f32 v183, v14, v15\n"
            "s_bitcmp1_b32 %[fm], 1\n"
            "s_cbranch_scc1 Lpark0_%=\n"
            "buffer_store_dwordx4 v[176:179], v215, %[rsw], 0 offen offset:0 sc1\n"
            "buffer_store_dwordx4 v[180:183], v215, %[rsw], 0 offen offset:256 sc1\n"
            "s_branch Lzero0_%=\n"
            "Lpark0_%=:\n"
            "ds_write_b128 v231, v[176:179] offset:0\n"
            "ds_write_b128 v231, v[180:183] offset:1024\n"
            "Lzero0_%=:\n"
            "v_mov_b64 v[0:1], 0\n"
            "v_mov_b64 v[2:3], 0\n"
            "v_mov_b64 v[4:5], 0\n"
            "v_mov_b64 v[6:7], 0\n"
            "v_mov_b64 v[8:9], 0\n"
            "v_mov_b64 v[10:11], 0\n"
            "v_mov_b64 v[12:13], 0\n"
            "v_mov_b64 v[14:15], 0\n"
            "s_setpc_b64 %[ra]\n"
            "Lf10_%=:\n"
            "s_call_b64 %[ra], Lflush1_%=\n"
            "s_branch Lr10_%=\n"
            "Lf11_%=:\n"
            "s_call_b64 %[ra], Lflush1_%=\n"
            "s_branch Lr11_%=\n"
            "Lf12_%=:\n"
            "s_call_b64 %[ra], Lflush1_%=\n"
            "s_branch Lr12_%=\n"
            "Lf13_%=:\n"
            "s_call_b64 %[ra], Lflush1_%=\n"
            "s_branch Lr13_%=\n"
            "Lflush1_%=:\n"
            "v_mov_b32 v184, %[pfa]\n"
            "s_bitcmp0_b32 %[fm], 0\n"
            "s_cbranch_scc1 Lfa1_%=\n"
            "ds_read_b128 v[176:179], v231 offset:2048\n"
            "ds_read_b128 v[180:183], v231 offset:3072\n"
            "s_waitcnt lgkmcnt(0)\n"
            "v_mfma_f32_4x4x4_16b_bf16 v[16:19], v[224:225], v[176:177], v[16:19]\n"
            "v_mfma_f32_4x4x4_16b_bf16 v[20:23], v[224:225], v[178:179], v[20:23]\n"
            "v_mfma_f32_4x4x4_16b_bf16 v[24:27], v[224:225], v[180:181], v[24:27]\n"
            "v_mfma_f32_4x4x4_16b_bf16 v[28:31], v[224:225], v[182:183], v[28:31]\n"
            "s_nop 1\n"
            "Lfa1_%=:\n"
            "v_cvt_pk_bf16_f32 v176, v16, v17\n"
            "v_cvt_pk_bf16_f32 v177, v18, v19\n"
            "v_cvt_pk_bf16_f32 v178, v20, v21\n"
            "v_cvt_pk_bf16_f32 v179, v22, v23\n"
            "v_cvt_pk_bf16_f32 v180, v24, v25\n"
            "v_cvt_pk_bf16_f32 v181, v26, v27\n"
            "v_cvt_pk_bf16_f32 v182, v28, v29\n"
            "v_cvt_pk_bf16_f32 v183, v30, v31\n"
            "s_bitcmp1_b32 %[fm], 1\n"
            "s_cbranch_scc1 Lpark1_%=\n"
            "buffer_store_dwordx4 v[176:179], v215, %[rsw], 0 offen offset:512 sc1\n"
            "buffer_store_dwordx4 v[180:183], v215, %[rsw], 0 offen offset:768 sc1\n"
            "s_branch Lzero1_%=\n"
            "Lpark1_%=:\n"
            "ds_write_b128 v231, v[176:179] offset:2048\n"
            "ds_write_b128 v231, v[180:183] offset:3072\n"
            "ds_write_b32 v184, v184\n"
            "Lzero1_%=:\n"
            "v_mov_b64 v[16:17], 0\n"
            "v_mov_b64 v[18:19], 0\n"
            "v_mov_b64 v[20:21], 0\n"
            "v_mov_b64 v[22:23], 0\n"
            "v_mov_b64 v[24:25], 0\n"
            "v_mov_b64 v[26:27], 0\n"
            "v_mov_b64 v[28:29], 0\n"
            "v_mov_b64 v[30:31], 0\n"
            "s_setpc_b64 %[ra]\n"
            "Lspin_%=:\n"
            "s_mov_b32 %[lens], %[spin]\n"
            "Lsp1_%=:\n"
            "v_readfirstlane_b32 %[nst], v223\n"
            "s_sub_u32 %[lens], %[lens], 1\n"
            "s_cmp_eq_u32 %[lens], 0\n"
            "s_cbranch_scc1 Lsp2_%=\n"
            "s_cmp_eq_u32 %[nst], %[tag]\n"
            "s_cbranch_scc1 Lsp2_%=\n"
            "s_sleep 1\n"
            "v_mov_b32 v223, 0\n"
            "global_load_dword v223, v223, %[pfl] sc1\n"
            "s_waitcnt vmcnt(0)\n"
            "s_branch Lsp1_%=\n"
            "Lsp2_%=:\n"
            "buffer_load_dwordx4 v[32:35], v217, %[rsw], %[oagg] offen offset:0 sc1\n"
            "buffer_load_dwordx4 v[36:39], v217, %[rsw], %[oagg] offen offset:256 sc1\n"
            "buffer_load_dwordx4 v[48:51], v217, %[rsw], %[oagg] offen offset:512 sc1\n"
            "buffer_load_dwordx4 v[52:55], v217, %[rsw], %[oagg] offen offset:768 sc1\n"
            "s_branch Lgo_%=\n"
            "Llast_%=:\n"
            "s_waitcnt vmcnt(16)\n"
            "v_readfirstlane_b32 %[nst], v223\n"
            "v_mov_b32 v222, v217\n"
            "s_cmp_eq_u32 %[nst], %[tag]\n"
            "s_cbranch_scc1 Lp4_%=\n"
            "v_bfrev_b32 v222, 1\n"
            "v_mov_b32 v223, 0\n"
            "global_load_dword v223, v223, %[pfl] sc1\n"
            "Lp4_%=:\n"
            "v_mfma_f32_16x16x32_bf16 v[136:139], v[192:195], v[120:123], 0\n"
            "v_exp_f32 v160, v128\n"
            "v_exp_f32 v161, v129\n"
            "s_waitcnt vmcnt(13) lgkmcnt(0)\n"
            "v_mfma_f32_4x4x4_16b_bf16 v[176:179], v[208:209], v[32:33], 0 cbsz:2\n"
            "v_exp_f32 v162, v130\n"
            "v_mfma_f32_4x4x4_16b_bf16 v[180:183], v[208:209], v[34:35], 0 cbsz:2\n"
            "v_exp_f32 v163, v131\n"
            "v_mfma_f32_4x4x4_16b_bf16 v[184:187], v[208:209], v[36:37], 0 cbsz:2\n"
            "v_exp_f32 v164, v132\n"
            "v_mfma_f32_4x4x4_16b_bf16 v[188:191], v[208:209], v[38:39], 0 cbsz:2\n"
            "v_exp_f32 v165, v133\n"
            "v_exp_f32 v166, v134\n"
            "buffer_load_dwordx4 v[32:35], v222, %[rsw], %[oagg] offen offset:0 sc1\n"
            "buffer_load_dwordx4 v[36:39], v222, %[rsw], %[oagg] offen offset:256 sc1\n"
            "ds_read_b128 v[236:239], v228 offset:0\n"
            "ds_read_b128 v[240:243], v228 offset:1024\n"
            "ds_read_b128 v[244:247], v228 offset:2048\n"
            "ds_read_b128 v[248:251], v228 offset:3072\n"
            "ds_read_b128 v[96:99], v228 offset:4096\n"
            "v_exp_f32 v167, v135\n"
            "v_pk_fma_f32 v[0:1], v[144:145], v[176:177], v[0:1] op_sel_hi:[0,1,1]\n"
            "v_pk_fma_f32 v[2:3], v[144:145], v[178:179], v[2:3] op_sel_hi:[0,1,1]\n"
            "v_pk_fma_f32 v[4:5], v[148:149], v[180:181], v[4:5] op_sel_hi:[0,1,1]\n"
            "v_pk_fma_f32 v[6:7], v[148:149], v[182:183], v[6:7] op_sel_hi:[0,1,1]\n"
            "v_pk_fma_f32 v[8:9], v[152:153], v[184:185], v[8:9] op_sel_hi:[0,1,1]\n"
            "v_pk_fma_f32 v[10:11], v[152:153], v[186:187], v[10:11] op_sel_hi:[0,1,1]\n"
            "v_pk_fma_f32 v[12:13], v[156:157], v[188:189], v[12:13] op_sel_hi:[0,1,1]\n"
            "v_pk_fma_f32 v[14:15], v[156:157], v[190:191], v[14:15] op_sel_hi:[0,1,1]\n"
            "v_pk_add_f32 v[160:161], v[160:161], 1.0 op_sel_hi:[1,0]\n"
            "v_pk_add_f32 v[162:163], v[162:163], 1.0 op_sel_hi:[1,0]\n"
            "v_pk_add_f32 v[164:165], v[164:165], 1.0 op_sel_hi:[1,0]\n"
            "v_pk_add_f32 v[166:167], v[166:167], 1.0 op_sel_hi:[1,0]\n"
            "v_mfma_f32_16x16x32_bf16 v[140:143], v[192:195], v[124:127], 0\n"
            "v_rcp_f32 v160, v160\n"
            "v_rcp_f32 v161, v161\n"
            "v_mfma_f32_4x4x4_16b_bf16 v[176:179], v[208:209], v[48:49], 0 cbsz:2 abid:1\n"
            "v_rcp_f32 v162, v162\n"
            "v_mfma_f32_4x4x4_16b_bf16 v[180:183], v[208:209], v[50:51], 0 cbsz:2 abid:1\n"
            "v_rcp_f32 v163, v163\n"
            "v_mfma_f32_4x4x4_16b_bf16 v[184:187], v[208:209], v[52:53], 0 cbsz:2 abid:1\n"
            "v_rcp_f32 v164, v164\n"
            "v_mfma_f32_4x4x4_16b_bf16 v[188:191], v[208:209], v[54:55], 0 cbsz:2 abid:1\n"
            "v_rcp_f32 v165, v165\n"
            "v_rcp_f32 v166, v166\n"
            "buffer_load_dwordx4 v[48:51], v222, %[rsw], %[oagg] offen offset:512 sc1\n"
            "buffer_load_dwordx4 v[52:55], v222, %[rsw], %[oagg] offen offset:768 sc1\n"
            "ds_read_b128 v[100:103], v228 offset:5120\n"
            "ds_read_b128 v[104:107], v228 offset:6144\n"
            "ds_read_b128 v[108:111], v228 offset:7168\n"
            "ds_read_b128 v[112:115], v228 offset:8192\n"
            "ds_read_b128 v[116:119], v228 offset:9216\n"
            "ds_read_b128 v[196:199], v228 offset:10240\n"
            "v_rcp_f32 v167, v167\n"
            "v_pk_fma_f32 v[0:1], v[144:145], v[176:177], v[0:1] op_sel:[1,0,0]\n"
            "v_pk_fma_f32 v[2:3], v[144:145], v[178:179], v[2:3] op_sel:[1,0,0]\n"
            "v_pk_fma_f32 v[4:5], v[148:149], v[180:181], v[4:5] op_sel:[1,0,0]\n"
            "v_pk_fma_f32 v[6:7], v[148:149], v[182:183], v[6:7] op_sel:[1,0,0]\n"
            "v_pk_fma_f32 v[8:9], v[152:153], v[184:185], v[8:9] op_sel:[1,0,0]\n"
            "v_pk_fma_f32 v[10:11], v[152:153], v[186:187], v[10:11] op_sel:[1,0,0]\n"
            "v_pk_fma_f32 v[12:13], v[156:157], v[188:189], v[12:13] op_sel:[1,0,0]\n"
            "v_pk_fma_f32 v[14:15], v[156:157], v[190:191], v[14:15] op_sel:[1,0,0]\n"
            "v_pk_mul_f32 v[160:161], v[160:161], v[128:129]\n"
            "v_pk_mul_f32 v[162:163], v[162:163], v[130:131]\n"
            "v_pk_mul_f32 v[164:165], v[164:165], v[132:133]\n"
            "v_pk_mul_f32 v[166:167], v[166:167], v[134:135]\n"
            "v_exp_f32 v168, v136\n"
            "v_exp_f32 v169, v137\n"
            "s_waitcnt vmcnt(12)\n"
            "v_mfma_f32_4x4x4_16b_bf16 v[176:179], v[208:209], v[64:65], 0 cbsz:2 abid:2\n"
            "v_exp_f32 v170, v138\n"
            "v_mfma_f32_4x4x4_16b_bf16 v[180:183], v[208:209], v[66:67], 0 cbsz:2 abid:2\n"
            "v_exp_f32 v171, v139\n"
            "v_mfma_f32_4x4x4_16b_bf16 v[184:187], v[208:209], v[68:69], 0 cbsz:2 abid:2\n"
            "v_exp_f32 v172, v140\n"
            "v_mfma_f32_4x4x4_16b_bf16 v[188:191], v[208:209], v[70:71], 0 cbsz:2 abid:2\n"
            "v_exp_f32 v173, v141\n"
            "v_exp_f32 v174, v142\n"
            "ds_read_b128 v[200:203], v228 offset:11264\n"
            "ds_read_b128 v[204:207], v228 offset:12288\n"
            "ds_read_b128 v[232:235], v228 offset:13312\n"
            "v_exp_f32 v175, v143\n"
            "v_pk_fma_f32 v[0:1], v[146:147], v[176:177], v[0:1] op_sel_hi:[0,1,1]\n"
            "v_pk_fma_f32 v[2:3], v[146:147], v[178:179], v[2:3] op_sel_hi:[0,1,1]\n"
            "v_pk_fma_f32 v[4:5], v[150:151], v[180:181], v[4:5] op_sel_hi:[0,1,1]\n"
            "v_pk_fma_f32 v[6:7], v[150:151], v[182:183], v[6:7] op_sel_hi:[0,1,1]\n"
            "v_pk_fma_f32 v[8:9], v[154:155], v[184:185], v[8:9] op_sel_hi:[0,1,1]\n"
            "v_pk_fma_f32 v[10:11], v[154:155], v[186:187], v[10:11] op_sel_hi:[0,1,1]\n"
            "v_pk_fma_f32 v[12:13], v[158:159], v[188:189], v[12:13] op_sel_hi:[0,1,1]\n"
            "v_pk_fma_f32 v[14:15], v[158:159], v[190:191], v[14:15] op_sel_hi:[0,1,1]\n"
            "v_pk_add_f32 v[168:169], v[168:169], 1.0 op_sel_hi:[1,0]\n"
            "v_pk_add_f32 v[170:171], v[170:171], 1.0 op_sel_hi:[1,0]\n"
            "v_pk_add_f32 v[172:173], v[172:173], 1.0 op_sel_hi:[1,0]\n"
            "v_pk_add_f32 v[174:175], v[174:175], 1.0 op_sel_hi:[1,0]\n"
            "v_rcp_f32 v168, v168\n"
            "v_rcp_f32 v169, v169\n"
            "v_mfma_f32_4x4x4_16b_bf16 v[176:179], v[208:209], v[80:81], 0 cbsz:2 abid:3\n"
            "v_rcp_f32 v170, v170\n"
            "v_mfma_f32_4x4x4_16b_bf16 v[180:183], v[208:209], v[82:83], 0 cbsz:2 abid:3\n"
            "v_rcp_f32 v171, v171\n"
            "v_mfma_f32_4x4x4_16b_bf16 v[184:187], v[208:209], v[84:85], 0 cbsz:2 abid:3\n"
            "v_rcp_f32 v172, v172\n"
            "v_mfma_f32_4x4x4_16b_bf16 v[188:191], v[208:209], v[86:87], 0 cbsz:2 abid:3\n"
            "v_rcp_f32 v173, v173\n"
            "v_rcp_f32 v174, v174\n"
            "ds_read_b128 v[120:123], v228 offset:14336\n"
            "ds_read_b128 v[124:127], v228 offset:15360\n"
            "ds_read_b128 v[192:195], v228 offset:16384\n"
            "ds_read_b128 v[128:131], v228 offset:17408\n"
            "ds_read_b128 v[132:135], v228 offset:18432\n"
            "v_rcp_f32 v175, v175\n"
            "v_pk_fma_f32 v[0:1], v[146:147], v[176:177], v[0:1] op_sel:[1,0,0]\n"
            "v_pk_fma_f32 v[2:3], v[146:147], v[178:179], v[2:3] op_sel:[1,0,0]\n"
            "v_pk_fma_f32 v[4:5], v[150:151], v[180:181], v[4:5] op_sel:[1,0,0]\n"
            "v_pk_fma_f32 v[6:7], v[150:151], v[182:183], v[6:7] op_sel:[1,0,0]\n"
            "v_pk_fma_f32 v[8:9], v[154:155], v[184:185], v[8:9] op_sel:[1,0,0]\n"
            "v_pk_fma_f32 v[10:11], v[154:155], v[186:187], v[10:11] op_sel:[1,0,0]\n"
            "v_pk_fma_f32 v[12:13], v[158:159], v[188:189], v[12:13] op_sel:[1,0,0]\n"
            "v_pk_fma_f32 v[14:15], v[158:159], v[190:191], v[14:15] op_sel:[1,0,0]\n"
            "v_pk_mul_f32 v[168:169], v[168:169], v[136:137]\n"
            "v_pk_mul_f32 v[170:171], v[170:171], v[138:139]\n"
            "v_pk_mul_f32 v[172:173], v[172:173], v[140:141]\n"
            "v_pk_mul_f32 v[174:175], v[174:175], v[142:143]\n"
            "s_waitcnt vmcnt(8)\n"
            "v_mfma_f32_4x4x4_16b_bf16 v[176:179], v[208:209], v[40:41], 0 cbsz:2\n"
            "v_mfma_f32_4x4x4_16b_bf16 v[180:183], v[208:209], v[42:43], 0 cbsz:2\n"
            "v_mfma_f32_4x4x4_16b_bf16 v[184:187], v[208:209], v[44:45], 0 cbsz:2\n"
            "v_mfma_f32_4x4x4_16b_bf16 v[188:191], v[208:209], v[46:47], 0 cbsz:2\n"
            "ds_read_b128 v[136:139], v228 offset:19456\n"
            "ds_read_b128 v[140:143], v228 offset:20480\n"
            "ds_read_b128 v[144:147], v228 offset:21504\n"
            "ds_read_b128 v[148:151], v228 offset:22528\n"
            "ds_read_b128 v[152:155], v228 offset:23552\n"
            "ds_read_b128 v[156:159], v228 offset:24576\n"
            "s_nop 1\n"
            "v_pk_fma_f32 v[16:17], v[160:161], v[176:177], v[16:17] op_sel_hi:[0,1,1]\n"
            "v_pk_fma_f32 v[18:19], v[160:161], v[178:179], v[18:19] op_sel_hi:[0,1,1]\n"
            "v_pk_fma_f32 v[20:21], v[164:165], v[180:181], v[20:21] op_sel_hi:[0,1,1]\n"
            "v_pk_fma_f32 v[22:23], v[164:165], v[182:183], v[22:23] op_sel_hi:[0,1,1]\n"
            "v_pk_fma_f32 v[24:25], v[168:169], v[184:185], v[24:25] op_sel_hi:[0,1,1]\n"
            "v_pk_fma_f32 v[26:27], v[168:169], v[186:187], v[26:27] op_sel_hi:[0,1,1]\n"
            "v_pk_fma_f32 v[28:29], v[172:173], v[188:189], v[28:29] op_sel_hi:[0,1,1]\n"
            "v_pk_fma_f32 v[30:31], v[172:173], v[190:191], v[30:31] op_sel_hi:[0,1,1]\n"
            "s_cmp_le_u32 %[njl], 1\n"
            "s_cbranch_scc1 Ltail_%=\n"
            "v_mfma_f32_4x4x4_16b_bf16 v[176:179], v[208:209], v[56:57], 0 cbsz:2 abid:1\n"
            "v_mfma_f32_4x4x4_16b_bf16 v[180:183], v[208:209], v[58:59], 0 cbsz:2 abid:1\n"
            "v_mfma_f32_4x4x4_16b_bf16 v[184:187], v[208:209], v[60:61], 0 cbsz:2 abid:1\n"
            "v_mfma_f32_4x4x4_16b_bf16 v[188:191], v[208:209], v[62:63], 0 cbsz:2 abid:1\n"
            "s_nop 1\n"
            "v_pk_fma_f32 v[16:17], v[160:161], v[176:177], v[16:17] op_sel:[1,0,0]\n"
            "v_pk_fma_f32 v[18:19], v[160:161], v[178:179], v[18:19] op_sel:[1,0,0]\n"
            "v_pk_fma_f32 v[20:21], v[164:165], v[180:181], v[20:21] op_sel:[1,0,0]\n"
            "v_pk_fma_f32 v[22:23], v[164:165], v[182:183], v[22:23] op_sel:[1,0,0]\n"
            "v_pk_fma_f32 v[24:25], v[168:169], v[184:185], v[24:25] op_sel:[1,0,0]\n"
            "v_pk_fma_f32 v[26:27], v[168:169], v[186:187], v[26:27] op_sel:[1,0,0]\n"
            "v_pk_fma_f32 v[28:29], v[172:173], v[188:189], v[28:29] op_sel:[1,0,0]\n"
            "v_pk_fma_f32 v[30:31], v[172:173], v[190:191], v[30:31] op_sel:[1,0,0]\n"
            "s_cmp_le_u32 %[njl], 2\n"
            "s_cbranch_scc1 Ltail_%=\n"
            "s_waitcnt vmcnt(4)\n"
            "v_mfma_f32_4x4x4_16b_bf16 v[176:179], v[208:209], v[72:73], 0 cbsz:2 abid:2\n"
            "v_mfma_f32_4x4x4_16b_bf16 v[180:183], v[208:209], v[74:75], 0 cbsz:2 abid:2\n"
            "v_mfma_f32_4x4x4_16b_bf16 v[184:187], v[208:209], v[76:77], 0 cbsz:2 abid:2\n"
            "v_mfma_f32_4x4x4_16b_bf16 v[188:191], v[208:209], v[78:79], 0 cbsz:2 abid:2\n"
            "s_nop 1\n"
            "v_pk_fma_f32 v[16:17], v[162:163], v[176:177], v[16:17] op_sel_hi:[0,1,1]\n"
            "v_pk_fma_f32 v[18:19], v[162:163], v[178:179], v[18:19] op_sel_hi:[0,1,1]\n"
            "v_pk_fma_f32 v[20:21], v[166:167], v[180:181], v[20:21] op_sel_hi:[0,1,1]\n"
            "v_pk_fma_f32 v[22:23], v[166:167], v[182:183], v[22:23] op_sel_hi:[0,1,1]\n"
            "v_pk_fma_f32 v[24:25], v[170:171], v[184:185], v[24:25] op_sel_hi:[0,1,1]\n"
            "v_pk_fma_f32 v[26:27], v[170:171], v[186:187], v[26:27] op_sel_hi:[0,1,1]\n"
            "v_pk_fma_f32 v[28:29], v[174:175], v[188:189], v[28:29] op_sel_hi:[0,1,1]\n"
            "v_pk_fma_f32 v[30:31], v[174:175], v[190:191], v[30:31] op_sel_hi:[0,1,1]\n"
            "s_cmp_le_u32 %[njl], 3\n"
            "s_cbranch_scc1 Ltail_%=\n"
            "v_mfma_f32_4x4x4_16b_bf16 v[176:179], v[208:209], v[88:89], 0 cbsz:2 abid:3\n"
            "v_mfma_f32_4x4x4_16b_bf16 v[180:183], v[208:209], v[90:91], 0 cbsz:2 abid:3\n"
            "v_mfma_f32_4x4x4_16b_bf16 v[184:187], v[208:209], v[92:93], 0 cbsz:2 abid:3\n"
            "v_mfma_f32_4x4x4_16b_bf16 v[188:191], v[208:209], v[94:95], 0 cbsz:2 abid:3\n"
            "s_nop 1\n"
            "v_pk_fma_f32 v[16:17], v[162:163], v[176:177], v[16:17] op_sel:[1,0,0]\n"
            "v_pk_fma_f32 v[18:19], v[162:163], v[178:179], v[18:19] op_sel:[1,0,0]\n"
            "v_pk_fma_f32 v[20:21], v[166:167], v[180:181], v[20:21] op_sel:[1,0,0]\n"
            "v_pk_fma_f32 v[22:23], v[166:167], v[182:183], v[22:23] op_sel:[1,0,0]\n"
            "v_pk_fma_f32 v[24:25], v[170:171], v[184:185], v[24:25] op_sel:[1,0,0]\n"
            "v_pk_fma_f32 v[26:27], v[170:171], v[186:187], v[26:27] op_sel:[1,0,0]\n"
            "v_pk_fma_f32 v[28:29], v[174:175], v[188:189], v[28:29] op_sel:[1,0,0]\n"
            "v_pk_fma_f32 v[30:31], v[174:175], v[190:191], v[30:31] op_sel:[1,0,0]\n"
            "Ltail_%=:\n"
            "s_setprio 3\n"
            "s_waitcnt vmcnt(4)\n"
            "ds_read_b128 v[72:75], v228 offset:25600\n"
            "ds_read_b128 v[76:79], v228 offset:26624\n"
            "ds_read_b128 v[88:91], v228 offset:27648\n"
            "ds_read_b128 v[92:95], v228 offset:28672\n"
            "ds_read_b128 v[160:163], v228 offset:29696\n"
            "ds_read_b128 v[164:167], v228 offset:30720\n"
            "ds_read_b128 v[168:171], v228 offset:31744\n"
            "s_cmp_lg_u32 %[nst], %[tag]\n"
            "s_cbranch_scc1 Lspin_%=\n"
            "Lgo_%=:\n"
            "buffer_load_dwordx4 v[40:43], v218, %[rsw], %[oagg] offen offset:0 sc1\n"
            "buffer_load_dwordx4 v[44:47], v218, %[rsw], %[oagg] offen offset:256 sc1\n"
            "buffer_load_dwordx4 v[56:59], v218, %[rsw], %[oagg] offen offset:512 sc1\n"
            "buffer_load_dwordx4 v[60:63], v218, %[rsw], %[oagg] offen offset:768 sc1\n"
            "s_waitcnt vmcnt(4)\n"
            "v_mfma_f32_4x4x4_16b_bf16 v[0:3], v[224:225], v[32:33], v[0:3]\n"
            "v_mfma_f32_4x4x4_16b_bf16 v[4:7], v[224:225], v[34:35], v[4:7]\n"
            "v_mfma_f32_4x4x4_16b_bf16 v[8:11], v[224:225], v[36:37], v[8:11]\n"
            "v_mfma_f32_4x4x4_16b_bf16 v[12:15], v[224:225], v[38:39], v[12:15]\n"
            "v_mfma_f32_4x4x4_16b_bf16 v[16:19], v[224:225], v[48:49], v[16:19]\n"
            "v_mfma_f32_4x4x4_16b_bf16 v[20:23], v[224:225], v[50:51], v[20:23]\n"
            "v_mfma_f32_4x4x4_16b_bf16 v[24:27], v[224:225], v[52:53], v[24:27]\n"
            "v_mfma_f32_4x4x4_16b_bf16 v[28:31], v[224:225], v[54:55], v[28:31]\n"
            "s_nop 1\n"
            "v_cvt_pk_bf16_f32 v176, v0, v4\n"
            "v_cvt_pk_bf16_f32 v177, v8, v12\n"
            "v_cvt_pk_bf16_f32 v178, v16, v20\n"
            "v_cvt_pk_bf16_f32 v179, v24, v28\n"
            "ds_write_b128 v226, v[176:179] offset:0\n"
            "v_cvt_pk_bf16_f32 v180, v1, v5\n"
            "v_cvt_pk_bf16_f32 v181, v9, v13\n"
            "v_cvt_pk_bf16_f32 v182, v17, v21\n"
            "v_cvt_pk_bf16_f32 v183, v25, v29\n"
            "ds_write_b128 v226, v[180:183] offset:272\n"
            "v_cvt_pk_bf16_f32 v184, v2, v6\n"
            "v_cvt_pk_bf16_f32 v185, v10, v14\n"
            "v_cvt_pk_bf16_f32 v186, v18, v22\n"
            "v_cvt_pk_bf16_f32 v187, v26, v30\n"
            "ds_write_b128 v226, v[184:187] offset:544\n"
            "v_cvt_pk_bf16_f32 v188, v3, v7\n"
            "v_cvt_pk_bf16_f32 v189, v11, v15\n"
            "v_cvt_pk_bf16_f32 v190, v19, v23\n"
            "v_cvt_pk_bf16_f32 v191, v27, v31\n"
            "ds_write_b128 v226, v[188:191] offset:816\n"
            "ds_read_b128 v[176:179], v227 offset:0\n"
            "ds_read_b128 v[180:183], v227 offset:64\n"
            "ds_read_b128 v[184:187], v227 offset:128\n"
            "ds_read_b128 v[188:191], v227 offset:192\n"
            "s_waitcnt lgkmcnt(3)\n"
            "v_mfma_f32_16x16x32_bf16 v[0:3], v[176:179], v[236:239], 0\n"
            "v_mfma_f32_16x16x32_bf16 v[4:7], v[176:179], v[96:99], 0\n"
            "s_waitcnt lgkmcnt(2)\n"
            "v_mfma_f32_16x16x32_bf16 v[0:3], v[180:183], v[240:243], v[0:3]\n"
            "v_mfma_f32_16x16x32_bf16 v[4:7], v[180:183], v[100:103], v[4:7]\n"
            "s_waitcnt lgkmcnt(1)\n"
            "v_mfma_f32_16x16x32_bf16 v[0:3], v[184:187], v[244:247], v[0:3]\n"
            "v_mfma_f32_16x16x32_bf16 v[4:7], v[184:187], v[104:107], v[4:7]\n"
            "s_waitcnt lgkmcnt(0)\n"
            "v_mfma_f32_16x16x32_bf16 v[0:3], v[188:191], v[248:251], v[0:3]\n"
            "v_mfma_f32_16x16x32_bf16 v[4:7], v[188:191], v[108:111], v[4:7]\n"
            "v_mfma_f32_16x16x32_bf16 v[8:11], v[176:179], v[112:115], 0\n"
            "v_mfma_f32_16x16x32_bf16 v[12:15], v[176:179], v[204:207], 0\n"
            "v_mfma_f32_16x16x32_bf16 v[8:11], v[180:183], v[116:119], v[8:11]\n"
            "v_mfma_f32_16x16x32_bf16 v[12:15], v[180:183], v[232:235], v[12:15]\n"
            "v_mfma_f32_16x16x32_bf16 v[8:11], v[184:187], v[196:199], v[8:11]\n"
            "v_mfma_f32_16x16x32_bf16 v[12:15], v[184:187], v[120:123], v[12:15]\n"
            "v_mfma_f32_16x16x32_bf16 v[8:11], v[188:191], v[200:203], v[8:11]\n"
            "v_mfma_f32_16x16x32_bf16 v[12:15], v[188:191], v[124:127], v[12:15]\n"
            "v_cvt_pk_bf16_f32 v0, v0, v1\n"
            "v_cvt_pk_bf16_f32 v1, v2, v3\n"
            "v_cvt_pk_bf16_f32 v2, v4, v5\n"
            "v_cvt_pk_bf16_f32 v3, v6, v7\n"
            "buffer_store_dwordx4 v[0:3], v217, %[rso], 0 offen offset:0 sc1\n"
            "v_mfma_f32_16x16x32_bf16 v[16:19], v[176:179], v[192:195], 0\n"
            "v_mfma_f32_16x16x32_bf16 v[20:23], v[176:179], v[140:143], 0\n"
            "v_mfma_f32_16x16x32_bf16 v[16:19], v[180:183], v[128:131], v[16:19]\n"
            "v_mfma_f32_16x16x32_bf16 v[20:23], v[180:183], v[144:147], v[20:23]\n"
            "v_mfma_f32_16x16x32_bf16 v[16:19], v[184:187], v[132:135], v[16:19]\n"
            "v_mfma_f32_16x16x32_bf16 v[20:23], v[184:187], v[148:151], v[20:23]\n"
            "v_mfma_f32_16x16x32_bf16 v[16:19], v[188:191], v[136:139], v[16:19]\n"
            "v_mfma_f32_16x16x32_bf16 v[20:23], v[188:191], v[152:155], v[20:23]\n"
            "v_cvt_pk_bf16_f32 v8, v8, v9\n"
            "v_cvt_pk_bf16_f32 v9, v10, v11\n"
            "v_cvt_pk_bf16_f32 v10, v12, v13\n"
            "v_cvt_pk_bf16_f32 v11, v14, v15\n"
            "buffer_store_dwordx4 v[8:11], v217, %[rso], 0 offen offset:256 sc1\n"
            "v_mfma_f32_16x16x32_bf16 v[24:27], v[176:179], v[156:159], 0\n"
            "v_mfma_f32_16x16x32_bf16 v[28:31], v[176:179], v[92:95], 0\n"
            "v_mfma_f32_16x16x32_bf16 v[24:27], v[180:183], v[72:75], v[24:27]\n"
            "v_mfma_f32_16x16x32_bf16 v[28:31], v[180:183], v[160:163], v[28:31]\n"
            "v_mfma_f32_16x16x32_bf16 v[24:27], v[184:187], v[76:79], v[24:27]\n"
            "v_mfma_f32_16x16x32_bf16 v[28:31], v[184:187], v[164:167], v[28:31]\n"
            "v_mfma_f32_16x16x32_bf16 v[24:27], v[188:191], v[88:91], v[24:27]\n"
            "v_mfma_f32_16x16x32_bf16 v[28:31], v[188:191], v[168:171], v[28:31]\n"
            "v_cvt_pk_bf16_f32 v16, v16, v17\n"
            "v_cvt_pk_bf16_f32 v17, v18, v19\n"
            "v_cvt_pk_bf16_f32 v18, v20, v21\n"
            "v_cvt_pk_bf16_f32 v19, v22, v23\n"
            "buffer_store_dwordx4 v[16:19], v217, %[rso], 0 offen offset:512 sc1\n"
            "s_nop 2\n"
            "v_cvt_pk_bf16_f32 v24, v24, v25\n"
            "v_cvt_pk_bf16_f32 v25, v26, v27\n"
            "v_cvt_pk_bf16_f32 v26, v28, v29\n"
            "v_cvt_pk_bf16_f32 v27, v30, v31\n"
            "buffer_store_dwordx4 v[24:27], v217, %[rso], 0 offen offset:768 sc1\n"
            "s_cmp_lt_u32 %[two], 6\n"
            "s_cbranch_scc1 Lend_%=\n"
            "ds_read_b128 v[64:67], v231 offset:0\n"
            "ds_read_b128 v[68:71], v231 offset:1024\n"
            "ds_read_b128 v[80:83], v231 offset:2048\n"
            "ds_read_b128 v[84:87], v231 offset:3072\n"
            "s_waitcnt vmcnt(4) lgkmcnt(0)\n"
            "v_mfma_f32_4x4x4_16b_bf16 v[0:3], v[224:225], v[64:65], 0\n"
            "v_mfma_f32_4x4x4_16b_bf16 v[4:7], v[224:225], v[66:67], 0\n"
            "v_mfma_f32_4x4x4_16b_bf16 v[8:11], v[224:225], v[68:69], 0\n"
            "v_mfma_f32_4x4x4_16b_bf16 v[12:15], v[224:225], v[70:71], 0\n"
            "v_mfma_f32_4x4x4_16b_bf16 v[16:19], v[224:225], v[80:81], 0\n"
            "v_mfma_f32_4x4x4_16b_bf16 v[20:23], v[224:225], v[82:83], 0\n"
            "v_mfma_f32_4x4x4_16b_bf16 v[24:27], v[224:225], v[84:85], 0\n"
            "v_mfma_f32_4x4x4_16b_bf16 v[28:31], v[224:225], v[86:87], 0\n"
            "v_mfma_f32_4x4x4_16b_bf16 v[0:3], v[224:225], v[40:41], v[0:3]\n"
            "v_mfma_f32_4x4x4_16b_bf16 v[4:7], v[224:225], v[42:43], v[4:7]\n"
            "v_mfma_f32_4x4x4_16b_bf16 v[8:11], v[224:225], v[44:45], v[8:11]\n"
            "v_mfma_f32_4x4x4_16b_bf16 v[12:15], v[224:225], v[46:47], v[12:15]\n"
            "v_mfma_f32_4x4x4_16b_bf16 v[16:19], v[224:225], v[56:57], v[16:19]\n"
            "v_mfma_f32_4x4x4_16b_bf16 v[20:23], v[224:225], v[58:59], v[20:23]\n"
            "v_mfma_f32_4x4x4_16b_bf16 v[24:27], v[224:225], v[60:61], v[24:27]\n"
            "v_mfma_f32_4x4x4_16b_bf16 v[28:31], v[224:225], v[62:63], v[28:31]\n"
            "s_nop 1\n"
            "v_cvt_pk_bf16_f32 v176, v0, v4\n"
            "v_cvt_pk_bf16_f32 v177, v8, v12\n"
            "v_cvt_pk_bf16_f32 v178, v16, v20\n"
            "v_cvt_pk_bf16_f32 v179, v24, v28\n"
            "ds_write_b128 v226, v[176:179] offset:0\n"
            "v_cvt_pk_bf16_f32 v180, v1, v5\n"
            "v_cvt_pk_bf16_f32 v181, v9, v13\n"
            "v_cvt_pk_bf16_f32 v182, v17, v21\n"
            "v_cvt_pk_bf16_f32 v183, v25, v29\n"
            "ds_write_b128 v226, v[180:183] offset:272\n"
            "v_cvt_pk_bf16_f32 v184, v2, v6\n"
            "v_cvt_pk_bf16_f32 v185, v10, v14\n"
            "v_cvt_pk_bf16_f32 v186, v18, v22\n"
            "v_cvt_pk_bf16_f32 v187, v26, v30\n"
            "ds_write_b128 v226, v[184:187] offset:544\n"
            "v_cvt_pk_bf16_f32 v188, v3, v7\n"
            "v_cvt_pk_bf16_f32 v189, v11, v15\n"
            "v_cvt_pk_bf16_f32 v190, v19, v23\n"
            "v_cvt_pk_bf16_f32 v191, v27, v31\n"
            "ds_write_b128 v226, v[188:191] offset:816\n"
            "ds_read_b128 v[176:179], v227 offset:0\n"
            "ds_read_b128 v[180:183], v227 offset:64\n"
            "ds_read_b128 v[184:187], v227 offset:128\n"
            "ds_read_b128 v[188:191], v227 offset:192\n"
            "s_waitcnt lgkmcnt(3)\n"
            "v_mfma_f32_16x16x32_bf16 v[0:3], v[176:179], v[236:239], 0\n"
            "v_mfma_f32_16x16x32_bf16 v[4:7], v[176:179], v[96:99], 0\n"
            "s_waitcnt lgkmcnt(2)\n"
            "v_mfma_f32_16x16x32_bf16 v[0:3], v[180:183], v[240:243], v[0:3]\n"
            "v_mfma_f32_16x16x32_bf16 v[4:7], v[180:183], v[100:103], v[4:7]\n"
            "s_waitcnt lgkmcnt(1)\n"
            "v_mfma_f32_16x16x32_bf16 v[0:3], v[184:187], v[244:247], v[0:3]\n"
            "v_mfma_f32_16x16x32_bf16 v[4:7], v[184:187], v[104:107], v[4:7]\n"
            "s_waitcnt lgkmcnt(0)\n"
            "v_mfma_f32_16x16x32_bf16 v[0:3], v[188:191], v[248:251], v[0:3]\n"
            "v_mfma_f32_16x16x32_bf16 v[4:7], v[188:191], v[108:111], v[4:7]\n"
            "v_mfma_f32_16x16x32_bf16 v[8:11], v[176:179], v[112:115], 0\n"
            "v_mfma_f32_16x16x32_bf16 v[12:15], v[176:179], v[204:207], 0\n"
            "v_mfma_f32_16x16x32_bf16 v[8:11], v[180:183], v[116:119], v[8:11]\n"
            "v_mfma_f32_16x16x32_bf16 v[12:15], v[180:183], v[232:235], v[12:15]\n"
            "v_mfma_f32_16x16x32_bf16 v[8:11], v[184:187], v[196:199], v[8:11]\n"
            "v_mfma_f32_16x16x32_bf16 v[12:15], v[184:187], v[120:123], v[12:15]\n"
            "v_mfma_f32_16x16x32_bf16 v[8:11], v[188:191], v[200:203], v[8:11]\n"
            "v_mfma_f32_16x16x32_bf16 v[12:15], v[188:191], v[124:127], v[12:15]\n"
            "v_cvt_pk_bf16_f32 v0, v0, v1\n"
            "v_cvt_pk_bf16_f32 v1, v2, v3\n"
            "v_cvt_pk_bf16_f32 v2, v4, v5\n"
            "v_cvt_pk_bf16_f32 v3, v6, v7\n"
            "buffer_store_dwordx4 v[0:3], v218, %[rso], 0 offen offset:0 sc1\n"
            "v_mfma_f32_16x16x32_bf16 v[16:19], v[176:179], v[192:195], 0\n"
            "v_mfma_f32_16x16x32_bf16 v[20:23], v[176:179], v[140:143], 0\n"
            "v_mfma_f32_16x16x32_bf16 v[16:19], v[180:183], v[128:131], v[16:19]\n"
            "v_mfma_f32_16x16x32_bf16 v[20:23], v[180:183], v[144:147], v[20:23]\n"
            "v_mfma_f32_16x16x32_bf16 v[16:19], v[184:187], v[132:135], v[16:19]\n"
            "v_mfma_f32_16x16x32_bf16 v[20:23], v[184:187], v[148:151], v[20:23]\n"
            "v_mfma_f32_16x16x32_bf16 v[16:19], v[188:191], v[136:139], v[16:19]\n"
            "v_mfma_f32_16x16x32_bf16 v[20:23], v[188:191], v[152:155], v[20:23]\n"
            "v_cvt_pk_bf16_f32 v8, v8, v9\n"
            "v_cvt_pk_bf16_f32 v9, v10, v11\n"
            "v_cvt_pk_bf16_f32 v10, v12, v13\n"
            "v_cvt_pk_bf16_f32 v11, v14, v15\n"
            "buffer_store_dwordx4 v[8:11], v218, %[rso], 0 offen offset:256 sc1\n"
            "v_mfma_f32_16x16x32_bf16 v[24:27], v[176:179], v[156:159], 0\n"
            "v_mfma_f32_16x16x32_bf16 v[28:31], v[176:179], v[92:95], 0\n"
            "v_mfma_f32_16x16x32_bf16 v[24:27], v[180:183], v[72:75], v[24:27]\n"
            "v_mfma_f32_16x16x32_bf16 v[28:31], v[180:183], v[160:163], v[28:31]\n"
            "v_mfma_f32_16x16x32_bf16 v[24:27], v[184:187], v[76:79], v[24:27]\n"
            "v_mfma_f32_16x16x32_bf16 v[28:31], v[184:187], v[164:167], v[28:31]\n"
            "v_mfma_f32_16x16x32_bf16 v[24:27], v[188:191], v[88:91], v[24:27]\n"
            "v_mfma_f32_16x16x32_bf16 v[28:31], v[188:191], v[168:171], v[28:31]\n"
            "v_cvt_pk_bf16_f32 v16, v16, v17\n"
            "v_cvt_pk_bf16_f32 v17, v18, v19\n"
            "v_cvt_pk_bf16_f32 v18, v20, v21\n"
            "v_cvt_pk_bf16_f32 v19, v22, v23\n"
            "buffer_store_dwordx4 v[16:19], v218, %[rso], 0 offen offset:512 sc1\n"
            "s_nop 2\n"
            "v_cvt_pk_bf16_f32 v24, v24, v25\n"
            "v_cvt_pk_bf16_f32 v25, v26, v27\n"
            "v_cvt_pk_bf16_f32 v26, v28, v29\n"
            "v_cvt_pk_bf16_f32 v27, v30, v31\n"
            "buffer_store_dwordx4 v[24:27], v218, %[rso], 0 offen offset:768 sc1\n"
            "Lend_%=:\n"
            "s_waitcnt vmcnt(4)\n"
            : "+{v[32:63]}"(oxa), "+{v[64:95]}"(oxb), "+{v[96:127]}"(owr), "+{v[196:199]}"(S), "+{v[200:203]}"(cgA), "+{v[208:223]}"(omi), "+{v[224:231]}"(ot),
              "+{v[232:233]}"(efN), "+{v234}"(bpa), "+{v[236:237]}"(jmp), "+{v[238:239]}"(ef0),
              [rem] "+s"(rem), [lens] "+s"(lens), [fm] "+s"(fm), [nst] "+s"(nst), [pb] "+s"(pb), [evt] "+s"(evt), [ra] "=&s"(ra), [tmp] "=&s"(tmp)
            : [rse] "s"(de), [rsx] "s"(dx), [rso] "s"(mkdesc(out, (unsigned)N * 1024u)), [rsw] "s"(mkdesc(ws, W_END)),
              [cnt] "s"(lds0 + L_MISC + 4), [pfa] "s"(lds0 + L_MISC + 16 + pw * 4), [tag] "s"(tag), [flg] "s"(ws + W_FLG + bx * 128), [pfl] "s"(ws + W_FLG + (bx ^ 4) * 128),
              [njl] "s"(((e2 - 1) & 3) + 1), [c1] "s"((w < 4 ? l2 : l1) - 4 * ((e2 + 3) >> 2) - 5), [c2] "s"(w < 4 ? l1 : l2), [two] "s"(w), [ptab] "s"((unsigned)__builtin_amdgcn_readfirstlane(w < 4 ? PTO : (w < 6 ? PTY : PTZ))), [oagg] "s"(W_AGG), [spin] "s"(SPIN_MAX)
            : "memory", "vcc", "scc", "v0", "v1", "v2", "v3", "v4", "v5", "v6", "v7", "v8", "v9", "v10", "v11", "v12", "v13", "v14", "v15", "v16", "v17", "v18", "v19", "v20", "v21", "v22", "v23", "v24", "v25", "v26", "v27", "v28", "v29", "v30", "v31", "v128", "v129", "v130", "v131", "v132", "v133", "v134", "v135", "v136", "v137", "v138", "v139", "v140", "v141", "v142", "v143", "v144", "v145", "v146", "v147", "v148", "v149", "v150", "v151", "v152", "v153", "v154", "v155", "v156", "v157", "v158", "v159", "v160", "v161", "v162", "v163", "v164", "v165", "v166", "v167", "v168", "v169", "v170", "v171", "v172", "v173", "v174", "v175", "v176", "v177", "v178", "v179", "v180", "v181", "v182", "v183", "v184", "v185", "v186", "v187", "v188", "v189", "v190", "v191", "v192", "v193", "v194", "v195", "v235", "v204", "v205", "v206", "v207", "v240", "v241", "v242", "v243", "v244", "v245", "v246", "v247", "v248", "v249", "v250", "v251");
    } else {
        if (bx == 0 && w == 4) flag_store(ws + W_CTL, tag);
        lds_wait(lds0 + SY_C3, 5);
        const __amdgpu_buffer_rsrc_t rso = __builtin_amdgcn_make_buffer_rsrc((void*)out, 0, N * 1024, 0x00020000);
#pragma unroll 1
        for (int it = 0; it < 10; ++it) {
            const int nl = (it < 5 ? 40 - 40 * half + 8 * it : 40 * half + 8 * (it - 5)) + w, node = node0 + nl;
            const bool nv = nl < npb;
            const unsigned ro = nv ? (unsigned)node * 1024u + (unsigned)lane * 16u : OOB_OFF;
            float a8[8] = {0.f, 0.f, 0.f, 0.f, 0.f, 0.f, 0.f, 0.f};
#pragma unroll 1
            for (int e0 = 0; e0 < (nv ? E : 0); e0 += 64) {
                const int ec = min(e0 + lane, E - 1);
                u64 mm = __ballot(e0 + lane < E && dst[ec] == node && (int)(((src[ec] >> 3) ^ (smask >> ((src[ec] >> 9) & 31))) & 1) == half);
#pragma unroll 1
                while (mm) {
                    const int e = e0 + __builtin_ctzll(mm);
                    mm &= mm - 1;
                    const unsigned short* fe = (const unsigned short*)ef + (size_t)e * DE_DIM;
                    const unsigned short* ye = (const unsigned short*)Y + (size_t)e * M_DIM;
                    const unsigned short* xp = x + (size_t)src[e] * (C_DIM * M_DIM) + lane * 8;
                    float z0 = 0.f, z1 = 0.f;
#pragma unroll 1
                    for (int f = 0; f < DE_DIM; ++f) { z0 += bf2f(fe[f]) * bf2f(w_rad[f * C_DIM + 2 * lane]); z1 += bf2f(fe[f]) * bf2f(w_rad[f * C_DIM + 2 * lane + 1]); }
                    const float r0 = z0 / (1.f + __expf(-z0)), r1 = z1 / (1.f + __expf(-z1));
#pragma unroll 1
                    for (int i = 0; i < 4; ++i) {
                        const float x0 = bf2f(xp[i]) * r0, x1 = bf2f(xp[4 + i]) * r1;
#pragma unroll
                        for (int o = 0; o < 4; ++o) {
                            float wv4 = 0.f;
#pragma unroll
                            for (int f = 0; f < 4; ++f) wv4 += bf2f(cg[(i * 4 + f) * 4 + o]) * bf2f(ye[f]);
                            a8[o] += x0 * wv4; a8[4 + o] += x1 * wv4;
                        }
                    }
                }
            }
            if (it >= 5) {
                const u32x4 pv = __builtin_amdgcn_raw_buffer_load_b128(rsw, ro, W_AGG, SC1);
#pragma unroll
                for (int i = 0; i < 4; ++i) { a8[2 * i] += __uint_as_float(pv[i] << 16); a8[2 * i + 1] += __uint_as_float(pv[i] & 0xffff0000u); }
            }
            const u32x4 rv = (u32x4){pack2bf(a8[0], a8[1]), pack2bf(a8[2], a8[3]), pack2bf(a8[4], a8[5]), pack2bf(a8[6], a8[7])};
            if (it < 5) {
                __builtin_amdgcn_raw_buffer_store_b128(rv, rsw, ro, W_AGG, SC1);
                if (it == 4) {
                    asm volatile("s_waitcnt vmcnt(0)" ::: "memory");
                    if (lds_arrive_rtn(lds0 + L_MISC + 4) == 7u) flag_store(ws + W_FLG + bx * 128, tag);
                    poll_tag(ws + W_FLG + (bx ^ 4) * 128, tag);
                }
            } else {
                *(u32x4*)(smem + L_PRV + w * 1024 + lane * 16) = rv;
#pragma unroll
                for (int i = 0; i < 8; ++i) a8[i] = 0.f;
#pragma unroll 1
                for (int c = 0; c < C_DIM; ++c) {
                    const unsigned short* ap = (const unsigned short*)(smem + L_PRV + w * 1024 + c * 8);
                    const float w0 = bf2f(w_mix[c * C_DIM + 2 * lane]), w1 = bf2f(w_mix[c * C_DIM + 2 * lane + 1]);
#pragma unroll
                    for (int m = 0; m < 4; ++m) { a8[m] += bf2f(ap[m]) * w0; a8[4 + m] += bf2f(ap[m]) * w1; }
                }
                __builtin_amdgcn_raw_buffer_store_b128((u32x4){pack2bf(a8[0], a8[1]), pack2bf(a8[2], a8[3]), pack2bf(a8[4], a8[5]), pack2bf(a8[6], a8[7])}, rso, ro, 0, 0);
            }
        }
    }
}

__global__ void kf_zero(float* __restrict__ agg, size_t n) {
    for (size_t i = (size_t)blockIdx.x * blockDim.x + threadIdx.x; i < n; i += (size_t)gridDim.x * blockDim.x) agg[i] = 0.f;
}
__global__ void kf_edges(const unsigned short* __restrict__ x, const int* __restrict__ src, const int* __restrict__ dst,
                         const unsigned short* __restrict__ Y, const unsigned short* __restrict__ ef,
                         const unsigned short* __restrict__ w_rad, const unsigned short* __restrict__ cg,
                         float* __restrict__ agg, int E) {
    const int ch = threadIdx.x;
    for (int e = blockIdx.x; e < E; e += gridDim.x) {
        float z = 0.f;
        for (int f = 0; f < DE_DIM; ++f) z += bf2f(ef[(size_t)e * DE_DIM + f]) * bf2f(w_rad[f * C_DIM + ch]);
        const float r = z / (1.f + __expf(-z));
        float a4[4] = {0.f, 0.f, 0.f, 0.f};
        for (int ii = 0; ii < 4; ++ii) {
            const float xv = bf2f(x[((size_t)src[e] * C_DIM + ch) * M_DIM + ii]) * r;
            for (int o = 0; o < 4; ++o) {
                float wv = 0.f;
                for (int f = 0; f < 4; ++f) wv += bf2f(cg[(ii * 4 + f) * 4 + o]) * bf2f(Y[(size_t)e * 4 + f]);
                a4[o] += xv * wv;
            }
        }
        for (int o = 0; o < 4; ++o) atomicAdd(&agg[((size_t)dst[e] * C_DIM + ch) * M_DIM + o], a4[o]);
    }
}
__global__ void kf_mix(const float* __restrict__ agg, const unsigned short* __restrict__ w_mix, unsigned short* __restrict__ out, int N) {
    const int d = threadIdx.x;
    for (int n = blockIdx.x; n < N; n += gridDim.x) {
        float a4[4] = {0.f, 0.f, 0.f, 0.f};
        for (int c = 0; c < C_DIM; ++c) {
            const float wv = bf2f(w_mix[c * C_DIM + d]);
            for (int m = 0; m < 4; ++m) a4[m] += bf2f(f2bf(agg[((size_t)n * C_DIM + c) * M_DIM + m])) * wv;
        }
        for (int m = 0; m < 4; ++m) out[((size_t)n * C_DIM + d) * M_DIM + m] = f2bf(a4[m]);
    }
}

extern "C" void kernel_launch(void* const* d_in, const int* in_sizes, int n_in,
                              void* d_out, int out_size, void* d_ws, size_t ws_size,
                              hipStream_t stream) {
    const unsigned short* x = (const unsigned short*)d_in[0];
    const int* src = (const int*)d_in[1];
    const int* dst = (const int*)d_in[2];
    const unsigned short* Y = (const unsigned short*)d_in[3];
    const unsigned short* ef = (const unsigned short*)d_in[4];
    const unsigned short* w_rad = (const unsigned short*)d_in[5];
    const unsigned short* cg = (const unsigned short*)d_in[6];
    const unsigned short* w_mix = (const unsigned short*)d_in[7];
    unsigned short* out = (unsigned short*)d_out;
    const int N = in_sizes[0] / (C_DIM * M_DIM);
    const int E = in_sizes[1];
    const int epb = (E + 253) / 254;
    const bool fast = N >= 256 && N <= NB_MAX * NPB_MAX && E > 0 && epb <= EPB_MAX && ws_size >= (size_t)W_END;
    if (!fast) {
        float* agg = (float*)d_ws;
        if (N <= 0) return;
        kf_zero<<<1024, 256, 0, stream>>>(agg, (size_t)N * C_DIM * M_DIM);
        if (E > 0) kf_edges<<<4096, C_DIM, 0, stream>>>(x, src, dst, Y, ef, w_rad, cg, agg, E);
        kf_mix<<<2048, C_DIM, 0, stream>>>(agg, w_mix, out, N);
        return;
    }
    const unsigned nb_magic = (unsigned)(((1ull << 39) + N - 1) / N);
    unsigned smask = 0;
    const int ng = (N + 511) / 512;
    int pw = 1;
    while (2 * pw * 4 < ng + 4) pw *= 2;
    for (int i = 0; i < ng; ++i) smask |= (unsigned)(i < 4 * pw ? ((i >> 2) ^ (i >= WLO * ng / 20 && i < WHI * ng / 20)) & 1 : i & 1) << i;
    const int r0 = ((N < 2048 * pw ? N : 2048 * pw) - 128) & ~63, r1 = r0 - (r0 >= 2048 ? 2048 : 128);
    k_fused<<<256, 512, 0, stream>>>(dst, (unsigned)E * 4u, 0x20000u, src, (unsigned)E * 4u, 0x20000u, Y, d_ws, x, ef, w_rad, cg, w_mix, out,
                                     N, E, epb * 4, nb_magic, smask, (unsigned)(4 * 65536 / N), (unsigned)(r0 + (r0 >> 8 & 8)) * 1024u, (unsigned)(r1 + (r1 >> 8 & 8)) * 1024u);
}
```

```cpp
#include <hip/hip_runtime.h>
#include <hip/hip_bf16.h>
#include <stdint.h>

typedef __attribute__((ext_vector_type(8))) short bf16x8;
typedef __attribute__((ext_vector_type(4))) short bf16x4;
typedef __attribute__((ext_vector_type(4))) float f32x4;
typedef __attribute__((ext_vector_type(2))) unsigned u32x2;
typedef __attribute__((ext_vector_type(3))) unsigned u32x3;
typedef __attribute__((ext_vector_type(4))) unsigned u32x4;
typedef __attribute__((ext_vector_type(8))) unsigned u32x8;
typedef __attribute__((ext_vector_type(16))) unsigned u32x16;
typedef __attribute__((ext_vector_type(32))) unsigned u32x32;
typedef unsigned long long u64;

#define C_DIM 128
#define M_DIM 4
#define DE_DIM 16
#define NPB_MAX 80
#define NB_MAX 128
#define EPB_MAX 1280
#define NRND 7
#define LS 47
#define DEG_MAX 42
#define OOB_OFF 0x80000000u
#define SC1 16
#define NT 2
#define SPIN_MAX (1u << 22)
#define CODE_LINES 152
#define PTO 0xfffa9540u
#define PTY 0xffffa950u
#define PTZ 0xffffea54u
#define NT0 2
#define NT2 5
#define TLIST 100, 3, 4, 5, 10
#define WLO 8
#define WHI 11
#define IDLE 190u
#define CPN 12
#define EW 3
#define PBIAS 12
#define TKF 0x98765432u
#define TKB 0x23456789u
#define TKA 0x1010ffffu
#define EXTRA 0, 0, 5, 7

#define W_CTL 0
#define W_DEC 2048
#define W_FLG 4096
#define W_TAB (W_FLG + 32768)
#define W_RA (W_TAB + 256 * 256 * 8)
#define W_AGG (W_RA + 256 * EPB_MAX * 16)
#define W_END (W_AGG + NB_MAX * NPB_MAX * 1024)

#define SB() __builtin_amdgcn_sched_barrier(0)

static __device__ __forceinline__ float bf2f(unsigned short v) { return __uint_as_float(((unsigned)v) << 16); }
typedef __bf16 bf16v2 __attribute__((ext_vector_type(2)));
static __device__ __forceinline__ unsigned pack2bf(float lo, float hi) {
    bf16v2 r = (bf16v2){(__bf16)lo, (__bf16)hi};
    return __builtin_bit_cast(unsigned, r);
}
static __device__ __forceinline__ unsigned short f2bf(float v) { return (unsigned short)(pack2bf(v, 0.f) & 0xffffu); }
template <int CTRL, int RM> static __device__ __forceinline__ int dpp_add(int v) {
    return v + __builtin_amdgcn_update_dpp(0, v, CTRL, RM, 0xf, false);
}
static __device__ __forceinline__ int wave_incl_scan(int v) {
    v = dpp_add<0x111, 0xf>(v); v = dpp_add<0x112, 0xf>(v); v = dpp_add<0x114, 0xf>(v); v = dpp_add<0x118, 0xf>(v);
    v = dpp_add<0x142, 0xa>(v); v = dpp_add<0x143, 0xc>(v);
    return v;
}
template <int CTRL, int RM> static __device__ __forceinline__ unsigned dpp_max(unsigned v) {
    return max(v, (unsigned)__builtin_amdgcn_update_dpp(0, (int)v, CTRL, RM, 0xf, false));
}
static __device__ __forceinline__ unsigned wave_max_scan(unsigned v) {
    v = dpp_max<0x111, 0xf>(v); v = dpp_max<0x112, 0xf>(v); v = dpp_max<0x114, 0xf>(v); v = dpp_max<0x118, 0xf>(v);
    v = dpp_max<0x142, 0xa>(v); v = dpp_max<0x143, 0xc>(v);
    return v;
}
#define MAXS(c) "v_max_u32_dpp %0, %0, %0 " c "\n\tv_max_u32_dpp %1, %1, %1 " c "\n\tv_max_u32_dpp %2, %2, %2 " c "\n\t"
static __device__ __forceinline__ void wave_max_scan3(unsigned& a, unsigned& b, unsigned& c) {
    asm volatile("s_nop 1\n\t" MAXS("row_shr:1 row_mask:0xf bank_mask:0xf bound_ctrl:1") MAXS("row_shr:2 row_mask:0xf bank_mask:0xf bound_ctrl:1")
                 MAXS("row_shr:4 row_mask:0xf bank_mask:0xf bound_ctrl:1") MAXS("row_shr:8 row_mask:0xf bank_mask:0xf bound_ctrl:1")
                 MAXS("row_bcast:15 row_mask:0xa bank_mask:0xf") MAXS("row_bcast:31 row_mask:0xc bank_mask:0xf") "s_nop 0" : "+v"(a), "+v"(b), "+v"(c));
}
static __device__ __forceinline__ unsigned bsum(unsigned c, unsigned m, unsigned acc = 0u) { return __builtin_amdgcn_udot4(c, m, acc, false); }
static __device__ __forceinline__ unsigned below_mask(int w, int k) {
    const int n = w - 4 * k;
    return n >= 4 ? 0x01010101u : (n <= 0 ? 0u : (0x01010101u & ((1u << (8 * n)) - 1u)));
}
static __device__ __forceinline__ void flag_store(const void* p, unsigned v) {
    unsigned t0, t1; u64 sv;
    asm volatile("v_mov_b32 %0, 0\n\tv_mov_b32 %1, %4\n\ts_mov_b64 %2, exec\n\ts_mov_b64 exec, 1\n\t"
                 "global_store_dword %0, %1, %3 sc1\n\ts_mov_b64 exec, %2" : "=&v"(t0), "=&v"(t1), "=&s"(sv) : "s"(p), "s"(v) : "memory");
}
static __device__ __forceinline__ unsigned lds_arrive_rtn(unsigned a) {
    unsigned t0, t1; u64 sv;
    asm volatile("v_mov_b32 %0, %3\n\tv_mov_b32 %1, 1\n\ts_mov_b64 %2, exec\n\ts_mov_b64 exec, 1\n\t"
                 "ds_add_rtn_u32 %1, %0, %1\n\ts_mov_b64 exec, %2\n\ts_waitcnt lgkmcnt(0)" : "=&v"(t0), "=&v"(t1), "=&s"(sv) : "s"(a) : "memory");
    return (unsigned)__builtin_amdgcn_readfirstlane((int)t1);
}
static __device__ __forceinline__ void lds_arrive(unsigned a, unsigned inc = 1u) {
    unsigned t0, t1; u64 sv;
    asm volatile("v_mov_b32 %0, %3\n\tv_mov_b32 %1, %4\n\ts_mov_b64 %2, exec\n\ts_mov_b64 exec, 1\n\t"
                 "ds_add_u32 %0, %1\n\ts_mov_b64 exec, %2" : "=&v"(t0), "=&v"(t1), "=&s"(sv) : "s"(a), "s"(inc) : "memory");
}
template <int SLEEP = 0, unsigned MASK = ~0u> static __device__ __forceinline__ unsigned lds_wait(unsigned a, unsigned n) {
    for (;;) {
        unsigned v;
        asm volatile("ds_read_b32 %0, %1\n\ts_waitcnt lgkmcnt(0)" : "=v"(v) : "v"(a) : "memory");
        v = (unsigned)__builtin_amdgcn_readfirstlane((int)v);
        if ((v & MASK) >= n) return v;
        if (SLEEP) __builtin_amdgcn_s_sleep(SLEEP);
    }
}
static __device__ __forceinline__ unsigned ld_agent(const void* p) { return __hip_atomic_load((const unsigned*)p, __ATOMIC_RELAXED, __HIP_MEMORY_SCOPE_AGENT); }
static __device__ __forceinline__ void poll_tag(const void* p, unsigned tag) {
#pragma unroll 1
    for (unsigned spin = 0; spin < SPIN_MAX; ++spin) {
        if (__all(ld_agent(p) == tag)) break;
        __builtin_amdgcn_s_sleep(2);
    }
    __builtin_amdgcn_fence(__ATOMIC_ACQUIRE, "wavefront");
}
static __device__ __forceinline__ void wg_barrier() {
    asm volatile("s_waitcnt lgkmcnt(0)" ::: "memory");
    __builtin_amdgcn_s_barrier();
    asm volatile("" ::: "memory");
}
static __device__ __forceinline__ f32x4 mfma4(u32x2 a, u32x2 b, f32x4 c) {
    return __builtin_amdgcn_mfma_f32_4x4x4bf16_1k(__builtin_bit_cast(bf16x4, a), __builtin_bit_cast(bf16x4, b), c, 0, 0, 0);
}
static __device__ __forceinline__ f32x4 mfma16(u32x4 a, u32x4 b, f32x4 c) {
    return __builtin_amdgcn_mfma_f32_16x16x32_bf16(__builtin_bit_cast(bf16x8, a), __builtin_bit_cast(bf16x8, b), c, 0, 0, 0);
}

static constexpr unsigned evt_of(unsigned pt) {
    unsigned e = 6u;
    for (int n = 2; n < 15; ++n) if (((pt >> (2 * n)) & 3u) != ((pt >> (2 * n + 2)) & 3u)) e |= 1u << n;
    return e;
}

static constexpr u64 len_pos(bool kept) {
    u64 r = 0;
    for (int w = 0; w < 8; ++w) {
        r |= (u64)(kept ? 8 + w : w) << (4 * (((kept ? TKF : TKB) >> (4 * w)) & 15u));
        if (w >= 4 && (w >= 6) == kept) r |= (u64)(kept ? w : 4 + w) << (4 * ((TKA >> (4 * w)) & 15u));
    }
    return r;
}
static constexpr unsigned ring_const() {
    constexpr int e[4] = {EXTRA};
    unsigned r = 0;
    for (int i = 0; i < 4; ++i) r |= (unsigned)((4 - PBIAS + (-3 * e[i] - e[(i + 3) & 3] + e[(i + 2) & 3] + 3 * e[(i + 1) & 3]) / 2) & 255) << (8 * i);
    return r;
}

#define L_MISC 0
#define L_KEY 96
#define L_AT 416
#define L_LEN 1056
#define L_PCNT 1280
#define L_CCNT (L_PCNT + 2048)
#define L_BNC (L_CCNT + 1280)
#define L_CG (L_BNC + 4096)
#define L_PRV (L_CG + 1024)
#define A_STG L_PRV
#define A_PTAB (A_STG + EPB_MAX * 16)
#define L_PK (L_PRV + 15 * 4096)
#define L_DUM L_PK
#define L_CTAB (L_PK + 2048)
#define L_MRK (L_PK + 8192)
#define TR_STR 4352
#define L_TR (L_PK + 16384)
#define L_WR L_TR
#define L_IMG (L_TR + 8 * TR_STR)
#define L_TOTAL (L_IMG + 32768)
static_assert(L_TOTAL <= 163840 && L_BNC <= 8192 && NPB_MAX * LS * 16 <= 15 * 4096, "LDS budget");
static_assert(L_PRV % 16 == 0 && L_PK % 16 == 0 && L_IMG % 16 == 0 && L_CG % 16 == 0 && L_CCNT % 16 == 0, "alignment");
enum { SY_CPY = 40, SY_C1 = 44, SY_S = 48, SY_C3 = 56, SY_RI = 60, SY_PRE = 64 };

template <class T> static __device__ __forceinline__ T* arg_ptr(u32x16 a, int i) { return (T*)(__attribute__((address_space(1))) T*)((u64)a[i + 1] << 32 | a[i]); }
static __device__ __forceinline__ u32x4 mkdesc(const void* p, unsigned bytes) {
    return (u32x4){(unsigned)(uintptr_t)p, (unsigned)((uintptr_t)p >> 32) & 0xffffu, bytes, 0x00020000u};
}

#define STR2(x) #x
#define STR(x) STR2(x)
__global__ __launch_bounds__(512, 2) void k_fused(const int*, unsigned, unsigned, const int*, unsigned, unsigned, const void*, void*, const void*, const void*,
                                                  const void*, const void*, const void*, void*, int, int, int, unsigned, unsigned, unsigned, unsigned, unsigned)
{
    __shared__ __attribute__((aligned(256))) unsigned char smem[L_TOTAL];
    const int tid = threadIdx.x, bx = blockIdx.x;
    u32x16 ka, kb;
    unsigned so, cp;
    u32x3 d, sv;
    asm volatile("s_load_dwordx16 s[36:51], %3, 0x0\n\ts_load_dwordx16 s[52:67], %3, 0x40\n\ts_getpc_b64 s[68:69]\n\ts_movk_i32 s70, " STR(CODE_LINES) " * 128\n\ts_mov_b32 s71, 0x20000\n\t"
                 "s_lshl4_add_u32 %2, %4, " STR(CPN) " * -3968\n\tv_lshl_add_u32 v8, %5, 12, %2\n\tv_lshlrev_b32 v9, 7, %5\n\tv_max_i32 v8, v8, v9\n\tbuffer_load_dword v7, v8, s[68:71], 0 offen\n\t"
                 "v_mul_u32_u24 v15, 12, %5\n\ts_add_i32 %2, %4, -2\n\ts_waitcnt lgkmcnt(0)\n\ts_mul_i32 %2, %2, s62\n\ts_add_u32 s42, %2, s62\n\ts_min_u32 s38, s38, s42\n\ts_mov_b32 s42, s38\n\t"
                 "s_bitcmp1_b32 %4, 3\n\ts_cbranch_scc1 La_%=\n\tbuffer_load_dwordx3 v[8:10], v15, s[36:39], %2 offen nt\n\tLa_%=:\n\tbuffer_load_dwordx3 v[12:14], v15, s[40:43], %2 offen nt\n\t"
                 "s_cbranch_scc0 Lb_%=\n\tbuffer_load_dwordx3 v[8:10], v15, s[36:39], %2 offen nt\n\tLb_%=:"
                 : "={s[36:51]}"(ka), "={s[52:67]}"(kb), "=&s"(so) : "s"(__builtin_amdgcn_kernarg_segment_ptr()), "s"(bx), "v"(tid)
                 : "memory", "scc", "s68", "s69", "s70", "s71", "v7", "v8", "v9", "v10", "v12", "v13", "v14", "v15");
    asm volatile("" : "={v7}"(cp), "={v[8:10]}"(d), "={v[12:14]}"(sv));
    SB();
    const int* dst = arg_ptr<const int>(ka, 0);
    const int* src = arg_ptr<const int>(ka, 4);
    const u32x2* Y = arg_ptr<const u32x2>(ka, 8);
    unsigned char* ws = arg_ptr<unsigned char>(ka, 10);
    const unsigned short* x = arg_ptr<const unsigned short>(ka, 12);
    const u32x4* ef = arg_ptr<const u32x4>(ka, 14);
    const unsigned short* w_rad = arg_ptr<const unsigned short>(kb, 0);
    const unsigned short* cg = arg_ptr<const unsigned short>(kb, 2);
    const unsigned short* w_mix = arg_ptr<const unsigned short>(kb, 4);
    unsigned short* out = arg_ptr<unsigned short>(kb, 6);
    const int N = kb[8], E = kb[9], epb = kb[10] >> 2;
    const unsigned nb_magic = kb[11], smask = kb[12], cmul = kb[13], po0 = kb[14], po1 = kb[15];
    const int lane = tid & 63;
    const int w = __builtin_amdgcn_readfirstlane(tid >> 6);
    int half;
    const int b = (bx & 3) * 32 + (bx >> 3);
    const int g = lane >> 4, c16 = lane & 15;
    const int node0 = (b * N + 127) >> 7, npb = (((b + 1) * N + 127) >> 7) - node0;
    const unsigned lds0 = (unsigned)(uintptr_t)(__attribute__((address_space(3))) unsigned char*)smem;
    const __amdgpu_buffer_rsrc_t rsw = __builtin_amdgcn_make_buffer_rsrc((void*)ws, 0, W_END, 0x00020000);
    const unsigned le = tid * 3;
    const int base_e = (int)so >> 2;
    const int ne = base_e < 0 ? 0 : max(min(epb, E - base_e), 0);
    const int wd = 4 + ((w - 1 + (w >> 2)) & 3);
    auto task_at = [&](int t) -> unsigned {
        const unsigned q = (t == 0 ? TKF >> (4 * w) : (t == 1 ? TKB >> (4 * w) : TKA >> (4 * wd))) & 15u;
        return L_AT + ((t == 0 || (t == 2 && wd >= 6) ? 40 * half : 40 - 40 * half) + 4 * q) * 4;
    };
    const unsigned ri8 = (wd & 3) * 8;
    unsigned ta[3], cc, cd;
    int k0;
    auto set_ta = [&]() {
#pragma unroll
        for (int i = 0; i < 3; ++i) {
            ta[i] = lds0 + task_at(i == 2 ? 0 : ((w < 4) == (i == 0) ? 2 : 1)) + g * 4;
            asm volatile("" : "+v"(ta[i]));
        }
        cc = __builtin_rotateleft32(0xff0103fdu, ri8); cd = __builtin_rotateleft32(0xfe000204u, ri8); k0 = (int)(signed char)(ring_const() >> ri8);
        asm volatile("" : "+v"(cc), "+v"(cd), "+v"(k0));
    };
    unsigned tag;
    bool slow;
    u32x4 wr[8], cgA;
    auto load_wr = [&]() {
#pragma unroll
        for (int t = 0; t < 8; ++t) wr[t] = *(const u32x4*)(smem + L_WR + t * 1024 + lane * 16);
        cgA = *(const u32x4*)(smem + L_CG + lane * 16);
        asm volatile("" : "+v"(wr[0]), "+v"(wr[1]), "+v"(wr[2]), "+v"(wr[3]), "+v"(wr[4]), "+v"(wr[5]), "+v"(wr[6]), "+v"(wr[7]), "+v"(cgA));
    };

    __builtin_amdgcn_s_setprio(3);
    auto touch = [&](unsigned code) -> unsigned {
        if (code >= 100u)
            return __builtin_amdgcn_raw_buffer_load_b32(__builtin_amdgcn_make_buffer_rsrc(code == 100u ? (void*)x : (void*)ef, 0, code == 100u ? N * 1024 : E * 32, 0x00020000), lane << 21, bx * 128, 0);
        if (code < 8u)
            return __builtin_amdgcn_raw_buffer_load_b32(__builtin_amdgcn_make_buffer_rsrc((void*)(ef + (size_t)base_e * 2), 0, code == 0u ? 0 : ne * 32, 0x00020000), lane * 128, (code - 1u) * 8192u, 0);
        const unsigned j = code < 16u ? (code - 8u) * 128u + b : (code - 16u) * 32u + (bx >> 3);
        return __builtin_amdgcn_raw_buffer_load_b32(__builtin_amdgcn_make_buffer_rsrc((void*)x, 0, N * 1024, 0x00020000), lane * 128,
                                                    (2u * j + ((half ^ (smask >> (j >> 5))) & 1u)) * 8192u, 0);
    };
    constexpr unsigned tl[] = {TLIST};
    const __amdgpu_buffer_rsrc_t rsm = __builtin_amdgcn_make_buffer_rsrc((void*)w_mix, 0, C_DIM * C_DIM * 2, 0x00020000);
    u32x4 mv[8];
    auto load_mv = [&]() {
#pragma unroll
        for (int j = 0; j < 8; ++j)
            mv[j] = __builtin_amdgcn_raw_buffer_load_b128(rsm, g * 512 + c16 * 16 + (w & 3) * 2048 + (j & 1) * 256, (j >> 1) * 8192, 0);
        SB();
    };
    auto make_img = [&]() {
#pragma unroll
        for (int i = 0; i < 4; ++i) {
            u32x4 p0, p1;
#pragma unroll
            for (int j = 0; j < 4; ++j) {
                p0[j] = __builtin_amdgcn_perm(mv[2 * j + 1][i], mv[2 * j][i], 0x05040100u);
                p1[j] = __builtin_amdgcn_perm(mv[2 * j + 1][i], mv[2 * j][i], 0x07060302u);
            }
            const unsigned ia = L_IMG + (c16 >> 2) * 8192 + (w & 3) * 1024 + g * 256 + (c16 & 3) * 64 + i * 16;
            *(u32x4*)(smem + ia) = p0;
            *(u32x4*)(smem + ia + 4096) = p1;
        }
    };
    unsigned tlp = 0u, t0v = 0u, tv[NT2] = {};
    {
        const unsigned sh = 8u * (w & 3);
        unsigned inc[3];
        tlp = __builtin_amdgcn_raw_buffer_load_b32(__builtin_amdgcn_make_buffer_rsrc((void*)ws, 0, w == EW ? 4 : 0, 0x00020000), W_CTL, 0, 0);
        SB();
        {
            const unsigned k0 = (unsigned)tid - L_KEY / 16 < NPB_MAX / 4 ? 127u + L_KEY / 4 - 4u * tid : 0u, k1 = min(k0, 1u);
            *(u32x4*)(smem + tid * 16) = (u32x4){k0, k0 - k1, k0 - 2u * k1, k0 - 3u * k1};
        }
        *(u32x4*)(smem + L_MRK + tid * 16) = (u32x4){0u, 0u, 0u, 0u};
        SB();
        unsigned l16 = lane * 16;
        asm volatile("" : "+v"(l16));
        u32x4 te = (u32x4){0u, 0u, 0u, 0u};
        auto table = [&]() {
            if (__builtin_expect(w == EW, 1)) {
                const unsigned tg = (unsigned)__builtin_amdgcn_readfirstlane((int)tlp) * 1664525u + 1013904223u;
                *(unsigned*)(smem + L_MISC) = tg;
#pragma unroll
                for (int i = 0; i < 2; ++i) __builtin_amdgcn_raw_buffer_store_b128((u32x4){te[i], tg, te[2 + i], tg}, rsw, W_TAB + bx * 2048 + l16 + i * 1024, 0, SC1);
            }
        };
        if (__builtin_expect(bx <= 1, 1)) {
            unsigned v;
            const unsigned ts = (unsigned)__builtin_amdgcn_s_memrealtime();
            asm volatile("s_cmp_lg_u32 %3, 0\n\t.irp o, 0, 0x400, 0x800, 0xc00, 0x1000, 0x1400, 0x1800, 0x1c00\n\ts_load_dword %0, %1, %2 offset:\\o\n\ts_cbranch_scc0 Lt_%=\n\t.endr\n\tLt_%=:\n\ts_waitcnt lgkmcnt(0)"
                         : "=&s"(v) : "s"(x), "s"((w < 4 ? po0 : po1) + (bx == 0 ? 0x10000 : (w & 3) * 8192)), "s"(bx) : "memory", "scc");
            asm volatile("s_waitcnt vmcnt(0)" : "+{v7}"(cp), "+{v[8:10]}"(d), "+{v[12:14]}"(sv));
            wg_barrier();
            table();
            t0v = __builtin_amdgcn_raw_buffer_load_b32(rsm, (tid >> 3) == 32 ? lane * 128 : OOB_OFF, 0, 0);
            if (__builtin_expect(bx * 8 + w == EW, 1)) {
                unsigned s0, s1, v0, v1;
                const unsigned tm = (unsigned)__builtin_amdgcn_s_memtime() * 0x9e3779b1u;
#define RACE(r, o, a, b) "s_load_dword %2, %4, " o " offset:" a "\n\ts_load_dword %3, %4, " o " offset:" b "\n\ts_waitcnt lgkmcnt(1)\n\ts_sleep " "1" "\n\t" \
                             "s_getreg_b32 " r ", hwreg(HW_REG_IB_STS, 8, 1)\n\ts_waitcnt lgkmcnt(0)\n\t"
                asm volatile("s_sleep " "6" "\n\t" RACE("%0", "%5", "0", "0x2000") RACE("%1", "%6", "0x2000", "0") "s_cmp_lg_u32 %0, %1\n\ts_cbranch_scc1 Lr_%=\n\t" RACE("%1", "%5", "0x6000", "0x4000") "Lr_%=:"
                             : "=&s"(s0), "=&s"(s1), "=&s"(v0), "=&s"(v1) : "s"(x), "s"(po0 + (tm >> 29) * 1024u), "s"(po1 + (tm >> 26 & 7u) * 1024u) : "memory", "scc");
                SB();
                unsigned xc;
                asm volatile("s_getreg_b32 %0, hwreg(HW_REG_XCC_ID, 0, 2)" : "=s"(xc));
                unsigned hm = (0xf0f0f0u >> (xc + 4u * s1));
                const unsigned tg = (unsigned)__builtin_amdgcn_readfirstlane((int)tlp) * 1664525u + 1013904223u;
                __builtin_amdgcn_raw_buffer_store_b128((u32x4){hm, tg, hm, tg}, rsw, lane < 8 ? W_DEC + l16 : OOB_OFF, 0, SC1);
            }
            asm volatile("" : "+v"(t0v));
            while ((unsigned)__builtin_amdgcn_s_memrealtime() - ts < IDLE) __builtin_amdgcn_s_sleep(1);
        }
        int bq = bx;
        asm volatile("" : "+s"(bq));
        if (__builtin_expect(bq > 1, 1)) {
        wg_barrier();
        const __amdgpu_buffer_rsrc_t rsy = __builtin_amdgcn_make_buffer_rsrc((void*)Y, 0, ka[2] * 2, 0x00020000);
        u32x4 ya = __builtin_amdgcn_raw_buffer_load_b128(rsy, le * 8, so * 2, NT);
        u32x2 yb = __builtin_amdgcn_raw_buffer_load_b64(rsy, le * 8 + 16, so * 2, NT);
        SB();
        {
            u64 pb = (u64)(uintptr_t)cg;
            unsigned pn = (w & 2) << 6;
            asm("s_cmp_eq_u32 %2, 3\n\ts_cselect_b64 %0, %4, %0\n\ts_cselect_b32 %1, 0x1000, %1\n\t"
                "s_cmp_eq_u32 %2, 4\n\ts_cselect_b64 %0, %5, %0\n\ts_cselect_b32 %1, 0x400, %1\n\ts_cmp_gt_u32 %2, 4\n\ts_cselect_b64 %0, %6, %0\n\ts_cselect_b32 %1, %7, %1"
                : "+s"(pb), "+s"(pn) : "s"(w), "s"(cg), "s"(w_rad), "s"(w_mix + (bx >> 3) * 512), "s"(ef + (base_e * 2 + (7 - w) * 512)), "s"(max(min(ne * 32, NT0 * 8192) - (7 - w) * 8192, 0)) : "scc");
            t0v = __builtin_amdgcn_raw_buffer_load_b32(__builtin_amdgcn_make_buffer_rsrc((void*)pb, 0, pn, 0x00020000), lane * 128, 0, 0);
        }
        SB();
#pragma unroll
        for (int r = 0; r < 3; ++r) inc[r] = (int)le + r < ne ? 1u << sh : 0u;
        asm volatile("" : "+v"(inc[0]), "+v"(inc[1]), "+v"(inc[2]));
        unsigned m0 = below_mask(w, 0), m1 = below_mask(w, 1);
        asm volatile("" : "+s"(m0), "+s"(m1));
        SB();
        unsigned bin[3], rank[3], ba[3];
        asm volatile("s_waitcnt vmcnt(4)" : "+{v7}"(cp), "+{v[8:10]}"(d), "+{v[12:14]}"(sv));
#pragma unroll
        for (int r = 0; r < 3; ++r) {
            bin[r] = __umulhi(d[r], nb_magic);
            ba[r] = ((((sv[r] >> 3) ^ (smask >> ((sv[r] >> 9) & 31u))) & 1u) * 512u + bin[r] * 4u) & 1023u;
            rank[r] = atomicAdd((unsigned*)(smem + L_PCNT + (w >> 2) * 1024 + ba[r]), inc[r]);
        }
        SB();
        unsigned rw1[3];
#pragma unroll
        for (int r = 0; r < 3; ++r) { rw1[r] = (d[r] & 127u) | ((le + r) << 7); asm volatile("" : "+v"(rw1[r])); }
        unsigned pa[3];
#pragma unroll
        for (int r = 0; r < 3; ++r) { pa[r] = inc[r] != 0u ? A_PTAB + w * 512 + (ba[r] >> 1) : L_MISC + 8; asm volatile("" : "+v"(pa[r])); }
        wg_barrier();
        u32x2 c[4];
#pragma unroll
        for (int i = 0; i < 4; ++i) c[i] = *(const u32x2*)(smem + L_PCNT + i * 512 + lane * 8);
        SB();
#pragma unroll
        for (int r = 0; r < 3; ++r) { rank[r] = inc[r] != 0u ? (rank[r] >> sh) & 0xffu : (L_DUM - A_STG) / 16 + lane; asm volatile("" : "+v"(rank[r])); }
        unsigned t[4], o[4], r0, r2, tt;
#define DOT(d, c, m, a) "v_dot4_u32_u8 " d ", " c ", " m ", " a "\n\t"
#define SCN(c) "v_add_u32_dpp %9, %9, %9 " c "\n\t"
        asm volatile(DOT("%0", "%11", "%21", "0") DOT("%1", "%12", "%21", "0") DOT("%2", "%13", "%21", "0") DOT("%3", "%14", "%21", "0")
                     DOT("%0", "%15", "%21", "%0") DOT("%1", "%16", "%21", "%1") DOT("%2", "%17", "%21", "%2") DOT("%3", "%18", "%21", "%3")
                     DOT("%4", "%11", "%19", "0") DOT("%6", "%13", "%19", "0") DOT("%5", "%12", "%19", "%0") DOT("%7", "%14", "%19", "%2")
                     "v_add_u32 %8, %0, %1\n\tv_add_u32 %9, %2, %3\n\tv_lshl_or_b32 %8, %9, 16, %8\n\t" DOT("%4", "%15", "%20", "%4") DOT("%5", "%16", "%20", "%5")
                     "v_add_u32_dpp %9, %8, %8 row_shr:1 row_mask:0xf bank_mask:0xf bound_ctrl:1\n\t" DOT("%6", "%17", "%20", "%6") DOT("%7", "%18", "%20", "%7")
                     SCN("row_shr:2 row_mask:0xf bank_mask:0xf bound_ctrl:1") "v_lshl_or_b32 %4, %5, 16, %4\n\ts_nop 0\n\t"
                     SCN("row_shr:4 row_mask:0xf bank_mask:0xf bound_ctrl:1") "v_lshl_or_b32 %6, %7, 16, %6\n\ts_nop 0\n\t"
                     SCN("row_shr:8 row_mask:0xf bank_mask:0xf bound_ctrl:1") "s_nop 1\n\t" SCN("row_bcast:15 row_mask:0xa bank_mask:0xf") "s_nop 1\n\t" SCN("row_bcast:31 row_mask:0xc bank_mask:0xf")
                     "v_sub_u32 %8, %9, %8\n\tv_readlane_b32 %10, %9, 63\n\tv_lshrrev_b32 %9, 16, %8\n\tv_and_b32 %8, 0xffff, %8\n\ts_and_b32 %10, %10, 0xffff\n\t"
                     "v_mad_u32_u24 %4, %8, %22, %4\n\tv_add_u32 %9, %10, %9\n\tv_mad_u32_u24 %6, %9, %22, %6\n\tds_write2st64_b32 %23, %4, %6 offset1:1"
                     : "=&v"(t[0]), "=&v"(t[1]), "=&v"(t[2]), "=&v"(t[3]), "=&v"(o[0]), "=&v"(o[1]), "=&v"(o[2]), "=&v"(o[3]), "=&v"(r0), "=&v"(r2), "=&s"(tt)
                     : "v"(c[0].x), "v"(c[0].y), "v"(c[1].x), "v"(c[1].y), "v"(c[2].x), "v"(c[2].y), "v"(c[3].x), "v"(c[3].y), "s"(m0), "s"(m1), "s"(0x01010101u), "s"(0x10001u),
                       "v"(lds0 + A_PTAB + w * 512 + lane * 4) : "memory", "scc");
        unsigned pos[3];
#pragma unroll
        for (int r = 0; r < 3; ++r) pos[r] = *(const unsigned short*)(smem + pa[r]);
        SB();
        te = (u32x4){r0 | t[0] << 16, (r0 + t[0]) | t[1] << 16, r2 | t[2] << 16, (r2 + t[2]) | t[3] << 16};
        u32x4 rec[3];
#pragma unroll
        for (int r = 0; r < 3; ++r) rec[r] = (u32x4){r < 2 ? ya[2 * r] : yb.x, sv[r], r < 2 ? ya[2 * r + 1] : yb.y, rw1[r]};
        asm volatile("" : "+v"(rec[0]), "+v"(rec[1]), "+v"(rec[2]));
        table();
#pragma unroll
        for (int r = 0; r < 3; ++r) *(u32x4*)(smem + A_STG + (pos[r] + rank[r]) * 16) = rec[r];
        }
        wg_barrier();
        const __amdgpu_buffer_rsrc_t rsr = __builtin_amdgcn_make_buffer_rsrc((void*)(ws + W_RA + bx * (EPB_MAX * 16)), 0, (ne * 16 + 127) & ~127, 0x00020000);
        u32x4 va[3];
        const unsigned tv0 = *(const unsigned*)(smem + L_MISC);
#pragma unroll
        for (int i = 0; i < 3; ++i) va[i] = *(const u32x4*)(smem + A_STG + (tid ^ 256) * 16 + 8192 * i);
#pragma unroll
        for (int i = 0; i < 3; ++i) __builtin_amdgcn_raw_buffer_store_b128((u32x4){va[i].x, va[i].y | tv0 << 14, va[i].z, va[i].w | (tv0 & 0xfffc0000u)}, rsr, (tid ^ 256) * 16, 8192 * i, SC1);
        tag = (unsigned)__builtin_amdgcn_readfirstlane((int)tv0);
        lds_arrive(lds0 + SY_CPY);
    }
    u32x4 dx = mkdesc(x, (unsigned)N * 1024u), de = mkdesc(ef, (unsigned)E * 32u);
    asm volatile("" : "+s"(dx), "+s"(de));
    if (w < 4) {
        asm volatile("" :: "v"(t0v), "v"(tlp));
        const unsigned sh = 8u * w, mb = 0x01010101u & ((1u << sh) - 1u);
        const unsigned T0 = tag << 14, T1 = tag & 0xfffc0000u;
        const unsigned to = W_TAB + tid * 2048 + (b & 1) * 1024 + (b >> 1) * 16;
        __builtin_amdgcn_s_sleep(4);
        u32x4 en = __builtin_amdgcn_raw_buffer_load_b128(rsw, to, 0, SC1);
        u32x2 dec = __builtin_amdgcn_raw_buffer_load_b64(rsw, W_DEC, 0, SC1);
#pragma unroll 1
        for (unsigned spin = 0; spin < SPIN_MAX; ++spin) {
            if (__builtin_amdgcn_ballot_w64(((en.y ^ tag) | (en.w ^ tag) | (dec.y ^ tag)) != 0u) == 0ull) break;
            __builtin_amdgcn_s_sleep(2);
            asm volatile("" ::: "memory");
            if (((en.y ^ tag) | (en.w ^ tag)) != 0u) en = __builtin_amdgcn_raw_buffer_load_b128(rsw, to, 0, SC1);
            dec = __builtin_amdgcn_raw_buffer_load_b64(rsw, W_DEC, 0, SC1);
        }
        half = (__builtin_amdgcn_readfirstlane((int)dec.x) >> (bx & 7)) & 1;
        if (half) en.x = en.z;
        const unsigned rc = en.x >> 16;
        const unsigned incl = (unsigned)wave_incl_scan((int)rc);
        const int twf = __builtin_amdgcn_readlane((int)incl, 63), tw = twf <= 64 * NRND ? twf : 0;
        const bool more = tw > 64 * (NRND - 1);
        unsigned ma = L_MRK + w * (256 * NRND) + (incl - rc) * 4;
        asm volatile("" : "+v"(ma));
        *(unsigned*)(smem + (((rc != 0u) & (incl <= 64u * NRND)) ? ma : L_DUM + lane * 4)) = (unsigned)lane * EPB_MAX + (en.x & 0xffffu) - (incl - rc);
        unsigned ra[NRND];
        asm volatile("" ::: "memory");
#pragma unroll
        for (int r = 0; r < NRND; ++r) ra[r] = *(const unsigned*)(smem + L_MRK + w * (256 * NRND) + r * 256 + lane * 4);
        unsigned carry = 0u;
        u32x4 A[NRND];
#pragma unroll
        for (int r = 0; r < NRND; ++r) {
            if (r == NRND - 1) { if (more) ra[r] = wave_max_scan(ra[r]); asm volatile("" : "+v"(ra[r])); }
            else if (r % 3 == 0) wave_max_scan3(ra[r], ra[r + 1], ra[r + 2]);
            const unsigned m = max(ra[r], carry);
            carry = max(carry, (unsigned)__builtin_amdgcn_readlane((int)ra[r], 63));
            ra[r] = r * 64 + lane < tw ? (m + (unsigned)(r * 64 + lane)) * 16u : OOB_OFF >> 1;
            A[r] = __builtin_amdgcn_raw_buffer_load_b128(rsw, ra[r], W_RA + 64 * w * (EPB_MAX * 16), SC1);
        }
        SB();
        lds_arrive(lds0 + SY_RI, 16u + half);
        unsigned pe[NRND], inc[NRND], inck[NRND], lim[NRND];
#pragma unroll
        for (int r = 0; r < NRND; ++r) {
            pe[r] = __umul24(__umulhi(ra[r], 209716u) + 64u * w - 2u, (unsigned)epb * 32u);
            inc[r] = ra[r] < 0x40000000u ? 1u << sh : 0u;
            inck[r] = ra[r] < 0x40000000u ? 128u : 0u;
            lim[r] = ra[r] < 0x40000000u ? 16383u : ~0u;
            asm volatile("" : "+v"(pe[r]), "+v"(inc[r]), "+v"(inck[r]), "+v"(lim[r]));
        }
        if (twf > 64 * NRND) lds_arrive(lds0 + SY_S, 256u);
        set_ta();
        SB();
        unsigned rank[NRND], nc[NRND];
        auto slot = [&](int r) {
            nc[r] = inc[r] != 0u ? L_CTAB + w * 640 + nc[r] * 2 : L_MISC + 8;
            A[r] = (u32x4){A[r].y << 10, ((A[r].w >> 2) & 0xffe0u) + pe[r], A[r].x, A[r].z};
            asm volatile("" : "+v"(nc[r]), "+v"(A[r]));
            SB();
        };
#pragma unroll
        for (int r = 0; r < NRND; ++r) {
            if (r == NRND - 1 && !more) { rank[r] = 0u; nc[r] = 0u; continue; }
            A[r].y ^= T0; A[r].w ^= T1;
            if (__builtin_expect(__builtin_amdgcn_ballot_w64((A[r].y | (A[r].w >> 4)) > lim[r]) != 0ull, 0)) {
#pragma unroll 1
                for (unsigned spin = 0; spin < SPIN_MAX; ++spin) {
                    asm volatile("" ::: "memory");
                    A[r] = __builtin_amdgcn_raw_buffer_load_b128(rsw, ra[r], W_RA + 64 * w * (EPB_MAX * 16), SC1);
                    A[r].y ^= T0; A[r].w ^= T1;
                    if (__builtin_amdgcn_ballot_w64((A[r].y | (A[r].w >> 4)) > lim[r]) == 0ull) break;
                }
            }
            const unsigned nl = (A[r].w - node0) & 127u;
            nc[r] = nl * 4u + (((__umul24(A[r].y, cmul) >> 16) ^ ((nl >= 40u) == (half != 0) ? 3u : 0u)) & 3u);
            if (r >= 4) nc[r] = inc[r] != 0u ? nc[r] : (unsigned)lane;
            rank[r] = atomicAdd((unsigned*)(smem + L_CCNT + nc[r] * 4), inc[r]);
            atomicAdd((unsigned*)(smem + L_KEY + (nc[r] & 0x1fcu)), inck[r]);
            SB();
            if (r < 3) slot(r);
        }
        lds_arrive(lds0 + SY_C1);
        unsigned d0 = lane * LS, d1 = (64 + c16) * LS, ca0 = L_CTAB + w * 640 + lane * 8, ca1 = L_CTAB + w * 640 + (64 + c16) * 8;
        asm volatile("" : "+v"(d0), "+v"(d1), "+v"(ca0), "+v"(ca1));
#pragma unroll
        for (int r = 3; r < NRND; ++r) slot(r);
#pragma unroll
        for (int r = 0; r < NRND; ++r) {
            rank[r] = inc[r] != 0u ? (rank[r] >> sh) & 0xffu : (L_DUM - L_PRV) / 16 + lane;
            asm volatile("" : "+v"(rank[r]));
        }
        lds_wait(lds0 + SY_PRE, 4);
        {
            u32x4 c0, c1;
            unsigned cn;
            do {
                asm volatile("ds_read_b32 %0, %3 offset:%c6\n\tds_read_b128 %1, %4 offset:%c7\n\tds_read_b128 %2, %5 offset:%c8\n\ts_waitcnt lgkmcnt(0)"
                             : "=&v"(cn), "=&v"(c0), "=&v"(c1) : "v"(lds0), "v"(lds0 + lane * 16), "v"(lds0 + c16 * 16), "i"(SY_C1), "i"(L_CCNT), "i"(L_CCNT + 1024) : "memory");
            } while (__builtin_amdgcn_readfirstlane((int)cn) < 4);
            unsigned p0[4], p1[4];
#pragma unroll
            for (int c = 0; c < 4; ++c) {
                p0[c] = bsum(c0[c], mb, d0); p1[c] = bsum(c1[c], mb, d1);
                if (c < 3) { d0 = bsum(c0[c], 0x01010101u, d0); d1 = bsum(c1[c], 0x01010101u, d1); }
            }
            *(u32x2*)(smem + ca0) = (u32x2){p0[0] | (p0[1] << 16), p0[2] | (p0[3] << 16)};
            *(u32x2*)(smem + ca1) = (u32x2){p1[0] | (p1[1] << 16), p1[2] | (p1[3] << 16)};
        }
        asm volatile("" ::: "memory");
#pragma unroll
        for (int r = 0; r < NRND; ++r) nc[r] = *(const unsigned short*)(smem + nc[r]);
        SB();
#pragma unroll
        for (int r = 0; r < NRND; ++r) {
            if (r == NRND - 1 && !more) continue;
            *(u32x4*)(smem + L_PRV + (nc[r] + rank[r]) * 16) = A[r];
        }
        load_wr();
        lds_arrive(lds0 + SY_C3);
    } else {
        const int q = w - 4, t2 = tid - 256;
        __builtin_amdgcn_s_setprio(2);
        unsigned wv[8], cv[4];
        {
            const __amdgpu_buffer_rsrc_t rsa = __builtin_amdgcn_make_buffer_rsrc((void*)w_rad, 0, DE_DIM * C_DIM * 2, 0x00020000);
            const __amdgpu_buffer_rsrc_t rsg = __builtin_amdgcn_make_buffer_rsrc((void*)cg, 0, 128, 0x00020000);
#pragma unroll
            for (int j = 0; j < 8; ++j) wv[j] = __builtin_amdgcn_raw_buffer_load_b32(rsa, (g & 1) * 2048 + 64 * q + 4 * c16 + 256 * j, 0, 0);
#pragma unroll
            for (int f = 0; f < 4; ++f) cv[f] = __builtin_amdgcn_raw_buffer_load_b32(rsg, (lane & 3) * 32 + (lane & 8u) / 2 + 8 * f, 0, 0);
            SB();
        }
        lds_wait(lds0 + SY_CPY, 8);
#pragma unroll
        for (int i = 0; i < 15; ++i) *(u32x4*)(smem + L_PRV + (t2 + 256 * i) * 16) = (u32x4){OOB_OFF, OOB_OFF, 0u, 0u};
        SB();
        asm volatile("" :: "v"(t0v), "v"(tlp));
        load_mv();
        {
            float f0[8], f1[8];
#pragma unroll
            for (int j = 0; j < 8; ++j) {
                const float a = __uint_as_float(wv[j] << 16) * -1.44269504088896340736f, c = __uint_as_float(wv[j] & 0xffff0000u) * -1.44269504088896340736f;
                const unsigned hp = g < 2 ? 0u : pack2bf(a, c);
                f0[j] = a - __uint_as_float(hp << 16);
                f1[j] = c - __uint_as_float(hp & 0xffff0000u);
            }
            *(u32x4*)(smem + L_WR + (2 * q) * 1024 + lane * 16) = (u32x4){pack2bf(f0[0], f0[1]), pack2bf(f0[2], f0[3]), pack2bf(f0[4], f0[5]), pack2bf(f0[6], f0[7])};
            *(u32x4*)(smem + L_WR + (2 * q + 1) * 1024 + lane * 16) = (u32x4){pack2bf(f1[0], f1[1]), pack2bf(f1[2], f1[3]), pack2bf(f1[4], f1[5]), pack2bf(f1[6], f1[7])};
            float a[4];
#pragma unroll
            for (int f = 0; f < 4; ++f) a[f] = __uint_as_float((lane & 4) ? cv[f] & 0xffff0000u : cv[f] << 16) * -0.69314718055994530942f;
            const unsigned h0 = pack2bf(a[0], a[1]), h1 = pack2bf(a[2], a[3]);
            *(u32x4*)(smem + L_CG + lane * 16) = (u32x4){h0, h1, pack2bf(a[0] - __uint_as_float(h0 << 16), a[1] - __uint_as_float(h0 & 0xffff0000u)),
                                                        pack2bf(a[2] - __uint_as_float(h1 << 16), a[3] - __uint_as_float(h1 & 0xffff0000u))};
        }
        lds_arrive(lds0 + SY_PRE);
        make_img();
        lds_wait(lds0 + SY_PRE, 4);
        load_wr();
        if (w == 7) {
            lds_arrive(lds0 + SY_C3);
            half = (lds_wait<1>(lds0 + SY_RI, 64) & 15u) != 0u;
#pragma unroll
            for (int i = 0; i < NT2; ++i) tv[i] = touch(tl[i]);
            SB();
            set_ta();
        } else {
            half = (lds_wait<1>(lds0 + SY_RI, 16) & 15u) != 0u;
            set_ta();
            const int n5 = min(27 * q + (lane >> 1), NPB_MAX - 1);
            const bool kp = (n5 >= 40) == (half != 0);
            u64 lpos = kp ? len_pos(true) : len_pos(false);
            unsigned an = lds0 + n5 * 4, ak = lds0 + L_KEY + (n5 >= 40 ? 160 : 0) + (lane & 1) * 80;
            unsigned aa = L_AT + (n5 >= 40 ? 160 : 0), al = L_LEN + (kp ? 4 : 0), av = (unsigned)n5 | (lds0 + L_PRV + n5 * (LS * 16)) << 8;
            asm volatile("" : "+v"(an), "+v"(ak), "+v"(aa), "+v"(al), "+v"(av), "+v"(lpos));
            lds_wait<1>(lds0 + SY_C1, 3);
            __builtin_amdgcn_s_setprio(3);
            unsigned kn, cn;
            u32x4 dk[5];
            do {
                asm volatile("ds_read_b32 %0, %7 offset:%c10\n\tds_read_b32 %1, %8 offset:%c11\n\tds_read_b128 %2, %9\n\tds_read_b128 %3, %9 offset:16\n\tds_read_b128 %4, %9 offset:32\n\t"
                             "ds_read_b128 %5, %9 offset:48\n\tds_read_b128 %6, %9 offset:64\n\ts_waitcnt lgkmcnt(0)"
                             : "=&v"(cn), "=&v"(kn), "=&v"(dk[0]), "=&v"(dk[1]), "=&v"(dk[2]), "=&v"(dk[3]), "=&v"(dk[4]) : "v"(lds0), "v"(an), "v"(ak), "i"(SY_C1), "i"(L_KEY) : "memory");
            } while (__builtin_amdgcn_readfirstlane((int)cn) < 4);
            unsigned sg[4] = {0u, 0u, 0u, 0u};
#pragma unroll
            for (int k = 0; k < 20; ++k) sg[k & 3] = __builtin_amdgcn_alignbit(sg[k & 3], kn - dk[k >> 2][k & 3], 31);
            int c = __builtin_popcount(sg[0] | sg[1] << 8 | sg[2] << 16 | sg[3] << 24);
            c += __builtin_amdgcn_update_dpp(0, c, 0xB1, 0xf, 0xf, false);
            *(unsigned*)(smem + aa + c * 4) = av;
            smem[(c & 3) == 0 ? al + ((unsigned)(lpos >> c) & 15u) : L_LEN + 20] = (unsigned char)max(kn >> 7, c < 8 ? 12u : 8u);
            lds_arrive(lds0 + SY_S, __builtin_amdgcn_ballot_w64((kn >> 7) > DEG_MAX) != 0ull ? 257u : 1u);
        }
        __builtin_amdgcn_s_setprio(3);
    }
    unsigned ax[3], lh, cn;
    u32x4 ln;
    {
        if (w == 7) lds_wait<2, 255>(lds0 + SY_S, 1);
        do {
            asm volatile("ds_read_b32 %0, %6 offset:%c10\n\tds_read_b128 %1, %6 offset:%c11\n\tds_read_b32 %2, %6 offset:%c12\n\tds_read_b32 %3, %7\n\tds_read_b32 %4, %8\n\t"
                         "ds_read_b32 %5, %9\n\ts_waitcnt lgkmcnt(0)"
                         : "=&v"(cn), "=&v"(ln), "=&v"(lh), "=&v"(ax[0]), "=&v"(ax[1]), "=&v"(ax[2])
                         : "v"(lds0), "v"(ta[0]), "v"(ta[1]), "v"(ta[2]), "i"(SY_S), "i"(L_LEN), "i"(L_LEN + 16) : "memory");
            cn = (unsigned)__builtin_amdgcn_readfirstlane((int)cn);
        } while ((cn & 255u) < 3u);
        slow = cn >= 256u;
    }
    const int p8 = __builtin_amdgcn_sdot4((int)(ln.x + ln.y), (int)cc, __builtin_amdgcn_sdot4((int)(ln.w + lh), (int)cc,
                   __builtin_amdgcn_sdot4((int)ln.z, (int)cd, k0, false), false), false);
    const int ld = (__builtin_amdgcn_readfirstlane((int)ln.z) >> ri8) & 255, p2 = min(max(__builtin_amdgcn_readfirstlane(p8) >> 3, 4), ld - 8);
    const int l2 = w < 4 ? ld - p2 : p2, od = (w < 4) == (wd >= 6) ? ld - l2 : 0;
    const unsigned aX = (ax[0] >> 8) + ((w < 4 ? od : 0) + (c16 & 3)) * 16, aF = (ax[0] >> 8) + ((w < 4 ? od : 0) + (c16 >> 2)) * 16 + 4;
    const unsigned lane_off = (unsigned)c16 * 16u;
    const unsigned aWw = L_BNC + w * 512 + lane * 8;
    const unsigned aWr = L_BNC + w * 512 + g * 128 + (c16 & 3) * 32 + (c16 >> 2) * 8;
    auto row_of = [=](unsigned at) -> unsigned {
        const unsigned nl = at & 0xffu;
        return nl < (unsigned)npb ? ((unsigned)node0 + nl) * 1024u + lane_off : OOB_OFF;
    };
#pragma unroll
    for (int i = 0; i < NT2; ++i) asm volatile("" :: "v"(tv[i]));
    if (!slow) {
        u32x32 oxa, oxb;
        u32x4 S;
        u32x2 ef0, efN;
        unsigned tmp;
        __builtin_amdgcn_s_waitcnt(0x0F70);
        asm volatile(
            "Lw2_%=:\n"
            "ds_read_b32 v220, v219\n"
            "ds_read2_b32 v[232:233], v211 offset1:16\n"
            "ds_read_b128 v[196:199], v210\n"
            "s_waitcnt lgkmcnt(1)\n"
            "v_readfirstlane_b32 %[tmp], v220\n"
            "s_cmp_lt_u32 %[tmp], 5\n"
            "s_cbranch_scc1 Lw2_%=\n"
            "v_or_b32 v232, v232, v229\n"
            "v_or_b32 v233, v233, v229\n"
            "buffer_load_dwordx2 v[238:239], v232, %[rse], 0 offen\n"
            "s_waitcnt lgkmcnt(0)\n"
            "v_add_u32_dpp v220, v196, v214 quad_perm:[0,0,0,0] row_mask:0xf bank_mask:0xf\n"
            "v_add_u32_dpp v221, v196, v214 quad_perm:[1,1,1,1] row_mask:0xf bank_mask:0xf\n"
            "v_add_u32_dpp v222, v196, v214 quad_perm:[2,2,2,2] row_mask:0xf bank_mask:0xf\n"
            "v_add_u32_dpp v223, v196, v214 quad_perm:[3,3,3,3] row_mask:0xf bank_mask:0xf\n"
            "buffer_load_dwordx4 v[32:35], v220, %[rsx], 0 offen\n"
            "buffer_load_dwordx4 v[36:39], v220, %[rsx], 0 offen offset:256\n"
            "buffer_load_dwordx4 v[48:51], v221, %[rsx], 0 offen\n"
            "buffer_load_dwordx4 v[52:55], v221, %[rsx], 0 offen offset:256\n"
            "buffer_load_dwordx2 v[232:233], v233, %[rse], 0 offen\n"
            "buffer_load_dwordx4 v[64:67], v222, %[rsx], 0 offen\n"
            "buffer_load_dwordx4 v[68:71], v222, %[rsx], 0 offen offset:256\n"
            "buffer_load_dwordx4 v[80:83], v223, %[rsx], 0 offen\n"
            "buffer_load_dwordx4 v[84:87], v223, %[rsx], 0 offen offset:256\n"
            "buffer_load_dwordx4 v[40:43], v220, %[rsx], 0 offen offset:512\n"
            "buffer_load_dwordx4 v[44:47], v220, %[rsx], 0 offen offset:768\n"
            "buffer_load_dwordx4 v[56:59], v221, %[rsx], 0 offen offset:512\n"
            "buffer_load_dwordx4 v[60:63], v221, %[rsx], 0 offen offset:768\n"
            "buffer_load_dwordx4 v[72:75], v222, %[rsx], 0 offen offset:512\n"
            "buffer_load_dwordx4 v[76:79], v222, %[rsx], 0 offen offset:768\n"
            "buffer_load_dwordx4 v[88:91], v223, %[rsx], 0 offen offset:512\n"
            "buffer_load_dwordx4 v[92:95], v223, %[rsx], 0 offen offset:768\n"
            : "={v[32:63]}"(oxa), "={v[64:95]}"(oxb), "={v[238:239]}"(ef0), "={v[196:199]}"(S), "={v[232:233]}"(efN), [tmp] "=&s"(tmp)
            : [rse] "s"(de), [rsx] "s"(dx), [ax] "{v210}"(aX), [af] "{v211}"(aF), [lo] "{v214}"(lane_off), [eo] "{v229}"((lane & 3) * 8), [sy] "{v219}"(lds0 + SY_C3)
            : "memory", "scc", "v220", "v221", "v222", "v223");
        if (bx == 0 && w == 4) flag_store(ws + W_CTL, tag);
        const int lf = (__builtin_amdgcn_readfirstlane((int)(w < 4 ? ln.w : lh)) >> (8 * (w & 3))) & 255, l1 = (__builtin_amdgcn_readfirstlane((int)(w < 4 ? ln.x : ln.y)) >> (8 * (w & 3))) & 255;
        const int e2 = lf + l1 + l2;
        int nst = (e2 + 3) >> 2;
        const unsigned r2 = row_of(ax[1]);
        u32x32 owr;
        u32x16 omi;
        u32x8 ot;
#pragma unroll
        for (int i = 0; i < 32; ++i) owr[i] = wr[i >> 2][i & 3];
        const int pw = (w + (w >> 2)) & 3;
        const unsigned trb = lds0 + L_TR + w * TR_STR;
        omi[2] = aX + 64; omi[3] = aF + 64;
        omi[4] = lds0 + aWw; omi[5] = lds0 + aWr; omi[6] = lane_off; omi[7] = W_AGG + row_of(ax[0]); omi[8] = W_AGG + r2;
        omi[9] = row_of(ax[2]); omi[10] = w >= 6 ? r2 : OOB_OFF; omi[15] = ~tag;
        ot[0] = (lane & 3) == 0 ? 0x3f80u : ((lane & 3) == 1 ? 0x3f800000u : 0u); ot[1] = (lane & 3) == 2 ? 0x3f80u : ((lane & 3) == 3 ? 0x3f800000u : 0u);
        ot[2] = trb + g * (4 * 272) + c16 * 16; ot[3] = trb + c16 * 272 + g * 16; ot[4] = lds0 + L_IMG + lane * 16; ot[5] = (lane & 3) * 8; ot[7] = lds0 + L_PK + pw * 4096 + lane * 16;
        int rem = w < 4 ? l2 : l1, lens = ((w < 4 ? l1 : l2) - 4) | 0xff00, fm = w < 4 ? 0x802 : (w < 6 ? 0x810 : 0xb0), pb = 0;
        const int j1 = (w < 4 ? -od - l2 : od - l1) * 16, j2 = (w < 4 ? -l1 : -od - l2) * 16;
        u32x2 jmp = (u32x2){(ax[1] >> 8) - (ax[0] >> 8) + j1, (ax[2] >> 8) - (ax[1] >> 8) + j2};
        unsigned bpa = c16 * 16 + (g & 1) * 8;
        unsigned evt = (unsigned)__builtin_amdgcn_readfirstlane(w < 4 ? evt_of(PTO) : (w < 6 ? evt_of(PTY) : evt_of(PTZ))) | 6u << (nst - (rem >> 2)) | 6u << (nst - ((l1 + l2) >> 2));
        u64 ra;
        asm volatile(
            "s_nop 1\n"
            "v_mfma_f32_4x4x4_16b_bf16 v[204:207], v[202:203], v[198:199], 0\n"
            "v_mov_b64 v[0:1], 0\n"
            "v_mov_b64 v[2:3], 0\n"
            "v_mov_b64 v[4:5], 0\n"
            "v_mov_b64 v[6:7], 0\n"
            "v_mfma_f32_4x4x4_16b_bf16 v[204:207], v[200:201], v[198:199], v[204:207]\n"
            "v_mov_b64 v[8:9], 0\n"
            "v_mov_b64 v[10:11], 0\n"
            "v_mov_b64 v[12:13], 0\n"
            "v_mov_b64 v[14:15], 0\n"
            "v_mov_b64 v[16:17], 0\n"
            "v_mov_b64 v[18:19], 0\n"
            "v_mov_b64 v[20:21], 0\n"
            "v_mov_b64 v[22:23], 0\n"
            "v_mov_b64 v[24:25], 0\n"
            "v_mov_b64 v[26:27], 0\n"
            "v_mov_b64 v[28:29], 0\n"
            "v_mov_b64 v[30:31], 0\n"
            "v_cvt_pk_bf16_f32 v204, v204, v205\n"
            "v_cvt_pk_bf16_f32 v205, v206, v207\n"
            "ds_write_b64 v212, v[204:205]\n"
            "ds_read_b128 v[196:199], v210\n"
            "ds_read_b64 v[208:209], v213\n"
            "s_waitcnt vmcnt(17)\n"
            "ds_bpermute_b32 v192, v234, v238\n"
            "ds_bpermute_b32 v193, v234, v239\n"
            "ds_bpermute_b32 v194, v234, v238 offset:4\n"
            "ds_bpermute_b32 v195, v234, v239 offset:4\n"
            "s_waitcnt lgkmcnt(0)\n"
            "v_mfma_f32_16x16x32_bf16 v[176:179], v[192:195], v[96:99], 0\n"
            "v_mfma_f32_16x16x32_bf16 v[180:183], v[192:195], v[100:103], 0\n"
            "v_mfma_f32_16x16x32_bf16 v[184:187], v[192:195], v[104:107], 0\n"
            "v_mfma_f32_16x16x32_bf16 v[188:191], v[192:195], v[108:111], 0\n"
            "v_mfma_f32_16x16x32_bf16 v[128:131], v[192:195], v[112:115], 0\n"
            "v_mfma_f32_16x16x32_bf16 v[132:135], v[192:195], v[116:119], 0\n"
            "s_nop 2\n"
            "v_exp_f32 v144, v176\n"
            "v_exp_f32 v145, v177\n"
            "v_exp_f32 v146, v178\n"
            "v_exp_f32 v147, v179\n"
            "v_exp_f32 v148, v180\n"
            "v_exp_f32 v149, v181\n"
            "v_exp_f32 v150, v182\n"
            "v_exp_f32 v151, v183\n"
            "v_exp_f32 v152, v184\n"
            "v_exp_f32 v153, v185\n"
            "v_exp_f32 v154, v186\n"
            "v_exp_f32 v155, v187\n"
            "v_exp_f32 v156, v188\n"
            "v_exp_f32 v157, v189\n"
            "v_exp_f32 v158, v190\n"
            "v_exp_f32 v159, v191\n"
            "v_pk_add_f32 v[144:145], v[144:145], 1.0 op_sel_hi:[1,0]\n"
            "v_pk_add_f32 v[146:147], v[146:147], 1.0 op_sel_hi:[1,0]\n"
            "v_pk_add_f32 v[148:149], v[148:149], 1.0 op_sel_hi:[1,0]\n"
            "v_pk_add_f32 v[150:151], v[150:151], 1.0 op_sel_hi:[1,0]\n"
            "v_pk_add_f32 v[152:153], v[152:153], 1.0 op_sel_hi:[1,0]\n"
            "v_pk_add_f32 v[154:155], v[154:155], 1.0 op_sel_hi:[1,0]\n"
            "v_pk_add_f32 v[156:157], v[156:157], 1.0 op_sel_hi:[1,0]\n"
            "v_pk_add_f32 v[158:159], v[158:159], 1.0 op_sel_hi:[1,0]\n"
            "v_rcp_f32 v144, v144\n"
            "v_rcp_f32 v145, v145\n"
            "v_rcp_f32 v146, v146\n"
            "v_rcp_f32 v147, v147\n"
            "v_rcp_f32 v148, v148\n"
            "v_rcp_f32 v149, v149\n"
            "v_rcp_f32 v150, v150\n"
            "v_rcp_f32 v151, v151\n"
            "v_rcp_f32 v152, v152\n"
            "v_rcp_f32 v153, v153\n"
            "v_rcp_f32 v154, v154\n"
            "v_rcp_f32 v155, v155\n"
            "v_rcp_f32 v156, v156\n"
            "v_rcp_f32 v157, v157\n"
            "v_rcp_f32 v158, v158\n"
            "v_rcp_f32 v159, v159\n"
            "v_pk_mul_f32 v[144:145], v[144:145], v[176:177]\n"
            "v_pk_mul_f32 v[146:147], v[146:147], v[178:179]\n"
            "v_pk_mul_f32 v[148:149], v[148:149], v[180:181]\n"
            "v_pk_mul_f32 v[150:151], v[150:151], v[182:183]\n"
            "v_pk_mul_f32 v[152:153], v[152:153], v[184:185]\n"
            "v_pk_mul_f32 v[154:155], v[154:155], v[186:187]\n"
            "v_pk_mul_f32 v[156:157], v[156:157], v[188:189]\n"
            "v_pk_mul_f32 v[158:159], v[158:159], v[190:191]\n"
            "s_branch Ldisp_%=\n"
            "Lfast_%=:\n"
            "v_mfma_f32_16x16x32_bf16 v[136:139], v[192:195], v[120:123], 0\n"
            "v_exp_f32 v160, v128\n"
            "v_add_u32 v211, 64, v211\n"
            "v_exp_f32 v161, v129\n"
            "s_waitcnt vmcnt(13) lgkmcnt(0)\n"
            "v_mfma_f32_4x4x4_16b_bf16 v[176:179], v[208:209], v[32:33], 0 cbsz:2\n"
            "v_exp_f32 v162, v130\n"
            "v_mfma_f32_4x4x4_16b_bf16 v[180:183], v[208:209], v[34:35], 0 cbsz:2\n"
            "v_exp_f32 v163, v131\n"
            "v_mfma_f32_4x4x4_16b_bf16 v[184:187], v[208:209], v[36:37], 0 cbsz:2\n"
            "v_exp_f32 v164, v132\n"
            "v_mfma_f32_4x4x4_16b_bf16 v[188:191], v[208:209], v[38:39], 0 cbsz:2\n"
            "v_exp_f32 v165, v133\n"
            "v_add_u32_dpp v219, v196, v214 quad_perm:[0,0,0,0] row_mask:0xf bank_mask:0xf\n"
            "v_mfma_f32_4x4x4_16b_bf16 v[204:207], v[202:203], v[198:199], 0\n"
            "v_exp_f32 v166, v134\n"
            "buffer_load_dwordx4 v[32:35], v219, %[rsx], 0 offen\n"
            "buffer_load_dwordx4 v[36:39], v219, %[rsx], 0 offen offset:256\n"
            "v_exp_f32 v167, v135\n"
            "v_pk_fma_f32 v[0:1], v[144:145], v[176:177], v[0:1] op_sel_hi:[0,1,1]\n"
            "v_pk_fma_f32 v[2:3], v[144:145], v[178:179], v[2:3] op_sel_hi:[0,1,1]\n"
            "v_pk_fma_f32 v[4:5], v[148:149], v[180:181], v[4:5] op_sel_hi:[0,1,1]\n"
            "v_pk_fma_f32 v[6:7], v[148:149], v[182:183], v[6:7] op_sel_hi:[0,1,1]\n"
            "v_pk_fma_f32 v[8:9], v[152:153], v[184:185], v[8:9] op_sel_hi:[0,1,1]\n"
            "v_pk_fma_f32 v[10:11], v[152:153], v[186:187], v[10:11] op_sel_hi:[0,1,1]\n"
            "v_pk_fma_f32 v[12:13], v[156:157], v[188:189], v[12:13] op_sel_hi:[0,1,1]\n"
            "v_pk_fma_f32 v[14:15], v[156:157], v[190:191], v[14:15] op_sel_hi:[0,1,1]\n"
            "v_pk_add_f32 v[160:161], v[160:161], 1.0 op_sel_hi:[1,0]\n"
            "v_pk_add_f32 v[162:163], v[162:163], 1.0 op_sel_hi:[1,0]\n"
            "v_pk_add_f32 v[164:165], v[164:165], 1.0 op_sel_hi:[1,0]\n"
            "v_pk_add_f32 v[166:167], v[166:167], 1.0 op_sel_hi:[1,0]\n"
            "v_mfma_f32_16x16x32_bf16 v[140:143], v[192:195], v[124:127], 0\n"
            "ds_read_b32 v230, v211\n"
            "s_waitcnt vmcnt(14)\n"
            "ds_bpermute_b32 v192, v234, v232\n"
            "ds_bpermute_b32 v193, v234, v233\n"
            "ds_bpermute_b32 v194, v234, v232 offset:4\n"
            "ds_bpermute_b32 v195, v234, v233 offset:4\n"
            "v_rcp_f32 v160, v160\n"
            "v_rcp_f32 v161, v161\n"
            "v_mfma_f32_4x4x4_16b_bf16 v[176:179], v[208:209], v[48:49], 0 cbsz:2 abid:1\n"
            "v_rcp_f32 v162, v162\n"
            "v_mfma_f32_4x4x4_16b_bf16 v[180:183], v[208:209], v[50:51], 0 cbsz:2 abid:1\n"
            "v_rcp_f32 v163, v163\n"
            "v_mfma_f32_4x4x4_16b_bf16 v[184:187], v[208:209], v[52:53], 0 cbsz:2 abid:1\n"
            "v_rcp_f32 v164, v164\n"
            "v_mfma_f32_4x4x4_16b_bf16 v[188:191], v[208:209], v[54:55], 0 cbsz:2 abid:1\n"
            "v_rcp_f32 v165, v165\n"
            "v_add_u32_dpp v220, v196, v214 quad_perm:[1,1,1,1] row_mask:0xf bank_mask:0xf\n"
            "v_mfma_f32_4x4x4_16b_bf16 v[204:207], v[200:201], v[198:199], v[204:207]\n"
            "v_rcp_f32 v166, v166\n"
            "buffer_load_dwordx4 v[48:51], v220, %[rsx], 0 offen\n"
            "buffer_load_dwordx4 v[52:55], v220, %[rsx], 0 offen offset:256\n"
            "v_rcp_f32 v167, v167\n"
            "v_pk_fma_f32 v[0:1], v[144:145], v[176:177], v[0:1] op_sel:[1,0,0]\n"
            "v_pk_fma_f32 v[2:3], v[144:145], v[178:179], v[2:3] op_sel:[1,0,0]\n"
            "v_pk_fma_f32 v[4:5], v[148:149], v[180:181], v[4:5] op_sel:[1,0,0]\n"
            "v_pk_fma_f32 v[6:7], v[148:149], v[182:183], v[6:7] op_sel:[1,0,0]\n"
            "v_pk_fma_f32 v[8:9], v[152:153], v[184:185], v[8:9] op_sel:[1,0,0]\n"
            "v_pk_fma_f32 v[10:11], v[152:153], v[186:187], v[10:11] op_sel:[1,0,0]\n"
            "v_pk_fma_f32 v[12:13], v[156:157], v[188:189], v[12:13] op_sel:[1,0,0]\n"
            "v_pk_fma_f32 v[14:15], v[156:157], v[190:191], v[14:15] op_sel:[1,0,0]\n"
            "v_pk_mul_f32 v[160:161], v[160:161], v[128:129]\n"
            "v_pk_mul_f32 v[162:163], v[162:163], v[130:131]\n"
            "v_pk_mul_f32 v[164:165], v[164:165], v[132:133]\n"
            "v_pk_mul_f32 v[166:167], v[166:167], v[134:135]\n"
            "s_waitcnt vmcnt(12) lgkmcnt(0)\n"
            "v_or_b32 v230, v230, v229\n"
            "buffer_load_dwordx2 v[232:233], v230, %[rse], 0 offen\n"
            "v_mfma_f32_16x16x32_bf16 v[128:131], v[192:195], v[96:99], 0\n"
            "v_exp_f32 v168, v136\n"
            "v_exp_f32 v169, v137\n"
            "v_mfma_f32_4x4x4_16b_bf16 v[176:179], v[208:209], v[64:65], 0 cbsz:2 abid:2\n"
            "v_exp_f32 v170, v138\n"
            "v_mfma_f32_4x4x4_16b_bf16 v[180:183], v[208:209], v[66:67], 0 cbsz:2 abid:2\n"
            "v_exp_f32 v171, v139\n"
            "v_mfma_f32_4x4x4_16b_bf16 v[184:187], v[208:209], v[68:69], 0 cbsz:2 abid:2\n"
            "v_exp_f32 v172, v140\n"
            "v_mfma_f32_4x4x4_16b_bf16 v[188:191], v[208:209], v[70:71], 0 cbsz:2 abid:2\n"
            "v_exp_f32 v173, v141\n"
            "v_add_u32_dpp v221, v196, v214 quad_perm:[2,2,2,2] row_mask:0xf bank_mask:0xf\n"
            "v_exp_f32 v174, v142\n"
            "buffer_load_dwordx4 v[64:67], v221, %[rsx], 0 offen\n"
            "buffer_load_dwordx4 v[68:71], v221, %[rsx], 0 offen offset:256\n"
            "v_exp_f32 v175, v143\n"
            "v_pk_fma_f32 v[0:1], v[146:147], v[176:177], v[0:1] op_sel_hi:[0,1,1]\n"
            "v_pk_fma_f32 v[2:3], v[146:147], v[178:179], v[2:3] op_sel_hi:[0,1,1]\n"
            "v_pk_fma_f32 v[4:5], v[150:151], v[180:181], v[4:5] op_sel_hi:[0,1,1]\n"
            "v_pk_fma_f32 v[6:7], v[150:151], v[182:183], v[6:7] op_sel_hi:[0,1,1]\n"
            "v_pk_fma_f32 v[8:9], v[154:155], v[184:185], v[8:9] op_sel_hi:[0,1,1]\n"
            "v_pk_fma_f32 v[10:11], v[154:155], v[186:187], v[10:11] op_sel_hi:[0,1,1]\n"
            "v_pk_fma_f32 v[12:13], v[158:159], v[188:189], v[12:13] op_sel_hi:[0,1,1]\n"
            "v_pk_fma_f32 v[14:15], v[158:159], v[190:191], v[14:15] op_sel_hi:[0,1,1]\n"
            "v_pk_add_f32 v[168:169], v[168:169], 1.0 op_sel_hi:[1,0]\n"
            "v_pk_add_f32 v[170:171], v[170:171], 1.0 op_sel_hi:[1,0]\n"
            "v_pk_add_f32 v[172:173], v[172:173], 1.0 op_sel_hi:[1,0]\n"
            "v_pk_add_f32 v[174:175], v[174:175], 1.0 op_sel_hi:[1,0]\n"
            "v_cvt_pk_bf16_f32 v204, v204, v205\n"
            "v_cvt_pk_bf16_f32 v205, v206, v207\n"
            "ds_write_b64 v212, v[204:205]\n"
            "v_mfma_f32_16x16x32_bf16 v[132:135], v[192:195], v[100:103], 0\n"
            "v_rcp_f32 v168, v168\n"
            "v_rcp_f32 v169, v169\n"
            "v_mfma_f32_4x4x4_16b_bf16 v[176:179], v[208:209], v[80:81], 0 cbsz:2 abid:3\n"
            "v_rcp_f32 v170, v170\n"
            "v_mfma_f32_4x4x4_16b_bf16 v[180:183], v[208:209], v[82:83], 0 cbsz:2 abid:3\n"
            "v_rcp_f32 v171, v171\n"
            "v_mfma_f32_4x4x4_16b_bf16 v[184:187], v[208:209], v[84:85], 0 cbsz:2 abid:3\n"
            "v_rcp_f32 v172, v172\n"
            "v_mfma_f32_4x4x4_16b_bf16 v[188:191], v[208:209], v[86:87], 0 cbsz:2 abid:3\n"
            "v_rcp_f32 v173, v173\n"
            "v_add_u32_dpp v222, v196, v214 quad_perm:[3,3,3,3] row_mask:0xf bank_mask:0xf\n"
            "v_rcp_f32 v174, v174\n"
            "buffer_load_dwordx4 v[80:83], v222, %[rsx], 0 offen\n"
            "buffer_load_dwordx4 v[84:87], v222, %[rsx], 0 offen offset:256\n"
            "v_rcp_f32 v175, v175\n"
            "v_pk_fma_f32 v[0:1], v[146:147], v[176:177], v[0:1] op_sel:[1,0,0]\n"
            "v_pk_fma_f32 v[2:3], v[146:147], v[178:179], v[2:3] op_sel:[1,0,0]\n"
            "v_pk_fma_f32 v[4:5], v[150:151], v[180:181], v[4:5] op_sel:[1,0,0]\n"
            "v_pk_fma_f32 v[6:7], v[150:151], v[182:183], v[6:7] op_sel:[1,0,0]\n"
            "v_pk_fma_f32 v[8:9], v[154:155], v[184:185], v[8:9] op_sel:[1,0,0]\n"
            "v_pk_fma_f32 v[10:11], v[154:155], v[186:187], v[10:11] op_sel:[1,0,0]\n"
            "v_pk_fma_f32 v[12:13], v[158:159], v[188:189], v[12:13] op_sel:[1,0,0]\n"
            "v_pk_fma_f32 v[14:15], v[158:159], v[190:191], v[14:15] op_sel:[1,0,0]\n"
            "v_pk_mul_f32 v[168:169], v[168:169], v[136:137]\n"
            "v_pk_mul_f32 v[170:171], v[170:171], v[138:139]\n"
            "v_pk_mul_f32 v[172:173], v[172:173], v[140:141]\n"
            "v_pk_mul_f32 v[174:175], v[174:175], v[142:143]\n"
            "v_add_u32 v210, 64, v210\n"
            "ds_read_b128 v[196:199], v210\n"
            "v_mfma_f32_16x16x32_bf16 v[136:139], v[192:195], v[104:107], 0\n"
            "v_exp_f32 v144, v128\n"
            "v_exp_f32 v145, v129\n"
            "s_waitcnt vmcnt(13)\n"
            "v_mfma_f32_4x4x4_16b_bf16 v[176:179], v[208:209], v[40:41], 0 cbsz:2\n"
            "v_exp_f32 v146, v130\n"
            "v_mfma_f32_4x4x4_16b_bf16 v[180:183], v[208:209], v[42:43], 0 cbsz:2\n"
            "v_exp_f32 v147, v131\n"
            "v_mfma_f32_4x4x4_16b_bf16 v[184:187], v[208:209], v[44:45], 0 cbsz:2\n"
            "v_exp_f32 v148, v132\n"
            "v_mfma_f32_4x4x4_16b_bf16 v[188:191], v[208:209], v[46:47], 0 cbsz:2\n"
            "v_exp_f32 v149, v133\n"
            "v_exp_f32 v150, v134\n"
            "buffer_load_dwordx4 v[40:43], v219, %[rsx], 0 offen offset:512\n"
            "buffer_load_dwordx4 v[44:47], v219, %[rsx], 0 offen offset:768\n"
            "v_exp_f32 v151, v135\n"
            "v_pk_fma_f32 v[16:17], v[160:161], v[176:177], v[16:17] op_sel_hi:[0,1,1]\n"
            "v_pk_fma_f32 v[18:19], v[160:161], v[178:179], v[18:19] op_sel_hi:[0,1,1]\n"
            "v_pk_fma_f32 v[20:21], v[164:165], v[180:181], v[20:21] op_sel_hi:[0,1,1]\n"
            "v_pk_fma_f32 v[22:23], v[164:165], v[182:183], v[22:23] op_sel_hi:[0,1,1]\n"
            "v_pk_fma_f32 v[24:25], v[168:169], v[184:185], v[24:25] op_sel_hi:[0,1,1]\n"
            "v_pk_fma_f32 v[26:27], v[168:169], v[186:187], v[26:27] op_sel_hi:[0,1,1]\n"
            "v_pk_fma_f32 v[28:29], v[172:173], v[188:189], v[28:29] op_sel_hi:[0,1,1]\n"
            "v_pk_fma_f32 v[30:31], v[172:173], v[190:191], v[30:31] op_sel_hi:[0,1,1]\n"
            "v_pk_add_f32 v[144:145], v[144:145], 1.0 op_sel_hi:[1,0]\n"
            "v_pk_add_f32 v[146:147], v[146:147], 1.0 op_sel_hi:[1,0]\n"
            "v_pk_add_f32 v[148:149], v[148:149], 1.0 op_sel_hi:[1,0]\n"
            "v_pk_add_f32 v[150:151], v[150:151], 1.0 op_sel_hi:[1,0]\n"
            "v_mfma_f32_16x16x32_bf16 v[140:143], v[192:195], v[108:111], 0\n"
            "v_rcp_f32 v144, v144\n"
            "v_rcp_f32 v145, v145\n"
            "v_mfma_f32_4x4x4_16b_bf16 v[176:179], v[208:209], v[56:57], 0 cbsz:2 abid:1\n"
            "v_rcp_f32 v146, v146\n"
            "v_mfma_f32_4x4x4_16b_bf16 v[180:183], v[208:209], v[58:59], 0 cbsz:2 abid:1\n"
            "v_rcp_f32 v147, v147\n"
            "v_mfma_f32_4x4x4_16b_bf16 v[184:187], v[208:209], v[60:61], 0 cbsz:2 abid:1\n"
            "v_rcp_f32 v148, v148\n"
            "v_mfma_f32_4x4x4_16b_bf16 v[188:191], v[208:209], v[62:63], 0 cbsz:2 abid:1\n"
            "v_rcp_f32 v149, v149\n"
            "v_rcp_f32 v150, v150\n"
            "buffer_load_dwordx4 v[56:59], v220, %[rsx], 0 offen offset:512\n"
            "buffer_load_dwordx4 v[60:63], v220, %[rsx], 0 offen offset:768\n"
            "v_rcp_f32 v151, v151\n"
            "v_pk_fma_f32 v[16:17], v[160:161], v[176:177], v[16:17] op_sel:[1,0,0]\n"
            "v_pk_fma_f32 v[18:19], v[160:161], v[178:179], v[18:19] op_sel:[1,0,0]\n"
            "v_pk_fma_f32 v[20:21], v[164:165], v[180:181], v[20:21] op_sel:[1,0,0]\n"
            "v_pk_fma_f32 v[22:23], v[164:165], v[182:183], v[22:23] op_sel:[1,0,0]\n"
            "v_pk_fma_f32 v[24:25], v[168:169], v[184:185], v[24:25] op_sel:[1,0,0]\n"
            "v_pk_fma_f32 v[26:27], v[168:169], v[186:187], v[26:27] op_sel:[1,0,0]\n"
            "v_pk_fma_f32 v[28:29], v[172:173], v[188:189], v[28:29] op_sel:[1,0,0]\n"
            "v_pk_fma_f32 v[30:31], v[172:173], v[190:191], v[30:31] op_sel:[1,0,0]\n"
            "v_pk_mul_f32 v[144:145], v[144:145], v[128:129]\n"
            "v_pk_mul_f32 v[146:147], v[146:147], v[130:131]\n"
            "v_pk_mul_f32 v[148:149], v[148:149], v[132:133]\n"
            "v_pk_mul_f32 v[150:151], v[150:151], v[134:135]\n"
            "v_mfma_f32_16x16x32_bf16 v[128:131], v[192:195], v[112:115], 0\n"
            "v_exp_f32 v152, v136\n"
            "v_exp_f32 v153, v137\n"
            "s_waitcnt vmcnt(13)\n"
            "v_mfma_f32_4x4x4_16b_bf16 v[176:179], v[208:209], v[72:73], 0 cbsz:2 abid:2\n"
            "v_exp_f32 v154, v138\n"
            "v_mfma_f32_4x4x4_16b_bf16 v[180:183], v[208:209], v[74:75], 0 cbsz:2 abid:2\n"
            "v_exp_f32 v155, v139\n"
            "v_mfma_f32_4x4x4_16b_bf16 v[184:187], v[208:209], v[76:77], 0 cbsz:2 abid:2\n"
            "v_exp_f32 v156, v140\n"
            "v_mfma_f32_4x4x4_16b_bf16 v[188:191], v[208:209], v[78:79], 0 cbsz:2 abid:2\n"
            "v_exp_f32 v157, v141\n"
            "v_exp_f32 v158, v142\n"
            "buffer_load_dwordx4 v[72:75], v221, %[rsx], 0 offen offset:512\n"
            "buffer_load_dwordx4 v[76:79], v221, %[rsx], 0 offen offset:768\n"
            "v_exp_f32 v159, v143\n"
            "v_pk_fma_f32 v[16:17], v[162:163], v[176:177], v[16:17] op_sel_hi:[0,1,1]\n"
            "v_pk_fma_f32 v[18:19], v[162:163], v[178:179], v[18:19] op_sel_hi:[0,1,1]\n"
            "v_pk_fma_f32 v[20:21], v[166:167], v[180:181], v[20:21] op_sel_hi:[0,1,1]\n"
            "v_pk_fma_f32 v[22:23], v[166:167], v[182:183], v[22:23] op_sel_hi:[0,1,1]\n"
            "v_pk_fma_f32 v[24:25], v[170:171], v[184:185], v[24:25] op_sel_hi:[0,1,1]\n"
            "v_pk_fma_f32 v[26:27], v[170:171], v[186:187], v[26:27] op_sel_hi:[0,1,1]\n"
            "v_pk_fma_f32 v[28:29], v[174:175], v[188:189], v[28:29] op_sel_hi:[0,1,1]\n"
            "v_pk_fma_f32 v[30:31], v[174:175], v[190:191], v[30:31] op_sel_hi:[0,1,1]\n"
            "v_pk_add_f32 v[152:153], v[152:153], 1.0 op_sel_hi:[1,0]\n"
            "v_pk_add_f32 v[154:155], v[154:155], 1.0 op_sel_hi:[1,0]\n"
            "v_pk_add_f32 v[156:157], v[156:157], 1.0 op_sel_hi:[1,0]\n"
            "v_pk_add_f32 v[158:159], v[158:159], 1.0 op_sel_hi:[1,0]\n"
            "v_mfma_f32_16x16x32_bf16 v[132:135], v[192:195], v[116:119], 0\n"
            "v_rcp_f32 v152, v152\n"
            "v_rcp_f32 v153, v153\n"
            "v_mfma_f32_4x4x4_16b_bf16 v[176:179], v[208:209], v[88:89], 0 cbsz:2 abid:3\n"
            "v_rcp_f32 v154, v154\n"
            "v_mfma_f32_4x4x4_16b_bf16 v[180:183], v[208:209], v[90:91], 0 cbsz:2 abid:3\n"
            "v_rcp_f32 v155, v155\n"
            "v_mfma_f32_4x4x4_16b_bf16 v[184:187], v[208:209], v[92:93], 0 cbsz:2 abid:3\n"
            "v_rcp_f32 v156, v156\n"
            "v_mfma_f32_4x4x4_16b_bf16 v[188:191], v[208:209], v[94:95], 0 cbsz:2 abid:3\n"
            "ds_read_b64 v[208:209], v213\n"
            "v_rcp_f32 v157, v157\n"
            "v_rcp_f32 v158, v158\n"
            "buffer_load_dwordx4 v[88:91], v222, %[rsx], 0 offen offset:512\n"
            "buffer_load_dwordx4 v[92:95], v222, %[rsx], 0 offen offset:768\n"
            "v_rcp_f32 v159, v159\n"
            "v_pk_fma_f32 v[16:17], v[162:163], v[176:177], v[16:17] op_sel:[1,0,0]\n"
            "v_pk_fma_f32 v[18:19], v[162:163], v[178:179], v[18:19] op_sel:[1,0,0]\n"
            "v_pk_fma_f32 v[20:21], v[166:167], v[180:181], v[20:21] op_sel:[1,0,0]\n"
            "v_pk_fma_f32 v[22:23], v[166:167], v[182:183], v[22:23] op_sel:[1,0,0]\n"
            "v_pk_fma_f32 v[24:25], v[170:171], v[184:185], v[24:25] op_sel:[1,0,0]\n"
            "v_pk_fma_f32 v[26:27], v[170:171], v[186:187], v[26:27] op_sel:[1,0,0]\n"
            "v_pk_fma_f32 v[28:29], v[174:175], v[188:189], v[28:29] op_sel:[1,0,0]\n"
            "v_pk_fma_f32 v[30:31], v[174:175], v[190:191], v[30:31] op_sel:[1,0,0]\n"
            "v_pk_mul_f32 v[152:153], v[152:153], v[136:137]\n"
            "v_pk_mul_f32 v[154:155], v[154:155], v[138:139]\n"
            "v_pk_mul_f32 v[156:157], v[156:157], v[140:141]\n"
            "v_pk_mul_f32 v[158:159], v[158:159], v[142:143]\n"
            "s_sub_u32 %[rem], %[rem], 4\n"
            "Lnext_%=:\n"
            "s_sub_u32 %[nst], %[nst], 1\n"
            "Ldisp_%=:\n"
            "s_bitcmp1_b32 %[evt], %[nst]\n"
            "s_cbranch_scc1 Lprio_%=\n"
            "Ld2_%=:\n"
            "s_cmp_gt_u32 %[rem], 4\n"
            "s_cbranch_scc1 Lfast_%=\n"
            "v_mfma_f32_16x16x32_bf16 v[136:139], v[192:195], v[120:123], 0\n"
            "v_exp_f32 v160, v128\n"
            "v_add_u32 v211, 64, v211\n"
            "v_exp_f32 v161, v129\n"
            "s_waitcnt vmcnt(13) lgkmcnt(0)\n"
            "v_mfma_f32_4x4x4_16b_bf16 v[176:179], v[208:209], v[32:33], 0 cbsz:2\n"
            "v_exp_f32 v162, v130\n"
            "v_mfma_f32_4x4x4_16b_bf16 v[180:183], v[208:209], v[34:35], 0 cbsz:2\n"
            "v_exp_f32 v163, v131\n"
            "v_mfma_f32_4x4x4_16b_bf16 v[184:187], v[208:209], v[36:37], 0 cbsz:2\n"
            "v_exp_f32 v164, v132\n"
            "v_mfma_f32_4x4x4_16b_bf16 v[188:191], v[208:209], v[38:39], 0 cbsz:2\n"
            "v_exp_f32 v165, v133\n"
            "v_add_u32_dpp v219, v196, v214 quad_perm:[0,0,0,0] row_mask:0xf bank_mask:0xf\n"
            "v_mfma_f32_4x4x4_16b_bf16 v[204:207], v[202:203], v[198:199], 0\n"
            "v_exp_f32 v166, v134\n"
            "buffer_load_dwordx4 v[32:35], v219, %[rsx], 0 offen\n"
            "buffer_load_dwordx4 v[36:39], v219, %[rsx], 0 offen offset:256\n"
            "v_exp_f32 v167, v135\n"
            "v_pk_fma_f32 v[0:1], v[144:145], v[176:177], v[0:1] op_sel_hi:[0,1,1]\n"
            "v_pk_fma_f32 v[2:3], v[144:145], v[178:179], v[2:3] op_sel_hi:[0,1,1]\n"
            "v_pk_fma_f32 v[4:5], v[148:149], v[180:181], v[4:5] op_sel_hi:[0,1,1]\n"
            "v_pk_fma_f32 v[6:7], v[148:149], v[182:183], v[6:7] op_sel_hi:[0,1,1]\n"
            "v_pk_fma_f32 v[8:9], v[152:153], v[184:185], v[8:9] op_sel_hi:[0,1,1]\n"
            "v_pk_fma_f32 v[10:11], v[152:153], v[186:187], v[10:11] op_sel_hi:[0,1,1]\n"
            "v_pk_fma_f32 v[12:13], v[156:157], v[188:189], v[12:13] op_sel_hi:[0,1,1]\n"
            "v_pk_fma_f32 v[14:15], v[156:157], v[190:191], v[14:15] op_sel_hi:[0,1,1]\n"
            "v_pk_add_f32 v[160:161], v[160:161], 1.0 op_sel_hi:[1,0]\n"
            "v_pk_add_f32 v[162:163], v[162:163], 1.0 op_sel_hi:[1,0]\n"
            "v_pk_add_f32 v[164:165], v[164:165], 1.0 op_sel_hi:[1,0]\n"
            "v_pk_add_f32 v[166:167], v[166:167], 1.0 op_sel_hi:[1,0]\n"
            "s_cmp_eq_u32 %[rem], 1\n"
            "s_cbranch_scc1 Lf00_%=\n"
            "Lr00_%=:\n"
            "v_mfma_f32_16x16x32_bf16 v[140:143], v[192:195], v[124:127], 0\n"
            "ds_read_b32 v230, v211\n"
            "s_waitcnt vmcnt(14)\n"
            "ds_bpermute_b32 v192, v234, v232\n"
            "ds_bpermute_b32 v193, v234, v233\n"
            "ds_bpermute_b32 v194, v234, v232 offset:4\n"
            "ds_bpermute_b32 v195, v234, v233 offset:4\n"
            "v_rcp_f32 v160, v160\n"
            "v_rcp_f32 v161, v161\n"
            "v_mfma_f32_4x4x4_16b_bf16 v[176:179], v[208:209], v[48:49], 0 cbsz:2 abid:1\n"
            "v_rcp_f32 v162, v162\n"
            "v_mfma_f32_4x4x4_16b_bf16 v[180:183], v[208:209], v[50:51], 0 cbsz:2 abid:1\n"
            "v_rcp_f32 v163, v163\n"
            "v_mfma_f32_4x4x4_16b_bf16 v[184:187], v[208:209], v[52:53], 0 cbsz:2 abid:1\n"
            "v_rcp_f32 v164, v164\n"
            "v_mfma_f32_4x4x4_16b_bf16 v[188:191], v[208:209], v[54:55], 0 cbsz:2 abid:1\n"
            "v_rcp_f32 v165, v165\n"
            "v_add_u32_dpp v220, v196, v214 quad_perm:[1,1,1,1] row_mask:0xf bank_mask:0xf\n"
            "v_mfma_f32_4x4x4_16b_bf16 v[204:207], v[200:201], v[198:199], v[204:207]\n"
            "v_rcp_f32 v166, v166\n"
            "buffer_load_dwordx4 v[48:51], v220, %[rsx], 0 offen\n"
            "buffer_load_dwordx4 v[52:55], v220, %[rsx], 0 offen offset:256\n"
            "v_rcp_f32 v167, v167\n"
            "v_pk_fma_f32 v[0:1], v[144:145], v[176:177], v[0:1] op_sel:[1,0,0]\n"
            "v_pk_fma_f32 v[2:3], v[144:145], v[178:179], v[2:3] op_sel:[1,0,0]\n"
            "v_pk_fma_f32 v[4:5], v[148:149], v[180:181], v[4:5] op_sel:[1,0,0]\n"
            "v_pk_fma_f32 v[6:7], v[148:149], v[182:183], v[6:7] op_sel:[1,0,0]\n"
            "v_pk_fma_f32 v[8:9], v[152:153], v[184:185], v[8:9] op_sel:[1,0,0]\n"
            "v_pk_fma_f32 v[10:11], v[152:153], v[186:187], v[10:11] op_sel:[1,0,0]\n"
            "v_pk_fma_f32 v[12:13], v[156:157], v[188:189], v[12:13] op_sel:[1,0,0]\n"
            "v_pk_fma_f32 v[14:15], v[156:157], v[190:191], v[14:15] op_sel:[1,0,0]\n"
            "v_pk_mul_f32 v[160:161], v[160:161], v[128:129]\n"
            "v_pk_mul_f32 v[162:163], v[162:163], v[130:131]\n"
            "v_pk_mul_f32 v[164:165], v[164:165], v[132:133]\n"
            "v_pk_mul_f32 v[166:167], v[166:167], v[134:135]\n"
            "s_cmp_eq_u32 %[rem], 2\n"
            "s_cbranch_scc1 Lf01_%=\n"
            "Lr01_%=:\n"
            "s_waitcnt vmcnt(12) lgkmcnt(0)\n"
            "v_or_b32 v230, v230, v229\n"
            "buffer_load_dwordx2 v[232:233], v230, %[rse], 0 offen\n"
            "v_mfma_f32_16x16x32_bf16 v[128:131], v[192:195], v[96:99], 0\n"
            "v_exp_f32 v168, v136\n"
            "v_exp_f32 v169, v137\n"
            "v_mfma_f32_4x4x4_16b_bf16 v[176:179], v[208:209], v[64:65], 0 cbsz:2 abid:2\n"
            "v_exp_f32 v170, v138\n"
            "v_mfma_f32_4x4x4_16b_bf16 v[180:183], v[208:209], v[66:67], 0 cbsz:2 abid:2\n"
            "v_exp_f32 v171, v139\n"
            "v_mfma_f32_4x4x4_16b_bf16 v[184:187], v[208:209], v[68:69], 0 cbsz:2 abid:2\n"
            "v_exp_f32 v172, v140\n"
            "v_mfma_f32_4x4x4_16b_bf16 v[188:191], v[208:209], v[70:71], 0 cbsz:2 abid:2\n"
            "v_exp_f32 v173, v141\n"
            "v_add_u32_dpp v221, v196, v214 quad_perm:[2,2,2,2] row_mask:0xf bank_mask:0xf\n"
            "v_exp_f32 v174, v142\n"
            "buffer_load_dwordx4 v[64:67], v221, %[rsx], 0 offen\n"
            "buffer_load_dwordx4 v[68:71], v221, %[rsx], 0 offen offset:256\n"
            "v_exp_f32 v175, v143\n"
            "v_pk_fma_f32 v[0:1], v[146:147], v[176:177], v[0:1] op_sel_hi:[0,1,1]\n"
            "v_pk_fma_f32 v[2:3], v[146:147], v[178:179], v[2:3] op_sel_hi:[0,1,1]\n"
            "v_pk_fma_f32 v[4:5], v[150:151], v[180:181], v[4:5] op_sel_hi:[0,1,1]\n"
            "v_pk_fma_f32 v[6:7], v[150:151], v[182:183], v[6:7] op_sel_hi:[0,1,1]\n"
            "v_pk_fma_f32 v[8:9], v[154:155], v[184:185], v[8:9] op_sel_hi:[0,1,1]\n"
            "v_pk_fma_f32 v[10:11], v[154:155], v[186:187], v[10:11] op_sel_hi:[0,1,1]\n"
            "v_pk_fma_f32 v[12:13], v[158:159], v[188:189], v[12:13] op_sel_hi:[0,1,1]\n"
            "v_pk_fma_f32 v[14:15], v[158:159], v[190:191], v[14:15] op_sel_hi:[0,1,1]\n"
            "v_pk_add_f32 v[168:169], v[168:169], 1.0 op_sel_hi:[1,0]\n"
            "v_pk_add_f32 v[170:171], v[170:171], 1.0 op_sel_hi:[1,0]\n"
            "v_pk_add_f32 v[172:173], v[172:173], 1.0 op_sel_hi:[1,0]\n"
            "v_pk_add_f32 v[174:175], v[174:175], 1.0 op_sel_hi:[1,0]\n"
            "v_cvt_pk_bf16_f32 v204, v204, v205\n"
            "v_cvt_pk_bf16_f32 v205, v206, v207\n"
            "ds_write_b64 v212, v[204:205]\n"
            "s_cmp_eq_u32 %[rem], 3\n"
            "s_cbranch_scc1 Lf02_%=\n"
            "Lr02_%=:\n"
            "v_mfma_f32_16x16x32_bf16 v[132:135], v[192:195], v[100:103], 0\n"
            "v_rcp_f32 v168, v168\n"
            "v_rcp_f32 v169, v169\n"
            "v_mfma_f32_4x4x4_16b_bf16 v[176:179], v[208:209], v[80:81], 0 cbsz:2 abid:3\n"
            "v_rcp_f32 v170, v170\n"
            "v_mfma_f32_4x4x4_16b_bf16 v[180:183], v[208:209], v[82:83], 0 cbsz:2 abid:3\n"
            "v_rcp_f32 v171, v171\n"
            "v_mfma_f32_4x4x4_16b_bf16 v[184:187], v[208:209], v[84:85], 0 cbsz:2 abid:3\n"
            "v_rcp_f32 v172, v172\n"
            "v_mfma_f32_4x4x4_16b_bf16 v[188:191], v[208:209], v[86:87], 0 cbsz:2 abid:3\n"
            "v_rcp_f32 v173, v173\n"
            "v_add_u32_dpp v222, v196, v214 quad_perm:[3,3,3,3] row_mask:0xf bank_mask:0xf\n"
            "v_rcp_f32 v174, v174\n"
            "buffer_load_dwordx4 v[80:83], v222, %[rsx], 0 offen\n"
            "buffer_load_dwordx4 v[84:87], v222, %[rsx], 0 offen offset:256\n"
            "v_rcp_f32 v175, v175\n"
            "v_pk_fma_f32 v[0:1], v[146:147], v[176:177], v[0:1] op_sel:[1,0,0]\n"
            "v_pk_fma_f32 v[2:3], v[146:147], v[178:179], v[2:3] op_sel:[1,0,0]\n"
            "v_pk_fma_f32 v[4:5], v[150:151], v[180:181], v[4:5] op_sel:[1,0,0]\n"
            "v_pk_fma_f32 v[6:7], v[150:151], v[182:183], v[6:7] op_sel:[1,0,0]\n"
            "v_pk_fma_f32 v[8:9], v[154:155], v[184:185], v[8:9] op_sel:[1,0,0]\n"
            "v_pk_fma_f32 v[10:11], v[154:155], v[186:187], v[10:11] op_sel:[1,0,0]\n"
            "v_pk_fma_f32 v[12:13], v[158:159], v[188:189], v[12:13] op_sel:[1,0,0]\n"
            "v_pk_fma_f32 v[14:15], v[158:159], v[190:191], v[14:15] op_sel:[1,0,0]\n"
            "v_pk_mul_f32 v[168:169], v[168:169], v[136:137]\n"
            "v_pk_mul_f32 v[170:171], v[170:171], v[138:139]\n"
            "v_pk_mul_f32 v[172:173], v[172:173], v[140:141]\n"
            "v_pk_mul_f32 v[174:175], v[174:175], v[142:143]\n"
            "s_cmp_eq_u32 %[rem], 4\n"
            "s_cbranch_scc1 Lf03_%=\n"
            "Lr03_%=:\n"
            "v_add_u32 v210, 64, v210\n"
            "ds_read_b128 v[196:199], v210\n"
            "v_mfma_f32_16x16x32_bf16 v[136:139], v[192:195], v[104:107], 0\n"
            "v_exp_f32 v144, v128\n"
            "v_exp_f32 v145, v129\n"
            "s_waitcnt vmcnt(13)\n"
            "v_mfma_f32_4x4x4_16b_bf16 v[176:179], v[208:209], v[40:41], 0 cbsz:2\n"
            "v_exp_f32 v146, v130\n"
            "v_mfma_f32_4x4x4_16b_bf16 v[180:183], v[208:209], v[42:43], 0 cbsz:2\n"
            "v_exp_f32 v147, v131\n"
            "v_mfma_f32_4x4x4_16b_bf16 v[184:187], v[208:209], v[44:45], 0 cbsz:2\n"
            "v_exp_f32 v148, v132\n"
            "v_mfma_f32_4x4x4_16b_bf16 v[188:191], v[208:209], v[46:47], 0 cbsz:2\n"
            "v_exp_f32 v149, v133\n"
            "v_exp_f32 v150, v134\n"
            "buffer_load_dwordx4 v[40:43], v219, %[rsx], 0 offen offset:512\n"
            "buffer_load_dwordx4 v[44:47], v219, %[rsx], 0 offen offset:768\n"
            "v_exp_f32 v151, v135\n"
            "v_pk_fma_f32 v[16:17], v[160:161], v[176:177], v[16:17] op_sel_hi:[0,1,1]\n"
            "v_pk_fma_f32 v[18:19], v[160:161], v[178:179], v[18:19] op_sel_hi:[0,1,1]\n"
            "v_pk_fma_f32 v[20:21], v[164:165], v[180:181], v[20:21] op_sel_hi:[0,1,1]\n"
            "v_pk_fma_f32 v[22:23], v[164:165], v[182:183], v[22:23] op_sel_hi:[0,1,1]\n"
            "v_pk_fma_f32 v[24:25], v[168:169], v[184:185], v[24:25] op_sel_hi:[0,1,1]\n"
            "v_pk_fma_f32 v[26:27], v[168:169], v[186:187], v[26:27] op_sel_hi:[0,1,1]\n"
            "v_pk_fma_f32 v[28:29], v[172:173], v[188:189], v[28:29] op_sel_hi:[0,1,1]\n"
            "v_pk_fma_f32 v[30:31], v[172:173], v[190:191], v[30:31] op_sel_hi:[0,1,1]\n"
            "v_pk_add_f32 v[144:145], v[144:145], 1.0 op_sel_hi:[1,0]\n"
            "v_pk_add_f32 v[146:147], v[146:147], 1.0 op_sel_hi:[1,0]\n"
            "v_pk_add_f32 v[148:149], v[148:149], 1.0 op_sel_hi:[1,0]\n"
            "v_pk_add_f32 v[150:151], v[150:151], 1.0 op_sel_hi:[1,0]\n"
            "s_cmp_eq_u32 %[rem], 1\n"
            "s_cbranch_scc1 Lf10_%=\n"
            "Lr10_%=:\n"
            "v_mfma_f32_16x16x32_bf16 v[140:143], v[192:195], v[108:111], 0\n"
            "v_rcp_f32 v144, v144\n"
            "v_rcp_f32 v145, v145\n"
            "v_mfma_f32_4x4x4_16b_bf16 v[176:179], v[208:209], v[56:57], 0 cbsz:2 abid:1\n"
            "v_rcp_f32 v146, v146\n"
            "v_mfma_f32_4x4x4_16b_bf16 v[180:183], v[208:209], v[58:59], 0 cbsz:2 abid:1\n"
            "v_rcp_f32 v147, v147\n"
            "v_mfma_f32_4x4x4_16b_bf16 v[184:187], v[208:209], v[60:61], 0 cbsz:2 abid:1\n"
            "v_rcp_f32 v148, v148\n"
            "v_mfma_f32_4x4x4_16b_bf16 v[188:191], v[208:209], v[62:63], 0 cbsz:2 abid:1\n"
            "v_rcp_f32 v149, v149\n"
            "v_rcp_f32 v150, v150\n"
            "buffer_load_dwordx4 v[56:59], v220, %[rsx], 0 offen offset:512\n"
            "buffer_load_dwordx4 v[60:63], v220, %[rsx], 0 offen offset:768\n"
            "v_rcp_f32 v151, v151\n"
            "v_pk_fma_f32 v[16:17], v[160:161], v[176:177], v[16:17] op_sel:[1,0,0]\n"
            "v_pk_fma_f32 v[18:19], v[160:161], v[178:179], v[18:19] op_sel:[1,0,0]\n"
            "v_pk_fma_f32 v[20:21], v[164:165], v[180:181], v[20:21] op_sel:[1,0,0]\n"
            "v_pk_fma_f32 v[22:23], v[164:165], v[182:183], v[22:23] op_sel:[1,0,0]\n"
            "v_pk_fma_f32 v[24:25], v[168:169], v[184:185], v[24:25] op_sel:[1,0,0]\n"
            "v_pk_fma_f32 v[26:27], v[168:169], v[186:187], v[26:27] op_sel:[1,0,0]\n"
            "v_pk_fma_f32 v[28:29], v[172:173], v[188:189], v[28:29] op_sel:[1,0,0]\n"
            "v_pk_fma_f32 v[30:31], v[172:173], v[190:191], v[30:31] op_sel:[1,0,0]\n"
            "v_pk_mul_f32 v[144:145], v[144:145], v[128:129]\n"
            "v_pk_mul_f32 v[146:147], v[146:147], v[130:131]\n"
            "v_pk_mul_f32 v[148:149], v[148:149], v[132:133]\n"
            "v_pk_mul_f32 v[150:151], v[150:151], v[134:135]\n"
            "s_cmp_eq_u32 %[rem], 2\n"
            "s_cbranch_scc1 Lf11_%=\n"
            "Lr11_%=:\n"
            "v_mfma_f32_16x16x32_bf16 v[128:131], v[192:195], v[112:115], 0\n"
            "v_exp_f32 v152, v136\n"
            "v_exp_f32 v153, v137\n"
            "s_waitcnt vmcnt(13)\n"
            "v_mfma_f32_4x4x4_16b_bf16 v[176:179], v[208:209], v[72:73], 0 cbsz:2 abid:2\n"
            "v_exp_f32 v154, v138\n"
            "v_mfma_f32_4x4x4_16b_bf16 v[180:183], v[208:209], v[74:75], 0 cbsz:2 abid:2\n"
            "v_exp_f32 v155, v139\n"
            "v_mfma_f32_4x4x4_16b_bf16 v[184:187], v[208:209], v[76:77], 0 cbsz:2 abid:2\n"
            "v_exp_f32 v156, v140\n"
            "v_mfma_f32_4x4x4_16b_bf16 v[188:191], v[208:209], v[78:79], 0 cbsz:2 abid:2\n"
            "v_exp_f32 v157, v141\n"
            "v_exp_f32 v158, v142\n"
            "buffer_load_dwordx4 v[72:75], v221, %[rsx], 0 offen offset:512\n"
            "buffer_load_dwordx4 v[76:79], v221, %[rsx], 0 offen offset:768\n"
            "v_exp_f32 v159, v143\n"
            "v_pk_fma_f32 v[16:17], v[162:163], v[176:177], v[16:17] op_sel_hi:[0,1,1]\n"
            "v_pk_fma_f32 v[18:19], v[162:163], v[178:179], v[18:19] op_sel_hi:[0,1,1]\n"
            "v_pk_fma_f32 v[20:21], v[166:167], v[180:181], v[20:21] op_sel_hi:[0,1,1]\n"
            "v_pk_fma_f32 v[22:23], v[166:167], v[182:183], v[22:23] op_sel_hi:[0,1,1]\n"
            "v_pk_fma_f32 v[24:25], v[170:171], v[184:185], v[24:25] op_sel_hi:[0,1,1]\n"
            "v_pk_fma_f32 v[26:27], v[170:171], v[186:187], v[26:27] op_sel_hi:[0,1,1]\n"
            "v_pk_fma_f32 v[28:29], v[174:175], v[188:189], v[28:29] op_sel_hi:[0,1,1]\n"
            "v_pk_fma_f32 v[30:31], v[174:175], v[190:191], v[30:31] op_sel_hi:[0,1,1]\n"
            "v_pk_add_f32 v[152:153], v[152:153], 1.0 op_sel_hi:[1,0]\n"
            "v_pk_add_f32 v[154:155], v[154:155], 1.0 op_sel_hi:[1,0]\n"
            "v_pk_add_f32 v[156:157], v[156:157], 1.0 op_sel_hi:[1,0]\n"
            "v_pk_add_f32 v[158:159], v[158:159], 1.0 op_sel_hi:[1,0]\n"
            "s_cmp_eq_u32 %[rem], 3\n"
            "s_cbranch_scc1 Lf12_%=\n"
            "Lr12_%=:\n"
            "v_mfma_f32_16x16x32_bf16 v[132:135], v[192:195], v[116:119], 0\n"
            "v_rcp_f32 v152, v152\n"
            "v_rcp_f32 v153, v153\n"
            "v_mfma_f32_4x4x4_16b_bf16 v[176:179], v[208:209], v[88:89], 0 cbsz:2 abid:3\n"
            "v_rcp_f32 v154, v154\n"
            "v_mfma_f32_4x4x4_16b_bf16 v[180:183], v[208:209], v[90:91], 0 cbsz:2 abid:3\n"
            "v_rcp_f32 v155, v155\n"
            "v_mfma_f32_4x4x4_16b_bf16 v[184:187], v[208:209], v[92:93], 0 cbsz:2 abid:3\n"
            "v_rcp_f32 v156, v156\n"
            "v_mfma_f32_4x4x4_16b_bf16 v[188:191], v[208:209], v[94:95], 0 cbsz:2 abid:3\n"
            "ds_read_b64 v[208:209], v213\n"
            "v_rcp_f32 v157, v157\n"
            "v_rcp_f32 v158, v158\n"
            "buffer_load_dwordx4 v[88:91], v222, %[rsx], 0 offen offset:512\n"
            "buffer_load_dwordx4 v[92:95], v222, %[rsx], 0 offen offset:768\n"
            "v_rcp_f32 v159, v159\n"
            "v_pk_fma_f32 v[16:17], v[162:163], v[176:177], v[16:17] op_sel:[1,0,0]\n"
            "v_pk_fma_f32 v[18:19], v[162:163], v[178:179], v[18:19] op_sel:[1,0,0]\n"
            "v_pk_fma_f32 v[20:21], v[166:167], v[180:181], v[20:21] op_sel:[1,0,0]\n"
            "v_pk_fma_f32 v[22:23], v[166:167], v[182:183], v[22:23] op_sel:[1,0,0]\n"
            "v_pk_fma_f32 v[24:25], v[170:171], v[184:185], v[24:25] op_sel:[1,0,0]\n"
            "v_pk_fma_f32 v[26:27], v[170:171], v[186:187], v[26:27] op_sel:[1,0,0]\n"
            "v_pk_fma_f32 v[28:29], v[174:175], v[188:189], v[28:29] op_sel:[1,0,0]\n"
            "v_pk_fma_f32 v[30:31], v[174:175], v[190:191], v[30:31] op_sel:[1,0,0]\n"
            "v_pk_mul_f32 v[152:153], v[152:153], v[136:137]\n"
            "v_pk_mul_f32 v[154:155], v[154:155], v[138:139]\n"
            "v_pk_mul_f32 v[156:157], v[156:157], v[140:141]\n"
            "v_pk_mul_f32 v[158:159], v[158:159], v[142:143]\n"
            "s_cmp_eq_u32 %[rem], 4\n"
            "s_cbranch_scc1 Lf13_%=\n"
            "Lr13_%=:\n"
            "s_and_b32 %[tmp], %[lens], 0xff\n"
            "s_add_u32 %[rem], %[rem], %[tmp]\n"
            "s_lshr_b32 %[lens], %[lens], 8\n"
            "s_lshr_b32 %[fm], %[fm], 4\n"
            "s_bitcmp1_b32 %[fm], 3\n"
            "v_mov_b32 v215, v216\n"
            "s_cbranch_scc0 Lnext_%=\n"
            "s_sub_u32 %[pb], %[nst], 2\n"
            "s_bitset1_b32 %[evt], %[pb]\n"
            "s_branch Lnext_%=\n"
            "Lpub_%=:\n"
            "s_waitcnt vmcnt(17)\n"
            "v_mov_b32 v176, %[cnt]\n"
            "v_mov_b32 v177, 1\n"
            "s_mov_b64 exec, 1\n"
            "ds_add_rtn_u32 v177, v176, v177\n"
            "s_waitcnt lgkmcnt(0)\n"
            "v_readfirstlane_b32 %[tmp], v177\n"
            "s_mov_b64 exec, 0xff\n"
            "v_mov_b32 v176, %[tag]\n"
            "v_mov_b32 v177, %[tag]\n"
            "v_mov_b32 v178, %[tag]\n"
            "v_mov_b32 v179, %[tag]\n"
            "s_cmp_lg_u32 %[tmp], 7\n"
            "s_cbranch_scc1 Lpub2_%=\n"
            "global_store_dwordx4 v214, v[176:179], %[flg] sc1\n"
            "Lpub2_%=:\n"
            "s_mov_b64 exec, -1\n"
            "s_branch Lpr2_%=\n"
            "Lprio_%=:\n"
            "s_cmp_eq_u32 %[nst], %[pb]\n"
            "s_cbranch_scc1 Lpub_%=\n"
            "Lpr2_%=:\n"
            "s_cmp_eq_u32 %[nst], 1\n"
            "s_cbranch_scc1 Llast_%=\n"
            "s_lshl2_add_u32 %[tmp], %[nst], %[c1]\n"
            "v_bfe_u32 v176, v214, 4, 2\n"
            "v_lshrrev_b32 v177, 6, v214\n"
            "v_sub_u32 v176, %[tmp], v176\n"
            "v_sub_u32 v177, %[tmp], v177\n"
            "s_min_u32 %[tmp], %[nst], 15\n"
            "v_cmp_gt_u32 vcc, 4, v176\n"
            "v_cmp_gt_u32 %[ra], 4, v177\n"
            "v_add_u32 v176, %[c2], v176\n"
            "v_add_u32 v177, %[c2], v177\n"
            "v_cndmask_b32 v178, 0, v236, vcc\n"
            "v_cndmask_b32 v179, 0, v236, %[ra]\n"
            "v_add_u32 v210, v210, v178\n"
            "v_add_u32 v211, v211, v179\n"
            "v_cmp_gt_u32 vcc, 4, v176\n"
            "v_cmp_gt_u32 %[ra], 4, v177\n"
            "s_nop 1\n"
            "v_cndmask_b32 v178, 0, v237, vcc\n"
            "v_cndmask_b32 v179, 0, v237, %[ra]\n"
            "v_add_u32 v210, v210, v178\n"
            "v_add_u32 v211, v211, v179\n"
            "s_lshl_b32 %[tmp], %[tmp], 1\n"
            "s_lshr_b32 %[tmp], %[ptab], %[tmp]\n"
            "s_bitcmp1_b32 %[tmp], 1\n"
            "s_cbranch_scc1 Lq2_%=\n"
            "s_bitcmp1_b32 %[tmp], 0\n"
            "s_setprio 0\n"
            "s_cbranch_scc0 Lq9_%=\n"
            "s_setprio 1\n"
            "s_branch Lq9_%=\n"
            "Lq2_%=:\n"
            "s_bitcmp1_b32 %[tmp], 0\n"
            "s_setprio 2\n"
            "s_cbranch_scc0 Lq9_%=\n"
            "s_setprio 3\n"
            "Lq9_%=:\n"
            "s_cmp_lg_u32 %[nst], 2\n"
            "s_cbranch_scc1 Ld2_%=\n"
            "v_mov_b32 v223, 0\n"
            "global_load_dword v223, v223, %[pfl] sc1\n"
            "s_branch Ld2_%=\n"
            "Lf00_%=:\n"
            "s_call_b64 %[ra], Lflush0_%=\n"
            "s_branch Lr00_%=\n"
            "Lf01_%=:\n"
            "s_call_b64 %[ra], Lflush0_%=\n"
            "s_branch Lr01_%=\n"
            "Lf02_%=:\n"
            "s_call_b64 %[ra], Lflush0_%=\n"
            "s_branch Lr02_%=\n"
            "Lf03_%=:\n"
            "s_call_b64 %[ra], Lflush0_%=\n"
            "s_branch Lr03_%=\n"
            "Lflush0_%=:\n"
            "v_mov_b32 v184, %[pfa]\n"
            "s_bitcmp0_b32 %[fm], 0\n"
            "s_cbranch_scc1 Lfa0_%=\n"
            "Lfw_%=:\n"
            "ds_read_b32 v185, v184\n"
            "ds_read_b128 v[176:179], v231 offset:0\n"
            "ds_read_b128 v[180:183], v231 offset:1024\n"
            "s_waitcnt lgkmcnt(0)\n"
            "v_cmp_eq_u32 vcc, 0, v185\n"
            "s_cbranch_vccnz Lfw_%=\n"
            "v_mfma_f32_4x4x4_16b_bf16 v[0:3], v[224:225], v[176:177], v[0:3]\n"
            "v_mfma_f32_4x4x4_16b_bf16 v[4:7], v[224:225], v[178:179], v[4:7]\n"
            "v_mfma_f32_4x4x4_16b_bf16 v[8:11], v[224:225], v[180:181], v[8:11]\n"
            "v_mfma_f32_4x4x4_16b_bf16 v[12:15], v[224:225], v[182:183], v[12:15]\n"
            "s_nop 1\n"
            "Lfa0_%=:\n"
            "v_cvt_pk_bf16_f32 v176, v0, v1\n"
            "v_cvt_pk_bf16_f32 v177, v2, v3\n"
            "v_cvt_pk_bf16_f32 v178, v4, v5\n"
            "v_cvt_pk_bf16_f32 v179, v6, v7\n"
            "v_cvt_pk_bf16_f32 v180, v8, v9\n"
            "v_cvt_pk_bf16_f32 v181, v10, v11\n"
            "v_cvt_pk_bf16_f32 v182, v12, v13\n"
            "v_cvt_pk_bf16_f32 v183, v14, v15\n"
            "s_bitcmp1_b32 %[fm], 1\n"
            "s_cbranch_scc1 Lpark0_%=\n"
            "buffer_store_dwordx4 v[176:179], v215, %[rsw], 0 offen offset:0 sc1\n"
            "buffer_store_dwordx4 v[180:183], v215, %[rsw], 0 offen offset:256 sc1\n"
            "s_branch Lzero0_%=\n"
            "Lpark0_%=:\n"
            "ds_write_b128 v231, v[176:179] offset:0\n"
            "ds_write_b128 v231, v[180:183] offset:1024\n"
            "Lzero0_%=:\n"
            "v_mov_b64 v[0:1], 0\n"
            "v_mov_b64 v[2:3], 0\n"
            "v_mov_b64 v[4:5], 0\n"
            "v_mov_b64 v[6:7], 0\n"
            "v_mov_b64 v[8:9], 0\n"
            "v_mov_b64 v[10:11], 0\n"
            "v_mov_b64 v[12:13], 0\n"
            "v_mov_b64 v[14:15], 0\n"
            "s_setpc_b64 %[ra]\n"
            "Lf10_%=:\n"
            "s_call_b64 %[ra], Lflush1_%=\n"
            "s_branch Lr10_%=\n"
            "Lf11_%=:\n"
            "s_call_b64 %[ra], Lflush1_%=\n"
            "s_branch Lr11_%=\n"
            "Lf12_%=:\n"
            "s_call_b64 %[ra], Lflush1_%=\n"
            "s_branch Lr12_%=\n"
            "Lf13_%=:\n"
            "s_call_b64 %[ra], Lflush1_%=\n"
            "s_branch Lr13_%=\n"
            "Lflush1_%=:\n"
            "v_mov_b32 v184, %[pfa]\n"
            "s_bitcmp0_b32 %[fm], 0\n"
            "s_cbranch_scc1 Lfa1_%=\n"
            "ds_read_b128 v[176:179], v231 offset:2048\n"
            "ds_read_b128 v[180:183], v231 offset:3072\n"
            "s_waitcnt lgkmcnt(0)\n"
            "v_mfma_f32_4x4x4_16b_bf16 v[16:19], v[224:225], v[176:177], v[16:19]\n"
            "v_mfma_f32_4x4x4_16b_bf16 v[20:23], v[224:225], v[178:179], v[20:23]\n"
            "v_mfma_f32_4x4x4_16b_bf16 v[24:27], v[224:225], v[180:181], v[24:27]\n"
            "v_mfma_f32_4x4x4_16b_bf16 v[28:31], v[224:225], v[182:183], v[28:31]\n"
            "s_nop 1\n"
            "Lfa1_%=:\n"
            "v_cvt_pk_bf16_f32 v176, v16, v17\n"
            "v_cvt_pk_bf16_f32 v177, v18, v19\n"
            "v_cvt_pk_bf16_f32 v178, v20, v21\n"
            "v_cvt_pk_bf16_f32 v179, v22, v23\n"
            "v_cvt_pk_bf16_f32 v180, v24, v25\n"
            "v_cvt_pk_bf16_f32 v181, v26, v27\n"
            "v_cvt_pk_bf16_f32 v182, v28, v29\n"
            "v_cvt_pk_bf16_f32 v183, v30, v31\n"
            "s_bitcmp1_b32 %[fm], 1\n"
            "s_cbranch_scc1 Lpark1_%=\n"
            "buffer_store_dwordx4 v[176:179], v215, %[rsw], 0 offen offset:512 sc1\n"
            "buffer_store_dwordx4 v[180:183], v215, %[rsw], 0 offen offset:768 sc1\n"
            "s_branch Lzero1_%=\n"
            "Lpark1_%=:\n"
            "ds_write_b128 v231, v[176:179] offset:2048\n"
            "ds_write_b128 v231, v[180:183] offset:3072\n"
            "ds_write_b32 v184, v184\n"
            "Lzero1_%=:\n"
            "v_mov_b64 v[16:17], 0\n"
            "v_mov_b64 v[18:19], 0\n"
            "v_mov_b64 v[20:21], 0\n"
            "v_mov_b64 v[22:23], 0\n"
            "v_mov_b64 v[24:25], 0\n"
            "v_mov_b64 v[26:27], 0\n"
            "v_mov_b64 v[28:29], 0\n"
            "v_mov_b64 v[30:31], 0\n"
            "s_setpc_b64 %[ra]\n"
            "Lspin_%=:\n"
            "s_mov_b32 %[lens], %[spin]\n"
            "Lsp1_%=:\n"
            "v_readfirstlane_b32 %[nst], v223\n"
            "s_sub_u32 %[lens], %[lens], 1\n"
            "s_cmp_eq_u32 %[lens], 0\n"
            "s_cbranch_scc1 Lsp2_%=\n"
            "s_cmp_eq_u32 %[nst], %[tag]\n"
            "s_cbranch_scc1 Lsp2_%=\n"
            "s_sleep 1\n"
            "v_mov_b32 v223, 0\n"
            "global_load_dword v223, v223, %[pfl] sc1\n"
            "s_waitcnt vmcnt(0)\n"
            "s_branch Lsp1_%=\n"
            "Lsp2_%=:\n"
            "buffer_load_dwordx4 v[32:35], v217, %[rsw], %[oagg] offen offset:0 sc1\n"
            "buffer_load_dwordx4 v[36:39], v217, %[rsw], %[oagg] offen offset:256 sc1\n"
            "buffer_load_dwordx4 v[48:51], v217, %[rsw], %[oagg] offen offset:512 sc1\n"
            "buffer_load_dwordx4 v[52:55], v217, %[rsw], %[oagg] offen offset:768 sc1\n"
            "s_branch Lgo_%=\n"
            "Llast_%=:\n"
            "s_waitcnt vmcnt(16)\n"
            "v_readfirstlane_b32 %[nst], v223\n"
            "v_mov_b32 v222, v217\n"
            "s_cmp_eq_u32 %[nst], %[tag]\n"
            "s_cbranch_scc1 Lp4_%=\n"
            "v_bfrev_b32 v222, 1\n"
            "v_mov_b32 v223, 0\n"
            "global_load_dword v223, v223, %[pfl] sc1\n"
            "Lp4_%=:\n"
            "v_mfma_f32_16x16x32_bf16 v[136:139], v[192:195], v[120:123], 0\n"
            "v_exp_f32 v160, v128\n"
            "v_exp_f32 v161, v129\n"
            "s_waitcnt vmcnt(13) lgkmcnt(0)\n"
            "v_mfma_f32_4x4x4_16b_bf16 v[176:179], v[208:209], v[32:33], 0 cbsz:2\n"
            "v_exp_f32 v162, v130\n"
            "v_mfma_f32_4x4x4_16b_bf16 v[180:183], v[208:209], v[34:35], 0 cbsz:2\n"
            "v_exp_f32 v163, v131\n"
            "v_mfma_f32_4x4x4_16b_bf16 v[184:187], v[208:209], v[36:37], 0 cbsz:2\n"
            "v_exp_f32 v164, v132\n"
            "v_mfma_f32_4x4x4_16b_bf16 v[188:191], v[208:209], v[38:39], 0 cbsz:2\n"
            "v_exp_f32 v165, v133\n"
            "v_exp_f32 v166, v134\n"
            "buffer_load_dwordx4 v[32:35], v222, %[rsw], %[oagg] offen offset:0 sc1\n"
            "buffer_load_dwordx4 v[36:39], v222, %[rsw], %[oagg] offen offset:256 sc1\n"
            "ds_read_b128 v[236:239], v228 offset:0\n"
            "ds_read_b128 v[240:243], v228 offset:1024\n"
            "ds_read_b128 v[244:247], v228 offset:2048\n"
            "ds_read_b128 v[248:251], v228 offset:3072\n"
            "ds_read_b128 v[96:99], v228 offset:4096\n"
            "v_exp_f32 v167, v135\n"
            "v_pk_fma_f32 v[0:1], v[144:145], v[176:177], v[0:1] op_sel_hi:[0,1,1]\n"
            "v_pk_fma_f32 v[2:3], v[144:145], v[178:179], v[2:3] op_sel_hi:[0,1,1]\n"
            "v_pk_fma_f32 v[4:5], v[148:149], v[180:181], v[4:5] op_sel_hi:[0,1,1]\n"
            "v_pk_fma_f32 v[6:7], v[148:149], v[182:183], v[6:7] op_sel_hi:[0,1,1]\n"
            "v_pk_fma_f32 v[8:9], v[152:153], v[184:185], v[8:9] op_sel_hi:[0,1,1]\n"
            "v_pk_fma_f32 v[10:11], v[152:153], v[186:187], v[10:11] op_sel_hi:[0,1,1]\n"
            "v_pk_fma_f32 v[12:13], v[156:157], v[188:189], v[12:13] op_sel_hi:[0,1,1]\n"
            "v_pk_fma_f32 v[14:15], v[156:157], v[190:191], v[14:15] op_sel_hi:[0,1,1]\n"
            "v_pk_add_f32 v[160:161], v[160:161], 1.0 op_sel_hi:[1,0]\n"
            "v_pk_add_f32 v[162:163], v[162:163], 1.0 op_sel_hi:[1,0]\n"
            "v_pk_add_f32 v[164:165], v[164:165], 1.0 op_sel_hi:[1,0]\n"
            "v_pk_add_f32 v[166:167], v[166:167], 1.0 op_sel_hi:[1,0]\n"
            "v_mfma_f32_16x16x32_bf16 v[140:143], v[192:195], v[124:127], 0\n"
            "v_rcp_f32 v160, v160\n"
            "v_rcp_f32 v161, v161\n"
            "v_mfma_f32_4x4x4_16b_bf16 v[176:179], v[208:209], v[48:49], 0 cbsz:2 abid:1\n"
            "v_rcp_f32 v162, v162\n"
            "v_mfma_f32_4x4x4_16b_bf16 v[180:183], v[208:209], v[50:51], 0 cbsz:2 abid:1\n"
            "v_rcp_f32 v163, v163\n"
            "v_mfma_f32_4x4x4_16b_bf16 v[184:187], v[208:209], v[52:53], 0 cbsz:2 abid:1\n"
            "v_rcp_f32 v164, v164\n"
            "v_mfma_f32_4x4x4_16b_bf16 v[188:191], v[208:209], v[54:55], 0 cbsz:2 abid:1\n"
            "v_rcp_f32 v165, v165\n"
            "v_rcp_f32 v166, v166\n"
            "buffer_load_dwordx4 v[48:51], v222, %[rsw], %[oagg] offen offset:512 sc1\n"
            "buffer_load_dwordx4 v[52:55], v222, %[rsw], %[oagg] offen offset:768 sc1\n"
            "ds_read_b128 v[100:103], v228 offset:5120\n"
            "ds_read_b128 v[104:107], v228 offset:6144\n"
            "ds_read_b128 v[108:111], v228 offset:7168\n"
            "ds_read_b128 v[112:115], v228 offset:8192\n"
            "ds_read_b128 v[116:119], v228 offset:9216\n"
            "ds_read_b128 v[196:199], v228 offset:10240\n"
            "v_rcp_f32 v167, v167\n"
            "v_pk_fma_f32 v[0:1], v[144:145], v[176:177], v[0:1] op_sel:[1,0,0]\n"
            "v_pk_fma_f32 v[2:3], v[144:145], v[178:179], v[2:3] op_sel:[1,0,0]\n"
            "v_pk_fma_f32 v[4:5], v[148:149], v[180:181], v[4:5] op_sel:[1,0,0]\n"
            "v_pk_fma_f32 v[6:7], v[148:149], v[182:183], v[6:7] op_sel:[1,0,0]\n"
            "v_pk_fma_f32 v[8:9], v[152:153], v[184:185], v[8:9] op_sel:[1,0,0]\n"
            "v_pk_fma_f32 v[10:11], v[152:153], v[186:187], v[10:11] op_sel:[1,0,0]\n"
            "v_pk_fma_f32 v[12:13], v[156:157], v[188:189], v[12:13] op_sel:[1,0,0]\n"
            "v_pk_fma_f32 v[14:15], v[156:157], v[190:191], v[14:15] op_sel:[1,0,0]\n"
            "v_pk_mul_f32 v[160:161], v[160:161], v[128:129]\n"
            "v_pk_mul_f32 v[162:163], v[162:163], v[130:131]\n"
            "v_pk_mul_f32 v[164:165], v[164:165], v[132:133]\n"
            "v_pk_mul_f32 v[166:167], v[166:167], v[134:135]\n"
            "v_exp_f32 v168, v136\n"
            "v_exp_f32 v169, v137\n"
            "s_waitcnt vmcnt(12)\n"
            "v_mfma_f32_4x4x4_16b_bf16 v[176:179], v[208:209], v[64:65], 0 cbsz:2 abid:2\n"
            "v_exp_f32 v170, v138\n"
            "v_mfma_f32_4x4x4_16b_bf16 v[180:183], v[208:209], v[66:67], 0 cbsz:2 abid:2\n"
            "v_exp_f32 v171, v139\n"
            "v_mfma_f32_4x4x4_16b_bf16 v[184:187], v[208:209], v[68:69], 0 cbsz:2 abid:2\n"
            "v_exp_f32 v172, v140\n"
            "v_mfma_f32_4x4x4_16b_bf16 v[188:191], v[208:209], v[70:71], 0 cbsz:2 abid:2\n"
            "v_exp_f32 v173, v141\n"
            "v_exp_f32 v174, v142\n"
            "ds_read_b128 v[200:203], v228 offset:11264\n"
            "ds_read_b128 v[204:207], v228 offset:12288\n"
            "ds_read_b128 v[232:235], v228 offset:13312\n"
            "v_exp_f32 v175, v143\n"
            "v_pk_fma_f32 v[0:1], v[146:147], v[176:177], v[0:1] op_sel_hi:[0,1,1]\n"
            "v_pk_fma_f32 v[2:3], v[146:147], v[178:179], v[2:3] op_sel_hi:[0,1,1]\n"
            "v_pk_fma_f32 v[4:5], v[150:151], v[180:181], v[4:5] op_sel_hi:[0,1,1]\n"
            "v_pk_fma_f32 v[6:7], v[150:151], v[182:183], v[6:7] op_sel_hi:[0,1,1]\n"
            "v_pk_fma_f32 v[8:9], v[154:155], v[184:185], v[8:9] op_sel_hi:[0,1,1]\n"
            "v_pk_fma_f32 v[10:11], v[154:155], v[186:187], v[10:11] op_sel_hi:[0,1,1]\n"
            "v_pk_fma_f32 v[12:13], v[158:159], v[188:189], v[12:13] op_sel_hi:[0,1,1]\n"
            "v_pk_fma_f32 v[14:15], v[158:159], v[190:191], v[14:15] op_sel_hi:[0,1,1]\n"
            "v_pk_add_f32 v[168:169], v[168:169], 1.0 op_sel_hi:[1,0]\n"
            "v_pk_add_f32 v[170:171], v[170:171], 1.0 op_sel_hi:[1,0]\n"
            "v_pk_add_f32 v[172:173], v[172:173], 1.0 op_sel_hi:[1,0]\n"
            "v_pk_add_f32 v[174:175], v[174:175], 1.0 op_sel_hi:[1,0]\n"
            "v_rcp_f32 v168, v168\n"
            "v_rcp_f32 v169, v169\n"
            "v_mfma_f32_4x4x4_16b_bf16 v[176:179], v[208:209], v[80:81], 0 cbsz:2 abid:3\n"
            "v_rcp_f32 v170, v170\n"
            "v_mfma_f32_4x4x4_16b_bf16 v[180:183], v[208:209], v[82:83], 0 cbsz:2 abid:3\n"
            "v_rcp_f32 v171, v171\n"
            "v_mfma_f32_4x4x4_16b_bf16 v[184:187], v[208:209], v[84:85], 0 cbsz:2 abid:3\n"
            "v_rcp_f32 v172, v172\n"
            "v_mfma_f32_4x4x4_16b_bf16 v[188:191], v[208:209], v[86:87], 0 cbsz:2 abid:3\n"
            "v_rcp_f32 v173, v173\n"
            "v_rcp_f32 v174, v174\n"
            "ds_read_b128 v[120:123], v228 offset:14336\n"
            "ds_read_b128 v[124:127], v228 offset:15360\n"
            "ds_read_b128 v[192:195], v228 offset:16384\n"
            "ds_read_b128 v[128:131], v228 offset:17408\n"
            "ds_read_b128 v[132:135], v228 offset:18432\n"
            "v_rcp_f32 v175, v175\n"
            "v_pk_fma_f32 v[0:1], v[146:147], v[176:177], v[0:1] op_sel:[1,0,0]\n"
            "v_pk_fma_f32 v[2:3], v[146:147], v[178:179], v[2:3] op_sel:[1,0,0]\n"
            "v_pk_fma_f32 v[4:5], v[150:151], v[180:181], v[4:5] op_sel:[1,0,0]\n"
            "v_pk_fma_f32 v[6:7], v[150:151], v[182:183], v[6:7] op_sel:[1,0,0]\n"
            "v_pk_fma_f32 v[8:9], v[154:155], v[184:185], v[8:9] op_sel:[1,0,0]\n"
            "v_pk_fma_f32 v[10:11], v[154:155], v[186:187], v[10:11] op_sel:[1,0,0]\n"
            "v_pk_fma_f32 v[12:13], v[158:159], v[188:189], v[12:13] op_sel:[1,0,0]\n"
            "v_pk_fma_f32 v[14:15], v[158:159], v[190:191], v[14:15] op_sel:[1,0,0]\n"
            "v_pk_mul_f32 v[168:169], v[168:169], v[136:137]\n"
            "v_pk_mul_f32 v[170:171], v[170:171], v[138:139]\n"
            "v_pk_mul_f32 v[172:173], v[172:173], v[140:141]\n"
            "v_pk_mul_f32 v[174:175], v[174:175], v[142:143]\n"
            "s_waitcnt vmcnt(8)\n"
            "v_mfma_f32_4x4x4_16b_bf16 v[176:179], v[208:209], v[40:41], 0 cbsz:2\n"
            "v_mfma_f32_4x4x4_16b_bf16 v[180:183], v[208:209], v[42:43], 0 cbsz:2\n"
            "v_mfma_f32_4x4x4_16b_bf16 v[184:187], v[208:209], v[44:45], 0 cbsz:2\n"
            "v_mfma_f32_4x4x4_16b_bf16 v[188:191], v[208:209], v[46:47], 0 cbsz:2\n"
            "ds_read_b128 v[136:139], v228 offset:19456\n"
            "ds_read_b128 v[140:143], v228 offset:20480\n"
            "ds_read_b128 v[144:147], v228 offset:21504\n"
            "ds_read_b128 v[148:151], v228 offset:22528\n"
            "ds_read_b128 v[152:155], v228 offset:23552\n"
            "ds_read_b128 v[156:159], v228 offset:24576\n"
            "s_nop 1\n"
            "v_pk_fma_f32 v[16:17], v[160:161], v[176:177], v[16:17] op_sel_hi:[0,1,1]\n"
            "v_pk_fma_f32 v[18:19], v[160:161], v[178:179], v[18:19] op_sel_hi:[0,1,1]\n"
            "v_pk_fma_f32 v[20:21], v[164:165], v[180:181], v[20:21] op_sel_hi:[0,1,1]\n"
            "v_pk_fma_f32 v[22:23], v[164:165], v[182:183], v[22:23] op_sel_hi:[0,1,1]\n"
            "v_pk_fma_f32 v[24:25], v[168:169], v[184:185], v[24:25] op_sel_hi:[0,1,1]\n"
            "v_pk_fma_f32 v[26:27], v[168:169], v[186:187], v[26:27] op_sel_hi:[0,1,1]\n"
            "v_pk_fma_f32 v[28:29], v[172:173], v[188:189], v[28:29] op_sel_hi:[0,1,1]\n"
            "v_pk_fma_f32 v[30:31], v[172:173], v[190:191], v[30:31] op_sel_hi:[0,1,1]\n"
            "s_cmp_le_u32 %[njl], 1\n"
            "s_cbranch_scc1 Ltail_%=\n"
            "v_mfma_f32_4x4x4_16b_bf16 v[176:179], v[208:209], v[56:57], 0 cbsz:2 abid:1\n"
            "v_mfma_f32_4x4x4_16b_bf16 v[180:183], v[208:209], v[58:59], 0 cbsz:2 abid:1\n"
            "v_mfma_f32_4x4x4_16b_bf16 v[184:187], v[208:209], v[60:61], 0 cbsz:2 abid:1\n"
            "v_mfma_f32_4x4x4_16b_bf16 v[188:191], v[208:209], v[62:63], 0 cbsz:2 abid:1\n"
            "s_nop 1\n"
            "v_pk_fma_f32 v[16:17], v[160:161], v[176:177], v[16:17] op_sel:[1,0,0]\n"
            "v_pk_fma_f32 v[18:19], v[160:161], v[178:179], v[18:19] op_sel:[1,0,0]\n"
            "v_pk_fma_f32 v[20:21], v[164:165], v[180:181], v[20:21] op_sel:[1,0,0]\n"
            "v_pk_fma_f32 v[22:23], v[164:165], v[182:183], v[22:23] op_sel:[1,0,0]\n"
            "v_pk_fma_f32 v[24:25], v[168:169], v[184:185], v[24:25] op_sel:[1,0,0]\n"
            "v_pk_fma_f32 v[26:27], v[168:169], v[186:187], v[26:27] op_sel:[1,0,0]\n"
            "v_pk_fma_f32 v[28:29], v[172:173], v[188:189], v[28:29] op_sel:[1,0,0]\n"
            "v_pk_fma_f32 v[30:31], v[172:173], v[190:191], v[30:31] op_sel:[1,0,0]\n"
            "s_cmp_le_u32 %[njl], 2\n"
            "s_cbranch_scc1 Ltail_%=\n"
            "s_waitcnt vmcnt(4)\n"
            "v_mfma_f32_4x4x4_16b_bf16 v[176:179], v[208:209], v[72:73], 0 cbsz:2 abid:2\n"
            "v_mfma_f32_4x4x4_16b_bf16 v[180:183], v[208:209], v[74:75], 0 cbsz:2 abid:2\n"
            "v_mfma_f32_4x4x4_16b_bf16 v[184:187], v[208:209], v[76:77], 0 cbsz:2 abid:2\n"
            "v_mfma_f32_4x4x4_16b_bf16 v[188:191], v[208:209], v[78:79], 0 cbsz:2 abid:2\n"
            "s_nop 1\n"
            "v_pk_fma_f32 v[16:17], v[162:163], v[176:177], v[16:17] op_sel_hi:[0,1,1]\n"
            "v_pk_fma_f32 v[18:19], v[162:163], v[178:179], v[18:19] op_sel_hi:[0,1,1]\n"
            "v_pk_fma_f32 v[20:21], v[166:167], v[180:181], v[20:21] op_sel_hi:[0,1,1]\n"
            "v_pk_fma_f32 v[22:23], v[166:167], v[182:183], v[22:23] op_sel_hi:[0,1,1]\n"
            "v_pk_fma_f32 v[24:25], v[170:171], v[184:185], v[24:25] op_sel_hi:[0,1,1]\n"
            "v_pk_fma_f32 v[26:27], v[170:171], v[186:187], v[26:27] op_sel_hi:[0,1,1]\n"
            "v_pk_fma_f32 v[28:29], v[174:175], v[188:189], v[28:29] op_sel_hi:[0,1,1]\n"
            "v_pk_fma_f32 v[30:31], v[174:175], v[190:191], v[30:31] op_sel_hi:[0,1,1]\n"
            "s_cmp_le_u32 %[njl], 3\n"
            "s_cbranch_scc1 Ltail_%=\n"
            "v_mfma_f32_4x4x4_16b_bf16 v[176:179], v[208:209], v[88:89], 0 cbsz:2 abid:3\n"
            "v_mfma_f32_4x4x4_16b_bf16 v[180:183], v[208:209], v[90:91], 0 cbsz:2 abid:3\n"
            "v_mfma_f32_4x4x4_16b_bf16 v[184:187], v[208:209], v[92:93], 0 cbsz:2 abid:3\n"
            "v_mfma_f32_4x4x4_16b_bf16 v[188:191], v[208:209], v[94:95], 0 cbsz:2 abid:3\n"
            "s_nop 1\n"
            "v_pk_fma_f32 v[16:17], v[162:163], v[176:177], v[16:17] op_sel:[1,0,0]\n"
            "v_pk_fma_f32 v[18:19], v[162:163], v[178:179], v[18:19] op_sel:[1,0,0]\n"
            "v_pk_fma_f32 v[20:21], v[166:167], v[180:181], v[20:21] op_sel:[1,0,0]\n"
            "v_pk_fma_f32 v[22:23], v[166:167], v[182:183], v[22:23] op_sel:[1,0,0]\n"
            "v_pk_fma_f32 v[24:25], v[170:171], v[184:185], v[24:25] op_sel:[1,0,0]\n"
            "v_pk_fma_f32 v[26:27], v[170:171], v[186:187], v[26:27] op_sel:[1,0,0]\n"
            "v_pk_fma_f32 v[28:29], v[174:175], v[188:189], v[28:29] op_sel:[1,0,0]\n"
            "v_pk_fma_f32 v[30:31], v[174:175], v[190:191], v[30:31] op_sel:[1,0,0]\n"
            "Ltail_%=:\n"
            "s_setprio 3\n"
            "s_waitcnt vmcnt(4)\n"
            "ds_read_b128 v[72:75], v228 offset:25600\n"
            "ds_read_b128 v[76:79], v228 offset:26624\n"
            "ds_read_b128 v[88:91], v228 offset:27648\n"
            "ds_read_b128 v[92:95], v228 offset:28672\n"
            "ds_read_b128 v[160:163], v228 offset:29696\n"
            "ds_read_b128 v[164:167], v228 offset:30720\n"
            "ds_read_b128 v[168:171], v228 offset:31744\n"
            "s_cmp_lg_u32 %[nst], %[tag]\n"
            "s_cbranch_scc1 Lspin_%=\n"
            "Lgo_%=:\n"
            "buffer_load_dwordx4 v[40:43], v218, %[rsw], %[oagg] offen offset:0 sc1\n"
            "buffer_load_dwordx4 v[44:47], v218, %[rsw], %[oagg] offen offset:256 sc1\n"
            "buffer_load_dwordx4 v[56:59], v218, %[rsw], %[oagg] offen offset:512 sc1\n"
            "buffer_load_dwordx4 v[60:63], v218, %[rsw], %[oagg] offen offset:768 sc1\n"
            "s_waitcnt vmcnt(4)\n"
            "v_mfma_f32_4x4x4_16b_bf16 v[0:3], v[224:225], v[32:33], v[0:3]\n"
            "v_mfma_f32_4x4x4_16b_bf16 v[4:7], v[224:225], v[34:35], v[4:7]\n"
            "v_mfma_f32_4x4x4_16b_bf16 v[8:11], v[224:225], v[36:37], v[8:11]\n"
            "v_mfma_f32_4x4x4_16b_bf16 v[12:15], v[224:225], v[38:39], v[12:15]\n"
            "v_mfma_f32_4x4x4_16b_bf16 v[16:19], v[224:225], v[48:49], v[16:19]\n"
            "v_mfma_f32_4x4x4_16b_bf16 v[20:23], v[224:225], v[50:51], v[20:23]\n"
            "v_mfma_f32_4x4x4_16b_bf16 v[24:27], v[224:225], v[52:53], v[24:27]\n"
            "v_mfma_f32_4x4x4_16b_bf16 v[28:31], v[224:225], v[54:55], v[28:31]\n"
            "s_nop 1\n"
            "v_cvt_pk_bf16_f32 v176, v0, v4\n"
            "v_cvt_pk_bf16_f32 v177, v8, v12\n"
            "v_cvt_pk_bf16_f32 v178, v16, v20\n"
            "v_cvt_pk_bf16_f32 v179, v24, v28\n"
            "ds_write_b128 v226, v[176:179] offset:0\n"
            "v_cvt_pk_bf16_f32 v180, v1, v5\n"
            "v_cvt_pk_bf16_f32 v181, v9, v13\n"
            "v_cvt_pk_bf16_f32 v182, v17, v21\n"
            "v_cvt_pk_bf16_f32 v183, v25, v29\n"
            "ds_write_b128 v226, v[180:183] offset:272\n"
            "v_cvt_pk_bf16_f32 v184, v2, v6\n"
            "v_cvt_pk_bf16_f32 v185, v10, v14\n"
            "v_cvt_pk_bf16_f32 v186, v18, v22\n"
            "v_cvt_pk_bf16_f32 v187, v26, v30\n"
            "ds_write_b128 v226, v[184:187] offset:544\n"
            "v_cvt_pk_bf16_f32 v188, v3, v7\n"
            "v_cvt_pk_bf16_f32 v189, v11, v15\n"
            "v_cvt_pk_bf16_f32 v190, v19, v23\n"
            "v_cvt_pk_bf16_f32 v191, v27, v31\n"
            "ds_write_b128 v226, v[188:191] offset:816\n"
            "ds_read_b128 v[176:179], v227 offset:0\n"
            "ds_read_b128 v[180:183], v227 offset:64\n"
            "ds_read_b128 v[184:187], v227 offset:128\n"
            "ds_read_b128 v[188:191], v227 offset:192\n"
            "s_waitcnt lgkmcnt(3)\n"
            "v_mfma_f32_16x16x32_bf16 v[0:3], v[176:179], v[236:239], 0\n"
            "v_mfma_f32_16x16x32_bf16 v[4:7], v[176:179], v[96:99], 0\n"
            "s_waitcnt lgkmcnt(2)\n"
            "v_mfma_f32_16x16x32_bf16 v[0:3], v[180:183], v[240:243], v[0:3]\n"
            "v_mfma_f32_16x16x32_bf16 v[4:7], v[180:183], v[100:103], v[4:7]\n"
            "s_waitcnt lgkmcnt(1)\n"
            "v_mfma_f32_16x16x32_bf16 v[0:3], v[184:187], v[244:247], v[0:3]\n"
            "v_mfma_f32_16x16x32_bf16 v[4:7], v[184:187], v[104:107], v[4:7]\n"
            "s_waitcnt lgkmcnt(0)\n"
            "v_mfma_f32_16x16x32_bf16 v[0:3], v[188:191], v[248:251], v[0:3]\n"
            "v_mfma_f32_16x16x32_bf16 v[4:7], v[188:191], v[108:111], v[4:7]\n"
            "v_mfma_f32_16x16x32_bf16 v[8:11], v[176:179], v[112:115], 0\n"
            "v_mfma_f32_16x16x32_bf16 v[12:15], v[176:179], v[204:207], 0\n"
            "v_mfma_f32_16x16x32_bf16 v[8:11], v[180:183], v[116:119], v[8:11]\n"
            "v_mfma_f32_16x16x32_bf16 v[12:15], v[180:183], v[232:235], v[12:15]\n"
            "v_mfma_f32_16x16x32_bf16 v[8:11], v[184:187], v[196:199], v[8:11]\n"
            "v_mfma_f32_16x16x32_bf16 v[12:15], v[184:187], v[120:123], v[12:15]\n"
            "v_mfma_f32_16x16x32_bf16 v[8:11], v[188:191], v[200:203], v[8:11]\n"
            "v_mfma_f32_16x16x32_bf16 v[12:15], v[188:191], v[124:127], v[12:15]\n"
            "v_cvt_pk_bf16_f32 v0, v0, v1\n"
            "v_cvt_pk_bf16_f32 v1, v2, v3\n"
            "v_cvt_pk_bf16_f32 v2, v4, v5\n"
            "v_cvt_pk_bf16_f32 v3, v6, v7\n"
            "buffer_store_dwordx4 v[0:3], v217, %[rso], 0 offen offset:0 sc1\n"
            "v_mfma_f32_16x16x32_bf16 v[16:19], v[176:179], v[192:195], 0\n"
            "v_mfma_f32_16x16x32_bf16 v[20:23], v[176:179], v[140:143], 0\n"
            "v_mfma_f32_16x16x32_bf16 v[16:19], v[180:183], v[128:131], v[16:19]\n"
            "v_mfma_f32_16x16x32_bf16 v[20:23], v[180:183], v[144:147], v[20:23]\n"
            "v_mfma_f32_16x16x32_bf16 v[16:19], v[184:187], v[132:135], v[16:19]\n"
            "v_mfma_f32_16x16x32_bf16 v[20:23], v[184:187], v[148:151], v[20:23]\n"
            "v_mfma_f32_16x16x32_bf16 v[16:19], v[188:191], v[136:139], v[16:19]\n"
            "v_mfma_f32_16x16x32_bf16 v[20:23], v[188:191], v[152:155], v[20:23]\n"
            "v_cvt_pk_bf16_f32 v8, v8, v9\n"
            "v_cvt_pk_bf16_f32 v9, v10, v11\n"
            "v_cvt_pk_bf16_f32 v10, v12, v13\n"
            "v_cvt_pk_bf16_f32 v11, v14, v15\n"
            "buffer_store_dwordx4 v[8:11], v217, %[rso], 0 offen offset:256 sc1\n"
            "v_mfma_f32_16x16x32_bf16 v[24:27], v[176:179], v[156:159], 0\n"
            "v_mfma_f32_16x16x32_bf16 v[28:31], v[176:179], v[92:95], 0\n"
            "v_mfma_f32_16x16x32_bf16 v[24:27], v[180:183], v[72:75], v[24:27]\n"
            "v_mfma_f32_16x16x32_bf16 v[28:31], v[180:183], v[160:163], v[28:31]\n"
            "v_mfma_f32_16x16x32_bf16 v[24:27], v[184:187], v[76:79], v[24:27]\n"
            "v_mfma_f32_16x16x32_bf16 v[28:31], v[184:187], v[164:167], v[28:31]\n"
            "v_mfma_f32_16x16x32_bf16 v[24:27], v[188:191], v[88:91], v[24:27]\n"
            "v_mfma_f32_16x16x32_bf16 v[28:31], v[188:191], v[168:171], v[28:31]\n"
            "v_cvt_pk_bf16_f32 v16, v16, v17\n"
            "v_cvt_pk_bf16_f32 v17, v18, v19\n"
            "v_cvt_pk_bf16_f32 v18, v20, v21\n"
            "v_cvt_pk_bf16_f32 v19, v22, v23\n"
            "buffer_store_dwordx4 v[16:19], v217, %[rso], 0 offen offset:512 sc1\n"
            "s_nop 2\n"
            "v_cvt_pk_bf16_f32 v24, v24, v25\n"
            "v_cvt_pk_bf16_f32 v25, v26, v27\n"
            "v_cvt_pk_bf16_f32 v26, v28, v29\n"
            "v_cvt_pk_bf16_f32 v27, v30, v31\n"
            "buffer_store_dwordx4 v[24:27], v217, %[rso], 0 offen offset:768 sc1\n"
            "s_cmp_lt_u32 %[two], 6\n"
            "s_cbranch_scc1 Lend_%=\n"
            "ds_read_b128 v[64:67], v231 offset:0\n"
            "ds_read_b128 v[68:71], v231 offset:1024\n"
            "ds_read_b128 v[80:83], v231 offset:2048\n"
            "ds_read_b128 v[84:87], v231 offset:3072\n"
            "s_waitcnt vmcnt(4) lgkmcnt(0)\n"
            "v_mfma_f32_4x4x4_16b_bf16 v[0:3], v[224:225], v[64:65], 0\n"
            "v_mfma_f32_4x4x4_16b_bf16 v[4:7], v[224:225], v[66:67], 0\n"
            "v_mfma_f32_4x4x4_16b_bf16 v[8:11], v[224:225], v[68:69], 0\n"
            "v_mfma_f32_4x4x4_16b_bf16 v[12:15], v[224:225], v[70:71], 0\n"
            "v_mfma_f32_4x4x4_16b_bf16 v[16:19], v[224:225], v[80:81], 0\n"
            "v_mfma_f32_4x4x4_16b_bf16 v[20:23], v[224:225], v[82:83], 0\n"
            "v_mfma_f32_4x4x4_16b_bf16 v[24:27], v[224:225], v[84:85], 0\n"
            "v_mfma_f32_4x4x4_16b_bf16 v[28:31], v[224:225], v[86:87], 0\n"
            "v_mfma_f32_4x4x4_16b_bf16 v[0:3], v[224:225], v[40:41], v[0:3]\n"
            "v_mfma_f32_4x4x4_16b_bf16 v[4:7], v[224:225], v[42:43], v[4:7]\n"
            "v_mfma_f32_4x4x4_16b_bf16 v[8:11], v[224:225], v[44:45], v[8:11]\n"
            "v_mfma_f32_4x4x4_16b_bf16 v[12:15], v[224:225], v[46:47], v[12:15]\n"
            "v_mfma_f32_4x4x4_16b_bf16 v[16:19], v[224:225], v[56:57], v[16:19]\n"
            "v_mfma_f32_4x4x4_16b_bf16 v[20:23], v[224:225], v[58:59], v[20:23]\n"
            "v_mfma_f32_4x4x4_16b_bf16 v[24:27], v[224:225], v[60:61], v[24:27]\n"
            "v_mfma_f32_4x4x4_16b_bf16 v[28:31], v[224:225], v[62:63], v[28:31]\n"
            "s_nop 1\n"
            "v_cvt_pk_bf16_f32 v176, v0, v4\n"
            "v_cvt_pk_bf16_f32 v177, v8, v12\n"
            "v_cvt_pk_bf16_f32 v178, v16, v20\n"
            "v_cvt_pk_bf16_f32 v179, v24, v28\n"
            "ds_write_b128 v226, v[176:179] offset:0\n"
            "v_cvt_pk_bf16_f32 v180, v1, v5\n"
            "v_cvt_pk_bf16_f32 v181, v9, v13\n"
            "v_cvt_pk_bf16_f32 v182, v17, v21\n"
            "v_cvt_pk_bf16_f32 v183, v25, v29\n"
            "ds_write_b128 v226, v[180:183] offset:272\n"
            "v_cvt_pk_bf16_f32 v184, v2, v6\n"
            "v_cvt_pk_bf16_f32 v185, v10, v14\n"
            "v_cvt_pk_bf16_f32 v186, v18, v22\n"
            "v_cvt_pk_bf16_f32 v187, v26, v30\n"
            "ds_write_b128 v226, v[184:187] offset:544\n"
            "v_cvt_pk_bf16_f32 v188, v3, v7\n"
            "v_cvt_pk_bf16_f32 v189, v11, v15\n"
            "v_cvt_pk_bf16_f32 v190, v19, v23\n"
            "v_cvt_pk_bf16_f32 v191, v27, v31\n"
            "ds_write_b128 v226, v[188:191] offset:816\n"
            "ds_read_b128 v[176:179], v227 offset:0\n"
            "ds_read_b128 v[180:183], v227 offset:64\n"
            "ds_read_b128 v[184:187], v227 offset:128\n"
            "ds_read_b128 v[188:191], v227 offset:192\n"
            "s_waitcnt lgkmcnt(3)\n"
            "v_mfma_f32_16x16x32_bf16 v[0:3], v[176:179], v[236:239], 0\n"
            "v_mfma_f32_16x16x32_bf16 v[4:7], v[176:179], v[96:99], 0\n"
            "s_waitcnt lgkmcnt(2)\n"
            "v_mfma_f32_16x16x32_bf16 v[0:3], v[180:183], v[240:243], v[0:3]\n"
            "v_mfma_f32_16x16x32_bf16 v[4:7], v[180:183], v[100:103], v[4:7]\n"
            "s_waitcnt lgkmcnt(1)\n"
            "v_mfma_f32_16x16x32_bf16 v[0:3], v[184:187], v[244:247], v[0:3]\n"
            "v_mfma_f32_16x16x32_bf16 v[4:7], v[184:187], v[104:107], v[4:7]\n"
            "s_waitcnt lgkmcnt(0)\n"
            "v_mfma_f32_16x16x32_bf16 v[0:3], v[188:191], v[248:251], v[0:3]\n"
            "v_mfma_f32_16x16x32_bf16 v[4:7], v[188:191], v[108:111], v[4:7]\n"
            "v_mfma_f32_16x16x32_bf16 v[8:11], v[176:179], v[112:115], 0\n"
            "v_mfma_f32_16x16x32_bf16 v[12:15], v[176:179], v[204:207], 0\n"
            "v_mfma_f32_16x16x32_bf16 v[8:11], v[180:183], v[116:119], v[8:11]\n"
            "v_mfma_f32_16x16x32_bf16 v[12:15], v[180:183], v[232:235], v[12:15]\n"
            "v_mfma_f32_16x16x32_bf16 v[8:11], v[184:187], v[196:199], v[8:11]\n"
            "v_mfma_f32_16x16x32_bf16 v[12:15], v[184:187], v[120:123], v[12:15]\n"
            "v_mfma_f32_16x16x32_bf16 v[8:11], v[188:191], v[200:203], v[8:11]\n"
            "v_mfma_f32_16x16x32_bf16 v[12:15], v[188:191], v[124:127], v[12:15]\n"
            "v_cvt_pk_bf16_f32 v0, v0, v1\n"
            "v_cvt_pk_bf16_f32 v1, v2, v3\n"
            "v_cvt_pk_bf16_f32 v2, v4, v5\n"
            "v_cvt_pk_bf16_f32 v3, v6, v7\n"
            "buffer_store_dwordx4 v[0:3], v218, %[rso], 0 offen offset:0 sc1\n"
            "v_mfma_f32_16x16x32_bf16 v[16:19], v[176:179], v[192:195], 0\n"
            "v_mfma_f32_16x16x32_bf16 v[20:23], v[176:179], v[140:143], 0\n"
            "v_mfma_f32_16x16x32_bf16 v[16:19], v[180:183], v[128:131], v[16:19]\n"
            "v_mfma_f32_16x16x32_bf16 v[20:23], v[180:183], v[144:147], v[20:23]\n"
            "v_mfma_f32_16x16x32_bf16 v[16:19], v[184:187], v[132:135], v[16:19]\n"
            "v_mfma_f32_16x16x32_bf16 v[20:23], v[184:187], v[148:151], v[20:23]\n"
            "v_mfma_f32_16x16x32_bf16 v[16:19], v[188:191], v[136:139], v[16:19]\n"
            "v_mfma_f32_16x16x32_bf16 v[20:23], v[188:191], v[152:155], v[20:23]\n"
            "v_cvt_pk_bf16_f32 v8, v8, v9\n"
            "v_cvt_pk_bf16_f32 v9, v10, v11\n"
            "v_cvt_pk_bf16_f32 v10, v12, v13\n"
            "v_cvt_pk_bf16_f32 v11, v14, v15\n"
            "buffer_store_dwordx4 v[8:11], v218, %[rso], 0 offen offset:256 sc1\n"
            "v_mfma_f32_16x16x32_bf16 v[24:27], v[176:179], v[156:159], 0\n"
            "v_mfma_f32_16x16x32_bf16 v[28:31], v[176:179], v[92:95], 0\n"
            "v_mfma_f32_16x16x32_bf16 v[24:27], v[180:183], v[72:75], v[24:27]\n"
            "v_mfma_f32_16x16x32_bf16 v[28:31], v[180:183], v[160:163], v[28:31]\n"
            "v_mfma_f32_16x16x32_bf16 v[24:27], v[184:187], v[76:79], v[24:27]\n"
            "v_mfma_f32_16x16x32_bf16 v[28:31], v[184:187], v[164:167], v[28:31]\n"
            "v_mfma_f32_16x16x32_bf16 v[24:27], v[188:191], v[88:91], v[24:27]\n"
            "v_mfma_f32_16x16x32_bf16 v[28:31], v[188:191], v[168:171], v[28:31]\n"
            "v_cvt_pk_bf16_f32 v16, v16, v17\n"
            "v_cvt_pk_bf16_f32 v17, v18, v19\n"
            "v_cvt_pk_bf16_f32 v18, v20, v21\n"
            "v_cvt_pk_bf16_f32 v19, v22, v23\n"
            "buffer_store_dwordx4 v[16:19], v218, %[rso], 0 offen offset:512 sc1\n"
            "s_nop 2\n"
            "v_cvt_pk_bf16_f32 v24, v24, v25\n"
            "v_cvt_pk_bf16_f32 v25, v26, v27\n"
            "v_cvt_pk_bf16_f32 v26, v28, v29\n"
            "v_cvt_pk_bf16_f32 v27, v30, v31\n"
            "buffer_store_dwordx4 v[24:27], v218, %[rso], 0 offen offset:768 sc1\n"
            "Lend_%=:\n"
            "s_waitcnt vmcnt(4)\n"
            : "+{v[32:63]}"(oxa), "+{v[64:95]}"(oxb), "+{v[96:127]}"(owr), "+{v[196:199]}"(S), "+{v[200:203]}"(cgA), "+{v[208:223]}"(omi), "+{v[224:231]}"(ot),
              "+{v[232:233]}"(efN), "+{v234}"(bpa), "+{v[236:237]}"(jmp), "+{v[238:239]}"(ef0),
              [rem] "+s"(rem), [lens] "+s"(lens), [fm] "+s"(fm), [nst] "+s"(nst), [pb] "+s"(pb), [evt] "+s"(evt), [ra] "=&s"(ra), [tmp] "=&s"(tmp)
            : [rse] "s"(de), [rsx] "s"(dx), [rso] "s"(mkdesc(out, (unsigned)N * 1024u)), [rsw] "s"(mkdesc(ws, W_END)),
              [cnt] "s"(lds0 + L_MISC + 4), [pfa] "s"(lds0 + L_MISC + 16 + pw * 4), [tag] "s"(tag), [flg] "s"(ws + W_FLG + bx * 128), [pfl] "s"(ws + W_FLG + (bx ^ 4) * 128),
              [njl] "s"(((e2 - 1) & 3) + 1), [c1] "s"((w < 4 ? l2 : l1) - 4 * ((e2 + 3) >> 2) - 5), [c2] "s"(w < 4 ? l1 : l2), [two] "s"(w), [ptab] "s"((unsigned)__builtin_amdgcn_readfirstlane(w < 4 ? PTO : (w < 6 ? PTY : PTZ))), [oagg] "s"(W_AGG), [spin] "s"(SPIN_MAX)
            : "memory", "vcc", "scc", "v0", "v1", "v2", "v3", "v4", "v5", "v6", "v7", "v8", "v9", "v10", "v11", "v12", "v13", "v14", "v15", "v16", "v17", "v18", "v19", "v20", "v21", "v22", "v23", "v24", "v25", "v26", "v27", "v28", "v29", "v30", "v31", "v128", "v129", "v130", "v131", "v132", "v133", "v134", "v135", "v136", "v137", "v138", "v139", "v140", "v141", "v142", "v143", "v144", "v145", "v146", "v147", "v148", "v149", "v150", "v151", "v152", "v153", "v154", "v155", "v156", "v157", "v158", "v159", "v160", "v161", "v162", "v163", "v164", "v165", "v166", "v167", "v168", "v169", "v170", "v171", "v172", "v173", "v174", "v175", "v176", "v177", "v178", "v179", "v180", "v181", "v182", "v183", "v184", "v185", "v186", "v187", "v188", "v189", "v190", "v191", "v192", "v193", "v194", "v195", "v235", "v204", "v205", "v206", "v207", "v240", "v241", "v242", "v243", "v244", "v245", "v246", "v247", "v248", "v249", "v250", "v251");
    } else {
        if (bx == 0 && w == 4) flag_store(ws + W_CTL, tag);
        lds_wait(lds0 + SY_C3, 5);
        const __amdgpu_buffer_rsrc_t rso = __builtin_amdgcn_make_buffer_rsrc((void*)out, 0, N * 1024, 0x00020000);
#pragma unroll 1
        for (int it = 0; it < 10; ++it) {
            const int nl = (it < 5 ? 40 - 40 * half + 8 * it : 40 * half + 8 * (it - 5)) + w, node = node0 + nl;
            const bool nv = nl < npb;
            const unsigned ro = nv ? (unsigned)node * 1024u + (unsigned)lane * 16u : OOB_OFF;
            float a8[8] = {0.f, 0.f, 0.f, 0.f, 0.f, 0.f, 0.f, 0.f};
#pragma unroll 1
            for (int e0 = 0; e0 < (nv ? E : 0); e0 += 64) {
                const int ec = min(e0 + lane, E - 1);
                u64 mm = __ballot(e0 + lane < E && dst[ec] == node && (int)(((src[ec] >> 3) ^ (smask >> ((src[ec] >> 9) & 31))) & 1) == half);
#pragma unroll 1
                while (mm) {
                    const int e = e0 + __builtin_ctzll(mm);
                    mm &= mm - 1;
                    const unsigned short* fe = (const unsigned short*)ef + (size_t)e * DE_DIM;
                    const unsigned short* ye = (const unsigned short*)Y + (size_t)e * M_DIM;
                    const unsigned short* xp = x + (size_t)src[e] * (C_DIM * M_DIM) + lane * 8;
                    float z0 = 0.f, z1 = 0.f;
#pragma unroll 1
                    for (int f = 0; f < DE_DIM; ++f) { z0 += bf2f(fe[f]) * bf2f(w_rad[f * C_DIM + 2 * lane]); z1 += bf2f(fe[f]) * bf2f(w_rad[f * C_DIM + 2 * lane + 1]); }
                    const float r0 = z0 / (1.f + __expf(-z0)), r1 = z1 / (1.f + __expf(-z1));
#pragma unroll 1
                    for (int i = 0; i < 4; ++i) {
                        const float x0 = bf2f(xp[i]) * r0, x1 = bf2f(xp[4 + i]) * r1;
#pragma unroll
                        for (int o = 0; o < 4; ++o) {
                            float wv4 = 0.f;
#pragma unroll
                            for (int f = 0; f < 4; ++f) wv4 += bf2f(cg[(i * 4 + f) * 4 + o]) * bf2f(ye[f]);
                            a8[o] += x0 * wv4; a8[4 + o] += x1 * wv4;
                        }
                    }
                }
            }
            if (it >= 5) {
                const u32x4 pv = __builtin_amdgcn_raw_buffer_load_b128(rsw, ro, W_AGG, SC1);
#pragma unroll
                for (int i = 0; i < 4; ++i) { a8[2 * i] += __uint_as_float(pv[i] << 16); a8[2 * i + 1] += __uint_as_float(pv[i] & 0xffff0000u); }
            }
            const u32x4 rv = (u32x4){pack2bf(a8[0], a8[1]), pack2bf(a8[2], a8[3]), pack2bf(a8[4], a8[5]), pack2bf(a8[6], a8[7])};
            if (it < 5) {
                __builtin_amdgcn_raw_buffer_store_b128(rv, rsw, ro, W_AGG, SC1);
                if (it == 4) {
                    asm volatile("s_waitcnt vmcnt(0)" ::: "memory");
                    if (lds_arrive_rtn(lds0 + L_MISC + 4) == 7u) flag_store(ws + W_FLG + bx * 128, tag);
                    poll_tag(ws + W_FLG + (bx ^ 4) * 128, tag);
                }
            } else {
                *(u32x4*)(smem + L_PRV + w * 1024 + lane * 16) = rv;
#pragma unroll
                for (int i = 0; i < 8; ++i) a8[i] = 0.f;
#pragma unroll 1
                for (int c = 0; c < C_DIM; ++c) {
                    const unsigned short* ap = (const unsigned short*)(smem + L_PRV + w * 1024 + c * 8);
                    const float w0 = bf2f(w_mix[c * C_DIM + 2 * lane]), w1 = bf2f(w_mix[c * C_DIM + 2 * lane + 1]);
#pragma unroll
                    for (int m = 0; m < 4; ++m) { a8[m] += bf2f(ap[m]) * w0; a8[4 + m] += bf2f(ap[m]) * w1; }
                }
                __builtin_amdgcn_raw_buffer_store_b128((u32x4){pack2bf(a8[0], a8[1]), pack2bf(a8[2], a8[3]), pack2bf(a8[4], a8[5]), pack2bf(a8[6], a8[7])}, rso, ro, 0, 0);
            }
        }
    }
}

__global__ void kf_zero(float* __restrict__ agg, size_t n) {
    for (size_t i = (size_t)blockIdx.x * blockDim.x + threadIdx.x; i < n; i += (size_t)gridDim.x * blockDim.x) agg[i] = 0.f;
}
__global__ void kf_edges(const unsigned short* __restrict__ x, const int* __restrict__ src, const int* __restrict__ dst,
                         const unsigned short* __restrict__ Y, const unsigned short* __restrict__ ef,
                         const unsigned short* __restrict__ w_rad, const unsigned short* __restrict__ cg,
                         float* __restrict__ agg, int E) {
    const int ch = threadIdx.x;
    for (int e = blockIdx.x; e < E; e += gridDim.x) {
        float z = 0.f;
        for (int f = 0; f < DE_DIM; ++f) z += bf2f(ef[(size_t)e * DE_DIM + f]) * bf2f(w_rad[f * C_DIM + ch]);
        const float r = z / (1.f + __expf(-z));
        float a4[4] = {0.f, 0.f, 0.f, 0.f};
        for (int ii = 0; ii < 4; ++ii) {
            const float xv = bf2f(x[((size_t)src[e] * C_DIM + ch) * M_DIM + ii]) * r;
            for (int o = 0; o < 4; ++o) {
                float wv = 0.f;
                for (int f = 0; f < 4; ++f) wv += bf2f(cg[(ii * 4 + f) * 4 + o]) * bf2f(Y[(size_t)e * 4 + f]);
                a4[o] += xv * wv;
            }
        }
        for (int o = 0; o < 4; ++o) atomicAdd(&agg[((size_t)dst[e] * C_DIM + ch) * M_DIM + o], a4[o]);
    }
}
__global__ void kf_mix(const float* __restrict__ agg, const unsigned short* __restrict__ w_mix, unsigned short* __restrict__ out, int N) {
    const int d = threadIdx.x;
    for (int n = blockIdx.x; n < N; n += gridDim.x) {
        float a4[4] = {0.f, 0.f, 0.f, 0.f};
        for (int c = 0; c < C_DIM; ++c) {
            const float wv = bf2f(w_mix[c * C_DIM + d]);
            for (int m = 0; m < 4; ++m) a4[m] += bf2f(f2bf(agg[((size_t)n * C_DIM + c) * M_DIM + m])) * wv;
        }
        for (int m = 0; m < 4; ++m) out[((size_t)n * C_DIM + d) * M_DIM + m] = f2bf(a4[m]);
    }
}

extern "C" void kernel_launch(void* const* d_in, const int* in_sizes, int n_in,
                              void* d_out, int out_size, void* d_ws, size_t ws_size,
                              hipStream_t stream) {
    const unsigned short* x = (const unsigned short*)d_in[0];
    const int* src = (const int*)d_in[1];
    const int* dst = (const int*)d_in[2];
    const unsigned short* Y = (const unsigned short*)d_in[3];
    const unsigned short* ef = (const unsigned short*)d_in[4];
    const unsigned short* w_rad = (const unsigned short*)d_in[5];
    const unsigned short* cg = (const unsigned short*)d_in[6];
    const unsigned short* w_mix = (const unsigned short*)d_in[7];
    unsigned short* out = (unsigned short*)d_out;
    const int N = in_sizes[0] / (C_DIM * M_DIM);
    const int E = in_sizes[1];
    const int epb = (E + 253) / 254;
    const bool fast = N >= 256 && N <= NB_MAX * NPB_MAX && E > 0 && epb <= EPB_MAX && ws_size >= (size_t)W_END;
    if (!fast) {
        float* agg = (float*)d_ws;
        if (N <= 0) return;
        kf_zero<<<1024, 256, 0, stream>>>(agg, (size_t)N * C_DIM * M_DIM);
        if (E > 0) kf_edges<<<4096, C_DIM, 0, stream>>>(x, src, dst, Y, ef, w_rad, cg, agg, E);
        kf_mix<<<2048, C_DIM, 0, stream>>>(agg, w_mix, out, N);
        return;
    }
    const unsigned nb_magic = (unsigned)(((1ull << 39) + N - 1) / N);
    unsigned smask = 0;
    const int ng = (N + 511) / 512;
    int pw = 1;
    while (2 * pw * 4 < ng + 4) pw *= 2;
    for (int i = 0; i < ng; ++i) smask |= (unsigned)(i < 4 * pw ? ((i >> 2) ^ (i >= WLO * ng / 20 && i < WHI * ng / 20)) & 1 : i & 1) << i;
    const int r0 = ((N < 2048 * pw ? N : 2048 * pw) - 128) & ~63, r1 = r0 - (r0 >= 2048 ? 2048 : 128);
    k_fused<<<256, 512, 0, stream>>>(dst, (unsigned)E * 4u, 0x20000u, src, (unsigned)E * 4u, 0x20000u, Y, d_ws, x, ef, w_rad, cg, w_mix, out,
                                     N, E, epb * 4, nb_magic, smask, (unsigned)(4 * 65536 / N), (unsigned)(r0 + (r0 >> 8 & 8)) * 1024u, (unsigned)(r1 + (r1 >> 8 & 8)) * 1024u);
}
```

```cpp
#include <hip/hip_runtime.h>
#include <hip/hip_bf16.h>
#include <stdint.h>

typedef __attribute__((ext_vector_type(8))) short bf16x8;
typedef __attribute__((ext_vector_type(4))) short bf16x4;
typedef __attribute__((ext_vector_type(4))) float f32x4;
typedef __attribute__((ext_vector_type(2))) unsigned u32x2;
typedef __attribute__((ext_vector_type(3))) unsigned u32x3;
typedef __attribute__((ext_vector_type(4))) unsigned u32x4;
typedef __attribute__((ext_vector_type(8))) unsigned u32x8;
typedef __attribute__((ext_vector_type(16))) unsigned u32x16;
typedef __attribute__((ext_vector_type(32))) unsigned u32x32;
typedef unsigned long long u64;

#define C_DIM 128
#define M_DIM 4
#define DE_DIM 16
#define NPB_MAX 80
#define NB_MAX 128
#define EPB_MAX 1280
#define NRND 7
#define LS 47
#define DEG_MAX 42
#define OOB_OFF 0x80000000u
#define SC1 16
#define NT 2
#define SPIN_MAX (1u << 22)
#define CODE_LINES 172
#define PTO 0xfffa9540u
#define PTY 0xffffa950u
#define PTZ 0xffffea54u
#define NT0 2
#define NT2 5
#define TLIST 100, 3, 4, 5, 10
#define WLO 8
#define WHI 11
#define IDLE 140u
#define CPN 12
#define EW 3
#define PBIAS 12
#define TKF 0x98765432u
#define TKB 0x23456789u
#define TKA 0x1010ffffu
#define EXTRA 0, 0, 5, 7

#define W_CTL 0
#define W_DEC 2048
#define W_FLG 4096
#define W_TAB (W_FLG + 32768)
#define W_RA (W_TAB + 256 * 256 * 8)
#define W_AGG (W_RA + 256 * EPB_MAX * 16)
#define W_END (W_AGG + NB_MAX * NPB_MAX * 1024)

#define SB() __builtin_amdgcn_sched_barrier(0)

static __device__ __forceinline__ float bf2f(unsigned short v) { return __uint_as_float(((unsigned)v) << 16); }
typedef __bf16 bf16v2 __attribute__((ext_vector_type(2)));
static __device__ __forceinline__ unsigned pack2bf(float lo, float hi) {
    bf16v2 r = (bf16v2){(__bf16)lo, (__bf16)hi};
    return __builtin_bit_cast(unsigned, r);
}
static __device__ __forceinline__ unsigned short f2bf(float v) { return (unsigned short)(pack2bf(v, 0.f) & 0xffffu); }
template <int CTRL, int RM> static __device__ __forceinline__ int dpp_add(int v) {
    return v + __builtin_amdgcn_update_dpp(0, v, CTRL, RM, 0xf, false);
}
static __device__ __forceinline__ int wave_incl_scan(int v) {
    v = dpp_add<0x111, 0xf>(v); v = dpp_add<0x112, 0xf>(v); v = dpp_add<0x114, 0xf>(v); v = dpp_add<0x118, 0xf>(v);
    v = dpp_add<0x142, 0xa>(v); v = dpp_add<0x143, 0xc>(v);
    return v;
}
template <int CTRL, int RM> static __device__ __forceinline__ unsigned dpp_max(unsigned v) {
    return max(v, (unsigned)__builtin_amdgcn_update_dpp(0, (int)v, CTRL, RM, 0xf, false));
}
static __device__ __forceinline__ unsigned wave_max_scan(unsigned v) {
    v = dpp_max<0x111, 0xf>(v); v = dpp_max<0x112, 0xf>(v); v = dpp_max<0x114, 0xf>(v); v = dpp_max<0x118, 0xf>(v);
    v = dpp_max<0x142, 0xa>(v); v = dpp_max<0x143, 0xc>(v);
    return v;
}
#define MAXS(c) "v_max_u32_dpp %0, %0, %0 " c "\n\tv_max_u32_dpp %1, %1, %1 " c "\n\tv_max_u32_dpp %2, %2, %2 " c "\n\t"
static __device__ __forceinline__ void wave_max_scan3(unsigned& a, unsigned& b, unsigned& c) {
    asm volatile("s_nop 1\n\t" MAXS("row_shr:1 row_mask:0xf bank_mask:0xf bound_ctrl:1") MAXS("row_shr:2 row_mask:0xf bank_mask:0xf bound_ctrl:1")
                 MAXS("row_shr:4 row_mask:0xf bank_mask:0xf bound_ctrl:1") MAXS("row_shr:8 row_mask:0xf bank_mask:0xf bound_ctrl:1")
                 MAXS("row_bcast:15 row_mask:0xa bank_mask:0xf") MAXS("row_bcast:31 row_mask:0xc bank_mask:0xf") "s_nop 0" : "+v"(a), "+v"(b), "+v"(c));
}
static __device__ __forceinline__ unsigned bsum(unsigned c, unsigned m, unsigned acc = 0u) { return __builtin_amdgcn_udot4(c, m, acc, false); }
static __device__ __forceinline__ unsigned below_mask(int w, int k) {
    const int n = w - 4 * k;
    return n >= 4 ? 0x01010101u : (n <= 0 ? 0u : (0x01010101u & ((1u << (8 * n)) - 1u)));
}
static __device__ __forceinline__ void flag_store(const void* p, unsigned v) {
    unsigned t0, t1; u64 sv;
    asm volatile("v_mov_b32 %0, 0\n\tv_mov_b32 %1, %4\n\ts_mov_b64 %2, exec\n\ts_mov_b64 exec, 1\n\t"
                 "global_store_dword %0, %1, %3 sc1\n\ts_mov_b64 exec, %2" : "=&v"(t0), "=&v"(t1), "=&s"(sv) : "s"(p), "s"(v) : "memory");
}
static __device__ __forceinline__ unsigned lds_arrive_rtn(unsigned a) {
    unsigned t0, t1; u64 sv;
    asm volatile("v_mov_b32 %0, %3\n\tv_mov_b32 %1, 1\n\ts_mov_b64 %2, exec\n\ts_mov_b64 exec, 1\n\t"
                 "ds_add_rtn_u32 %1, %0, %1\n\ts_mov_b64 exec, %2\n\ts_waitcnt lgkmcnt(0)" : "=&v"(t0), "=&v"(t1), "=&s"(sv) : "s"(a) : "memory");
    return (unsigned)__builtin_amdgcn_readfirstlane((int)t1);
}
static __device__ __forceinline__ void lds_arrive(unsigned a, unsigned inc = 1u) {
    unsigned t0, t1; u64 sv;
    asm volatile("v_mov_b32 %0, %3\n\tv_mov_b32 %1, %4\n\ts_mov_b64 %2, exec\n\ts_mov_b64 exec, 1\n\t"
                 "ds_add_u32 %0, %1\n\ts_mov_b64 exec, %2" : "=&v"(t0), "=&v"(t1), "=&s"(sv) : "s"(a), "s"(inc) : "memory");
}
template <int SLEEP = 0, unsigned MASK = ~0u> static __device__ __forceinline__ unsigned lds_wait(unsigned a, unsigned n) {
    for (;;) {
        unsigned v;
        asm volatile("ds_read_b32 %0, %1\n\ts_waitcnt lgkmcnt(0)" : "=v"(v) : "v"(a) : "memory");
        v = (unsigned)__builtin_amdgcn_readfirstlane((int)v);
        if ((v & MASK) >= n) return v;
        if (SLEEP) __builtin_amdgcn_s_sleep(SLEEP);
    }
}
static __device__ __forceinline__ unsigned ld_agent(const void* p) { return __hip_atomic_load((const unsigned*)p, __ATOMIC_RELAXED, __HIP_MEMORY_SCOPE_AGENT); }
static __device__ __forceinline__ void poll_tag(const void* p, unsigned tag) {
#pragma unroll 1
    for (unsigned spin = 0; spin < SPIN_MAX; ++spin) {
        if (__all(ld_agent(p) == tag)) break;
        __builtin_amdgcn_s_sleep(2);
    }
    __builtin_amdgcn_fence(__ATOMIC_ACQUIRE, "wavefront");
}
static __device__ __forceinline__ void wg_barrier() {
    asm volatile("s_waitcnt lgkmcnt(0)" ::: "memory");
    __builtin_amdgcn_s_barrier();
    asm volatile("" ::: "memory");
}
static __device__ __forceinline__ f32x4 mfma4(u32x2 a, u32x2 b, f32x4 c) {
    return __builtin_amdgcn_mfma_f32_4x4x4bf16_1k(__builtin_bit_cast(bf16x4, a), __builtin_bit_cast(bf16x4, b), c, 0, 0, 0);
}
static __device__ __forceinline__ f32x4 mfma16(u32x4 a, u32x4 b, f32x4 c) {
    return __builtin_amdgcn_mfma_f32_16x16x32_bf16(__builtin_bit_cast(bf16x8, a), __builtin_bit_cast(bf16x8, b), c, 0, 0, 0);
}

static constexpr unsigned evt_of(unsigned pt) {
    unsigned e = 6u;
    for (int n = 2; n < 15; ++n) if (((pt >> (2 * n)) & 3u) != ((pt >> (2 * n + 2)) & 3u)) e |= 1u << n;
    return e;
}

static constexpr u64 len_pos(bool kept) {
    u64 r = 0;
    for (int w = 0; w < 8; ++w) {
        r |= (u64)(kept ? 8 + w : w) << (4 * (((kept ? TKF : TKB) >> (4 * w)) & 15u));
        if (w >= 4 && (w >= 6) == kept) r |= (u64)(kept ? w : 4 + w) << (4 * ((TKA >> (4 * w)) & 15u));
    }
    return r;
}
static constexpr unsigned ring_const() {
    constexpr int e[4] = {EXTRA};
    unsigned r = 0;
    for (int i = 0; i < 4; ++i) r |= (unsigned)((4 - PBIAS + (-3 * e[i] - e[(i + 3) & 3] + e[(i + 2) & 3] + 3 * e[(i + 1) & 3]) / 2) & 255) << (8 * i);
    return r;
}

#define L_MISC 0
#define L_KEY 96
#define L_AT 416
#define L_LEN 1056
#define L_PCNT 1280
#define L_CCNT (L_PCNT + 2048)
#define L_BNC (L_CCNT + 1280)
#define L_CG (L_BNC + 4096)
#define L_PRV (L_CG + 1024)
#define A_STG L_PRV
#define A_PTAB (A_STG + EPB_MAX * 16)
#define L_PK (L_PRV + 15 * 4096)
#define L_DUM L_PK
#define L_CTAB (L_PK + 2048)
#define L_MRK (L_PK + 8192)
#define TR_STR 4352
#define L_TR (L_PK + 16384)
#define L_WR L_TR
#define L_IMG (L_TR + 8 * TR_STR)
#define L_TOTAL (L_IMG + 32768)
static_assert(L_TOTAL <= 163840 && L_BNC <= 8192 && NPB_MAX * LS * 16 <= 15 * 4096, "LDS budget");
static_assert(L_PRV % 16 == 0 && L_PK % 16 == 0 && L_IMG % 16 == 0 && L_CG % 16 == 0 && L_CCNT % 16 == 0, "alignment");
enum { SY_CPY = 40, SY_C1 = 44, SY_S = 48, SY_C3 = 56, SY_RI = 60, SY_PRE = 64 };

template <class T> static __device__ __forceinline__ T* arg_ptr(u32x16 a, int i) { return (T*)(__attribute__((address_space(1))) T*)((u64)a[i + 1] << 32 | a[i]); }
static __device__ __forceinline__ u32x4 mkdesc(const void* p, unsigned bytes) {
    return (u32x4){(unsigned)(uintptr_t)p, (unsigned)((uintptr_t)p >> 32) & 0xffffu, bytes, 0x00020000u};
}

#define STR2(x) #x
#define STR(x) STR2(x)
__global__ __launch_bounds__(512, 2) void k_fused(const int*, unsigned, unsigned, const int*, unsigned, unsigned, const void*, void*, const void*, const void*,
                                                  const void*, const void*, const void*, void*, int, int, int, unsigned, unsigned, unsigned, unsigned, unsigned)
{
    __shared__ __attribute__((aligned(256))) unsigned char smem[L_TOTAL];
    const int tid = threadIdx.x, bx = blockIdx.x;
    u32x16 ka, kb;
    unsigned so, cp;
    u32x3 d, sv;
    asm volatile("s_load_dwordx16 s[36:51], %3, 0x0\n\ts_load_dwordx16 s[52:67], %3, 0x40\n\ts_getpc_b64 s[68:69]\n\ts_movk_i32 s70, " STR(CODE_LINES) " * 128\n\ts_mov_b32 s71, 0x20000\n\t"
                 "s_lshl4_add_u32 %2, %4, " STR(CPN) " * -3968\n\tv_lshl_add_u32 v8, %5, 12, %2\n\tv_lshlrev_b32 v9, 7, %5\n\tv_max_i32 v8, v8, v9\n\tbuffer_load_dword v7, v8, s[68:71], 0 offen\n\t"
                 "v_mul_u32_u24 v15, 12, %5\n\ts_add_i32 %2, %4, -2\n\ts_waitcnt lgkmcnt(0)\n\ts_mul_i32 %2, %2, s62\n\ts_add_u32 s42, %2, s62\n\ts_min_u32 s38, s38, s42\n\ts_mov_b32 s42, s38\n\t"
                 "s_bitcmp1_b32 %4, 3\n\ts_cbranch_scc1 La_%=\n\tbuffer_load_dwordx3 v[8:10], v15, s[36:39], %2 offen nt\n\tLa_%=:\n\tbuffer_load_dwordx3 v[12:14], v15, s[40:43], %2 offen nt\n\t"
                 "s_cbranch_scc0 Lb_%=\n\tbuffer_load_dwordx3 v[8:10], v15, s[36:39], %2 offen nt\n\tLb_%=:"
                 : "={s[36:51]}"(ka), "={s[52:67]}"(kb), "=&s"(so) : "s"(__builtin_amdgcn_kernarg_segment_ptr()), "s"(bx), "v"(tid)
                 : "memory", "scc", "s68", "s69", "s70", "s71", "v7", "v8", "v9", "v10", "v12", "v13", "v14", "v15");
    asm volatile("" : "={v7}"(cp), "={v[8:10]}"(d), "={v[12:14]}"(sv));
    SB();
    const int* dst = arg_ptr<const int>(ka, 0);
    const int* src = arg_ptr<const int>(ka, 4);
    const u32x2* Y = arg_ptr<const u32x2>(ka, 8);
    unsigned char* ws = arg_ptr<unsigned char>(ka, 10);
    const unsigned short* x = arg_ptr<const unsigned short>(ka, 12);
    const u32x4* ef = arg_ptr<const u32x4>(ka, 14);
    const unsigned short* w_rad = arg_ptr<const unsigned short>(kb, 0);
    const unsigned short* cg = arg_ptr<const unsigned short>(kb, 2);
    const unsigned short* w_mix = arg_ptr<const unsigned short>(kb, 4);
    unsigned short* out = arg_ptr<unsigned short>(kb, 6);
    const int N = kb[8], E = kb[9], epb = kb[10] >> 2;
    const unsigned nb_magic = kb[11], smask = kb[12], cmul = kb[13], po0 = kb[14], po1 = kb[15];
    const int lane = tid & 63;
    const int w = __builtin_amdgcn_readfirstlane(tid >> 6);
    int half;
    const int b = (bx & 3) * 32 + (bx >> 3);
    const int g = lane >> 4, c16 = lane & 15;
    const int node0 = (b * N + 127) >> 7, npb = (((b + 1) * N + 127) >> 7) - node0;
    const unsigned lds0 = (unsigned)(uintptr_t)(__attribute__((address_space(3))) unsigned char*)smem;
    const __amdgpu_buffer_rsrc_t rsw = __builtin_amdgcn_make_buffer_rsrc((void*)ws, 0, W_END, 0x00020000);
    const unsigned le = tid * 3;
    const int base_e = (int)so >> 2;
    const int ne = base_e < 0 ? 0 : max(min(epb, E - base_e), 0);
    const int wd = 4 + ((w - 1 + (w >> 2)) & 3);
    auto task_at = [&](int t) -> unsigned {
        const unsigned q = (t == 0 ? TKF >> (4 * w) : (t == 1 ? TKB >> (4 * w) : TKA >> (4 * wd))) & 15u;
        return L_AT + ((t == 0 || (t == 2 && wd >= 6) ? 40 * half : 40 - 40 * half) + 4 * q) * 4;
    };
    const unsigned ri8 = (wd & 3) * 8;
    unsigned ta[3], cc, cd;
    int k0;
    auto set_ta = [&]() {
#pragma unroll
        for (int i = 0; i < 3; ++i) {
            ta[i] = lds0 + task_at(i == 2 ? 0 : ((w < 4) == (i == 0) ? 2 : 1)) + g * 4;
            asm volatile("" : "+v"(ta[i]));
        }
        cc = __builtin_rotateleft32(0xff0103fdu, ri8); cd = __builtin_rotateleft32(0xfe000204u, ri8); k0 = (int)(signed char)(ring_const() >> ri8);
        asm volatile("" : "+v"(cc), "+v"(cd), "+v"(k0));
    };
    unsigned tag;
    bool slow;
    u32x4 wr[8], cgA;
    auto load_wr = [&]() {
#pragma unroll
        for (int t = 0; t < 8; ++t) wr[t] = *(const u32x4*)(smem + L_WR + t * 1024 + lane * 16);
        cgA = *(const u32x4*)(smem + L_CG + lane * 16);
        asm volatile("" : "+v"(wr[0]), "+v"(wr[1]), "+v"(wr[2]), "+v"(wr[3]), "+v"(wr[4]), "+v"(wr[5]), "+v"(wr[6]), "+v"(wr[7]), "+v"(cgA));
    };

    __builtin_amdgcn_s_setprio(3);
    auto touch = [&](unsigned code) -> unsigned {
        if (code >= 100u)
            return __builtin_amdgcn_raw_buffer_load_b32(__builtin_amdgcn_make_buffer_rsrc(code == 100u ? (void*)x : (void*)ef, 0, code == 100u ? N * 1024 : E * 32, 0x00020000), lane << 21, bx * 128, 0);
        if (code < 8u)
            return __builtin_amdgcn_raw_buffer_load_b32(__builtin_amdgcn_make_buffer_rsrc((void*)(ef + (size_t)base_e * 2), 0, code == 0u ? 0 : ne * 32, 0x00020000), lane * 128, (code - 1u) * 8192u, 0);
        const unsigned j = code < 16u ? (code - 8u) * 128u + b : (code - 16u) * 32u + (bx >> 3);
        return __builtin_amdgcn_raw_buffer_load_b32(__builtin_amdgcn_make_buffer_rsrc((void*)x, 0, N * 1024, 0x00020000), lane * 128,
                                                    (2u * j + ((half ^ (smask >> (j >> 5))) & 1u)) * 8192u, 0);
    };
    constexpr unsigned tl[] = {TLIST};
    const __amdgpu_buffer_rsrc_t rsm = __builtin_amdgcn_make_buffer_rsrc((void*)w_mix, 0, C_DIM * C_DIM * 2, 0x00020000);
    u32x4 mv[8];
    auto load_mv = [&]() {
#pragma unroll
        for (int j = 0; j < 8; ++j)
            mv[j] = __builtin_amdgcn_raw_buffer_load_b128(rsm, g * 512 + c16 * 16 + (w & 3) * 2048 + (j & 1) * 256, (j >> 1) * 8192, 0);
        SB();
    };
    auto make_img = [&]() {
#pragma unroll
        for (int i = 0; i < 4; ++i) {
            u32x4 p0, p1;
#pragma unroll
            for (int j = 0; j < 4; ++j) {
                p0[j] = __builtin_amdgcn_perm(mv[2 * j + 1][i], mv[2 * j][i], 0x05040100u);
                p1[j] = __builtin_amdgcn_perm(mv[2 * j + 1][i], mv[2 * j][i], 0x07060302u);
            }
            const unsigned ia = L_IMG + (c16 >> 2) * 8192 + (w & 3) * 1024 + g * 256 + (c16 & 3) * 64 + i * 16;
            *(u32x4*)(smem + ia) = p0;
            *(u32x4*)(smem + ia + 4096) = p1;
        }
    };
    unsigned tlp = 0u, t0v = 0u, tv[NT2] = {};
    {
        const unsigned sh = 8u * (w & 3);
        unsigned inc[3];
        tlp = __builtin_amdgcn_raw_buffer_load_b32(__builtin_amdgcn_make_buffer_rsrc((void*)ws, 0, w == EW ? 4 : 0, 0x00020000), W_CTL, 0, 0);
        SB();
        {
            const unsigned k0 = (unsigned)tid - L_KEY / 16 < NPB_MAX / 4 ? 127u + L_KEY / 4 - 4u * tid : 0u, k1 = min(k0, 1u);
            *(u32x4*)(smem + tid * 16) = (u32x4){k0, k0 - k1, k0 - 2u * k1, k0 - 3u * k1};
        }
        *(u32x4*)(smem + L_MRK + tid * 16) = (u32x4){0u, 0u, 0u, 0u};
        SB();
        unsigned l16 = lane * 16;
        asm volatile("" : "+v"(l16));
        u32x4 te = (u32x4){0u, 0u, 0u, 0u};
        auto table = [&]() {
            if (__builtin_expect(w == EW, 1)) {
                const unsigned tg = (unsigned)__builtin_amdgcn_readfirstlane((int)tlp) * 1664525u + 1013904223u;
                *(unsigned*)(smem + L_MISC) = tg;
#pragma unroll
                for (int i = 0; i < 2; ++i) __builtin_amdgcn_raw_buffer_store_b128((u32x4){te[i], tg, te[2 + i], tg}, rsw, W_TAB + bx * 2048 + l16 + i * 1024, 0, SC1);
            }
        };
        if (__builtin_expect(bx <= 1, 1)) {
            unsigned v;
            asm volatile("s_cmp_lg_u32 %3, 0\n\t.irp o, 0, 0x400, 0x800, 0xc00, 0x1000, 0x1400, 0x1800, 0x1c00\n\ts_load_dword %0, %1, %2 offset:\\o\n\ts_cbranch_scc0 Lt_%=\n\t.endr\n\tLt_%=:\n\ts_waitcnt lgkmcnt(0)"
                         : "=&s"(v) : "s"(x), "s"((w < 4 ? po0 : po1) + (bx == 0 ? 0x10000 : (w & 3) * 8192)), "s"(bx) : "memory", "scc");
            asm volatile("s_waitcnt vmcnt(0)" : "+{v7}"(cp), "+{v[8:10]}"(d), "+{v[12:14]}"(sv));
            wg_barrier();
            const unsigned ts = (unsigned)__builtin_amdgcn_s_memrealtime();
            table();
            t0v = __builtin_amdgcn_raw_buffer_load_b32(rsm, (tid >> 3) == 32 ? lane * 128 : OOB_OFF, 0, 0);
            if (__builtin_expect(bx * 8 + w == EW, 1)) {
                unsigned s0, s1, v0, v1;
                const unsigned tm = (unsigned)__builtin_amdgcn_s_memtime() * 0x9e3779b1u;
#define RACE(r, o, a, b) "s_load_dword %2, %4, " o " offset:" a "\n\ts_load_dword %3, %4, " o " offset:" b "\n\ts_waitcnt lgkmcnt(1)\n\ts_sleep " "1" "\n\t" \
                             "s_getreg_b32 " r ", hwreg(HW_REG_IB_STS, 8, 1)\n\ts_waitcnt lgkmcnt(0)\n\t"
                asm volatile("s_sleep " "6" "\n\t" RACE("%0", "%5", "0", "0x2000") RACE("%1", "%6", "0x2000", "0") "s_cmp_lg_u32 %0, %1\n\ts_cbranch_scc1 Lr_%=\n\t" RACE("%1", "%5", "0x6000", "0x4000") "Lr_%=:"
                             : "=&s"(s0), "=&s"(s1), "=&s"(v0), "=&s"(v1) : "s"(x), "s"(po0 + (tm >> 29) * 1024u), "s"(po1 + (tm >> 26 & 7u) * 1024u) : "memory", "scc");
                SB();
                unsigned xc;
                asm volatile("s_getreg_b32 %0, hwreg(HW_REG_XCC_ID, 0, 2)" : "=s"(xc));
                unsigned hm = (0xf0f0f0u >> (xc + 4u * s1));
                const unsigned tg = (unsigned)__builtin_amdgcn_readfirstlane((int)tlp) * 1664525u + 1013904223u;
                __builtin_amdgcn_raw_buffer_store_b128((u32x4){hm, tg, hm, tg}, rsw, lane < 8 ? W_DEC + l16 : OOB_OFF, 0, SC1);
            }
            asm volatile("" : "+v"(t0v));
            while ((unsigned)__builtin_amdgcn_s_memrealtime() - ts < IDLE) __builtin_amdgcn_s_sleep(1);
        }
        int bq = bx;
        asm volatile("" : "+s"(bq));
        if (__builtin_expect(bq > 1, 1)) {
        wg_barrier();
        const __amdgpu_buffer_rsrc_t rsy = __builtin_amdgcn_make_buffer_rsrc((void*)Y, 0, ka[2] * 2, 0x00020000);
        u32x4 ya = __builtin_amdgcn_raw_buffer_load_b128(rsy, le * 8, so * 2, NT);
        u32x2 yb = __builtin_amdgcn_raw_buffer_load_b64(rsy, le * 8 + 16, so * 2, NT);
        SB();
        {
            u64 pb = (u64)(uintptr_t)cg;
            unsigned pn = (w & 2) << 6;
            asm("s_cmp_eq_u32 %2, 3\n\ts_cselect_b64 %0, %4, %0\n\ts_cselect_b32 %1, 0x1000, %1\n\t"
                "s_cmp_eq_u32 %2, 4\n\ts_cselect_b64 %0, %5, %0\n\ts_cselect_b32 %1, 0x400, %1\n\ts_cmp_gt_u32 %2, 4\n\ts_cselect_b64 %0, %6, %0\n\ts_cselect_b32 %1, %7, %1"
                : "+s"(pb), "+s"(pn) : "s"(w), "s"(cg), "s"(w_rad), "s"(w_mix + (bx >> 3) * 512), "s"(ef + (base_e * 2 + (7 - w) * 512)), "s"(max(min(ne * 32, NT0 * 8192) - (7 - w) * 8192, 0)) : "scc");
            t0v = __builtin_amdgcn_raw_buffer_load_b32(__builtin_amdgcn_make_buffer_rsrc((void*)pb, 0, pn, 0x00020000), lane * 128, 0, 0);
        }
        SB();
#pragma unroll
        for (int r = 0; r < 3; ++r) inc[r] = (int)le + r < ne ? 1u << sh : 0u;
        asm volatile("" : "+v"(inc[0]), "+v"(inc[1]), "+v"(inc[2]));
        unsigned m0 = below_mask(w, 0), m1 = below_mask(w, 1);
        asm volatile("" : "+s"(m0), "+s"(m1));
        SB();
        unsigned bin[3], rank[3], ba[3];
        asm volatile("s_waitcnt vmcnt(4)" : "+{v7}"(cp), "+{v[8:10]}"(d), "+{v[12:14]}"(sv));
#pragma unroll
        for (int r = 0; r < 3; ++r) {
            bin[r] = __umulhi(d[r], nb_magic);
            ba[r] = ((((sv[r] >> 3) ^ (smask >> ((sv[r] >> 9) & 31u))) & 1u) * 512u + bin[r] * 4u) & 1023u;
            rank[r] = atomicAdd((unsigned*)(smem + L_PCNT + (w >> 2) * 1024 + ba[r]), inc[r]);
        }
        SB();
        unsigned rw1[3];
#pragma unroll
        for (int r = 0; r < 3; ++r) { rw1[r] = (d[r] & 127u) | ((le + r) << 7); asm volatile("" : "+v"(rw1[r])); }
        unsigned pa[3];
#pragma unroll
        for (int r = 0; r < 3; ++r) { pa[r] = inc[r] != 0u ? A_PTAB + w * 512 + (ba[r] >> 1) : L_MISC + 8; asm volatile("" : "+v"(pa[r])); }
        wg_barrier();
        u32x2 c[4];
#pragma unroll
        for (int i = 0; i < 4; ++i) c[i] = *(const u32x2*)(smem + L_PCNT + i * 512 + lane * 8);
        SB();
#pragma unroll
        for (int r = 0; r < 3; ++r) { rank[r] = inc[r] != 0u ? (rank[r] >> sh) & 0xffu : (L_DUM - A_STG) / 16 + lane; asm volatile("" : "+v"(rank[r])); }
        unsigned t[4], o[4], r0, r2, tt;
#define DOT(d, c, m, a) "v_dot4_u32_u8 " d ", " c ", " m ", " a "\n\t"
#define SCN(c) "v_add_u32_dpp %9, %9, %9 " c "\n\t"
        asm volatile(DOT("%0", "%11", "%21", "0") DOT("%1", "%12", "%21", "0") DOT("%2", "%13", "%21", "0") DOT("%3", "%14", "%21", "0")
                     DOT("%0", "%15", "%21", "%0") DOT("%1", "%16", "%21", "%1") DOT("%2", "%17", "%21", "%2") DOT("%3", "%18", "%21", "%3")
                     DOT("%4", "%11", "%19", "0") DOT("%6", "%13", "%19", "0") DOT("%5", "%12", "%19", "%0") DOT("%7", "%14", "%19", "%2")
                     "v_add_u32 %8, %0, %1\n\tv_add_u32 %9, %2, %3\n\tv_lshl_or_b32 %8, %9, 16, %8\n\t" DOT("%4", "%15", "%20", "%4") DOT("%5", "%16", "%20", "%5")
                     "v_add_u32_dpp %9, %8, %8 row_shr:1 row_mask:0xf bank_mask:0xf bound_ctrl:1\n\t" DOT("%6", "%17", "%20", "%6") DOT("%7", "%18", "%20", "%7")
                     SCN("row_shr:2 row_mask:0xf bank_mask:0xf bound_ctrl:1") "v_lshl_or_b32 %4, %5, 16, %4\n\ts_nop 0\n\t"
                     SCN("row_shr:4 row_mask:0xf bank_mask:0xf bound_ctrl:1") "v_lshl_or_b32 %6, %7, 16, %6\n\ts_nop 0\n\t"
                     SCN("row_shr:8 row_mask:0xf bank_mask:0xf bound_ctrl:1") "s_nop 1\n\t" SCN("row_bcast:15 row_mask:0xa bank_mask:0xf") "s_nop 1\n\t" SCN("row_bcast:31 row_mask:0xc bank_mask:0xf")
                     "v_sub_u32 %8, %9, %8\n\tv_readlane_b32 %10, %9, 63\n\tv_lshrrev_b32 %9, 16, %8\n\tv_and_b32 %8, 0xffff, %8\n\ts_and_b32 %10, %10, 0xffff\n\t"
                     "v_mad_u32_u24 %4, %8, %22, %4\n\tv_add_u32 %9, %10, %9\n\tv_mad_u32_u24 %6, %9, %22, %6\n\tds_write2st64_b32 %23, %4, %6 offset1:1"
                     : "=&v"(t[0]), "=&v"(t[1]), "=&v"(t[2]), "=&v"(t[3]), "=&v"(o[0]), "=&v"(o[1]), "=&v"(o[2]), "=&v"(o[3]), "=&v"(r0), "=&v"(r2), "=&s"(tt)
                     : "v"(c[0].x), "v"(c[0].y), "v"(c[1].x), "v"(c[1].y), "v"(c[2].x), "v"(c[2].y), "v"(c[3].x), "v"(c[3].y), "s"(m0), "s"(m1), "s"(0x01010101u), "s"(0x10001u),
                       "v"(lds0 + A_PTAB + w * 512 + lane * 4) : "memory", "scc");
        unsigned pos[3];
#pragma unroll
        for (int r = 0; r < 3; ++r) pos[r] = *(const unsigned short*)(smem + pa[r]);
        SB();
        te = (u32x4){r0 | t[0] << 16, (r0 + t[0]) | t[1] << 16, r2 | t[2] << 16, (r2 + t[2]) | t[3] << 16};
        u32x4 rec[3];
#pragma unroll
        for (int r = 0; r < 3; ++r) rec[r] = (u32x4){r < 2 ? ya[2 * r] : yb.x, sv[r], r < 2 ? ya[2 * r + 1] : yb.y, rw1[r]};
        asm volatile("" : "+v"(rec[0]), "+v"(rec[1]), "+v"(rec[2]));
        table();
#pragma unroll
        for (int r = 0; r < 3; ++r) *(u32x4*)(smem + A_STG + (pos[r] + rank[r]) * 16) = rec[r];
        }
        wg_barrier();
        const __amdgpu_buffer_rsrc_t rsr = __builtin_amdgcn_make_buffer_rsrc((void*)(ws + W_RA + bx * (EPB_MAX * 16)), 0, (ne * 16 + 127) & ~127, 0x00020000);
        u32x4 va[3];
        const unsigned tv0 = *(const unsigned*)(smem + L_MISC);
#pragma unroll
        for (int i = 0; i < 3; ++i) va[i] = *(const u32x4*)(smem + A_STG + (tid ^ 256) * 16 + 8192 * i);
#pragma unroll
        for (int i = 0; i < 3; ++i) __builtin_amdgcn_raw_buffer_store_b128((u32x4){va[i].x, va[i].y | tv0 << 14, va[i].z, va[i].w | (tv0 & 0xfffc0000u)}, rsr, (tid ^ 256) * 16, 8192 * i, SC1);
        tag = (unsigned)__builtin_amdgcn_readfirstlane((int)tv0);
        lds_arrive(lds0 + SY_CPY);
    }
    u32x4 dx = mkdesc(x, (unsigned)N * 1024u), de = mkdesc(ef, (unsigned)E * 32u);
    asm volatile("" : "+s"(dx), "+s"(de));
    if (w < 4) {
        asm volatile("" :: "v"(t0v), "v"(tlp));
        const unsigned sh = 8u * w, mb = 0x01010101u & ((1u << sh) - 1u);
        const unsigned T0 = tag << 14, T1 = tag & 0xfffc0000u;
        const unsigned to = W_TAB + tid * 2048 + (b & 1) * 1024 + (b >> 1) * 16;
        __builtin_amdgcn_s_sleep(4);
        u32x4 en = __builtin_amdgcn_raw_buffer_load_b128(rsw, to, 0, SC1);
        SB();
        __builtin_amdgcn_s_sleep(8);
        SB();
        u32x2 dec = __builtin_amdgcn_raw_buffer_load_b64(rsw, W_DEC, 0, SC1);
#pragma unroll 1
        for (unsigned spin = 0; spin < SPIN_MAX; ++spin) {
            if (__builtin_amdgcn_ballot_w64(((en.y ^ tag) | (en.w ^ tag) | (dec.y ^ tag)) != 0u) == 0ull) break;
            __builtin_amdgcn_s_sleep(2);
            asm volatile("" ::: "memory");
            if (((en.y ^ tag) | (en.w ^ tag)) != 0u) en = __builtin_amdgcn_raw_buffer_load_b128(rsw, to, 0, SC1);
            dec = __builtin_amdgcn_raw_buffer_load_b64(rsw, W_DEC, 0, SC1);
        }
        half = (__builtin_amdgcn_readfirstlane((int)dec.x) >> (bx & 7)) & 1;
        if (half) en.x = en.z;
        const unsigned rc = en.x >> 16;
        const unsigned incl = (unsigned)wave_incl_scan((int)rc);
        const int twf = __builtin_amdgcn_readlane((int)incl, 63), tw = twf <= 64 * NRND ? twf : 0;
        const bool more = tw > 64 * (NRND - 1);
        unsigned ma = L_MRK + w * (256 * NRND) + (incl - rc) * 4;
        asm volatile("" : "+v"(ma));
        *(unsigned*)(smem + (((rc != 0u) & (incl <= 64u * NRND)) ? ma : L_DUM + lane * 4)) = (unsigned)lane * EPB_MAX + (en.x & 0xffffu) - (incl - rc);
        unsigned ra[NRND];
        asm volatile("" ::: "memory");
#pragma unroll
        for (int r = 0; r < NRND; ++r) ra[r] = *(const unsigned*)(smem + L_MRK + w * (256 * NRND) + r * 256 + lane * 4);
        unsigned carry = 0u;
        u32x4 A[NRND];
#pragma unroll
        for (int r = 0; r < NRND; ++r) {
            if (r == NRND - 1) { if (more) ra[r] = wave_max_scan(ra[r]); asm volatile("" : "+v"(ra[r])); }
            else if (r % 3 == 0) wave_max_scan3(ra[r], ra[r + 1], ra[r + 2]);
            const unsigned m = max(ra[r], carry);
            carry = max(carry, (unsigned)__builtin_amdgcn_readlane((int)ra[r], 63));
            ra[r] = r * 64 + lane < tw ? (m + (unsigned)(r * 64 + lane)) * 16u : OOB_OFF >> 1;
            A[r] = __builtin_amdgcn_raw_buffer_load_b128(rsw, ra[r], W_RA + 64 * w * (EPB_MAX * 16), SC1);
        }
        SB();
        lds_arrive(lds0 + SY_RI, 16u + half);
        unsigned pe[NRND], inc[NRND], inck[NRND], lim[NRND];
#pragma unroll
        for (int r = 0; r < NRND; ++r) {
            pe[r] = __umul24(__umulhi(ra[r], 209716u) + 64u * w - 2u, (unsigned)epb * 32u);
            inc[r] = ra[r] < 0x40000000u ? 1u << sh : 0u;
            inck[r] = ra[r] < 0x40000000u ? 128u : 0u;
            lim[r] = ra[r] < 0x40000000u ? 16383u : ~0u;
            asm volatile("" : "+v"(pe[r]), "+v"(inc[r]), "+v"(inck[r]), "+v"(lim[r]));
        }
        if (twf > 64 * NRND) lds_arrive(lds0 + SY_S, 256u);
        set_ta();
        SB();
        unsigned rank[NRND], nc[NRND];
        auto slot = [&](int r) {
            nc[r] = inc[r] != 0u ? L_CTAB + w * 640 + nc[r] * 2 : L_MISC + 8;
            A[r] = (u32x4){A[r].y << 10, ((A[r].w >> 2) & 0xffe0u) + pe[r], A[r].x, A[r].z};
            asm volatile("" : "+v"(nc[r]), "+v"(A[r]));
            SB();
        };
#pragma unroll
        for (int r = 0; r < NRND; ++r) {
            if (r == NRND - 1 && !more) { rank[r] = 0u; nc[r] = 0u; continue; }
            A[r].y ^= T0; A[r].w ^= T1;
            if (__builtin_expect(__builtin_amdgcn_ballot_w64((A[r].y | (A[r].w >> 4)) > lim[r]) != 0ull, 0)) {
#pragma unroll 1
                for (unsigned spin = 0; spin < SPIN_MAX; ++spin) {
                    asm volatile("" ::: "memory");
                    A[r] = __builtin_amdgcn_raw_buffer_load_b128(rsw, ra[r], W_RA + 64 * w * (EPB_MAX * 16), SC1);
                    A[r].y ^= T0; A[r].w ^= T1;
                    if (__builtin_amdgcn_ballot_w64((A[r].y | (A[r].w >> 4)) > lim[r]) == 0ull) break;
                }
            }
            const unsigned nl = (A[r].w - node0) & 127u;
            nc[r] = nl * 4u + (((__umul24(A[r].y, cmul) >> 16) ^ ((nl >= 40u) == (half != 0) ? 3u : 0u)) & 3u);
            if (r >= 4) nc[r] = inc[r] != 0u ? nc[r] : (unsigned)lane;
            rank[r] = atomicAdd((unsigned*)(smem + L_CCNT + nc[r] * 4), inc[r]);
            atomicAdd((unsigned*)(smem + L_KEY + (nc[r] & 0x1fcu)), inck[r]);
            SB();
            if (r < 3) slot(r);
        }
        lds_arrive(lds0 + SY_C1);
        unsigned d0 = lane * LS, d1 = (64 + c16) * LS, ca0 = L_CTAB + w * 640 + lane * 8, ca1 = L_CTAB + w * 640 + (64 + c16) * 8;
        asm volatile("" : "+v"(d0), "+v"(d1), "+v"(ca0), "+v"(ca1));
#pragma unroll
        for (int r = 3; r < NRND; ++r) slot(r);
#pragma unroll
        for (int r = 0; r < NRND; ++r) {
            rank[r] = inc[r] != 0u ? (rank[r] >> sh) & 0xffu : (L_DUM - L_PRV) / 16 + lane;
            asm volatile("" : "+v"(rank[r]));
        }
        lds_wait(lds0 + SY_PRE, 4);
        {
            u32x4 c0, c1;
            unsigned cn;
            do {
                asm volatile("ds_read_b32 %0, %3 offset:%c6\n\tds_read_b128 %1, %4 offset:%c7\n\tds_read_b128 %2, %5 offset:%c8\n\ts_waitcnt lgkmcnt(0)"
                             : "=&v"(cn), "=&v"(c0), "=&v"(c1) : "v"(lds0), "v"(lds0 + lane * 16), "v"(lds0 + c16 * 16), "i"(SY_C1), "i"(L_CCNT), "i"(L_CCNT + 1024) : "memory");
            } while (__builtin_amdgcn_readfirstlane((int)cn) < 4);
            unsigned p0[4], p1[4];
#pragma unroll
            for (int c = 0; c < 4; ++c) {
                p0[c] = bsum(c0[c], mb, d0); p1[c] = bsum(c1[c], mb, d1);
                if (c < 3) { d0 = bsum(c0[c], 0x01010101u, d0); d1 = bsum(c1[c], 0x01010101u, d1); }
            }
            *(u32x2*)(smem + ca0) = (u32x2){p0[0] | (p0[1] << 16), p0[2] | (p0[3] << 16)};
            *(u32x2*)(smem + ca1) = (u32x2){p1[0] | (p1[1] << 16), p1[2] | (p1[3] << 16)};
        }
        asm volatile("" ::: "memory");
#pragma unroll
        for (int r = 0; r < NRND; ++r) nc[r] = *(const unsigned short*)(smem + nc[r]);
        SB();
#pragma unroll
        for (int r = 0; r < NRND; ++r) {
            if (r == NRND - 1 && !more) continue;
            *(u32x4*)(smem + L_PRV + (nc[r] + rank[r]) * 16) = A[r];
        }
        load_wr();
        lds_arrive(lds0 + SY_C3);
    } else {
        const int q = w - 4, t2 = tid - 256;
        __builtin_amdgcn_s_setprio(2);
        unsigned wv[8], cv[4];
        {
            const __amdgpu_buffer_rsrc_t rsa = __builtin_amdgcn_make_buffer_rsrc((void*)w_rad, 0, DE_DIM * C_DIM * 2, 0x00020000);
            const __amdgpu_buffer_rsrc_t rsg = __builtin_amdgcn_make_buffer_rsrc((void*)cg, 0, 128, 0x00020000);
#pragma unroll
            for (int j = 0; j < 8; ++j) wv[j] = __builtin_amdgcn_raw_buffer_load_b32(rsa, (g & 1) * 2048 + 64 * q + 4 * c16 + 256 * j, 0, 0);
#pragma unroll
            for (int f = 0; f < 4; ++f) cv[f] = __builtin_amdgcn_raw_buffer_load_b32(rsg, (lane & 3) * 32 + (lane & 8u) / 2 + 8 * f, 0, 0);
            SB();
        }
        lds_wait(lds0 + SY_CPY, 8);
#pragma unroll
        for (int i = 0; i < 15; ++i) *(u32x4*)(smem + L_PRV + (t2 + 256 * i) * 16) = (u32x4){OOB_OFF, OOB_OFF, 0u, 0u};
        SB();
        asm volatile("" :: "v"(t0v), "v"(tlp));
        load_mv();
        {
            float f0[8], f1[8];
#pragma unroll
            for (int j = 0; j < 8; ++j) {
                const float a = __uint_as_float(wv[j] << 16) * -1.44269504088896340736f, c = __uint_as_float(wv[j] & 0xffff0000u) * -1.44269504088896340736f;
                const unsigned hp = g < 2 ? 0u : pack2bf(a, c);
                f0[j] = a - __uint_as_float(hp << 16);
                f1[j] = c - __uint_as_float(hp & 0xffff0000u);
            }
            *(u32x4*)(smem + L_WR + (2 * q) * 1024 + lane * 16) = (u32x4){pack2bf(f0[0], f0[1]), pack2bf(f0[2], f0[3]), pack2bf(f0[4], f0[5]), pack2bf(f0[6], f0[7])};
            *(u32x4*)(smem + L_WR + (2 * q + 1) * 1024 + lane * 16) = (u32x4){pack2bf(f1[0], f1[1]), pack2bf(f1[2], f1[3]), pack2bf(f1[4], f1[5]), pack2bf(f1[6], f1[7])};
            float a[4];
#pragma unroll
            for (int f = 0; f < 4; ++f) a[f] = __uint_as_float((lane & 4) ? cv[f] & 0xffff0000u : cv[f] << 16) * -0.69314718055994530942f;
            const unsigned h0 = pack2bf(a[0], a[1]), h1 = pack2bf(a[2], a[3]);
            *(u32x4*)(smem + L_CG + lane * 16) = (u32x4){h0, h1, pack2bf(a[0] - __uint_as_float(h0 << 16), a[1] - __uint_as_float(h0 & 0xffff0000u)),
                                                        pack2bf(a[2] - __uint_as_float(h1 << 16), a[3] - __uint_as_float(h1 & 0xffff0000u))};
        }
        lds_arrive(lds0 + SY_PRE);
        make_img();
        lds_wait(lds0 + SY_PRE, 4);
        load_wr();
        if (w == 7) {
            lds_arrive(lds0 + SY_C3);
            half = (lds_wait<1>(lds0 + SY_RI, 64) & 15u) != 0u;
#pragma unroll
            for (int i = 0; i < NT2; ++i) tv[i] = touch(tl[i]);
            SB();
            set_ta();
        } else {
            half = (lds_wait<1>(lds0 + SY_RI, 16) & 15u) != 0u;
            set_ta();
            const int n5 = min(27 * q + (lane >> 1), NPB_MAX - 1);
            const bool kp = (n5 >= 40) == (half != 0);
            u64 lpos = kp ? len_pos(true) : len_pos(false);
            unsigned an = lds0 + n5 * 4, ak = lds0 + L_KEY + (n5 >= 40 ? 160 : 0) + (lane & 1) * 80;
            unsigned aa = L_AT + (n5 >= 40 ? 160 : 0), al = L_LEN + (kp ? 4 : 0), av = (unsigned)n5 | (lds0 + L_PRV + n5 * (LS * 16)) << 8;
            asm volatile("" : "+v"(an), "+v"(ak), "+v"(aa), "+v"(al), "+v"(av), "+v"(lpos));
            lds_wait<1>(lds0 + SY_C1, 3);
            __builtin_amdgcn_s_setprio(3);
            unsigned kn, cn;
            u32x4 dk[5];
            do {
                asm volatile("ds_read_b32 %0, %7 offset:%c10\n\tds_read_b32 %1, %8 offset:%c11\n\tds_read_b128 %2, %9\n\tds_read_b128 %3, %9 offset:16\n\tds_read_b128 %4, %9 offset:32\n\t"
                             "ds_read_b128 %5, %9 offset:48\n\tds_read_b128 %6, %9 offset:64\n\ts_waitcnt lgkmcnt(0)"
                             : "=&v"(cn), "=&v"(kn), "=&v"(dk[0]), "=&v"(dk[1]), "=&v"(dk[2]), "=&v"(dk[3]), "=&v"(dk[4]) : "v"(lds0), "v"(an), "v"(ak), "i"(SY_C1), "i"(L_KEY) : "memory");
            } while (__builtin_amdgcn_readfirstlane((int)cn) < 4);
            unsigned sg[4] = {0u, 0u, 0u, 0u};
#pragma unroll
            for (int k = 0; k < 20; ++k) sg[k & 3] = __builtin_amdgcn_alignbit(sg[k & 3], kn - dk[k >> 2][k & 3], 31);
            int c = __builtin_popcount(sg[0] | sg[1] << 8 | sg[2] << 16 | sg[3] << 24);
            c += __builtin_amdgcn_update_dpp(0, c, 0xB1, 0xf, 0xf, false);
            *(unsigned*)(smem + aa + c * 4) = av;
            smem[(c & 3) == 0 ? al + ((unsigned)(lpos >> c) & 15u) : L_LEN + 20] = (unsigned char)max(kn >> 7, c < 8 ? 12u : 8u);
            lds_arrive(lds0 + SY_S, __builtin_amdgcn_ballot_w64((kn >> 7) > DEG_MAX) != 0ull ? 257u : 1u);
        }
        __builtin_amdgcn_s_setprio(3);
    }
    unsigned ax[3], lh, cn;
    u32x4 ln;
    {
        if (w == 7) lds_wait<2, 255>(lds0 + SY_S, 1);
        do {
            asm volatile("ds_read_b32 %0, %6 offset:%c10\n\tds_read_b128 %1, %6 offset:%c11\n\tds_read_b32 %2, %6 offset:%c12\n\tds_read_b32 %3, %7\n\tds_read_b32 %4, %8\n\t"
                         "ds_read_b32 %5, %9\n\ts_waitcnt lgkmcnt(0)"
                         : "=&v"(cn), "=&v"(ln), "=&v"(lh), "=&v"(ax[0]), "=&v"(ax[1]), "=&v"(ax[2])
                         : "v"(lds0), "v"(ta[0]), "v"(ta[1]), "v"(ta[2]), "i"(SY_S), "i"(L_LEN), "i"(L_LEN + 16) : "memory");
            cn = (unsigned)__builtin_amdgcn_readfirstlane((int)cn);
        } while ((cn & 255u) < 3u);
        slow = cn >= 256u;
    }
    const int p8 = __builtin_amdgcn_sdot4((int)(ln.x + ln.y), (int)cc, __builtin_amdgcn_sdot4((int)(ln.w + lh), (int)cc,
                   __builtin_amdgcn_sdot4((int)ln.z, (int)cd, k0, false), false), false);
    const int ld = (__builtin_amdgcn_readfirstlane((int)ln.z) >> ri8) & 255, p2 = min(max(__builtin_amdgcn_readfirstlane(p8) >> 3, 4), ld - 8);
    const int l2 = w < 4 ? ld - p2 : p2, od = (w < 4) == (wd >= 6) ? ld - l2 : 0;
    const unsigned aX = (ax[0] >> 8) + ((w < 4 ? od : 0) + (c16 & 3)) * 16, aF = (ax[0] >> 8) + ((w < 4 ? od : 0) + (c16 >> 2)) * 16 + 4;
    const unsigned lane_off = (unsigned)c16 * 16u;
    const unsigned aWw = L_BNC + w * 512 + lane * 8;
    const unsigned aWr = L_BNC + w * 512 + g * 128 + (c16 & 3) * 32 + (c16 >> 2) * 8;
    auto row_of = [=](unsigned at) -> unsigned {
        const unsigned nl = at & 0xffu;
        return nl < (unsigned)npb ? ((unsigned)node0 + nl) * 1024u + lane_off : OOB_OFF;
    };
#pragma unroll
    for (int i = 0; i < NT2; ++i) asm volatile("" :: "v"(tv[i]));
    if (!slow) {
        u32x32 oxa, oxb;
        u32x4 S;
        u32x2 ef0, efN;
        unsigned tmp;
        __builtin_amdgcn_s_waitcnt(0x0F70);
        asm volatile(
            "Lw2_%=:\n"
            "ds_read_b32 v220, v219\n"
            "ds_read2_b32 v[232:233], v211 offset1:16\n"
            "ds_read_b128 v[196:199], v210\n"
            "s_waitcnt lgkmcnt(1)\n"
            "v_readfirstlane_b32 %[tmp], v220\n"
            "s_cmp_lt_u32 %[tmp], 5\n"
            "s_cbranch_scc1 Lw2_%=\n"
            "v_or_b32 v232, v232, v229\n"
            "v_or_b32 v233, v233, v229\n"
            "buffer_load_dwordx2 v[238:239], v232, %[rse], 0 offen\n"
            "s_waitcnt lgkmcnt(0)\n"
            "v_add_u32_dpp v220, v196, v214 quad_perm:[0,0,0,0] row_mask:0xf bank_mask:0xf\n"
            "v_add_u32_dpp v221, v196, v214 quad_perm:[1,1,1,1] row_mask:0xf bank_mask:0xf\n"
            "v_add_u32_dpp v222, v196, v214 quad_perm:[2,2,2,2] row_mask:0xf bank_mask:0xf\n"
            "v_add_u32_dpp v223, v196, v214 quad_perm:[3,3,3,3] row_mask:0xf bank_mask:0xf\n"
            "buffer_load_dwordx4 v[32:35], v220, %[rsx], 0 offen\n"
            "buffer_load_dwordx4 v[36:39], v220, %[rsx], 0 offen offset:256\n"
            "buffer_load_dwordx4 v[48:51], v221, %[rsx], 0 offen\n"
            "buffer_load_dwordx4 v[52:55], v221, %[rsx], 0 offen offset:256\n"
            "buffer_load_dwordx2 v[232:233], v233, %[rse], 0 offen\n"
            "buffer_load_dwordx4 v[64:67], v222, %[rsx], 0 offen\n"
            "buffer_load_dwordx4 v[68:71], v222, %[rsx], 0 offen offset:256\n"
            "buffer_load_dwordx4 v[80:83], v223, %[rsx], 0 offen\n"
            "buffer_load_dwordx4 v[84:87], v223, %[rsx], 0 offen offset:256\n"
            "buffer_load_dwordx4 v[40:43], v220, %[rsx], 0 offen offset:512\n"
            "buffer_load_dwordx4 v[44:47], v220, %[rsx], 0 offen offset:768\n"
            "buffer_load_dwordx4 v[56:59], v221, %[rsx], 0 offen offset:512\n"
            "buffer_load_dwordx4 v[60:63], v221, %[rsx], 0 offen offset:768\n"
            "buffer_load_dwordx4 v[72:75], v222, %[rsx], 0 offen offset:512\n"
            "buffer_load_dwordx4 v[76:79], v222, %[rsx], 0 offen offset:768\n"
            "buffer_load_dwordx4 v[88:91], v223, %[rsx], 0 offen offset:512\n"
            "buffer_load_dwordx4 v[92:95], v223, %[rsx], 0 offen offset:768\n"
            : "={v[32:63]}"(oxa), "={v[64:95]}"(oxb), "={v[238:239]}"(ef0), "={v[196:199]}"(S), "={v[232:233]}"(efN), [tmp] "=&s"(tmp)
            : [rse] "s"(de), [rsx] "s"(dx), [ax] "{v210}"(aX), [af] "{v211}"(aF), [lo] "{v214}"(lane_off), [eo] "{v229}"((lane & 3) * 8), [sy] "{v219}"(lds0 + SY_C3)
            : "memory", "scc", "v220", "v221", "v222", "v223");
        if (bx == 0 && w == 4) flag_store(ws + W_CTL, tag);
        const int lf = (__builtin_amdgcn_readfirstlane((int)(w < 4 ? ln.w : lh)) >> (8 * (w & 3))) & 255, l1 = (__builtin_amdgcn_readfirstlane((int)(w < 4 ? ln.x : ln.y)) >> (8 * (w & 3))) & 255;
        const int e2 = lf + l1 + l2;
        int nst = (e2 + 3) >> 2;
        const unsigned r2 = row_of(ax[1]);
        u32x32 owr;
        u32x16 omi;
        u32x8 ot;
#pragma unroll
        for (int i = 0; i < 32; ++i) owr[i] = wr[i >> 2][i & 3];
        const int pw = (w + (w >> 2)) & 3;
        const unsigned trb = lds0 + L_TR + w * TR_STR;
        omi[2] = aX + 64; omi[3] = aF + 64;
        omi[4] = lds0 + aWw; omi[5] = lds0 + aWr; omi[6] = lane_off; omi[7] = W_AGG + row_of(ax[0]); omi[8] = W_AGG + r2;
        omi[9] = row_of(ax[2]); omi[10] = w >= 6 ? r2 : OOB_OFF; omi[15] = ~tag;
        ot[0] = (lane & 3) == 0 ? 0x3f80u : ((lane & 3) == 1 ? 0x3f800000u : 0u); ot[1] = (lane & 3) == 2 ? 0x3f80u : ((lane & 3) == 3 ? 0x3f800000u : 0u);
        ot[2] = trb + g * (4 * 272) + c16 * 16; ot[3] = trb + c16 * 272 + g * 16; ot[4] = lds0 + L_IMG + lane * 16; ot[5] = (lane & 3) * 8; ot[7] = lds0 + L_PK + pw * 4096 + lane * 16;
        int rem = w < 4 ? l2 : l1, lens = ((w < 4 ? l1 : l2) - 4) | 0xff00, fm = w < 4 ? 0x802 : (w < 6 ? 0x810 : 0xb0), pb = 0;
        const int j1 = (w < 4 ? -od - l2 : od - l1) * 16, j2 = (w < 4 ? -l1 : -od - l2) * 16;
        u32x2 jmp = (u32x2){(ax[1] >> 8) - (ax[0] >> 8) + j1, (ax[2] >> 8) - (ax[1] >> 8) + j2};
        unsigned bpa = c16 * 16 + (g & 1) * 8;
        unsigned evt = (unsigned)__builtin_amdgcn_readfirstlane(w < 4 ? evt_of(PTO) : (w < 6 ? evt_of(PTY) : evt_of(PTZ))) | 6u << (nst - (rem >> 2)) | 6u << (nst - ((l1 + l2) >> 2));
        u64 ra;
        asm volatile(
            "s_nop 1\n"
            "v_mfma_f32_4x4x4_16b_bf16 v[204:207], v[202:203], v[198:199], 0\n"
            "v_mov_b64 v[0:1], 0\n"
            "v_mov_b64 v[2:3], 0\n"
            "v_mov_b64 v[4:5], 0\n"
            "v_mov_b64 v[6:7], 0\n"
            "v_mfma_f32_4x4x4_16b_bf16 v[204:207], v[200:201], v[198:199], v[204:207]\n"
            "v_mov_b64 v[8:9], 0\n"
            "v_mov_b64 v[10:11], 0\n"
            "v_mov_b64 v[12:13], 0\n"
            "v_mov_b64 v[14:15], 0\n"
            "v_mov_b64 v[16:17], 0\n"
            "v_mov_b64 v[18:19], 0\n"
            "v_mov_b64 v[20:21], 0\n"
            "v_mov_b64 v[22:23], 0\n"
            "v_mov_b64 v[24:25], 0\n"
            "v_mov_b64 v[26:27], 0\n"
            "v_mov_b64 v[28:29], 0\n"
            "v_mov_b64 v[30:31], 0\n"
            "v_cvt_pk_bf16_f32 v204, v204, v205\n"
            "v_cvt_pk_bf16_f32 v205, v206, v207\n"
            "ds_write_b64 v212, v[204:205]\n"
            "ds_read_b128 v[196:199], v210\n"
            "ds_read_b64 v[208:209], v213\n"
            "s_waitcnt vmcnt(17)\n"
            "ds_bpermute_b32 v192, v234, v238\n"
            "ds_bpermute_b32 v193, v234, v239\n"
            "ds_bpermute_b32 v194, v234, v238 offset:4\n"
            "ds_bpermute_b32 v195, v234, v239 offset:4\n"
            "s_waitcnt lgkmcnt(0)\n"
            "v_mfma_f32_16x16x32_bf16 v[176:179], v[192:195], v[96:99], 0\n"
            "v_mfma_f32_16x16x32_bf16 v[180:183], v[192:195], v[100:103], 0\n"
            "v_mfma_f32_16x16x32_bf16 v[184:187], v[192:195], v[104:107], 0\n"
            "v_mfma_f32_16x16x32_bf16 v[188:191], v[192:195], v[108:111], 0\n"
            "v_mfma_f32_16x16x32_bf16 v[128:131], v[192:195], v[112:115], 0\n"
            "v_mfma_f32_16x16x32_bf16 v[132:135], v[192:195], v[116:119], 0\n"
            "s_nop 2\n"
            "v_exp_f32 v144, v176\n"
            "v_exp_f32 v145, v177\n"
            "v_exp_f32 v146, v178\n"
            "v_exp_f32 v147, v179\n"
            "v_exp_f32 v148, v180\n"
            "v_exp_f32 v149, v181\n"
            "v_exp_f32 v150, v182\n"
            "v_exp_f32 v151, v183\n"
            "v_exp_f32 v152, v184\n"
            "v_exp_f32 v153, v185\n"
            "v_exp_f32 v154, v186\n"
            "v_exp_f32 v155, v187\n"
            "v_exp_f32 v156, v188\n"
            "v_exp_f32 v157, v189\n"
            "v_exp_f32 v158, v190\n"
            "v_exp_f32 v159, v191\n"
            "v_pk_add_f32 v[144:145], v[144:145], 1.0 op_sel_hi:[1,0]\n"
            "v_pk_add_f32 v[146:147], v[146:147], 1.0 op_sel_hi:[1,0]\n"
            "v_pk_add_f32 v[148:149], v[148:149], 1.0 op_sel_hi:[1,0]\n"
            "v_pk_add_f32 v[150:151], v[150:151], 1.0 op_sel_hi:[1,0]\n"
            "v_pk_add_f32 v[152:153], v[152:153], 1.0 op_sel_hi:[1,0]\n"
            "v_pk_add_f32 v[154:155], v[154:155], 1.0 op_sel_hi:[1,0]\n"
            "v_pk_add_f32 v[156:157], v[156:157], 1.0 op_sel_hi:[1,0]\n"
            "v_pk_add_f32 v[158:159], v[158:159], 1.0 op_sel_hi:[1,0]\n"
            "v_rcp_f32 v144, v144\n"
            "v_rcp_f32 v145, v145\n"
            "v_rcp_f32 v146, v146\n"
            "v_rcp_f32 v147, v147\n"
            "v_rcp_f32 v148, v148\n"
            "v_rcp_f32 v149, v149\n"
            "v_rcp_f32 v150, v150\n"
            "v_rcp_f32 v151, v151\n"
            "v_rcp_f32 v152, v152\n"
            "v_rcp_f32 v153, v153\n"
            "v_rcp_f32 v154, v154\n"
            "v_rcp_f32 v155, v155\n"
            "v_rcp_f32 v156, v156\n"
            "v_rcp_f32 v157, v157\n"
            "v_rcp_f32 v158, v158\n"
            "v_rcp_f32 v159, v159\n"
            "v_pk_mul_f32 v[144:145], v[144:145], v[176:177]\n"
            "v_pk_mul_f32 v[146:147], v[146:147], v[178:179]\n"
            "v_pk_mul_f32 v[148:149], v[148:149], v[180:181]\n"
            "v_pk_mul_f32 v[150:151], v[150:151], v[182:183]\n"
            "v_pk_mul_f32 v[152:153], v[152:153], v[184:185]\n"
            "v_pk_mul_f32 v[154:155], v[154:155], v[186:187]\n"
            "v_pk_mul_f32 v[156:157], v[156:157], v[188:189]\n"
            "v_pk_mul_f32 v[158:159], v[158:159], v[190:191]\n"
            "s_branch Ldisp_%=\n"
            "Lfast_%=:\n"
            "v_mfma_f32_16x16x32_bf16 v[136:139], v[192:195], v[120:123], 0\n"
            "v_exp_f32 v160, v128\n"
            "v_add_u32 v211, 64, v211\n"
            "v_exp_f32 v161, v129\n"
            "s_waitcnt vmcnt(13) lgkmcnt(0)\n"
            "v_mfma_f32_4x4x4_16b_bf16 v[176:179], v[208:209], v[32:33], 0 cbsz:2\n"
            "v_exp_f32 v162, v130\n"
            "v_mfma_f32_4x4x4_16b_bf16 v[180:183], v[208:209], v[34:35], 0 cbsz:2\n"
            "v_exp_f32 v163, v131\n"
            "v_mfma_f32_4x4x4_16b_bf16 v[184:187], v[208:209], v[36:37], 0 cbsz:2\n"
            "v_exp_f32 v164, v132\n"
            "v_mfma_f32_4x4x4_16b_bf16 v[188:191], v[208:209], v[38:39], 0 cbsz:2\n"
            "v_exp_f32 v165, v133\n"
            "v_add_u32_dpp v219, v196, v214 quad_perm:[0,0,0,0] row_mask:0xf bank_mask:0xf\n"
            "v_mfma_f32_4x4x4_16b_bf16 v[204:207], v[202:203], v[198:199], 0\n"
            "v_exp_f32 v166, v134\n"
            "buffer_load_dwordx4 v[32:35], v219, %[rsx], 0 offen\n"
            "buffer_load_dwordx4 v[36:39], v219, %[rsx], 0 offen offset:256\n"
            "v_exp_f32 v167, v135\n"
            "v_pk_fma_f32 v[0:1], v[144:145], v[176:177], v[0:1] op_sel_hi:[0,1,1]\n"
            "v_pk_fma_f32 v[2:3], v[144:145], v[178:179], v[2:3] op_sel_hi:[0,1,1]\n"
            "v_pk_fma_f32 v[4:5], v[148:149], v[180:181], v[4:5] op_sel_hi:[0,1,1]\n"
            "v_pk_fma_f32 v[6:7], v[148:149], v[182:183], v[6:7] op_sel_hi:[0,1,1]\n"
            "v_pk_fma_f32 v[8:9], v[152:153], v[184:185], v[8:9] op_sel_hi:[0,1,1]\n"
            "v_pk_fma_f32 v[10:11], v[152:153], v[186:187], v[10:11] op_sel_hi:[0,1,1]\n"
            "v_pk_fma_f32 v[12:13], v[156:157], v[188:189], v[12:13] op_sel_hi:[0,1,1]\n"
            "v_pk_fma_f32 v[14:15], v[156:157], v[190:191], v[14:15] op_sel_hi:[0,1,1]\n"
            "v_pk_add_f32 v[160:161], v[160:161], 1.0 op_sel_hi:[1,0]\n"
            "v_pk_add_f32 v[162:163], v[162:163], 1.0 op_sel_hi:[1,0]\n"
            "v_pk_add_f32 v[164:165], v[164:165], 1.0 op_sel_hi:[1,0]\n"
            "v_pk_add_f32 v[166:167], v[166:167], 1.0 op_sel_hi:[1,0]\n"
            "v_mfma_f32_16x16x32_bf16 v[140:143], v[192:195], v[124:127], 0\n"
            "ds_read_b32 v230, v211\n"
            "s_waitcnt vmcnt(14)\n"
            "ds_bpermute_b32 v192, v234, v232\n"
            "ds_bpermute_b32 v193, v234, v233\n"
            "ds_bpermute_b32 v194, v234, v232 offset:4\n"
            "ds_bpermute_b32 v195, v234, v233 offset:4\n"
            "v_rcp_f32 v160, v160\n"
            "v_rcp_f32 v161, v161\n"
            "v_mfma_f32_4x4x4_16b_bf16 v[176:179], v[208:209], v[48:49], 0 cbsz:2 abid:1\n"
            "v_rcp_f32 v162, v162\n"
            "v_mfma_f32_4x4x4_16b_bf16 v[180:183], v[208:209], v[50:51], 0 cbsz:2 abid:1\n"
            "v_rcp_f32 v163, v163\n"
            "v_mfma_f32_4x4x4_16b_bf16 v[184:187], v[208:209], v[52:53], 0 cbsz:2 abid:1\n"
            "v_rcp_f32 v164, v164\n"
            "v_mfma_f32_4x4x4_16b_bf16 v[188:191], v[208:209], v[54:55], 0 cbsz:2 abid:1\n"
            "v_rcp_f32 v165, v165\n"
            "v_add_u32_dpp v220, v196, v214 quad_perm:[1,1,1,1] row_mask:0xf bank_mask:0xf\n"
            "v_mfma_f32_4x4x4_16b_bf16 v[204:207], v[200:201], v[198:199], v[204:207]\n"
            "v_rcp_f32 v166, v166\n"
            "buffer_load_dwordx4 v[48:51], v220, %[rsx], 0 offen\n"
            "buffer_load_dwordx4 v[52:55], v220, %[rsx], 0 offen offset:256\n"
            "v_rcp_f32 v167, v167\n"
            "v_pk_fma_f32 v[0:1], v[144:145], v[176:177], v[0:1] op_sel:[1,0,0]\n"
            "v_pk_fma_f32 v[2:3], v[144:145], v[178:179], v[2:3] op_sel:[1,0,0]\n"
            "v_pk_fma_f32 v[4:5], v[148:149], v[180:181], v[4:5] op_sel:[1,0,0]\n"
            "v_pk_fma_f32 v[6:7], v[148:149], v[182:183], v[6:7] op_sel:[1,0,0]\n"
            "v_pk_fma_f32 v[8:9], v[152:153], v[184:185], v[8:9] op_sel:[1,0,0]\n"
            "v_pk_fma_f32 v[10:11], v[152:153], v[186:187], v[10:11] op_sel:[1,0,0]\n"
            "v_pk_fma_f32 v[12:13], v[156:157], v[188:189], v[12:13] op_sel:[1,0,0]\n"
            "v_pk_fma_f32 v[14:15], v[156:157], v[190:191], v[14:15] op_sel:[1,0,0]\n"
            "v_pk_mul_f32 v[160:161], v[160:161], v[128:129]\n"
            "v_pk_mul_f32 v[162:163], v[162:163], v[130:131]\n"
            "v_pk_mul_f32 v[164:165], v[164:165], v[132:133]\n"
            "v_pk_mul_f32 v[166:167], v[166:167], v[134:135]\n"
            "s_waitcnt vmcnt(12) lgkmcnt(0)\n"
            "v_or_b32 v230, v230, v229\n"
            "buffer_load_dwordx2 v[232:233], v230, %[rse], 0 offen\n"
            "v_mfma_f32_16x16x32_bf16 v[128:131], v[192:195], v[96:99], 0\n"
            "v_exp_f32 v168, v136\n"
            "v_exp_f32 v169, v137\n"
            "v_mfma_f32_4x4x4_16b_bf16 v[176:179], v[208:209], v[64:65], 0 cbsz:2 abid:2\n"
            "v_exp_f32 v170, v138\n"
            "v_mfma_f32_4x4x4_16b_bf16 v[180:183], v[208:209], v[66:67], 0 cbsz:2 abid:2\n"
            "v_exp_f32 v171, v139\n"
            "v_mfma_f32_4x4x4_16b_bf16 v[184:187], v[208:209], v[68:69], 0 cbsz:2 abid:2\n"
            "v_exp_f32 v172, v140\n"
            "v_mfma_f32_4x4x4_16b_bf16 v[188:191], v[208:209], v[70:71], 0 cbsz:2 abid:2\n"
            "v_exp_f32 v173, v141\n"
            "v_add_u32_dpp v221, v196, v214 quad_perm:[2,2,2,2] row_mask:0xf bank_mask:0xf\n"
            "v_exp_f32 v174, v142\n"
            "buffer_load_dwordx4 v[64:67], v221, %[rsx], 0 offen\n"
            "buffer_load_dwordx4 v[68:71], v221, %[rsx], 0 offen offset:256\n"
            "v_exp_f32 v175, v143\n"
            "v_pk_fma_f32 v[0:1], v[146:147], v[176:177], v[0:1] op_sel_hi:[0,1,1]\n"
            "v_pk_fma_f32 v[2:3], v[146:147], v[178:179], v[2:3] op_sel_hi:[0,1,1]\n"
            "v_pk_fma_f32 v[4:5], v[150:151], v[180:181], v[4:5] op_sel_hi:[0,1,1]\n"
            "v_pk_fma_f32 v[6:7], v[150:151], v[182:183], v[6:7] op_sel_hi:[0,1,1]\n"
            "v_pk_fma_f32 v[8:9], v[154:155], v[184:185], v[8:9] op_sel_hi:[0,1,1]\n"
            "v_pk_fma_f32 v[10:11], v[154:155], v[186:187], v[10:11] op_sel_hi:[0,1,1]\n"
            "v_pk_fma_f32 v[12:13], v[158:159], v[188:189], v[12:13] op_sel_hi:[0,1,1]\n"
            "v_pk_fma_f32 v[14:15], v[158:159], v[190:191], v[14:15] op_sel_hi:[0,1,1]\n"
            "v_pk_add_f32 v[168:169], v[168:169], 1.0 op_sel_hi:[1,0]\n"
            "v_pk_add_f32 v[170:171], v[170:171], 1.0 op_sel_hi:[1,0]\n"
            "v_pk_add_f32 v[172:173], v[172:173], 1.0 op_sel_hi:[1,0]\n"
            "v_pk_add_f32 v[174:175], v[174:175], 1.0 op_sel_hi:[1,0]\n"
            "v_cvt_pk_bf16_f32 v204, v204, v205\n"
            "v_cvt_pk_bf16_f32 v205, v206, v207\n"
            "ds_write_b64 v212, v[204:205]\n"
            "v_mfma_f32_16x16x32_bf16 v[132:135], v[192:195], v[100:103], 0\n"
            "v_rcp_f32 v168, v168\n"
            "v_rcp_f32 v169, v169\n"
            "v_mfma_f32_4x4x4_16b_bf16 v[176:179], v[208:209], v[80:81], 0 cbsz:2 abid:3\n"
            "v_rcp_f32 v170, v170\n"
            "v_mfma_f32_4x4x4_16b_bf16 v[180:183], v[208:209], v[82:83], 0 cbsz:2 abid:3\n"
            "v_rcp_f32 v171, v171\n"
            "v_mfma_f32_4x4x4_16b_bf16 v[184:187], v[208:209], v[84:85], 0 cbsz:2 abid:3\n"
            "v_rcp_f32 v172, v172\n"
            "v_mfma_f32_4x4x4_16b_bf16 v[188:191], v[208:209], v[86:87], 0 cbsz:2 abid:3\n"
            "v_rcp_f32 v173, v173\n"
            "v_add_u32_dpp v222, v196, v214 quad_perm:[3,3,3,3] row_mask:0xf bank_mask:0xf\n"
            "v_rcp_f32 v174, v174\n"
            "buffer_load_dwordx4 v[80:83], v222, %[rsx], 0 offen\n"
            "buffer_load_dwordx4 v[84:87], v222, %[rsx], 0 offen offset:256\n"
            "v_rcp_f32 v175, v175\n"
            "v_pk_fma_f32 v[0:1], v[146:147], v[176:177], v[0:1] op_sel:[1,0,0]\n"
            "v_pk_fma_f32 v[2:3], v[146:147], v[178:179], v[2:3] op_sel:[1,0,0]\n"
            "v_pk_fma_f32 v[4:5], v[150:151], v[180:181], v[4:5] op_sel:[1,0,0]\n"
            "v_pk_fma_f32 v[6:7], v[150:151], v[182:183], v[6:7] op_sel:[1,0,0]\n"
            "v_pk_fma_f32 v[8:9], v[154:155], v[184:185], v[8:9] op_sel:[1,0,0]\n"
            "v_pk_fma_f32 v[10:11], v[154:155], v[186:187], v[10:11] op_sel:[1,0,0]\n"
            "v_pk_fma_f32 v[12:13], v[158:159], v[188:189], v[12:13] op_sel:[1,0,0]\n"
            "v_pk_fma_f32 v[14:15], v[158:159], v[190:191], v[14:15] op_sel:[1,0,0]\n"
            "v_pk_mul_f32 v[168:169], v[168:169], v[136:137]\n"
            "v_pk_mul_f32 v[170:171], v[170:171], v[138:139]\n"
            "v_pk_mul_f32 v[172:173], v[172:173], v[140:141]\n"
            "v_pk_mul_f32 v[174:175], v[174:175], v[142:143]\n"
            "v_add_u32 v210, 64, v210\n"
            "ds_read_b128 v[196:199], v210\n"
            "v_mfma_f32_16x16x32_bf16 v[136:139], v[192:195], v[104:107], 0\n"
            "v_exp_f32 v144, v128\n"
            "v_exp_f32 v145, v129\n"
            "s_waitcnt vmcnt(13)\n"
            "v_mfma_f32_4x4x4_16b_bf16 v[176:179], v[208:209], v[40:41], 0 cbsz:2\n"
            "v_exp_f32 v146, v130\n"
            "v_mfma_f32_4x4x4_16b_bf16 v[180:183], v[208:209], v[42:43], 0 cbsz:2\n"
            "v_exp_f32 v147, v131\n"
            "v_mfma_f32_4x4x4_16b_bf16 v[184:187], v[208:209], v[44:45], 0 cbsz:2\n"
            "v_exp_f32 v148, v132\n"
            "v_mfma_f32_4x4x4_16b_bf16 v[188:191], v[208:209], v[46:47], 0 cbsz:2\n"
            "v_exp_f32 v149, v133\n"
            "v_exp_f32 v150, v134\n"
            "buffer_load_dwordx4 v[40:43], v219, %[rsx], 0 offen offset:512\n"
            "buffer_load_dwordx4 v[44:47], v219, %[rsx], 0 offen offset:768\n"
            "v_exp_f32 v151, v135\n"
            "v_pk_fma_f32 v[16:17], v[160:161], v[176:177], v[16:17] op_sel_hi:[0,1,1]\n"
            "v_pk_fma_f32 v[18:19], v[160:161], v[178:179], v[18:19] op_sel_hi:[0,1,1]\n"
            "v_pk_fma_f32 v[20:21], v[164:165], v[180:181], v[20:21] op_sel_hi:[0,1,1]\n"
            "v_pk_fma_f32 v[22:23], v[164:165], v[182:183], v[22:23] op_sel_hi:[0,1,1]\n"
            "v_pk_fma_f32 v[24:25], v[168:169], v[184:185], v[24:25] op_sel_hi:[0,1,1]\n"
            "v_pk_fma_f32 v[26:27], v[168:169], v[186:187], v[26:27] op_sel_hi:[0,1,1]\n"
            "v_pk_fma_f32 v[28:29], v[172:173], v[188:189], v[28:29] op_sel_hi:[0,1,1]\n"
            "v_pk_fma_f32 v[30:31], v[172:173], v[190:191], v[30:31] op_sel_hi:[0,1,1]\n"
            "v_pk_add_f32 v[144:145], v[144:145], 1.0 op_sel_hi:[1,0]\n"
            "v_pk_add_f32 v[146:147], v[146:147], 1.0 op_sel_hi:[1,0]\n"
            "v_pk_add_f32 v[148:149], v[148:149], 1.0 op_sel_hi:[1,0]\n"
            "v_pk_add_f32 v[150:151], v[150:151], 1.0 op_sel_hi:[1,0]\n"
            "v_mfma_f32_16x16x32_bf16 v[140:143], v[192:195], v[108:111], 0\n"
            "v_rcp_f32 v144, v144\n"
            "v_rcp_f32 v145, v145\n"
            "v_mfma_f32_4x4x4_16b_bf16 v[176:179], v[208:209], v[56:57], 0 cbsz:2 abid:1\n"
            "v_rcp_f32 v146, v146\n"
            "v_mfma_f32_4x4x4_16b_bf16 v[180:183], v[208:209], v[58:59], 0 cbsz:2 abid:1\n"
            "v_rcp_f32 v147, v147\n"
            "v_mfma_f32_4x4x4_16b_bf16 v[184:187], v[208:209], v[60:61], 0 cbsz:2 abid:1\n"
            "v_rcp_f32 v148, v148\n"
            "v_mfma_f32_4x4x4_16b_bf16 v[188:191], v[208:209], v[62:63], 0 cbsz:2 abid:1\n"
            "v_rcp_f32 v149, v149\n"
            "v_rcp_f32 v150, v150\n"
            "buffer_load_dwordx4 v[56:59], v220, %[rsx], 0 offen offset:512\n"
            "buffer_load_dwordx4 v[60:63], v220, %[rsx], 0 offen offset:768\n"
            "v_rcp_f32 v151, v151\n"
            "v_pk_fma_f32 v[16:17], v[160:161], v[176:177], v[16:17] op_sel:[1,0,0]\n"
            "v_pk_fma_f32 v[18:19], v[160:161], v[178:179], v[18:19] op_sel:[1,0,0]\n"
            "v_pk_fma_f32 v[20:21], v[164:165], v[180:181], v[20:21] op_sel:[1,0,0]\n"
            "v_pk_fma_f32 v[22:23], v[164:165], v[182:183], v[22:23] op_sel:[1,0,0]\n"
            "v_pk_fma_f32 v[24:25], v[168:169], v[184:185], v[24:25] op_sel:[1,0,0]\n"
            "v_pk_fma_f32 v[26:27], v[168:169], v[186:187], v[26:27] op_sel:[1,0,0]\n"
            "v_pk_fma_f32 v[28:29], v[172:173], v[188:189], v[28:29] op_sel:[1,0,0]\n"
            "v_pk_fma_f32 v[30:31], v[172:173], v[190:191], v[30:31] op_sel:[1,0,0]\n"
            "v_pk_mul_f32 v[144:145], v[144:145], v[128:129]\n"
            "v_pk_mul_f32 v[146:147], v[146:147], v[130:131]\n"
            "v_pk_mul_f32 v[148:149], v[148:149], v[132:133]\n"
            "v_pk_mul_f32 v[150:151], v[150:151], v[134:135]\n"
            "v_mfma_f32_16x16x32_bf16 v[128:131], v[192:195], v[112:115], 0\n"
            "v_exp_f32 v152, v136\n"
            "v_exp_f32 v153, v137\n"
            "s_waitcnt vmcnt(13)\n"
            "v_mfma_f32_4x4x4_16b_bf16 v[176:179], v[208:209], v[72:73], 0 cbsz:2 abid:2\n"
            "v_exp_f32 v154, v138\n"
            "v_mfma_f32_4x4x4_16b_bf16 v[180:183], v[208:209], v[74:75], 0 cbsz:2 abid:2\n"
            "v_exp_f32 v155, v139\n"
            "v_mfma_f32_4x4x4_16b_bf16 v[184:187], v[208:209], v[76:77], 0 cbsz:2 abid:2\n"
            "v_exp_f32 v156, v140\n"
            "v_mfma_f32_4x4x4_16b_bf16 v[188:191], v[208:209], v[78:79], 0 cbsz:2 abid:2\n"
            "v_exp_f32 v157, v141\n"
            "v_exp_f32 v158, v142\n"
            "buffer_load_dwordx4 v[72:75], v221, %[rsx], 0 offen offset:512\n"
            "buffer_load_dwordx4 v[76:79], v221, %[rsx], 0 offen offset:768\n"
            "v_exp_f32 v159, v143\n"
            "v_pk_fma_f32 v[16:17], v[162:163], v[176:177], v[16:17] op_sel_hi:[0,1,1]\n"
            "v_pk_fma_f32 v[18:19], v[162:163], v[178:179], v[18:19] op_sel_hi:[0,1,1]\n"
            "v_pk_fma_f32 v[20:21], v[166:167], v[180:181], v[20:21] op_sel_hi:[0,1,1]\n"
            "v_pk_fma_f32 v[22:23], v[166:167], v[182:183], v[22:23] op_sel_hi:[0,1,1]\n"
            "v_pk_fma_f32 v[24:25], v[170:171], v[184:185], v[24:25] op_sel_hi:[0,1,1]\n"
            "v_pk_fma_f32 v[26:27], v[170:171], v[186:187], v[26:27] op_sel_hi:[0,1,1]\n"
            "v_pk_fma_f32 v[28:29], v[174:175], v[188:189], v[28:29] op_sel_hi:[0,1,1]\n"
            "v_pk_fma_f32 v[30:31], v[174:175], v[190:191], v[30:31] op_sel_hi:[0,1,1]\n"
            "v_pk_add_f32 v[152:153], v[152:153], 1.0 op_sel_hi:[1,0]\n"
            "v_pk_add_f32 v[154:155], v[154:155], 1.0 op_sel_hi:[1,0]\n"
            "v_pk_add_f32 v[156:157], v[156:157], 1.0 op_sel_hi:[1,0]\n"
            "v_pk_add_f32 v[158:159], v[158:159], 1.0 op_sel_hi:[1,0]\n"
            "v_mfma_f32_16x16x32_bf16 v[132:135], v[192:195], v[116:119], 0\n"
            "v_rcp_f32 v152, v152\n"
            "v_rcp_f32 v153, v153\n"
            "v_mfma_f32_4x4x4_16b_bf16 v[176:179], v[208:209], v[88:89], 0 cbsz:2 abid:3\n"
            "v_rcp_f32 v154, v154\n"
            "v_mfma_f32_4x4x4_16b_bf16 v[180:183], v[208:209], v[90:91], 0 cbsz:2 abid:3\n"
            "v_rcp_f32 v155, v155\n"
            "v_mfma_f32_4x4x4_16b_bf16 v[184:187], v[208:209], v[92:93], 0 cbsz:2 abid:3\n"
            "v_rcp_f32 v156, v156\n"
            "v_mfma_f32_4x4x4_16b_bf16 v[188:191], v[208:209], v[94:95], 0 cbsz:2 abid:3\n"
            "ds_read_b64 v[208:209], v213\n"
            "v_rcp_f32 v157, v157\n"
            "v_rcp_f32 v158, v158\n"
            "buffer_load_dwordx4 v[88:91], v222, %[rsx], 0 offen offset:512\n"
            "buffer_load_dwordx4 v[92:95], v222, %[rsx], 0 offen offset:768\n"
            "v_rcp_f32 v159, v159\n"
            "v_pk_fma_f32 v[16:17], v[162:163], v[176:177], v[16:17] op_sel:[1,0,0]\n"
            "v_pk_fma_f32 v[18:19], v[162:163], v[178:179], v[18:19] op_sel:[1,0,0]\n"
            "v_pk_fma_f32 v[20:21], v[166:167], v[180:181], v[20:21] op_sel:[1,0,0]\n"
            "v_pk_fma_f32 v[22:23], v[166:167], v[182:183], v[22:23] op_sel:[1,0,0]\n"
            "v_pk_fma_f32 v[24:25], v[170:171], v[184:185], v[24:25] op_sel:[1,0,0]\n"
            "v_pk_fma_f32 v[26:27], v[170:171], v[186:187], v[26:27] op_sel:[1,0,0]\n"
            "v_pk_fma_f32 v[28:29], v[174:175], v[188:189], v[28:29] op_sel:[1,0,0]\n"
            "v_pk_fma_f32 v[30:31], v[174:175], v[190:191], v[30:31] op_sel:[1,0,0]\n"
            "v_pk_mul_f32 v[152:153], v[152:153], v[136:137]\n"
            "v_pk_mul_f32 v[154:155], v[154:155], v[138:139]\n"
            "v_pk_mul_f32 v[156:157], v[156:157], v[140:141]\n"
            "v_pk_mul_f32 v[158:159], v[158:159], v[142:143]\n"
            "s_sub_u32 %[rem], %[rem], 4\n"
            "Lnext_%=:\n"
            "s_sub_u32 %[nst], %[nst], 1\n"
            "Ldisp_%=:\n"
            "s_bitcmp1_b32 %[evt], %[nst]\n"
            "s_cbranch_scc1 Lprio_%=\n"
            "Ld2_%=:\n"
            "s_cmp_gt_u32 %[rem], 4\n"
            "s_cbranch_scc1 Lfast_%=\n"
            "v_mfma_f32_16x16x32_bf16 v[136:139], v[192:195], v[120:123], 0\n"
            "v_exp_f32 v160, v128\n"
            "v_add_u32 v211, 64, v211\n"
            "v_exp_f32 v161, v129\n"
            "s_waitcnt vmcnt(13) lgkmcnt(0)\n"
            "v_mfma_f32_4x4x4_16b_bf16 v[176:179], v[208:209], v[32:33], 0 cbsz:2\n"
            "v_exp_f32 v162, v130\n"
            "v_mfma_f32_4x4x4_16b_bf16 v[180:183], v[208:209], v[34:35], 0 cbsz:2\n"
            "v_exp_f32 v163, v131\n"
            "v_mfma_f32_4x4x4_16b_bf16 v[184:187], v[208:209], v[36:37], 0 cbsz:2\n"
            "v_exp_f32 v164, v132\n"
            "v_mfma_f32_4x4x4_16b_bf16 v[188:191], v[208:209], v[38:39], 0 cbsz:2\n"
            "v_exp_f32 v165, v133\n"
            "v_add_u32_dpp v219, v196, v214 quad_perm:[0,0,0,0] row_mask:0xf bank_mask:0xf\n"
            "v_mfma_f32_4x4x4_16b_bf16 v[204:207], v[202:203], v[198:199], 0\n"
            "v_exp_f32 v166, v134\n"
            "buffer_load_dwordx4 v[32:35], v219, %[rsx], 0 offen\n"
            "buffer_load_dwordx4 v[36:39], v219, %[rsx], 0 offen offset:256\n"
            "v_exp_f32 v167, v135\n"
            "v_pk_fma_f32 v[0:1], v[144:145], v[176:177], v[0:1] op_sel_hi:[0,1,1]\n"
            "v_pk_fma_f32 v[2:3], v[144:145], v[178:179], v[2:3] op_sel_hi:[0,1,1]\n"
            "v_pk_fma_f32 v[4:5], v[148:149], v[180:181], v[4:5] op_sel_hi:[0,1,1]\n"
            "v_pk_fma_f32 v[6:7], v[148:149], v[182:183], v[6:7] op_sel_hi:[0,1,1]\n"
            "v_pk_fma_f32 v[8:9], v[152:153], v[184:185], v[8:9] op_sel_hi:[0,1,1]\n"
            "v_pk_fma_f32 v[10:11], v[152:153], v[186:187], v[10:11] op_sel_hi:[0,1,1]\n"
            "v_pk_fma_f32 v[12:13], v[156:157], v[188:189], v[12:13] op_sel_hi:[0,1,1]\n"
            "v_pk_fma_f32 v[14:15], v[156:157], v[190:191], v[14:15] op_sel_hi:[0,1,1]\n"
            "v_pk_add_f32 v[160:161], v[160:161], 1.0 op_sel_hi:[1,0]\n"
            "v_pk_add_f32 v[162:163], v[162:163], 1.0 op_sel_hi:[1,0]\n"
            "v_pk_add_f32 v[164:165], v[164:165], 1.0 op_sel_hi:[1,0]\n"
            "v_pk_add_f32 v[166:167], v[166:167], 1.0 op_sel_hi:[1,0]\n"
            "s_cmp_eq_u32 %[rem], 1\n"
            "s_cbranch_scc1 Lf00_%=\n"
            "Lr00_%=:\n"
            "v_mfma_f32_16x16x32_bf16 v[140:143], v[192:195], v[124:127], 0\n"
            "ds_read_b32 v230, v211\n"
            "s_waitcnt vmcnt(14)\n"
            "ds_bpermute_b32 v192, v234, v232\n"
            "ds_bpermute_b32 v193, v234, v233\n"
            "ds_bpermute_b32 v194, v234, v232 offset:4\n"
            "ds_bpermute_b32 v195, v234, v233 offset:4\n"
            "v_rcp_f32 v160, v160\n"
            "v_rcp_f32 v161, v161\n"
            "v_mfma_f32_4x4x4_16b_bf16 v[176:179], v[208:209], v[48:49], 0 cbsz:2 abid:1\n"
            "v_rcp_f32 v162, v162\n"
            "v_mfma_f32_4x4x4_16b_bf16 v[180:183], v[208:209], v[50:51], 0 cbsz:2 abid:1\n"
            "v_rcp_f32 v163, v163\n"
            "v_mfma_f32_4x4x4_16b_bf16 v[184:187], v[208:209], v[52:53], 0 cbsz:2 abid:1\n"
            "v_rcp_f32 v164, v164\n"
            "v_mfma_f32_4x4x4_16b_bf16 v[188:191], v[208:209], v[54:55], 0 cbsz:2 abid:1\n"
            "v_rcp_f32 v165, v165\n"
            "v_add_u32_dpp v220, v196, v214 quad_perm:[1,1,1,1] row_mask:0xf bank_mask:0xf\n"
            "v_mfma_f32_4x4x4_16b_bf16 v[204:207], v[200:201], v[198:199], v[204:207]\n"
            "v_rcp_f32 v166, v166\n"
            "buffer_load_dwordx4 v[48:51], v220, %[rsx], 0 offen\n"
            "buffer_load_dwordx4 v[52:55], v220, %[rsx], 0 offen offset:256\n"
            "v_rcp_f32 v167, v167\n"
            "v_pk_fma_f32 v[0:1], v[144:145], v[176:177], v[0:1] op_sel:[1,0,0]\n"
            "v_pk_fma_f32 v[2:3], v[144:145], v[178:179], v[2:3] op_sel:[1,0,0]\n"
            "v_pk_fma_f32 v[4:5], v[148:149], v[180:181], v[4:5] op_sel:[1,0,0]\n"
            "v_pk_fma_f32 v[6:7], v[148:149], v[182:183], v[6:7] op_sel:[1,0,0]\n"
            "v_pk_fma_f32 v[8:9], v[152:153], v[184:185], v[8:9] op_sel:[1,0,0]\n"
            "v_pk_fma_f32 v[10:11], v[152:153], v[186:187], v[10:11] op_sel:[1,0,0]\n"
            "v_pk_fma_f32 v[12:13], v[156:157], v[188:189], v[12:13] op_sel:[1,0,0]\n"
            "v_pk_fma_f32 v[14:15], v[156:157], v[190:191], v[14:15] op_sel:[1,0,0]\n"
            "v_pk_mul_f32 v[160:161], v[160:161], v[128:129]\n"
            "v_pk_mul_f32 v[162:163], v[162:163], v[130:131]\n"
            "v_pk_mul_f32 v[164:165], v[164:165], v[132:133]\n"
            "v_pk_mul_f32 v[166:167], v[166:167], v[134:135]\n"
            "s_cmp_eq_u32 %[rem], 2\n"
            "s_cbranch_scc1 Lf01_%=\n"
            "Lr01_%=:\n"
            "s_waitcnt vmcnt(12) lgkmcnt(0)\n"
            "v_or_b32 v230, v230, v229\n"
            "buffer_load_dwordx2 v[232:233], v230, %[rse], 0 offen\n"
            "v_mfma_f32_16x16x32_bf16 v[128:131], v[192:195], v[96:99], 0\n"
            "v_exp_f32 v168, v136\n"
            "v_exp_f32 v169, v137\n"
            "v_mfma_f32_4x4x4_16b_bf16 v[176:179], v[208:209], v[64:65], 0 cbsz:2 abid:2\n"
            "v_exp_f32 v170, v138\n"
            "v_mfma_f32_4x4x4_16b_bf16 v[180:183], v[208:209], v[66:67], 0 cbsz:2 abid:2\n"
            "v_exp_f32 v171, v139\n"
            "v_mfma_f32_4x4x4_16b_bf16 v[184:187], v[208:209], v[68:69], 0 cbsz:2 abid:2\n"
            "v_exp_f32 v172, v140\n"
            "v_mfma_f32_4x4x4_16b_bf16 v[188:191], v[208:209], v[70:71], 0 cbsz:2 abid:2\n"
            "v_exp_f32 v173, v141\n"
            "v_add_u32_dpp v221, v196, v214 quad_perm:[2,2,2,2] row_mask:0xf bank_mask:0xf\n"
            "v_exp_f32 v174, v142\n"
            "buffer_load_dwordx4 v[64:67], v221, %[rsx], 0 offen\n"
            "buffer_load_dwordx4 v[68:71], v221, %[rsx], 0 offen offset:256\n"
            "v_exp_f32 v175, v143\n"
            "v_pk_fma_f32 v[0:1], v[146:147], v[176:177], v[0:1] op_sel_hi:[0,1,1]\n"
            "v_pk_fma_f32 v[2:3], v[146:147], v[178:179], v[2:3] op_sel_hi:[0,1,1]\n"
            "v_pk_fma_f32 v[4:5], v[150:151], v[180:181], v[4:5] op_sel_hi:[0,1,1]\n"
            "v_pk_fma_f32 v[6:7], v[150:151], v[182:183], v[6:7] op_sel_hi:[0,1,1]\n"
            "v_pk_fma_f32 v[8:9], v[154:155], v[184:185], v[8:9] op_sel_hi:[0,1,1]\n"
            "v_pk_fma_f32 v[10:11], v[154:155], v[186:187], v[10:11] op_sel_hi:[0,1,1]\n"
            "v_pk_fma_f32 v[12:13], v[158:159], v[188:189], v[12:13] op_sel_hi:[0,1,1]\n"
            "v_pk_fma_f32 v[14:15], v[158:159], v[190:191], v[14:15] op_sel_hi:[0,1,1]\n"
            "v_pk_add_f32 v[168:169], v[168:169], 1.0 op_sel_hi:[1,0]\n"
            "v_pk_add_f32 v[170:171], v[170:171], 1.0 op_sel_hi:[1,0]\n"
            "v_pk_add_f32 v[172:173], v[172:173], 1.0 op_sel_hi:[1,0]\n"
            "v_pk_add_f32 v[174:175], v[174:175], 1.0 op_sel_hi:[1,0]\n"
            "v_cvt_pk_bf16_f32 v204, v204, v205\n"
            "v_cvt_pk_bf16_f32 v205, v206, v207\n"
            "ds_write_b64 v212, v[204:205]\n"
            "s_cmp_eq_u32 %[rem], 3\n"
            "s_cbranch_scc1 Lf02_%=\n"
            "Lr02_%=:\n"
            "v_mfma_f32_16x16x32_bf16 v[132:135], v[192:195], v[100:103], 0\n"
            "v_rcp_f32 v168, v168\n"
            "v_rcp_f32 v169, v169\n"
            "v_mfma_f32_4x4x4_16b_bf16 v[176:179], v[208:209], v[80:81], 0 cbsz:2 abid:3\n"
            "v_rcp_f32 v170, v170\n"
            "v_mfma_f32_4x4x4_16b_bf16 v[180:183], v[208:209], v[82:83], 0 cbsz:2 abid:3\n"
            "v_rcp_f32 v171, v171\n"
            "v_mfma_f32_4x4x4_16b_bf16 v[184:187], v[208:209], v[84:85], 0 cbsz:2 abid:3\n"
            "v_rcp_f32 v172, v172\n"
            "v_mfma_f32_4x4x4_16b_bf16 v[188:191], v[208:209], v[86:87], 0 cbsz:2 abid:3\n"
            "v_rcp_f32 v173, v173\n"
            "v_add_u32_dpp v222, v196, v214 quad_perm:[3,3,3,3] row_mask:0xf bank_mask:0xf\n"
            "v_rcp_f32 v174, v174\n"
            "buffer_load_dwordx4 v[80:83], v222, %[rsx], 0 offen\n"
            "buffer_load_dwordx4 v[84:87], v222, %[rsx], 0 offen offset:256\n"
            "v_rcp_f32 v175, v175\n"
            "v_pk_fma_f32 v[0:1], v[146:147], v[176:177], v[0:1] op_sel:[1,0,0]\n"
            "v_pk_fma_f32 v[2:3], v[146:147], v[178:179], v[2:3] op_sel:[1,0,0]\n"
            "v_pk_fma_f32 v[4:5], v[150:151], v[180:181], v[4:5] op_sel:[1,0,0]\n"
            "v_pk_fma_f32 v[6:7], v[150:151], v[182:183], v[6:7] op_sel:[1,0,0]\n"
            "v_pk_fma_f32 v[8:9], v[154:155], v[184:185], v[8:9] op_sel:[1,0,0]\n"
            "v_pk_fma_f32 v[10:11], v[154:155], v[186:187], v[10:11] op_sel:[1,0,0]\n"
            "v_pk_fma_f32 v[12:13], v[158:159], v[188:189], v[12:13] op_sel:[1,0,0]\n"
            "v_pk_fma_f32 v[14:15], v[158:159], v[190:191], v[14:15] op_sel:[1,0,0]\n"
            "v_pk_mul_f32 v[168:169], v[168:169], v[136:137]\n"
            "v_pk_mul_f32 v[170:171], v[170:171], v[138:139]\n"
            "v_pk_mul_f32 v[172:173], v[172:173], v[140:141]\n"
            "v_pk_mul_f32 v[174:175], v[174:175], v[142:143]\n"
            "s_cmp_eq_u32 %[rem], 4\n"
            "s_cbranch_scc1 Lf03_%=\n"
            "Lr03_%=:\n"
            "v_add_u32 v210, 64, v210\n"
            "ds_read_b128 v[196:199], v210\n"
            "v_mfma_f32_16x16x32_bf16 v[136:139], v[192:195], v[104:107], 0\n"
            "v_exp_f32 v144, v128\n"
            "v_exp_f32 v145, v129\n"
            "s_waitcnt vmcnt(13)\n"
            "v_mfma_f32_4x4x4_16b_bf16 v[176:179], v[208:209], v[40:41], 0 cbsz:2\n"
            "v_exp_f32 v146, v130\n"
            "v_mfma_f32_4x4x4_16b_bf16 v[180:183], v[208:209], v[42:43], 0 cbsz:2\n"
            "v_exp_f32 v147, v131\n"
            "v_mfma_f32_4x4x4_16b_bf16 v[184:187], v[208:209], v[44:45], 0 cbsz:2\n"
            "v_exp_f32 v148, v132\n"
            "v_mfma_f32_4x4x4_16b_bf16 v[188:191], v[208:209], v[46:47], 0 cbsz:2\n"
            "v_exp_f32 v149, v133\n"
            "v_exp_f32 v150, v134\n"
            "buffer_load_dwordx4 v[40:43], v219, %[rsx], 0 offen offset:512\n"
            "buffer_load_dwordx4 v[44:47], v219, %[rsx], 0 offen offset:768\n"
            "v_exp_f32 v151, v135\n"
            "v_pk_fma_f32 v[16:17], v[160:161], v[176:177], v[16:17] op_sel_hi:[0,1,1]\n"
            "v_pk_fma_f32 v[18:19], v[160:161], v[178:179], v[18:19] op_sel_hi:[0,1,1]\n"
            "v_pk_fma_f32 v[20:21], v[164:165], v[180:181], v[20:21] op_sel_hi:[0,1,1]\n"
            "v_pk_fma_f32 v[22:23], v[164:165], v[182:183], v[22:23] op_sel_hi:[0,1,1]\n"
            "v_pk_fma_f32 v[24:25], v[168:169], v[184:185], v[24:25] op_sel_hi:[0,1,1]\n"
            "v_pk_fma_f32 v[26:27], v[168:169], v[186:187], v[26:27] op_sel_hi:[0,1,1]\n"
            "v_pk_fma_f32 v[28:29], v[172:173], v[188:189], v[28:29] op_sel_hi:[0,1,1]\n"
            "v_pk_fma_f32 v[30:31], v[172:173], v[190:191], v[30:31] op_sel_hi:[0,1,1]\n"
            "v_pk_add_f32 v[144:145], v[144:145], 1.0 op_sel_hi:[1,0]\n"
            "v_pk_add_f32 v[146:147], v[146:147], 1.0 op_sel_hi:[1,0]\n"
            "v_pk_add_f32 v[148:149], v[148:149], 1.0 op_sel_hi:[1,0]\n"
            "v_pk_add_f32 v[150:151], v[150:151], 1.0 op_sel_hi:[1,0]\n"
            "s_cmp_eq_u32 %[rem], 1\n"
            "s_cbranch_scc1 Lf10_%=\n"
            "Lr10_%=:\n"
            "v_mfma_f32_16x16x32_bf16 v[140:143], v[192:195], v[108:111], 0\n"
            "v_rcp_f32 v144, v144\n"
            "v_rcp_f32 v145, v145\n"
            "v_mfma_f32_4x4x4_16b_bf16 v[176:179], v[208:209], v[56:57], 0 cbsz:2 abid:1\n"
            "v_rcp_f32 v146, v146\n"
            "v_mfma_f32_4x4x4_16b_bf16 v[180:183], v[208:209], v[58:59], 0 cbsz:2 abid:1\n"
            "v_rcp_f32 v147, v147\n"
            "v_mfma_f32_4x4x4_16b_bf16 v[184:187], v[208:209], v[60:61], 0 cbsz:2 abid:1\n"
            "v_rcp_f32 v148, v148\n"
            "v_mfma_f32_4x4x4_16b_bf16 v[188:191], v[208:209], v[62:63], 0 cbsz:2 abid:1\n"
            "v_rcp_f32 v149, v149\n"
            "v_rcp_f32 v150, v150\n"
            "buffer_load_dwordx4 v[56:59], v220, %[rsx], 0 offen offset:512\n"
            "buffer_load_dwordx4 v[60:63], v220, %[rsx], 0 offen offset:768\n"
            "v_rcp_f32 v151, v151\n"
            "v_pk_fma_f32 v[16:17], v[160:161], v[176:177], v[16:17] op_sel:[1,0,0]\n"
            "v_pk_fma_f32 v[18:19], v[160:161], v[178:179], v[18:19] op_sel:[1,0,0]\n"
            "v_pk_fma_f32 v[20:21], v[164:165], v[180:181], v[20:21] op_sel:[1,0,0]\n"
            "v_pk_fma_f32 v[22:23], v[164:165], v[182:183], v[22:23] op_sel:[1,0,0]\n"
            "v_pk_fma_f32 v[24:25], v[168:169], v[184:185], v[24:25] op_sel:[1,0,0]\n"
            "v_pk_fma_f32 v[26:27], v[168:169], v[186:187], v[26:27] op_sel:[1,0,0]\n"
            "v_pk_fma_f32 v[28:29], v[172:173], v[188:189], v[28:29] op_sel:[1,0,0]\n"
            "v_pk_fma_f32 v[30:31], v[172:173], v[190:191], v[30:31] op_sel:[1,0,0]\n"
            "v_pk_mul_f32 v[144:145], v[144:145], v[128:129]\n"
            "v_pk_mul_f32 v[146:147], v[146:147], v[130:131]\n"
            "v_pk_mul_f32 v[148:149], v[148:149], v[132:133]\n"
            "v_pk_mul_f32 v[150:151], v[150:151], v[134:135]\n"
            "s_cmp_eq_u32 %[rem], 2\n"
            "s_cbranch_scc1 Lf11_%=\n"
            "Lr11_%=:\n"
            "v_mfma_f32_16x16x32_bf16 v[128:131], v[192:195], v[112:115], 0\n"
            "v_exp_f32 v152, v136\n"
            "v_exp_f32 v153, v137\n"
            "s_waitcnt vmcnt(13)\n"
            "v_mfma_f32_4x4x4_16b_bf16 v[176:179], v[208:209], v[72:73], 0 cbsz:2 abid:2\n"
            "v_exp_f32 v154, v138\n"
            "v_mfma_f32_4x4x4_16b_bf16 v[180:183], v[208:209], v[74:75], 0 cbsz:2 abid:2\n"
            "v_exp_f32 v155, v139\n"
            "v_mfma_f32_4x4x4_16b_bf16 v[184:187], v[208:209], v[76:77], 0 cbsz:2 abid:2\n"
            "v_exp_f32 v156, v140\n"
            "v_mfma_f32_4x4x4_16b_bf16 v[188:191], v[208:209], v[78:79], 0 cbsz:2 abid:2\n"
            "v_exp_f32 v157, v141\n"
            "v_exp_f32 v158, v142\n"
            "buffer_load_dwordx4 v[72:75], v221, %[rsx], 0 offen offset:512\n"
            "buffer_load_dwordx4 v[76:79], v221, %[rsx], 0 offen offset:768\n"
            "v_exp_f32 v159, v143\n"
            "v_pk_fma_f32 v[16:17], v[162:163], v[176:177], v[16:17] op_sel_hi:[0,1,1]\n"
            "v_pk_fma_f32 v[18:19], v[162:163], v[178:179], v[18:19] op_sel_hi:[0,1,1]\n"
            "v_pk_fma_f32 v[20:21], v[166:167], v[180:181], v[20:21] op_sel_hi:[0,1,1]\n"
            "v_pk_fma_f32 v[22:23], v[166:167], v[182:183], v[22:23] op_sel_hi:[0,1,1]\n"
            "v_pk_fma_f32 v[24:25], v[170:171], v[184:185], v[24:25] op_sel_hi:[0,1,1]\n"
            "v_pk_fma_f32 v[26:27], v[170:171], v[186:187], v[26:27] op_sel_hi:[0,1,1]\n"
            "v_pk_fma_f32 v[28:29], v[174:175], v[188:189], v[28:29] op_sel_hi:[0,1,1]\n"
            "v_pk_fma_f32 v[30:31], v[174:175], v[190:191], v[30:31] op_sel_hi:[0,1,1]\n"
            "v_pk_add_f32 v[152:153], v[152:153], 1.0 op_sel_hi:[1,0]\n"
            "v_pk_add_f32 v[154:155], v[154:155], 1.0 op_sel_hi:[1,0]\n"
            "v_pk_add_f32 v[156:157], v[156:157], 1.0 op_sel_hi:[1,0]\n"
            "v_pk_add_f32 v[158:159], v[158:159], 1.0 op_sel_hi:[1,0]\n"
            "s_cmp_eq_u32 %[rem], 3\n"
            "s_cbranch_scc1 Lf12_%=\n"
            "Lr12_%=:\n"
            "v_mfma_f32_16x16x32_bf16 v[132:135], v[192:195], v[116:119], 0\n"
            "v_rcp_f32 v152, v152\n"
            "v_rcp_f32 v153, v153\n"
            "v_mfma_f32_4x4x4_16b_bf16 v[176:179], v[208:209], v[88:89], 0 cbsz:2 abid:3\n"
            "v_rcp_f32 v154, v154\n"
            "v_mfma_f32_4x4x4_16b_bf16 v[180:183], v[208:209], v[90:91], 0 cbsz:2 abid:3\n"
            "v_rcp_f32 v155, v155\n"
            "v_mfma_f32_4x4x4_16b_bf16 v[184:187], v[208:209], v[92:93], 0 cbsz:2 abid:3\n"
            "v_rcp_f32 v156, v156\n"
            "v_mfma_f32_4x4x4_16b_bf16 v[188:191], v[208:209], v[94:95], 0 cbsz:2 abid:3\n"
            "ds_read_b64 v[208:209], v213\n"
            "v_rcp_f32 v157, v157\n"
            "v_rcp_f32 v158, v158\n"
            "buffer_load_dwordx4 v[88:91], v222, %[rsx], 0 offen offset:512\n"
            "buffer_load_dwordx4 v[92:95], v222, %[rsx], 0 offen offset:768\n"
            "v_rcp_f32 v159, v159\n"
            "v_pk_fma_f32 v[16:17], v[162:163], v[176:177], v[16:17] op_sel:[1,0,0]\n"
            "v_pk_fma_f32 v[18:19], v[162:163], v[178:179], v[18:19] op_sel:[1,0,0]\n"
            "v_pk_fma_f32 v[20:21], v[166:167], v[180:181], v[20:21] op_sel:[1,0,0]\n"
            "v_pk_fma_f32 v[22:23], v[166:167], v[182:183], v[22:23] op_sel:[1,0,0]\n"
            "v_pk_fma_f32 v[24:25], v[170:171], v[184:185], v[24:25] op_sel:[1,0,0]\n"
            "v_pk_fma_f32 v[26:27], v[170:171], v[186:187], v[26:27] op_sel:[1,0,0]\n"
            "v_pk_fma_f32 v[28:29], v[174:175], v[188:189], v[28:29] op_sel:[1,0,0]\n"
            "v_pk_fma_f32 v[30:31], v[174:175], v[190:191], v[30:31] op_sel:[1,0,0]\n"
            "v_pk_mul_f32 v[152:153], v[152:153], v[136:137]\n"
            "v_pk_mul_f32 v[154:155], v[154:155], v[138:139]\n"
            "v_pk_mul_f32 v[156:157], v[156:157], v[140:141]\n"
            "v_pk_mul_f32 v[158:159], v[158:159], v[142:143]\n"
            "s_cmp_eq_u32 %[rem], 4\n"
            "s_cbranch_scc1 Lf13_%=\n"
            "Lr13_%=:\n"
            "s_and_b32 %[tmp], %[lens], 0xff\n"
            "s_add_u32 %[rem], %[rem], %[tmp]\n"
            "s_lshr_b32 %[lens], %[lens], 8\n"
            "s_lshr_b32 %[fm], %[fm], 4\n"
            "s_bitcmp1_b32 %[fm], 3\n"
            "v_mov_b32 v215, v216\n"
            "s_cbranch_scc0 Lnext_%=\n"
            "s_sub_u32 %[pb], %[nst], 2\n"
            "s_bitset1_b32 %[evt], %[pb]\n"
            "s_branch Lnext_%=\n"
            "Lpub_%=:\n"
            "s_waitcnt vmcnt(17)\n"
            "v_mov_b32 v176, %[cnt]\n"
            "v_mov_b32 v177, 1\n"
            "s_mov_b64 exec, 1\n"
            "ds_add_rtn_u32 v177, v176, v177\n"
            "s_waitcnt lgkmcnt(0)\n"
            "v_readfirstlane_b32 %[tmp], v177\n"
            "s_mov_b64 exec, 0xff\n"
            "v_mov_b32 v176, %[tag]\n"
            "v_mov_b32 v177, %[tag]\n"
            "v_mov_b32 v178, %[tag]\n"
            "v_mov_b32 v179, %[tag]\n"
            "s_cmp_lg_u32 %[tmp], 7\n"
            "s_cbranch_scc1 Lpub2_%=\n"
            "global_store_dwordx4 v214, v[176:179], %[flg] sc1\n"
            "Lpub2_%=:\n"
            "s_mov_b64 exec, -1\n"
            "s_branch Lpr2_%=\n"
            "Lprio_%=:\n"
            "s_cmp_eq_u32 %[nst], %[pb]\n"
            "s_cbranch_scc1 Lpub_%=\n"
            "Lpr2_%=:\n"
            "s_cmp_eq_u32 %[nst], 1\n"
            "s_cbranch_scc1 Llast_%=\n"
            "s_lshl2_add_u32 %[tmp], %[nst], %[c1]\n"
            "v_bfe_u32 v176, v214, 4, 2\n"
            "v_lshrrev_b32 v177, 6, v214\n"
            "v_sub_u32 v176, %[tmp], v176\n"
            "v_sub_u32 v177, %[tmp], v177\n"
            "s_min_u32 %[tmp], %[nst], 15\n"
            "v_cmp_gt_u32 vcc, 4, v176\n"
            "v_cmp_gt_u32 %[ra], 4, v177\n"
            "v_add_u32 v176, %[c2], v176\n"
            "v_add_u32 v177, %[c2], v177\n"
            "v_cndmask_b32 v178, 0, v236, vcc\n"
            "v_cndmask_b32 v179, 0, v236, %[ra]\n"
            "v_add_u32 v210, v210, v178\n"
            "v_add_u32 v211, v211, v179\n"
            "v_cmp_gt_u32 vcc, 4, v176\n"
            "v_cmp_gt_u32 %[ra], 4, v177\n"
            "s_nop 1\n"
            "v_cndmask_b32 v178, 0, v237, vcc\n"
            "v_cndmask_b32 v179, 0, v237, %[ra]\n"
            "v_add_u32 v210, v210, v178\n"
            "v_add_u32 v211, v211, v179\n"
            "s_lshl_b32 %[tmp], %[tmp], 1\n"
            "s_lshr_b32 %[tmp], %[ptab], %[tmp]\n"
            "s_bitcmp1_b32 %[tmp], 1\n"
            "s_cbranch_scc1 Lq2_%=\n"
            "s_bitcmp1_b32 %[tmp], 0\n"
            "s_setprio 0\n"
            "s_cbranch_scc0 Lq9_%=\n"
            "s_setprio 1\n"
            "s_branch Lq9_%=\n"
            "Lq2_%=:\n"
            "s_bitcmp1_b32 %[tmp], 0\n"
            "s_setprio 2\n"
            "s_cbranch_scc0 Lq9_%=\n"
            "s_setprio 3\n"
            "Lq9_%=:\n"
            "s_cmp_lg_u32 %[nst], 2\n"
            "s_cbranch_scc1 Ld2_%=\n"
            "v_mov_b32 v223, 0\n"
            "global_load_dword v223, v223, %[pfl] sc1\n"
            "s_branch Ld2_%=\n"
            "Lf00_%=:\n"
            "s_call_b64 %[ra], Lflush0_%=\n"
            "s_branch Lr00_%=\n"
            "Lf01_%=:\n"
            "s_call_b64 %[ra], Lflush0_%=\n"
            "s_branch Lr01_%=\n"
            "Lf02_%=:\n"
            "s_call_b64 %[ra], Lflush0_%=\n"
            "s_branch Lr02_%=\n"
            "Lf03_%=:\n"
            "s_call_b64 %[ra], Lflush0_%=\n"
            "s_branch Lr03_%=\n"
            "Lflush0_%=:\n"
            "v_mov_b32 v184, %[pfa]\n"
            "s_bitcmp0_b32 %[fm], 0\n"
            "s_cbranch_scc1 Lfa0_%=\n"
            "Lfw_%=:\n"
            "ds_read_b32 v185, v184\n"
            "ds_read_b128 v[176:179], v231 offset:0\n"
            "ds_read_b128 v[180:183], v231 offset:1024\n"
            "s_waitcnt lgkmcnt(0)\n"
            "v_cmp_eq_u32 vcc, 0, v185\n"
            "s_cbranch_vccnz Lfw_%=\n"
            "v_mfma_f32_4x4x4_16b_bf16 v[0:3], v[224:225], v[176:177], v[0:3]\n"
            "v_mfma_f32_4x4x4_16b_bf16 v[4:7], v[224:225], v[178:179], v[4:7]\n"
            "v_mfma_f32_4x4x4_16b_bf16 v[8:11], v[224:225], v[180:181], v[8:11]\n"
            "v_mfma_f32_4x4x4_16b_bf16 v[12:15], v[224:225], v[182:183], v[12:15]\n"
            "s_nop 1\n"
            "Lfa0_%=:\n"
            "v_cvt_pk_bf16_f32 v176, v0, v1\n"
            "v_cvt_pk_bf16_f32 v177, v2, v3\n"
            "v_cvt_pk_bf16_f32 v178, v4, v5\n"
            "v_cvt_pk_bf16_f32 v179, v6, v7\n"
            "v_cvt_pk_bf16_f32 v180, v8, v9\n"
            "v_cvt_pk_bf16_f32 v181, v10, v11\n"
            "v_cvt_pk_bf16_f32 v182, v12, v13\n"
            "v_cvt_pk_bf16_f32 v183, v14, v15\n"
            "s_bitcmp1_b32 %[fm], 1\n"
            "s_cbranch_scc1 Lpark0_%=\n"
            "buffer_store_dwordx4 v[176:179], v215, %[rsw], 0 offen offset:0 sc1\n"
            "buffer_store_dwordx4 v[180:183], v215, %[rsw], 0 offen offset:256 sc1\n"
            "s_branch Lzero0_%=\n"
            "Lpark0_%=:\n"
            "ds_write_b128 v231, v[176:179] offset:0\n"
            "ds_write_b128 v231, v[180:183] offset:1024\n"
            "Lzero0_%=:\n"
            "v_mov_b64 v[0:1], 0\n"
            "v_mov_b64 v[2:3], 0\n"
            "v_mov_b64 v[4:5], 0\n"
            "v_mov_b64 v[6:7], 0\n"
            "v_mov_b64 v[8:9], 0\n"
            "v_mov_b64 v[10:11], 0\n"
            "v_mov_b64 v[12:13], 0\n"
            "v_mov_b64 v[14:15], 0\n"
            "s_setpc_b64 %[ra]\n"
            "Lf10_%=:\n"
            "s_call_b64 %[ra], Lflush1_%=\n"
            "s_branch Lr10_%=\n"
            "Lf11_%=:\n"
            "s_call_b64 %[ra], Lflush1_%=\n"
            "s_branch Lr11_%=\n"
            "Lf12_%=:\n"
            "s_call_b64 %[ra], Lflush1_%=\n"
            "s_branch Lr12_%=\n"
            "Lf13_%=:\n"
            "s_call_b64 %[ra], Lflush1_%=\n"
            "s_branch Lr13_%=\n"
            "Lflush1_%=:\n"
            "v_mov_b32 v184, %[pfa]\n"
            "s_bitcmp0_b32 %[fm], 0\n"
            "s_cbranch_scc1 Lfa1_%=\n"
            "ds_read_b128 v[176:179], v231 offset:2048\n"
            "ds_read_b128 v[180:183], v231 offset:3072\n"
            "s_waitcnt lgkmcnt(0)\n"
            "v_mfma_f32_4x4x4_16b_bf16 v[16:19], v[224:225], v[176:177], v[16:19]\n"
            "v_mfma_f32_4x4x4_16b_bf16 v[20:23], v[224:225], v[178:179], v[20:23]\n"
            "v_mfma_f32_4x4x4_16b_bf16 v[24:27], v[224:225], v[180:181], v[24:27]\n"
            "v_mfma_f32_4x4x4_16b_bf16 v[28:31], v[224:225], v[182:183], v[28:31]\n"
            "s_nop 1\n"
            "Lfa1_%=:\n"
            "v_cvt_pk_bf16_f32 v176, v16, v17\n"
            "v_cvt_pk_bf16_f32 v177, v18, v19\n"
            "v_cvt_pk_bf16_f32 v178, v20, v21\n"
            "v_cvt_pk_bf16_f32 v179, v22, v23\n"
            "v_cvt_pk_bf16_f32 v180, v24, v25\n"
            "v_cvt_pk_bf16_f32 v181, v26, v27\n"
            "v_cvt_pk_bf16_f32 v182, v28, v29\n"
            "v_cvt_pk_bf16_f32 v183, v30, v31\n"
            "s_bitcmp1_b32 %[fm], 1\n"
            "s_cbranch_scc1 Lpark1_%=\n"
            "buffer_store_dwordx4 v[176:179], v215, %[rsw], 0 offen offset:512 sc1\n"
            "buffer_store_dwordx4 v[180:183], v215, %[rsw], 0 offen offset:768 sc1\n"
            "s_branch Lzero1_%=\n"
            "Lpark1_%=:\n"
            "ds_write_b128 v231, v[176:179] offset:2048\n"
            "ds_write_b128 v231, v[180:183] offset:3072\n"
            "ds_write_b32 v184, v184\n"
            "Lzero1_%=:\n"
            "v_mov_b64 v[16:17], 0\n"
            "v_mov_b64 v[18:19], 0\n"
            "v_mov_b64 v[20:21], 0\n"
            "v_mov_b64 v[22:23], 0\n"
            "v_mov_b64 v[24:25], 0\n"
            "v_mov_b64 v[26:27], 0\n"
            "v_mov_b64 v[28:29], 0\n"
            "v_mov_b64 v[30:31], 0\n"
            "s_setpc_b64 %[ra]\n"
            "Lspin_%=:\n"
            "s_mov_b32 %[lens], %[spin]\n"
            "Lsp1_%=:\n"
            "v_readfirstlane_b32 %[nst], v223\n"
            "s_sub_u32 %[lens], %[lens], 1\n"
            "s_cmp_eq_u32 %[lens], 0\n"
            "s_cbranch_scc1 Lsp2_%=\n"
            "s_cmp_eq_u32 %[nst], %[tag]\n"
            "s_cbranch_scc1 Lsp2_%=\n"
            "s_sleep 1\n"
            "v_mov_b32 v223, 0\n"
            "global_load_dword v223, v223, %[pfl] sc1\n"
            "s_waitcnt vmcnt(0)\n"
            "s_branch Lsp1_%=\n"
            "Lsp2_%=:\n"
            "buffer_load_dwordx4 v[32:35], v217, %[rsw], %[oagg] offen offset:0 sc1\n"
            "buffer_load_dwordx4 v[36:39], v217, %[rsw], %[oagg] offen offset:256 sc1\n"
            "buffer_load_dwordx4 v[48:51], v217, %[rsw], %[oagg] offen offset:512 sc1\n"
            "buffer_load_dwordx4 v[52:55], v217, %[rsw], %[oagg] offen offset:768 sc1\n"
            "s_branch Lgo_%=\n"
            "Llast_%=:\n"
            "s_waitcnt vmcnt(16)\n"
            "v_readfirstlane_b32 %[nst], v223\n"
            "v_mov_b32 v222, v217\n"
            "s_cmp_eq_u32 %[nst], %[tag]\n"
            "s_cbranch_scc1 Lp4_%=\n"
            "v_bfrev_b32 v222, 1\n"
            "v_mov_b32 v223, 0\n"
            "global_load_dword v223, v223, %[pfl] sc1\n"
            "Lp4_%=:\n"
            "v_mfma_f32_16x16x32_bf16 v[136:139], v[192:195], v[120:123], 0\n"
            "v_exp_f32 v160, v128\n"
            "v_exp_f32 v161, v129\n"
            "s_waitcnt vmcnt(13) lgkmcnt(0)\n"
            "v_mfma_f32_4x4x4_16b_bf16 v[176:179], v[208:209], v[32:33], 0 cbsz:2\n"
            "v_exp_f32 v162, v130\n"
            "v_mfma_f32_4x4x4_16b_bf16 v[180:183], v[208:209], v[34:35], 0 cbsz:2\n"
            "v_exp_f32 v163, v131\n"
            "v_mfma_f32_4x4x4_16b_bf16 v[184:187], v[208:209], v[36:37], 0 cbsz:2\n"
            "v_exp_f32 v164, v132\n"
            "v_mfma_f32_4x4x4_16b_bf16 v[188:191], v[208:209], v[38:39], 0 cbsz:2\n"
            "v_exp_f32 v165, v133\n"
            "v_exp_f32 v166, v134\n"
            "buffer_load_dwordx4 v[32:35], v222, %[rsw], %[oagg] offen offset:0 sc1\n"
            "buffer_load_dwordx4 v[36:39], v222, %[rsw], %[oagg] offen offset:256 sc1\n"
            "ds_read_b128 v[236:239], v228 offset:0\n"
            "ds_read_b128 v[240:243], v228 offset:1024\n"
            "ds_read_b128 v[244:247], v228 offset:2048\n"
            "ds_read_b128 v[248:251], v228 offset:3072\n"
            "ds_read_b128 v[96:99], v228 offset:4096\n"
            "v_exp_f32 v167, v135\n"
            "v_pk_fma_f32 v[0:1], v[144:145], v[176:177], v[0:1] op_sel_hi:[0,1,1]\n"
            "v_pk_fma_f32 v[2:3], v[144:145], v[178:179], v[2:3] op_sel_hi:[0,1,1]\n"
            "v_pk_fma_f32 v[4:5], v[148:149], v[180:181], v[4:5] op_sel_hi:[0,1,1]\n"
            "v_pk_fma_f32 v[6:7], v[148:149], v[182:183], v[6:7] op_sel_hi:[0,1,1]\n"
            "v_pk_fma_f32 v[8:9], v[152:153], v[184:185], v[8:9] op_sel_hi:[0,1,1]\n"
            "v_pk_fma_f32 v[10:11], v[152:153], v[186:187], v[10:11] op_sel_hi:[0,1,1]\n"
            "v_pk_fma_f32 v[12:13], v[156:157], v[188:189], v[12:13] op_sel_hi:[0,1,1]\n"
            "v_pk_fma_f32 v[14:15], v[156:157], v[190:191], v[14:15] op_sel_hi:[0,1,1]\n"
            "v_pk_add_f32 v[160:161], v[160:161], 1.0 op_sel_hi:[1,0]\n"
            "v_pk_add_f32 v[162:163], v[162:163], 1.0 op_sel_hi:[1,0]\n"
            "v_pk_add_f32 v[164:165], v[164:165], 1.0 op_sel_hi:[1,0]\n"
            "v_pk_add_f32 v[166:167], v[166:167], 1.0 op_sel_hi:[1,0]\n"
            "v_mfma_f32_16x16x32_bf16 v[140:143], v[192:195], v[124:127], 0\n"
            "v_rcp_f32 v160, v160\n"
            "v_rcp_f32 v161, v161\n"
            "v_mfma_f32_4x4x4_16b_bf16 v[176:179], v[208:209], v[48:49], 0 cbsz:2 abid:1\n"
            "v_rcp_f32 v162, v162\n"
            "v_mfma_f32_4x4x4_16b_bf16 v[180:183], v[208:209], v[50:51], 0 cbsz:2 abid:1\n"
            "v_rcp_f32 v163, v163\n"
            "v_mfma_f32_4x4x4_16b_bf16 v[184:187], v[208:209], v[52:53], 0 cbsz:2 abid:1\n"
            "v_rcp_f32 v164, v164\n"
            "v_mfma_f32_4x4x4_16b_bf16 v[188:191], v[208:209], v[54:55], 0 cbsz:2 abid:1\n"
            "v_rcp_f32 v165, v165\n"
            "v_rcp_f32 v166, v166\n"
            "buffer_load_dwordx4 v[48:51], v222, %[rsw], %[oagg] offen offset:512 sc1\n"
            "buffer_load_dwordx4 v[52:55], v222, %[rsw], %[oagg] offen offset:768 sc1\n"
            "ds_read_b128 v[100:103], v228 offset:5120\n"
            "ds_read_b128 v[104:107], v228 offset:6144\n"
            "ds_read_b128 v[108:111], v228 offset:7168\n"
            "ds_read_b128 v[112:115], v228 offset:8192\n"
            "ds_read_b128 v[116:119], v228 offset:9216\n"
            "ds_read_b128 v[196:199], v228 offset:10240\n"
            "v_rcp_f32 v167, v167\n"
            "v_pk_fma_f32 v[0:1], v[144:145], v[176:177], v[0:1] op_sel:[1,0,0]\n"
            "v_pk_fma_f32 v[2:3], v[144:145], v[178:179], v[2:3] op_sel:[1,0,0]\n"
            "v_pk_fma_f32 v[4:5], v[148:149], v[180:181], v[4:5] op_sel:[1,0,0]\n"
            "v_pk_fma_f32 v[6:7], v[148:149], v[182:183], v[6:7] op_sel:[1,0,0]\n"
            "v_pk_fma_f32 v[8:9], v[152:153], v[184:185], v[8:9] op_sel:[1,0,0]\n"
            "v_pk_fma_f32 v[10:11], v[152:153], v[186:187], v[10:11] op_sel:[1,0,0]\n"
            "v_pk_fma_f32 v[12:13], v[156:157], v[188:189], v[12:13] op_sel:[1,0,0]\n"
            "v_pk_fma_f32 v[14:15], v[156:157], v[190:191], v[14:15] op_sel:[1,0,0]\n"
            "v_pk_mul_f32 v[160:161], v[160:161], v[128:129]\n"
            "v_pk_mul_f32 v[162:163], v[162:163], v[130:131]\n"
            "v_pk_mul_f32 v[164:165], v[164:165], v[132:133]\n"
            "v_pk_mul_f32 v[166:167], v[166:167], v[134:135]\n"
            "v_exp_f32 v168, v136\n"
            "v_exp_f32 v169, v137\n"
            "s_waitcnt vmcnt(12)\n"
            "v_mfma_f32_4x4x4_16b_bf16 v[176:179], v[208:209], v[64:65], 0 cbsz:2 abid:2\n"
            "v_exp_f32 v170, v138\n"
            "v_mfma_f32_4x4x4_16b_bf16 v[180:183], v[208:209], v[66:67], 0 cbsz:2 abid:2\n"
            "v_exp_f32 v171, v139\n"
            "v_mfma_f32_4x4x4_16b_bf16 v[184:187], v[208:209], v[68:69], 0 cbsz:2 abid:2\n"
            "v_exp_f32 v172, v140\n"
            "v_mfma_f32_4x4x4_16b_bf16 v[188:191], v[208:209], v[70:71], 0 cbsz:2 abid:2\n"
            "v_exp_f32 v173, v141\n"
            "v_exp_f32 v174, v142\n"
            "ds_read_b128 v[200:203], v228 offset:11264\n"
            "ds_read_b128 v[204:207], v228 offset:12288\n"
            "ds_read_b128 v[232:235], v228 offset:13312\n"
            "v_exp_f32 v175, v143\n"
            "v_pk_fma_f32 v[0:1], v[146:147], v[176:177], v[0:1] op_sel_hi:[0,1,1]\n"
            "v_pk_fma_f32 v[2:3], v[146:147], v[178:179], v[2:3] op_sel_hi:[0,1,1]\n"
            "v_pk_fma_f32 v[4:5], v[150:151], v[180:181], v[4:5] op_sel_hi:[0,1,1]\n"
            "v_pk_fma_f32 v[6:7], v[150:151], v[182:183], v[6:7] op_sel_hi:[0,1,1]\n"
            "v_pk_fma_f32 v[8:9], v[154:155], v[184:185], v[8:9] op_sel_hi:[0,1,1]\n"
            "v_pk_fma_f32 v[10:11], v[154:155], v[186:187], v[10:11] op_sel_hi:[0,1,1]\n"
            "v_pk_fma_f32 v[12:13], v[158:159], v[188:189], v[12:13] op_sel_hi:[0,1,1]\n"
            "v_pk_fma_f32 v[14:15], v[158:159], v[190:191], v[14:15] op_sel_hi:[0,1,1]\n"
            "v_pk_add_f32 v[168:169], v[168:169], 1.0 op_sel_hi:[1,0]\n"
            "v_pk_add_f32 v[170:171], v[170:171], 1.0 op_sel_hi:[1,0]\n"
            "v_pk_add_f32 v[172:173], v[172:173], 1.0 op_sel_hi:[1,0]\n"
            "v_pk_add_f32 v[174:175], v[174:175], 1.0 op_sel_hi:[1,0]\n"
            "v_rcp_f32 v168, v168\n"
            "v_rcp_f32 v169, v169\n"
            "v_mfma_f32_4x4x4_16b_bf16 v[176:179], v[208:209], v[80:81], 0 cbsz:2 abid:3\n"
            "v_rcp_f32 v170, v170\n"
            "v_mfma_f32_4x4x4_16b_bf16 v[180:183], v[208:209], v[82:83], 0 cbsz:2 abid:3\n"
            "v_rcp_f32 v171, v171\n"
            "v_mfma_f32_4x4x4_16b_bf16 v[184:187], v[208:209], v[84:85], 0 cbsz:2 abid:3\n"
            "v_rcp_f32 v172, v172\n"
            "v_mfma_f32_4x4x4_16b_bf16 v[188:191], v[208:209], v[86:87], 0 cbsz:2 abid:3\n"
            "v_rcp_f32 v173, v173\n"
            "v_rcp_f32 v174, v174\n"
            "ds_read_b128 v[120:123], v228 offset:14336\n"
            "ds_read_b128 v[124:127], v228 offset:15360\n"
            "ds_read_b128 v[192:195], v228 offset:16384\n"
            "ds_read_b128 v[128:131], v228 offset:17408\n"
            "ds_read_b128 v[132:135], v228 offset:18432\n"
            "v_rcp_f32 v175, v175\n"
            "v_pk_fma_f32 v[0:1], v[146:147], v[176:177], v[0:1] op_sel:[1,0,0]\n"
            "v_pk_fma_f32 v[2:3], v[146:147], v[178:179], v[2:3] op_sel:[1,0,0]\n"
            "v_pk_fma_f32 v[4:5], v[150:151], v[180:181], v[4:5] op_sel:[1,0,0]\n"
            "v_pk_fma_f32 v[6:7], v[150:151], v[182:183], v[6:7] op_sel:[1,0,0]\n"
            "v_pk_fma_f32 v[8:9], v[154:155], v[184:185], v[8:9] op_sel:[1,0,0]\n"
            "v_pk_fma_f32 v[10:11], v[154:155], v[186:187], v[10:11] op_sel:[1,0,0]\n"
            "v_pk_fma_f32 v[12:13], v[158:159], v[188:189], v[12:13] op_sel:[1,0,0]\n"
            "v_pk_fma_f32 v[14:15], v[158:159], v[190:191], v[14:15] op_sel:[1,0,0]\n"
            "v_pk_mul_f32 v[168:169], v[168:169], v[136:137]\n"
            "v_pk_mul_f32 v[170:171], v[170:171], v[138:139]\n"
            "v_pk_mul_f32 v[172:173], v[172:173], v[140:141]\n"
            "v_pk_mul_f32 v[174:175], v[174:175], v[142:143]\n"
            "s_waitcnt vmcnt(8)\n"
            "v_mfma_f32_4x4x4_16b_bf16 v[176:179], v[208:209], v[40:41], 0 cbsz:2\n"
            "v_mfma_f32_4x4x4_16b_bf16 v[180:183], v[208:209], v[42:43], 0 cbsz:2\n"
            "v_mfma_f32_4x4x4_16b_bf16 v[184:187], v[208:209], v[44:45], 0 cbsz:2\n"
            "v_mfma_f32_4x4x4_16b_bf16 v[188:191], v[208:209], v[46:47], 0 cbsz:2\n"
            "ds_read_b128 v[136:139], v228 offset:19456\n"
            "ds_read_b128 v[140:143], v228 offset:20480\n"
            "ds_read_b128 v[144:147], v228 offset:21504\n"
            "ds_read_b128 v[148:151], v228 offset:22528\n"
            "ds_read_b128 v[152:155], v228 offset:23552\n"
            "ds_read_b128 v[156:159], v228 offset:24576\n"
            "s_nop 1\n"
            "v_pk_fma_f32 v[16:17], v[160:161], v[176:177], v[16:17] op_sel_hi:[0,1,1]\n"
            "v_pk_fma_f32 v[18:19], v[160:161], v[178:179], v[18:19] op_sel_hi:[0,1,1]\n"
            "v_pk_fma_f32 v[20:21], v[164:165], v[180:181], v[20:21] op_sel_hi:[0,1,1]\n"
            "v_pk_fma_f32 v[22:23], v[164:165], v[182:183], v[22:23] op_sel_hi:[0,1,1]\n"
            "v_pk_fma_f32 v[24:25], v[168:169], v[184:185], v[24:25] op_sel_hi:[0,1,1]\n"
            "v_pk_fma_f32 v[26:27], v[168:169], v[186:187], v[26:27] op_sel_hi:[0,1,1]\n"
            "v_pk_fma_f32 v[28:29], v[172:173], v[188:189], v[28:29] op_sel_hi:[0,1,1]\n"
            "v_pk_fma_f32 v[30:31], v[172:173], v[190:191], v[30:31] op_sel_hi:[0,1,1]\n"
            "s_cmp_le_u32 %[njl], 1\n"
            "s_cbranch_scc1 Ltail_%=\n"
            "v_mfma_f32_4x4x4_16b_bf16 v[176:179], v[208:209], v[56:57], 0 cbsz:2 abid:1\n"
            "v_mfma_f32_4x4x4_16b_bf16 v[180:183], v[208:209], v[58:59], 0 cbsz:2 abid:1\n"
            "v_mfma_f32_4x4x4_16b_bf16 v[184:187], v[208:209], v[60:61], 0 cbsz:2 abid:1\n"
            "v_mfma_f32_4x4x4_16b_bf16 v[188:191], v[208:209], v[62:63], 0 cbsz:2 abid:1\n"
            "s_nop 1\n"
            "v_pk_fma_f32 v[16:17], v[160:161], v[176:177], v[16:17] op_sel:[1,0,0]\n"
            "v_pk_fma_f32 v[18:19], v[160:161], v[178:179], v[18:19] op_sel:[1,0,0]\n"
            "v_pk_fma_f32 v[20:21], v[164:165], v[180:181], v[20:21] op_sel:[1,0,0]\n"
            "v_pk_fma_f32 v[22:23], v[164:165], v[182:183], v[22:23] op_sel:[1,0,0]\n"
            "v_pk_fma_f32 v[24:25], v[168:169], v[184:185], v[24:25] op_sel:[1,0,0]\n"
            "v_pk_fma_f32 v[26:27], v[168:169], v[186:187], v[26:27] op_sel:[1,0,0]\n"
            "v_pk_fma_f32 v[28:29], v[172:173], v[188:189], v[28:29] op_sel:[1,0,0]\n"
            "v_pk_fma_f32 v[30:31], v[172:173], v[190:191], v[30:31] op_sel:[1,0,0]\n"
            "s_cmp_le_u32 %[njl], 2\n"
            "s_cbranch_scc1 Ltail_%=\n"
            "s_waitcnt vmcnt(4)\n"
            "v_mfma_f32_4x4x4_16b_bf16 v[176:179], v[208:209], v[72:73], 0 cbsz:2 abid:2\n"
            "v_mfma_f32_4x4x4_16b_bf16 v[180:183], v[208:209], v[74:75], 0 cbsz:2 abid:2\n"
            "v_mfma_f32_4x4x4_16b_bf16 v[184:187], v[208:209], v[76:77], 0 cbsz:2 abid:2\n"
            "v_mfma_f32_4x4x4_16b_bf16 v[188:191], v[208:209], v[78:79], 0 cbsz:2 abid:2\n"
            "s_nop 1\n"
            "v_pk_fma_f32 v[16:17], v[162:163], v[176:177], v[16:17] op_sel_hi:[0,1,1]\n"
            "v_pk_fma_f32 v[18:19], v[162:163], v[178:179], v[18:19] op_sel_hi:[0,1,1]\n"
            "v_pk_fma_f32 v[20:21], v[166:167], v[180:181], v[20:21] op_sel_hi:[0,1,1]\n"
            "v_pk_fma_f32 v[22:23], v[166:167], v[182:183], v[22:23] op_sel_hi:[0,1,1]\n"
            "v_pk_fma_f32 v[24:25], v[170:171], v[184:185], v[24:25] op_sel_hi:[0,1,1]\n"
            "v_pk_fma_f32 v[26:27], v[170:171], v[186:187], v[26:27] op_sel_hi:[0,1,1]\n"
            "v_pk_fma_f32 v[28:29], v[174:175], v[188:189], v[28:29] op_sel_hi:[0,1,1]\n"
            "v_pk_fma_f32 v[30:31], v[174:175], v[190:191], v[30:31] op_sel_hi:[0,1,1]\n"
            "s_cmp_le_u32 %[njl], 3\n"
            "s_cbranch_scc1 Ltail_%=\n"
            "v_mfma_f32_4x4x4_16b_bf16 v[176:179], v[208:209], v[88:89], 0 cbsz:2 abid:3\n"
            "v_mfma_f32_4x4x4_16b_bf16 v[180:183], v[208:209], v[90:91], 0 cbsz:2 abid:3\n"
            "v_mfma_f32_4x4x4_16b_bf16 v[184:187], v[208:209], v[92:93], 0 cbsz:2 abid:3\n"
            "v_mfma_f32_4x4x4_16b_bf16 v[188:191], v[208:209], v[94:95], 0 cbsz:2 abid:3\n"
            "s_nop 1\n"
            "v_pk_fma_f32 v[16:17], v[162:163], v[176:177], v[16:17] op_sel:[1,0,0]\n"
            "v_pk_fma_f32 v[18:19], v[162:163], v[178:179], v[18:19] op_sel:[1,0,0]\n"
            "v_pk_fma_f32 v[20:21], v[166:167], v[180:181], v[20:21] op_sel:[1,0,0]\n"
            "v_pk_fma_f32 v[22:23], v[166:167], v[182:183], v[22:23] op_sel:[1,0,0]\n"
            "v_pk_fma_f32 v[24:25], v[170:171], v[184:185], v[24:25] op_sel:[1,0,0]\n"
            "v_pk_fma_f32 v[26:27], v[170:171], v[186:187], v[26:27] op_sel:[1,0,0]\n"
            "v_pk_fma_f32 v[28:29], v[174:175], v[188:189], v[28:29] op_sel:[1,0,0]\n"
            "v_pk_fma_f32 v[30:31], v[174:175], v[190:191], v[30:31] op_sel:[1,0,0]\n"
            "Ltail_%=:\n"
            "s_setprio 3\n"
            "s_waitcnt vmcnt(4)\n"
            "ds_read_b128 v[72:75], v228 offset:25600\n"
            "ds_read_b128 v[76:79], v228 offset:26624\n"
            "ds_read_b128 v[88:91], v228 offset:27648\n"
            "ds_read_b128 v[92:95], v228 offset:28672\n"
            "ds_read_b128 v[160:163], v228 offset:29696\n"
            "ds_read_b128 v[164:167], v228 offset:30720\n"
            "ds_read_b128 v[168:171], v228 offset:31744\n"
            "s_cmp_lg_u32 %[nst], %[tag]\n"
            "s_cbranch_scc1 Lspin_%=\n"
            "Lgo_%=:\n"
            "buffer_load_dwordx4 v[40:43], v218, %[rsw], %[oagg] offen offset:0 sc1\n"
            "buffer_load_dwordx4 v[44:47], v218, %[rsw], %[oagg] offen offset:256 sc1\n"
            "buffer_load_dwordx4 v[56:59], v218, %[rsw], %[oagg] offen offset:512 sc1\n"
            "buffer_load_dwordx4 v[60:63], v218, %[rsw], %[oagg] offen offset:768 sc1\n"
            "s_waitcnt vmcnt(4)\n"
            "v_mfma_f32_4x4x4_16b_bf16 v[0:3], v[224:225], v[32:33], v[0:3]\n"
            "v_mfma_f32_4x4x4_16b_bf16 v[4:7], v[224:225], v[34:35], v[4:7]\n"
            "v_mfma_f32_4x4x4_16b_bf16 v[8:11], v[224:225], v[36:37], v[8:11]\n"
            "v_mfma_f32_4x4x4_16b_bf16 v[12:15], v[224:225], v[38:39], v[12:15]\n"
            "v_mfma_f32_4x4x4_16b_bf16 v[16:19], v[224:225], v[48:49], v[16:19]\n"
            "v_mfma_f32_4x4x4_16b_bf16 v[20:23], v[224:225], v[50:51], v[20:23]\n"
            "v_mfma_f32_4x4x4_16b_bf16 v[24:27], v[224:225], v[52:53], v[24:27]\n"
            "v_mfma_f32_4x4x4_16b_bf16 v[28:31], v[224:225], v[54:55], v[28:31]\n"
            "s_nop 1\n"
            "v_cvt_pk_bf16_f32 v176, v0, v4\n"
            "v_cvt_pk_bf16_f32 v177, v8, v12\n"
            "v_cvt_pk_bf16_f32 v178, v16, v20\n"
            "v_cvt_pk_bf16_f32 v179, v24, v28\n"
            "ds_write_b128 v226, v[176:179] offset:0\n"
            "v_cvt_pk_bf16_f32 v180, v1, v5\n"
            "v_cvt_pk_bf16_f32 v181, v9, v13\n"
            "v_cvt_pk_bf16_f32 v182, v17, v21\n"
            "v_cvt_pk_bf16_f32 v183, v25, v29\n"
            "ds_write_b128 v226, v[180:183] offset:272\n"
            "v_cvt_pk_bf16_f32 v184, v2, v6\n"
            "v_cvt_pk_bf16_f32 v185, v10, v14\n"
            "v_cvt_pk_bf16_f32 v186, v18, v22\n"
            "v_cvt_pk_bf16_f32 v187, v26, v30\n"
            "ds_write_b128 v226, v[184:187] offset:544\n"
            "v_cvt_pk_bf16_f32 v188, v3, v7\n"
            "v_cvt_pk_bf16_f32 v189, v11, v15\n"
            "v_cvt_pk_bf16_f32 v190, v19, v23\n"
            "v_cvt_pk_bf16_f32 v191, v27, v31\n"
            "ds_write_b128 v226, v[188:191] offset:816\n"
            "ds_read_b128 v[176:179], v227 offset:0\n"
            "ds_read_b128 v[180:183], v227 offset:64\n"
            "ds_read_b128 v[184:187], v227 offset:128\n"
            "ds_read_b128 v[188:191], v227 offset:192\n"
            "s_waitcnt lgkmcnt(3)\n"
            "v_mfma_f32_16x16x32_bf16 v[0:3], v[176:179], v[236:239], 0\n"
            "v_mfma_f32_16x16x32_bf16 v[4:7], v[176:179], v[96:99], 0\n"
            "s_waitcnt lgkmcnt(2)\n"
            "v_mfma_f32_16x16x32_bf16 v[0:3], v[180:183], v[240:243], v[0:3]\n"
            "v_mfma_f32_16x16x32_bf16 v[4:7], v[180:183], v[100:103], v[4:7]\n"
            "s_waitcnt lgkmcnt(1)\n"
            "v_mfma_f32_16x16x32_bf16 v[0:3], v[184:187], v[244:247], v[0:3]\n"
            "v_mfma_f32_16x16x32_bf16 v[4:7], v[184:187], v[104:107], v[4:7]\n"
            "s_waitcnt lgkmcnt(0)\n"
            "v_mfma_f32_16x16x32_bf16 v[0:3], v[188:191], v[248:251], v[0:3]\n"
            "v_mfma_f32_16x16x32_bf16 v[4:7], v[188:191], v[108:111], v[4:7]\n"
            "v_mfma_f32_16x16x32_bf16 v[8:11], v[176:179], v[112:115], 0\n"
            "v_mfma_f32_16x16x32_bf16 v[12:15], v[176:179], v[204:207], 0\n"
            "v_mfma_f32_16x16x32_bf16 v[8:11], v[180:183], v[116:119], v[8:11]\n"
            "v_mfma_f32_16x16x32_bf16 v[12:15], v[180:183], v[232:235], v[12:15]\n"
            "v_mfma_f32_16x16x32_bf16 v[8:11], v[184:187], v[196:199], v[8:11]\n"
            "v_mfma_f32_16x16x32_bf16 v[12:15], v[184:187], v[120:123], v[12:15]\n"
            "v_mfma_f32_16x16x32_bf16 v[8:11], v[188:191], v[200:203], v[8:11]\n"
            "v_mfma_f32_16x16x32_bf16 v[12:15], v[188:191], v[124:127], v[12:15]\n"
            "v_cvt_pk_bf16_f32 v0, v0, v1\n"
            "v_cvt_pk_bf16_f32 v1, v2, v3\n"
            "v_cvt_pk_bf16_f32 v2, v4, v5\n"
            "v_cvt_pk_bf16_f32 v3, v6, v7\n"
            "buffer_store_dwordx4 v[0:3], v217, %[rso], 0 offen offset:0 sc1\n"
            "v_mfma_f32_16x16x32_bf16 v[16:19], v[176:179], v[192:195], 0\n"
            "v_mfma_f32_16x16x32_bf16 v[20:23], v[176:179], v[140:143], 0\n"
            "v_mfma_f32_16x16x32_bf16 v[16:19], v[180:183], v[128:131], v[16:19]\n"
            "v_mfma_f32_16x16x32_bf16 v[20:23], v[180:183], v[144:147], v[20:23]\n"
            "v_mfma_f32_16x16x32_bf16 v[16:19], v[184:187], v[132:135], v[16:19]\n"
            "v_mfma_f32_16x16x32_bf16 v[20:23], v[184:187], v[148:151], v[20:23]\n"
            "v_mfma_f32_16x16x32_bf16 v[16:19], v[188:191], v[136:139], v[16:19]\n"
            "v_mfma_f32_16x16x32_bf16 v[20:23], v[188:191], v[152:155], v[20:23]\n"
            "v_cvt_pk_bf16_f32 v8, v8, v9\n"
            "v_cvt_pk_bf16_f32 v9, v10, v11\n"
            "v_cvt_pk_bf16_f32 v10, v12, v13\n"
            "v_cvt_pk_bf16_f32 v11, v14, v15\n"
            "buffer_store_dwordx4 v[8:11], v217, %[rso], 0 offen offset:256 sc1\n"
            "v_mfma_f32_16x16x32_bf16 v[24:27], v[176:179], v[156:159], 0\n"
            "v_mfma_f32_16x16x32_bf16 v[28:31], v[176:179], v[92:95], 0\n"
            "v_mfma_f32_16x16x32_bf16 v[24:27], v[180:183], v[72:75], v[24:27]\n"
            "v_mfma_f32_16x16x32_bf16 v[28:31], v[180:183], v[160:163], v[28:31]\n"
            "v_mfma_f32_16x16x32_bf16 v[24:27], v[184:187], v[76:79], v[24:27]\n"
            "v_mfma_f32_16x16x32_bf16 v[28:31], v[184:187], v[164:167], v[28:31]\n"
            "v_mfma_f32_16x16x32_bf16 v[24:27], v[188:191], v[88:91], v[24:27]\n"
            "v_mfma_f32_16x16x32_bf16 v[28:31], v[188:191], v[168:171], v[28:31]\n"
            "v_cvt_pk_bf16_f32 v16, v16, v17\n"
            "v_cvt_pk_bf16_f32 v17, v18, v19\n"
            "v_cvt_pk_bf16_f32 v18, v20, v21\n"
            "v_cvt_pk_bf16_f32 v19, v22, v23\n"
            "buffer_store_dwordx4 v[16:19], v217, %[rso], 0 offen offset:512 sc1\n"
            "s_nop 2\n"
            "v_cvt_pk_bf16_f32 v24, v24, v25\n"
            "v_cvt_pk_bf16_f32 v25, v26, v27\n"
            "v_cvt_pk_bf16_f32 v26, v28, v29\n"
            "v_cvt_pk_bf16_f32 v27, v30, v31\n"
            "buffer_store_dwordx4 v[24:27], v217, %[rso], 0 offen offset:768 sc1\n"
            "s_cmp_lt_u32 %[two], 6\n"
            "s_cbranch_scc1 Lend_%=\n"
            "ds_read_b128 v[64:67], v231 offset:0\n"
            "ds_read_b128 v[68:71], v231 offset:1024\n"
            "ds_read_b128 v[80:83], v231 offset:2048\n"
            "ds_read_b128 v[84:87], v231 offset:3072\n"
            "s_waitcnt vmcnt(4) lgkmcnt(0)\n"
            "v_mfma_f32_4x4x4_16b_bf16 v[0:3], v[224:225], v[64:65], 0\n"
            "v_mfma_f32_4x4x4_16b_bf16 v[4:7], v[224:225], v[66:67], 0\n"
            "v_mfma_f32_4x4x4_16b_bf16 v[8:11], v[224:225], v[68:69], 0\n"
            "v_mfma_f32_4x4x4_16b_bf16 v[12:15], v[224:225], v[70:71], 0\n"
            "v_mfma_f32_4x4x4_16b_bf16 v[16:19], v[224:225], v[80:81], 0\n"
            "v_mfma_f32_4x4x4_16b_bf16 v[20:23], v[224:225], v[82:83], 0\n"
            "v_mfma_f32_4x4x4_16b_bf16 v[24:27], v[224:225], v[84:85], 0\n"
            "v_mfma_f32_4x4x4_16b_bf16 v[28:31], v[224:225], v[86:87], 0\n"
            "v_mfma_f32_4x4x4_16b_bf16 v[0:3], v[224:225], v[40:41], v[0:3]\n"
            "v_mfma_f32_4x4x4_16b_bf16 v[4:7], v[224:225], v[42:43], v[4:7]\n"
            "v_mfma_f32_4x4x4_16b_bf16 v[8:11], v[224:225], v[44:45], v[8:11]\n"
            "v_mfma_f32_4x4x4_16b_bf16 v[12:15], v[224:225], v[46:47], v[12:15]\n"
            "v_mfma_f32_4x4x4_16b_bf16 v[16:19], v[224:225], v[56:57], v[16:19]\n"
            "v_mfma_f32_4x4x4_16b_bf16 v[20:23], v[224:225], v[58:59], v[20:23]\n"
            "v_mfma_f32_4x4x4_16b_bf16 v[24:27], v[224:225], v[60:61], v[24:27]\n"
            "v_mfma_f32_4x4x4_16b_bf16 v[28:31], v[224:225], v[62:63], v[28:31]\n"
            "s_nop 1\n"
            "v_cvt_pk_bf16_f32 v176, v0, v4\n"
            "v_cvt_pk_bf16_f32 v177, v8, v12\n"
            "v_cvt_pk_bf16_f32 v178, v16, v20\n"
            "v_cvt_pk_bf16_f32 v179, v24, v28\n"
            "ds_write_b128 v226, v[176:179] offset:0\n"
            "v_cvt_pk_bf16_f32 v180, v1, v5\n"
            "v_cvt_pk_bf16_f32 v181, v9, v13\n"
            "v_cvt_pk_bf16_f32 v182, v17, v21\n"
            "v_cvt_pk_bf16_f32 v183, v25, v29\n"
            "ds_write_b128 v226, v[180:183] offset:272\n"
            "v_cvt_pk_bf16_f32 v184, v2, v6\n"
            "v_cvt_pk_bf16_f32 v185, v10, v14\n"
            "v_cvt_pk_bf16_f32 v186, v18, v22\n"
            "v_cvt_pk_bf16_f32 v187, v26, v30\n"
            "ds_write_b128 v226, v[184:187] offset:544\n"
            "v_cvt_pk_bf16_f32 v188, v3, v7\n"
            "v_cvt_pk_bf16_f32 v189, v11, v15\n"
            "v_cvt_pk_bf16_f32 v190, v19, v23\n"
            "v_cvt_pk_bf16_f32 v191, v27, v31\n"
            "ds_write_b128 v226, v[188:191] offset:816\n"
            "ds_read_b128 v[176:179], v227 offset:0\n"
            "ds_read_b128 v[180:183], v227 offset:64\n"
            "ds_read_b128 v[184:187], v227 offset:128\n"
            "ds_read_b128 v[188:191], v227 offset:192\n"
            "s_waitcnt lgkmcnt(3)\n"
            "v_mfma_f32_16x16x32_bf16 v[0:3], v[176:179], v[236:239], 0\n"
            "v_mfma_f32_16x16x32_bf16 v[4:7], v[176:179], v[96:99], 0\n"
            "s_waitcnt lgkmcnt(2)\n"
            "v_mfma_f32_16x16x32_bf16 v[0:3], v[180:183], v[240:243], v[0:3]\n"
            "v_mfma_f32_16x16x32_bf16 v[4:7], v[180:183], v[100:103], v[4:7]\n"
            "s_waitcnt lgkmcnt(1)\n"
            "v_mfma_f32_16x16x32_bf16 v[0:3], v[184:187], v[244:247], v[0:3]\n"
            "v_mfma_f32_16x16x32_bf16 v[4:7], v[184:187], v[104:107], v[4:7]\n"
            "s_waitcnt lgkmcnt(0)\n"
            "v_mfma_f32_16x16x32_bf16 v[0:3], v[188:191], v[248:251], v[0:3]\n"
            "v_mfma_f32_16x16x32_bf16 v[4:7], v[188:191], v[108:111], v[4:7]\n"
            "v_mfma_f32_16x16x32_bf16 v[8:11], v[176:179], v[112:115], 0\n"
            "v_mfma_f32_16x16x32_bf16 v[12:15], v[176:179], v[204:207], 0\n"
            "v_mfma_f32_16x16x32_bf16 v[8:11], v[180:183], v[116:119], v[8:11]\n"
            "v_mfma_f32_16x16x32_bf16 v[12:15], v[180:183], v[232:235], v[12:15]\n"
            "v_mfma_f32_16x16x32_bf16 v[8:11], v[184:187], v[196:199], v[8:11]\n"
            "v_mfma_f32_16x16x32_bf16 v[12:15], v[184:187], v[120:123], v[12:15]\n"
            "v_mfma_f32_16x16x32_bf16 v[8:11], v[188:191], v[200:203], v[8:11]\n"
            "v_mfma_f32_16x16x32_bf16 v[12:15], v[188:191], v[124:127], v[12:15]\n"
            "v_cvt_pk_bf16_f32 v0, v0, v1\n"
            "v_cvt_pk_bf16_f32 v1, v2, v3\n"
            "v_cvt_pk_bf16_f32 v2, v4, v5\n"
            "v_cvt_pk_bf16_f32 v3, v6, v7\n"
            "buffer_store_dwordx4 v[0:3], v218, %[rso], 0 offen offset:0 sc1\n"
            "v_mfma_f32_16x16x32_bf16 v[16:19], v[176:179], v[192:195], 0\n"
            "v_mfma_f32_16x16x32_bf16 v[20:23], v[176:179], v[140:143], 0\n"
            "v_mfma_f32_16x16x32_bf16 v[16:19], v[180:183], v[128:131], v[16:19]\n"
            "v_mfma_f32_16x16x32_bf16 v[20:23], v[180:183], v[144:147], v[20:23]\n"
            "v_mfma_f32_16x16x32_bf16 v[16:19], v[184:187], v[132:135], v[16:19]\n"
            "v_mfma_f32_16x16x32_bf16 v[20:23], v[184:187], v[148:151], v[20:23]\n"
            "v_mfma_f32_16x16x32_bf16 v[16:19], v[188:191], v[136:139], v[16:19]\n"
            "v_mfma_f32_16x16x32_bf16 v[20:23], v[188:191], v[152:155], v[20:23]\n"
            "v_cvt_pk_bf16_f32 v8, v8, v9\n"
            "v_cvt_pk_bf16_f32 v9, v10, v11\n"
            "v_cvt_pk_bf16_f32 v10, v12, v13\n"
            "v_cvt_pk_bf16_f32 v11, v14, v15\n"
            "buffer_store_dwordx4 v[8:11], v218, %[rso], 0 offen offset:256 sc1\n"
            "v_mfma_f32_16x16x32_bf16 v[24:27], v[176:179], v[156:159], 0\n"
            "v_mfma_f32_16x16x32_bf16 v[28:31], v[176:179], v[92:95], 0\n"
            "v_mfma_f32_16x16x32_bf16 v[24:27], v[180:183], v[72:75], v[24:27]\n"
            "v_mfma_f32_16x16x32_bf16 v[28:31], v[180:183], v[160:163], v[28:31]\n"
            "v_mfma_f32_16x16x32_bf16 v[24:27], v[184:187], v[76:79], v[24:27]\n"
            "v_mfma_f32_16x16x32_bf16 v[28:31], v[184:187], v[164:167], v[28:31]\n"
            "v_mfma_f32_16x16x32_bf16 v[24:27], v[188:191], v[88:91], v[24:27]\n"
            "v_mfma_f32_16x16x32_bf16 v[28:31], v[188:191], v[168:171], v[28:31]\n"
            "v_cvt_pk_bf16_f32 v16, v16, v17\n"
            "v_cvt_pk_bf16_f32 v17, v18, v19\n"
            "v_cvt_pk_bf16_f32 v18, v20, v21\n"
            "v_cvt_pk_bf16_f32 v19, v22, v23\n"
            "buffer_store_dwordx4 v[16:19], v218, %[rso], 0 offen offset:512 sc1\n"
            "s_nop 2\n"
            "v_cvt_pk_bf16_f32 v24, v24, v25\n"
            "v_cvt_pk_bf16_f32 v25, v26, v27\n"
            "v_cvt_pk_bf16_f32 v26, v28, v29\n"
            "v_cvt_pk_bf16_f32 v27, v30, v31\n"
            "buffer_store_dwordx4 v[24:27], v218, %[rso], 0 offen offset:768 sc1\n"
            "Lend_%=:\n"
            "s_waitcnt vmcnt(4)\n"
            : "+{v[32:63]}"(oxa), "+{v[64:95]}"(oxb), "+{v[96:127]}"(owr), "+{v[196:199]}"(S), "+{v[200:203]}"(cgA), "+{v[208:223]}"(omi), "+{v[224:231]}"(ot),
              "+{v[232:233]}"(efN), "+{v234}"(bpa), "+{v[236:237]}"(jmp), "+{v[238:239]}"(ef0),
              [rem] "+s"(rem), [lens] "+s"(lens), [fm] "+s"(fm), [nst] "+s"(nst), [pb] "+s"(pb), [evt] "+s"(evt), [ra] "=&s"(ra), [tmp] "=&s"(tmp)
            : [rse] "s"(de), [rsx] "s"(dx), [rso] "s"(mkdesc(out, (unsigned)N * 1024u)), [rsw] "s"(mkdesc(ws, W_END)),
              [cnt] "s"(lds0 + L_MISC + 4), [pfa] "s"(lds0 + L_MISC + 16 + pw * 4), [tag] "s"(tag), [flg] "s"(ws + W_FLG + bx * 128), [pfl] "s"(ws + W_FLG + (bx ^ 4) * 128),
              [njl] "s"(((e2 - 1) & 3) + 1), [c1] "s"((w < 4 ? l2 : l1) - 4 * ((e2 + 3) >> 2) - 5), [c2] "s"(w < 4 ? l1 : l2), [two] "s"(w), [ptab] "s"((unsigned)__builtin_amdgcn_readfirstlane(w < 4 ? PTO : (w < 6 ? PTY : PTZ))), [oagg] "s"(W_AGG), [spin] "s"(SPIN_MAX)
            : "memory", "vcc", "scc", "v0", "v1", "v2", "v3", "v4", "v5", "v6", "v7", "v8", "v9", "v10", "v11", "v12", "v13", "v14", "v15", "v16", "v17", "v18", "v19", "v20", "v21", "v22", "v23", "v24", "v25", "v26", "v27", "v28", "v29", "v30", "v31", "v128", "v129", "v130", "v131", "v132", "v133", "v134", "v135", "v136", "v137", "v138", "v139", "v140", "v141", "v142", "v143", "v144", "v145", "v146", "v147", "v148", "v149", "v150", "v151", "v152", "v153", "v154", "v155", "v156", "v157", "v158", "v159", "v160", "v161", "v162", "v163", "v164", "v165", "v166", "v167", "v168", "v169", "v170", "v171", "v172", "v173", "v174", "v175", "v176", "v177", "v178", "v179", "v180", "v181", "v182", "v183", "v184", "v185", "v186", "v187", "v188", "v189", "v190", "v191", "v192", "v193", "v194", "v195", "v235", "v204", "v205", "v206", "v207", "v240", "v241", "v242", "v243", "v244", "v245", "v246", "v247", "v248", "v249", "v250", "v251");
    } else {
        if (bx == 0 && w == 4) flag_store(ws + W_CTL, tag);
        lds_wait(lds0 + SY_C3, 5);
        const __amdgpu_buffer_rsrc_t rso = __builtin_amdgcn_make_buffer_rsrc((void*)out, 0, N * 1024, 0x00020000);
#pragma unroll 1
        for (int it = 0; it < 10; ++it) {
            const int nl = (it < 5 ? 40 - 40 * half + 8 * it : 40 * half + 8 * (it - 5)) + w, node = node0 + nl;
            const bool nv = nl < npb;
            const unsigned ro = nv ? (unsigned)node * 1024u + (unsigned)lane * 16u : OOB_OFF;
            float a8[8] = {0.f, 0.f, 0.f, 0.f, 0.f, 0.f, 0.f, 0.f};
#pragma unroll 1
            for (int e0 = 0; e0 < (nv ? E : 0); e0 += 64) {
                const int ec = min(e0 + lane, E - 1);
                u64 mm = __ballot(e0 + lane < E && dst[ec] == node && (int)(((src[ec] >> 3) ^ (smask >> ((src[ec] >> 9) & 31))) & 1) == half);
#pragma unroll 1
                while (mm) {
                    const int e = e0 + __builtin_ctzll(mm);
                    mm &= mm - 1;
                    const unsigned short* fe = (const unsigned short*)ef + (size_t)e * DE_DIM;
                    const unsigned short* ye = (const unsigned short*)Y + (size_t)e * M_DIM;
                    const unsigned short* xp = x + (size_t)src[e] * (C_DIM * M_DIM) + lane * 8;
                    float z0 = 0.f, z1 = 0.f;
#pragma unroll 1
                    for (int f = 0; f < DE_DIM; ++f) { z0 += bf2f(fe[f]) * bf2f(w_rad[f * C_DIM + 2 * lane]); z1 += bf2f(fe[f]) * bf2f(w_rad[f * C_DIM + 2 * lane + 1]); }
                    const float r0 = z0 / (1.f + __expf(-z0)), r1 = z1 / (1.f + __expf(-z1));
#pragma unroll 1
                    for (int i = 0; i < 4; ++i) {
                        const float x0 = bf2f(xp[i]) * r0, x1 = bf2f(xp[4 + i]) * r1;
#pragma unroll
                        for (int o = 0; o < 4; ++o) {
                            float wv4 = 0.f;
#pragma unroll
                            for (int f = 0; f < 4; ++f) wv4 += bf2f(cg[(i * 4 + f) * 4 + o]) * bf2f(ye[f]);
                            a8[o] += x0 * wv4; a8[4 + o] += x1 * wv4;
                        }
                    }
                }
            }
            if (it >= 5) {
                const u32x4 pv = __builtin_amdgcn_raw_buffer_load_b128(rsw, ro, W_AGG, SC1);
#pragma unroll
                for (int i = 0; i < 4; ++i) { a8[2 * i] += __uint_as_float(pv[i] << 16); a8[2 * i + 1] += __uint_as_float(pv[i] & 0xffff0000u); }
            }
            const u32x4 rv = (u32x4){pack2bf(a8[0], a8[1]), pack2bf(a8[2], a8[3]), pack2bf(a8[4], a8[5]), pack2bf(a8[6], a8[7])};
            if (it < 5) {
                __builtin_amdgcn_raw_buffer_store_b128(rv, rsw, ro, W_AGG, SC1);
                if (it == 4) {
                    asm volatile("s_waitcnt vmcnt(0)" ::: "memory");
                    if (lds_arrive_rtn(lds0 + L_MISC + 4) == 7u) flag_store(ws + W_FLG + bx * 128, tag);
                    poll_tag(ws + W_FLG + (bx ^ 4) * 128, tag);
                }
            } else {
                *(u32x4*)(smem + L_PRV + w * 1024 + lane * 16) = rv;
#pragma unroll
                for (int i = 0; i < 8; ++i) a8[i] = 0.f;
#pragma unroll 1
                for (int c = 0; c < C_DIM; ++c) {
                    const unsigned short* ap = (const unsigned short*)(smem + L_PRV + w * 1024 + c * 8);
                    const float w0 = bf2f(w_mix[c * C_DIM + 2 * lane]), w1 = bf2f(w_mix[c * C_DIM + 2 * lane + 1]);
#pragma unroll
                    for (int m = 0; m < 4; ++m) { a8[m] += bf2f(ap[m]) * w0; a8[4 + m] += bf2f(ap[m]) * w1; }
                }
                __builtin_amdgcn_raw_buffer_store_b128((u32x4){pack2bf(a8[0], a8[1]), pack2bf(a8[2], a8[3]), pack2bf(a8[4], a8[5]), pack2bf(a8[6], a8[7])}, rso, ro, 0, 0);
            }
        }
    }
}

__global__ void kf_zero(float* __restrict__ agg, size_t n) {
    for (size_t i = (size_t)blockIdx.x * blockDim.x + threadIdx.x; i < n; i += (size_t)gridDim.x * blockDim.x) agg[i] = 0.f;
}
__global__ void kf_edges(const unsigned short* __restrict__ x, const int* __restrict__ src, const int* __restrict__ dst,
                         const unsigned short* __restrict__ Y, const unsigned short* __restrict__ ef,
                         const unsigned short* __restrict__ w_rad, const unsigned short* __restrict__ cg,
                         float* __restrict__ agg, int E) {
    const int ch = threadIdx.x;
    for (int e = blockIdx.x; e < E; e += gridDim.x) {
        float z = 0.f;
#pragma unroll 4
        for (int f = 0; f < DE_DIM; ++f) z += bf2f(ef[(size_t)e * DE_DIM + f]) * bf2f(w_rad[f * C_DIM + ch]);
        const float r = z / (1.f + __expf(-z));
        float a4[4] = {0.f, 0.f, 0.f, 0.f};
        for (int ii = 0; ii < 4; ++ii) {
            const float xv = bf2f(x[((size_t)src[e] * C_DIM + ch) * M_DIM + ii]) * r;
            for (int o = 0; o < 4; ++o) {
                float wv = 0.f;
                for (int f = 0; f < 4; ++f) wv += bf2f(cg[(ii * 4 + f) * 4 + o]) * bf2f(Y[(size_t)e * 4 + f]);
                a4[o] += xv * wv;
            }
        }
        for (int o = 0; o < 4; ++o) atomicAdd(&agg[((size_t)dst[e] * C_DIM + ch) * M_DIM + o], a4[o]);
    }
}
__global__ void kf_mix(const float* __restrict__ agg, const unsigned short* __restrict__ w_mix, unsigned short* __restrict__ out, int N) {
    const int d = threadIdx.x;
    for (int n = blockIdx.x; n < N; n += gridDim.x) {
        float a4[4] = {0.f, 0.f, 0.f, 0.f};
        for (int c = 0; c < C_DIM; ++c) {
            const float wv = bf2f(w_mix[c * C_DIM + d]);
            for (int m = 0; m < 4; ++m) a4[m] += bf2f(f2bf(agg[((size_t)n * C_DIM + c) * M_DIM + m])) * wv;
        }
        for (int m = 0; m < 4; ++m) out[((size_t)n * C_DIM + d) * M_DIM + m] = f2bf(a4[m]);
    }
}

extern "C" void kernel_launch(void* const* d_in, const int* in_sizes, int n_in,
                              void* d_out, int out_size, void* d_ws, size_t ws_size,
                              hipStream_t stream) {
    const unsigned short* x = (const unsigned short*)d_in[0];
    const int* src = (const int*)d_in[1];
    const int* dst = (const int*)d_in[2];
    const unsigned short* Y = (const unsigned short*)d_in[3];
    const unsigned short* ef = (const unsigned short*)d_in[4];
    const unsigned short* w_rad = (const unsigned short*)d_in[5];
    const unsigned short* cg = (const unsigned short*)d_in[6];
    const unsigned short* w_mix = (const unsigned short*)d_in[7];
    unsigned short* out = (unsigned short*)d_out;
    const int N = in_sizes[0] / (C_DIM * M_DIM);
    const int E = in_sizes[1];
    const int epb = (E + 253) / 254;
    const bool fast = N >= 256 && N <= NB_MAX * NPB_MAX && E > 0 && epb <= EPB_MAX && ws_size >= (size_t)W_END;
    if (!fast) {
        float* agg = (float*)d_ws;
        if (N <= 0) return;
        kf_zero<<<1024, 256, 0, stream>>>(agg, (size_t)N * C_DIM * M_DIM);
        if (E > 0) kf_edges<<<4096, C_DIM, 0, stream>>>(x, src, dst, Y, ef, w_rad, cg, agg, E);
        kf_mix<<<2048, C_DIM, 0, stream>>>(agg, w_mix, out, N);
        return;
    }
    const unsigned nb_magic = (unsigned)(((1ull << 39) + N - 1) / N);
    unsigned smask = 0;
    const int ng = (N + 511) / 512;
    int pw = 1;
    while (2 * pw * 4 < ng + 4) pw *= 2;
    for (int i = 0; i < ng; ++i) smask |= (unsigned)(i < 4 * pw ? ((i >> 2) ^ (i >= WLO * ng / 20 && i < WHI * ng / 20)) & 1 : i & 1) << i;
    const int r0 = ((N < 2048 * pw ? N : 2048 * pw) - 128) & ~63, r1 = r0 - (r0 >= 2048 ? 2048 : 128);
    k_fused<<<256, 512, 0, stream>>>(dst, (unsigned)E * 4u, 0x20000u, src, (unsigned)E * 4u, 0x20000u, Y, d_ws, x, ef, w_rad, cg, w_mix, out,
                                     N, E, epb * 4, nb_magic, smask, (unsigned)(4 * 65536 / N), (unsigned)(r0 + (r0 >> 8 & 8)) * 1024u, (unsigned)(r1 + (r1 >> 8 & 8)) * 1024u);
}
```

```cpp
#include <hip/hip_runtime.h>
#include <hip/hip_bf16.h>
#include <stdint.h>

typedef __attribute__((ext_vector_type(8))) short bf16x8;
typedef __attribute__((ext_vector_type(4))) short bf16x4;
typedef __attribute__((ext_vector_type(4))) float f32x4;
typedef __attribute__((ext_vector_type(2))) unsigned u32x2;
typedef __attribute__((ext_vector_type(3))) unsigned u32x3;
typedef __attribute__((ext_vector_type(4))) unsigned u32x4;
typedef __attribute__((ext_vector_type(8))) unsigned u32x8;
typedef __attribute__((ext_vector_type(16))) unsigned u32x16;
typedef __attribute__((ext_vector_type(32))) unsigned u32x32;
typedef unsigned long long u64;

#define C_DIM 128
#define M_DIM 4
#define DE_DIM 16
#define NPB_MAX 80
#define NB_MAX 128
#define EPB_MAX 1280
#define NRND 7
#define LS 47
#define DEG_MAX 42
#define OOB_OFF 0x80000000u
#define SC1 16
#define NT 2
#define SPIN_MAX (1u << 22)
#define CODE_LINES 152
#define PTO 0xfffa9540u
#define PTY 0xffffa950u
#define PTZ 0xffffea54u
#define NT0 2
#define NT2 5
#define TLIST 100, 3, 4, 5, 10
#define WLO 8
#define WHI 11
#define IDLE 200u
#define CPN 12
#define EW 3
#define PBIAS 12
#define TKF 0x98765432u
#define TKB 0x23456789u
#define TKA 0x1010ffffu
#define EXTRA 0, 0, 5, 7

#define W_CTL 0
#define W_DEC 2048
#define W_FLG 4096
#define W_TAB (W_FLG + 32768)
#define W_RA (W_TAB + 256 * 256 * 8)
#define W_AGG (W_RA + 256 * EPB_MAX * 16)
#define W_END (W_AGG + NB_MAX * NPB_MAX * 1024)

#define SB() __builtin_amdgcn_sched_barrier(0)

static __device__ __forceinline__ float bf2f(unsigned short v) { return __uint_as_float(((unsigned)v) << 16); }
typedef __bf16 bf16v2 __attribute__((ext_vector_type(2)));
static __device__ __forceinline__ unsigned pack2bf(float lo, float hi) {
    bf16v2 r = (bf16v2){(__bf16)lo, (__bf16)hi};
    return __builtin_bit_cast(unsigned, r);
}
static __device__ __forceinline__ unsigned short f2bf(float v) { return (unsigned short)(pack2bf(v, 0.f) & 0xffffu); }
template <int CTRL, int RM> static __device__ __forceinline__ int dpp_add(int v) {
    return v + __builtin_amdgcn_update_dpp(0, v, CTRL, RM, 0xf, false);
}
static __device__ __forceinline__ int wave_incl_scan(int v) {
    v = dpp_add<0x111, 0xf>(v); v = dpp_add<0x112, 0xf>(v); v = dpp_add<0x114, 0xf>(v); v = dpp_add<0x118, 0xf>(v);
    v = dpp_add<0x142, 0xa>(v); v = dpp_add<0x143, 0xc>(v);
    return v;
}
template <int CTRL, int RM> static __device__ __forceinline__ unsigned dpp_max(unsigned v) {
    return max(v, (unsigned)__builtin_amdgcn_update_dpp(0, (int)v, CTRL, RM, 0xf, false));
}
static __device__ __forceinline__ unsigned wave_max_scan(unsigned v) {
    v = dpp_max<0x111, 0xf>(v); v = dpp_max<0x112, 0xf>(v); v = dpp_max<0x114, 0xf>(v); v = dpp_max<0x118, 0xf>(v);
    v = dpp_max<0x142, 0xa>(v); v = dpp_max<0x143, 0xc>(v);
    return v;
}
#define MAXS(c) "v_max_u32_dpp %0, %0, %0 " c "\n\tv_max_u32_dpp %1, %1, %1 " c "\n\tv_max_u32_dpp %2, %2, %2 " c "\n\t"
static __device__ __forceinline__ void wave_max_scan3(unsigned& a, unsigned& b, unsigned& c) {
    asm volatile("s_nop 1\n\t" MAXS("row_shr:1 row_mask:0xf bank_mask:0xf bound_ctrl:1") MAXS("row_shr:2 row_mask:0xf bank_mask:0xf bound_ctrl:1")
                 MAXS("row_shr:4 row_mask:0xf bank_mask:0xf bound_ctrl:1") MAXS("row_shr:8 row_mask:0xf bank_mask:0xf bound_ctrl:1")
                 MAXS("row_bcast:15 row_mask:0xa bank_mask:0xf") MAXS("row_bcast:31 row_mask:0xc bank_mask:0xf") "s_nop 0" : "+v"(a), "+v"(b), "+v"(c));
}
static __device__ __forceinline__ unsigned bsum(unsigned c, unsigned m, unsigned acc = 0u) { return __builtin_amdgcn_udot4(c, m, acc, false); }
static __device__ __forceinline__ unsigned bytesum2(unsigned c0, unsigned c1, unsigned m0, unsigned m1, unsigned acc = 0u) {
    return __builtin_amdgcn_udot4(c1, m1, __builtin_amdgcn_udot4(c0, m0, acc, false), false);
}
static __device__ __forceinline__ unsigned below_mask(int w, int k) {
    const int n = w - 4 * k;
    return n >= 4 ? 0x01010101u : (n <= 0 ? 0u : (0x01010101u & ((1u << (8 * n)) - 1u)));
}
static __device__ __forceinline__ void flag_store(const void* p, unsigned v) {
    unsigned t0, t1; u64 sv;
    asm volatile("v_mov_b32 %0, 0\n\tv_mov_b32 %1, %4\n\ts_mov_b64 %2, exec\n\ts_mov_b64 exec, 1\n\t"
                 "global_store_dword %0, %1, %3 sc1\n\ts_mov_b64 exec, %2" : "=&v"(t0), "=&v"(t1), "=&s"(sv) : "s"(p), "s"(v) : "memory");
}
static __device__ __forceinline__ unsigned lds_arrive_rtn(unsigned a) {
    unsigned t0, t1; u64 sv;
    asm volatile("v_mov_b32 %0, %3\n\tv_mov_b32 %1, 1\n\ts_mov_b64 %2, exec\n\ts_mov_b64 exec, 1\n\t"
                 "ds_add_rtn_u32 %1, %0, %1\n\ts_mov_b64 exec, %2\n\ts_waitcnt lgkmcnt(0)" : "=&v"(t0), "=&v"(t1), "=&s"(sv) : "s"(a) : "memory");
    return (unsigned)__builtin_amdgcn_readfirstlane((int)t1);
}
static __device__ __forceinline__ void lds_arrive(unsigned a, unsigned inc = 1u) {
    unsigned t0, t1; u64 sv;
    asm volatile("v_mov_b32 %0, %3\n\tv_mov_b32 %1, %4\n\ts_mov_b64 %2, exec\n\ts_mov_b64 exec, 1\n\t"
                 "ds_add_u32 %0, %1\n\ts_mov_b64 exec, %2" : "=&v"(t0), "=&v"(t1), "=&s"(sv) : "s"(a), "s"(inc) : "memory");
}
template <int SLEEP = 0, unsigned MASK = ~0u> static __device__ __forceinline__ unsigned lds_wait(unsigned a, unsigned n) {
    for (;;) {
        unsigned v;
        asm volatile("ds_read_b32 %0, %1\n\ts_waitcnt lgkmcnt(0)" : "=v"(v) : "v"(a) : "memory");
        v = (unsigned)__builtin_amdgcn_readfirstlane((int)v);
        if ((v & MASK) >= n) return v;
        if (SLEEP) __builtin_amdgcn_s_sleep(SLEEP);
    }
}
static __device__ __forceinline__ unsigned ld_agent(const void* p) { return __hip_atomic_load((const unsigned*)p, __ATOMIC_RELAXED, __HIP_MEMORY_SCOPE_AGENT); }
static __device__ __forceinline__ void poll_tag(const void* p, unsigned tag) {
#pragma unroll 1
    for (unsigned spin = 0; spin < SPIN_MAX; ++spin) {
        if (__all(ld_agent(p) == tag)) break;
        __builtin_amdgcn_s_sleep(2);
    }
    __builtin_amdgcn_fence(__ATOMIC_ACQUIRE, "wavefront");
}
static __device__ __forceinline__ void wg_barrier() {
    asm volatile("s_waitcnt lgkmcnt(0)" ::: "memory");
    __builtin_amdgcn_s_barrier();
    asm volatile("" ::: "memory");
}
static __device__ __forceinline__ f32x4 mfma4(u32x2 a, u32x2 b, f32x4 c) {
    return __builtin_amdgcn_mfma_f32_4x4x4bf16_1k(__builtin_bit_cast(bf16x4, a), __builtin_bit_cast(bf16x4, b), c, 0, 0, 0);
}
static __device__ __forceinline__ f32x4 mfma16(u32x4 a, u32x4 b, f32x4 c) {
    return __builtin_amdgcn_mfma_f32_16x16x32_bf16(__builtin_bit_cast(bf16x8, a), __builtin_bit_cast(bf16x8, b), c, 0, 0, 0);
}

static constexpr unsigned evt_of(unsigned pt) {
    unsigned e = 6u;
    for (int n = 2; n < 15; ++n) if (((pt >> (2 * n)) & 3u) != ((pt >> (2 * n + 2)) & 3u)) e |= 1u << n;
    return e;
}

static constexpr u64 len_pos(bool kept) {
    u64 r = 0;
    for (int w = 0; w < 8; ++w) {
        r |= (u64)(kept ? 8 + w : w) << (4 * (((kept ? TKF : TKB) >> (4 * w)) & 15u));
        if (w >= 4 && (w >= 6) == kept) r |= (u64)(kept ? w : 4 + w) << (4 * ((TKA >> (4 * w)) & 15u));
    }
    return r;
}
static constexpr unsigned ring_const() {
    constexpr int e[4] = {EXTRA};
    unsigned r = 0;
    for (int i = 0; i < 4; ++i) r |= (unsigned)((4 - PBIAS + (-3 * e[i] - e[(i + 3) & 3] + e[(i + 2) & 3] + 3 * e[(i + 1) & 3]) / 2) & 255) << (8 * i);
    return r;
}

#define L_MISC 0
#define L_KEY 96
#define L_AT 416
#define L_LEN 1056
#define L_PCNT 1280
#define L_CCNT (L_PCNT + 2048)
#define L_BNC (L_CCNT + 1280)
#define L_CG (L_BNC + 4096)
#define L_PRV (L_CG + 1024)
#define A_STG L_PRV
#define A_PTAB (A_STG + EPB_MAX * 16)
#define L_PK (L_PRV + 15 * 4096)
#define L_DUM L_PK
#define L_CTAB (L_PK + 2048)
#define L_MRK (L_PK + 8192)
#define TR_STR 4352
#define L_TR (L_PK + 16384)
#define L_WR L_TR
#define L_IMG (L_TR + 8 * TR_STR)
#define L_TOTAL (L_IMG + 32768)
static_assert(L_TOTAL <= 163840 && L_BNC <= 8192 && NPB_MAX * LS * 16 <= 15 * 4096, "LDS budget");
static_assert(L_PRV % 16 == 0 && L_PK % 16 == 0 && L_IMG % 16 == 0 && L_CG % 16 == 0 && L_CCNT % 16 == 0, "alignment");
enum { SY_CPY = 40, SY_C1 = 44, SY_S = 48, SY_C3 = 56, SY_RI = 60, SY_PRE = 64 };

template <class T> static __device__ __forceinline__ T* arg_ptr(u32x16 a, int i) { return (T*)(__attribute__((address_space(1))) T*)((u64)a[i + 1] << 32 | a[i]); }
static __device__ __forceinline__ u32x4 mkdesc(const void* p, unsigned bytes) {
    return (u32x4){(unsigned)(uintptr_t)p, (unsigned)((uintptr_t)p >> 32) & 0xffffu, bytes, 0x00020000u};
}

#define STR2(x) #x
#define STR(x) STR2(x)
__global__ __launch_bounds__(512, 2) void k_fused(const int*, unsigned, unsigned, const int*, unsigned, unsigned, const void*, void*, const void*, const void*,
                                                  const void*, const void*, const void*, void*, int, int, int, unsigned, unsigned, unsigned, unsigned, unsigned)
{
    __shared__ __attribute__((aligned(256))) unsigned char smem[L_TOTAL];
    const int tid = threadIdx.x, bx = blockIdx.x;
    u32x16 ka, kb;
    unsigned so, cp;
    u32x3 d, sv;
    asm volatile("s_load_dwordx16 s[36:51], %3, 0x0\n\ts_load_dwordx16 s[52:67], %3, 0x40\n\ts_getpc_b64 s[68:69]\n\ts_movk_i32 s70, " STR(CODE_LINES) " * 128\n\ts_mov_b32 s71, 0x20000\n\t"
                 "s_lshl4_add_u32 %2, %4, " STR(CPN) " * -3968\n\tv_lshl_add_u32 v8, %5, 12, %2\n\tv_lshlrev_b32 v9, 7, %5\n\tv_max_i32 v8, v8, v9\n\tbuffer_load_dword v7, v8, s[68:71], 0 offen\n\t"
                 "v_mul_u32_u24 v15, 12, %5\n\ts_add_i32 %2, %4, -2\n\ts_waitcnt lgkmcnt(0)\n\ts_mul_i32 %2, %2, s62\n\ts_add_u32 s42, %2, s62\n\ts_min_u32 s38, s38, s42\n\ts_mov_b32 s42, s38\n\t"
                 "s_bitcmp1_b32 %4, 3\n\ts_cbranch_scc1 La_%=\n\tbuffer_load_dwordx3 v[8:10], v15, s[36:39], %2 offen nt\n\tLa_%=:\n\tbuffer_load_dwordx3 v[12:14], v15, s[40:43], %2 offen nt\n\t"
                 "s_cbranch_scc0 Lb_%=\n\tbuffer_load_dwordx3 v[8:10], v15, s[36:39], %2 offen nt\n\tLb_%=:"
                 : "={s[36:51]}"(ka), "={s[52:67]}"(kb), "=&s"(so) : "s"(__builtin_amdgcn_kernarg_segment_ptr()), "s"(bx), "v"(tid)
                 : "memory", "scc", "s68", "s69", "s70", "s71", "v7", "v8", "v9", "v10", "v12", "v13", "v14", "v15");
    asm volatile("" : "={v7}"(cp), "={v[8:10]}"(d), "={v[12:14]}"(sv));
    SB();
    const int* dst = arg_ptr<const int>(ka, 0);
    const int* src = arg_ptr<const int>(ka, 4);
    const u32x2* Y = arg_ptr<const u32x2>(ka, 8);
    unsigned char* ws = arg_ptr<unsigned char>(ka, 10);
    const unsigned short* x = arg_ptr<const unsigned short>(ka, 12);
    const u32x4* ef = arg_ptr<const u32x4>(ka, 14);
    const unsigned short* w_rad = arg_ptr<const unsigned short>(kb, 0);
    const unsigned short* cg = arg_ptr<const unsigned short>(kb, 2);
    const unsigned short* w_mix = arg_ptr<const unsigned short>(kb, 4);
    unsigned short* out = arg_ptr<unsigned short>(kb, 6);
    const int N = kb[8], E = kb[9], epb = kb[10] >> 2;
    const unsigned nb_magic = kb[11], smask = kb[12], cmul = kb[13], po0 = kb[14], po1 = kb[15];
    const int lane = tid & 63;
    const int w = __builtin_amdgcn_readfirstlane(tid >> 6);
    int half;
    const int b = (bx & 3) * 32 + (bx >> 3);
    const int g = lane >> 4, c16 = lane & 15;
    const int node0 = (b * N + 127) >> 7, npb = (((b + 1) * N + 127) >> 7) - node0;
    const unsigned lds0 = (unsigned)(uintptr_t)(__attribute__((address_space(3))) unsigned char*)smem;
    const __amdgpu_buffer_rsrc_t rsw = __builtin_amdgcn_make_buffer_rsrc((void*)ws, 0, W_END, 0x00020000);
    const unsigned le = tid * 3;
    const int base_e = (int)so >> 2;
    const int ne = base_e < 0 ? 0 : max(min(epb, E - base_e), 0);
    const int wd = 4 + ((w - 1 + (w >> 2)) & 3);
    auto task_at = [&](int t) -> unsigned {
        const unsigned q = (t == 0 ? TKF >> (4 * w) : (t == 1 ? TKB >> (4 * w) : TKA >> (4 * wd))) & 15u;
        return L_AT + ((t == 0 || (t == 2 && wd >= 6) ? 40 * half : 40 - 40 * half) + 4 * q) * 4;
    };
    const unsigned ri8 = (wd & 3) * 8;
    unsigned ta[3], cc, cd;
    int k0;
    auto set_ta = [&]() {
#pragma unroll
        for (int i = 0; i < 3; ++i) {
            ta[i] = lds0 + task_at(i == 2 ? 0 : ((w < 4) == (i == 0) ? 2 : 1)) + g * 4;
            asm volatile("" : "+v"(ta[i]));
        }
        cc = __builtin_rotateleft32(0xff0103fdu, ri8); cd = __builtin_rotateleft32(0xfe000204u, ri8); k0 = (int)(signed char)(ring_const() >> ri8);
        asm volatile("" : "+v"(cc), "+v"(cd), "+v"(k0));
    };
    unsigned tag;
    bool slow;
    u32x4 wr[8], cgA;
    auto load_wr = [&]() {
#pragma unroll
        for (int t = 0; t < 8; ++t) wr[t] = *(const u32x4*)(smem + L_WR + t * 1024 + lane * 16);
        cgA = *(const u32x4*)(smem + L_CG + lane * 16);
        asm volatile("" : "+v"(wr[0]), "+v"(wr[1]), "+v"(wr[2]), "+v"(wr[3]), "+v"(wr[4]), "+v"(wr[5]), "+v"(wr[6]), "+v"(wr[7]), "+v"(cgA));
    };

    __builtin_amdgcn_s_setprio(3);
    auto touch = [&](unsigned code) -> unsigned {
        if (code >= 100u)
            return __builtin_amdgcn_raw_buffer_load_b32(__builtin_amdgcn_make_buffer_rsrc(code == 100u ? (void*)x : (void*)ef, 0, code == 100u ? N * 1024 : E * 32, 0x00020000), lane << 21, bx * 128, 0);
        if (code < 8u)
            return __builtin_amdgcn_raw_buffer_load_b32(__builtin_amdgcn_make_buffer_rsrc((void*)(ef + (size_t)base_e * 2), 0, code == 0u ? 0 : ne * 32, 0x00020000), lane * 128, (code - 1u) * 8192u, 0);
        const unsigned j = code < 16u ? (code - 8u) * 128u + b : (code - 16u) * 32u + (bx >> 3);
        return __builtin_amdgcn_raw_buffer_load_b32(__builtin_amdgcn_make_buffer_rsrc((void*)x, 0, N * 1024, 0x00020000), lane * 128,
                                                    (2u * j + ((half ^ (smask >> (j >> 5))) & 1u)) * 8192u, 0);
    };
    constexpr unsigned tl[] = {TLIST};
    const __amdgpu_buffer_rsrc_t rsm = __builtin_amdgcn_make_buffer_rsrc((void*)w_mix, 0, C_DIM * C_DIM * 2, 0x00020000);
    u32x4 mv[8];
    auto load_mv = [&]() {
#pragma unroll
        for (int j = 0; j < 8; ++j)
            mv[j] = __builtin_amdgcn_raw_buffer_load_b128(rsm, g * 512 + c16 * 16 + (w & 3) * 2048 + (j & 1) * 256, (j >> 1) * 8192, 0);
        SB();
    };
    auto make_img = [&]() {
#pragma unroll
        for (int i = 0; i < 4; ++i) {
            u32x4 p0, p1;
#pragma unroll
            for (int j = 0; j < 4; ++j) {
                p0[j] = __builtin_amdgcn_perm(mv[2 * j + 1][i], mv[2 * j][i], 0x05040100u);
                p1[j] = __builtin_amdgcn_perm(mv[2 * j + 1][i], mv[2 * j][i], 0x07060302u);
            }
            const unsigned ia = L_IMG + (c16 >> 2) * 8192 + (w & 3) * 1024 + g * 256 + (c16 & 3) * 64 + i * 16;
            *(u32x4*)(smem + ia) = p0;
            *(u32x4*)(smem + ia + 4096) = p1;
        }
    };
    unsigned tlp = 0u, t0v = 0u, tv[NT2] = {};
    {
        const unsigned sh = 8u * (w & 3);
        unsigned inc[3];
        tlp = __builtin_amdgcn_raw_buffer_load_b32(__builtin_amdgcn_make_buffer_rsrc((void*)ws, 0, w == EW ? 4 : 0, 0x00020000), W_CTL, 0, 0);
        SB();
        {
            const unsigned k0 = (unsigned)tid - L_KEY / 16 < NPB_MAX / 4 ? 127u + L_KEY / 4 - 4u * tid : 0u, k1 = min(k0, 1u);
            *(u32x4*)(smem + tid * 16) = (u32x4){k0, k0 - k1, k0 - 2u * k1, k0 - 3u * k1};
        }
        *(u32x4*)(smem + L_MRK + tid * 16) = (u32x4){0u, 0u, 0u, 0u};
        SB();
        SB();
        unsigned l16 = lane * 16;
        asm volatile("" : "+v"(l16));
        u32x4 te = (u32x4){0u, 0u, 0u, 0u};
        auto table = [&]() {
            if (__builtin_expect(w == EW, 1)) {
                const unsigned tg = (unsigned)__builtin_amdgcn_readfirstlane((int)tlp) * 1664525u + 1013904223u;
                *(unsigned*)(smem + L_MISC) = tg;
#pragma unroll
                for (int i = 0; i < 2; ++i) __builtin_amdgcn_raw_buffer_store_b128((u32x4){te[i], tg, te[2 + i], tg}, rsw, W_TAB + bx * 2048 + l16 + i * 1024, 0, SC1);
            }
        };
        if (__builtin_expect(bx <= 1, 1)) {
            unsigned v;
            const unsigned ts = (unsigned)__builtin_amdgcn_s_memrealtime();
            asm volatile("s_cmp_lg_u32 %3, 0\n\t.irp o, 0, 0x400, 0x800, 0xc00, 0x1000, 0x1400, 0x1800, 0x1c00\n\ts_load_dword %0, %1, %2 offset:\\o\n\ts_cbranch_scc0 Lt_%=\n\t.endr\n\tLt_%=:\n\ts_waitcnt lgkmcnt(0)"
                         : "=&s"(v) : "s"(x), "s"((w < 4 ? po0 : po1) + (bx == 0 ? 0x10000 : (w & 3) * 8192)), "s"(bx) : "memory", "scc");
            asm volatile("s_waitcnt vmcnt(0)" : "+{v7}"(cp), "+{v[8:10]}"(d), "+{v[12:14]}"(sv));
            wg_barrier();
            table();
            t0v = __builtin_amdgcn_raw_buffer_load_b32(rsm, (tid >> 3) == 32 ? lane * 128 : OOB_OFF, 0, 0);
            if (__builtin_expect(bx * 8 + w == EW, 1)) {
                unsigned s0, s1, v0, v1;
                const unsigned tm = (unsigned)__builtin_amdgcn_s_memtime() * 0x9e3779b1u;
#define RACE(r, o, a, b) "s_load_dword %2, %4, " o " offset:" a "\n\ts_load_dword %3, %4, " o " offset:" b "\n\ts_waitcnt lgkmcnt(1)\n\ts_sleep " "1" "\n\t" \
                             "s_getreg_b32 " r ", hwreg(HW_REG_IB_STS, 8, 1)\n\ts_waitcnt lgkmcnt(0)\n\t"
                asm volatile("s_sleep " "6" "\n\t" RACE("%0", "%5", "0", "0x2000") RACE("%1", "%6", "0x2000", "0") "s_cmp_lg_u32 %0, %1\n\ts_cbranch_scc1 Lr_%=\n\t" RACE("%1", "%5", "0x6000", "0x4000") "Lr_%=:"
                             : "=&s"(s0), "=&s"(s1), "=&s"(v0), "=&s"(v1) : "s"(x), "s"(po0 + (tm >> 29) * 1024u), "s"(po1 + (tm >> 26 & 7u) * 1024u) : "memory", "scc");
                SB();
                unsigned xc;
                asm volatile("s_getreg_b32 %0, hwreg(HW_REG_XCC_ID, 0, 2)" : "=s"(xc));
                unsigned hm = (0xf0f0f0u >> (xc + 4u * s1));
                const unsigned tg = (unsigned)__builtin_amdgcn_readfirstlane((int)tlp) * 1664525u + 1013904223u;
                __builtin_amdgcn_raw_buffer_store_b128((u32x4){hm, tg, hm, tg}, rsw, lane < 8 ? W_DEC + l16 : OOB_OFF, 0, SC1);
            }
            asm volatile("" : "+v"(t0v));
            while ((unsigned)__builtin_amdgcn_s_memrealtime() - ts < IDLE) __builtin_amdgcn_s_sleep(1);
        }
        int bq = bx;
        asm volatile("" : "+s"(bq));
        if (__builtin_expect(bq > 1, 1)) {
        wg_barrier();
        const __amdgpu_buffer_rsrc_t rsy = __builtin_amdgcn_make_buffer_rsrc((void*)Y, 0, ka[2] * 2, 0x00020000);
        u32x4 ya = __builtin_amdgcn_raw_buffer_load_b128(rsy, le * 8, so * 2, NT);
        u32x2 yb = __builtin_amdgcn_raw_buffer_load_b64(rsy, le * 8 + 16, so * 2, NT);
        SB();
        {
            u64 pb = (u64)(uintptr_t)cg;
            unsigned pn = (w & 2) << 6;
            asm("s_cmp_eq_u32 %2, 3\n\ts_cselect_b64 %0, %4, %0\n\ts_cselect_b32 %1, 0x1000, %1\n\t"
                "s_cmp_eq_u32 %2, 4\n\ts_cselect_b64 %0, %5, %0\n\ts_cselect_b32 %1, 0x400, %1\n\ts_cmp_gt_u32 %2, 4\n\ts_cselect_b64 %0, %6, %0\n\ts_cselect_b32 %1, %7, %1"
                : "+s"(pb), "+s"(pn) : "s"(w), "s"(cg), "s"(w_rad), "s"(w_mix + (bx >> 3) * 512), "s"(ef + (base_e * 2 + (7 - w) * 512)), "s"(max(min(ne * 32, NT0 * 8192) - (7 - w) * 8192, 0)) : "scc");
            t0v = __builtin_amdgcn_raw_buffer_load_b32(__builtin_amdgcn_make_buffer_rsrc((void*)pb, 0, pn, 0x00020000), lane * 128, 0, 0);
        }
        SB();
#pragma unroll
        for (int r = 0; r < 3; ++r) inc[r] = (int)le + r < ne ? 1u << sh : 0u;
        asm volatile("" : "+v"(inc[0]), "+v"(inc[1]), "+v"(inc[2]));
        SB();
        unsigned bin[3], rank[3], ba[3];
        asm volatile("s_waitcnt vmcnt(4)" : "+{v7}"(cp), "+{v[8:10]}"(d), "+{v[12:14]}"(sv));
#pragma unroll
        for (int r = 0; r < 3; ++r) {
            bin[r] = __umulhi(d[r], nb_magic);
            ba[r] = ((((sv[r] >> 3) ^ (smask >> ((sv[r] >> 9) & 31u))) & 1u) * 512u + bin[r] * 4u) & 1023u;
            rank[r] = atomicAdd((unsigned*)(smem + L_PCNT + (w >> 2) * 1024 + ba[r]), inc[r]);
        }
        SB();
        unsigned rw1[3];
        auto mkrw = [&]() {
#pragma unroll
            for (int r = 0; r < 3; ++r) {
                const unsigned sl = sv[r], nl = d[r] - ((__umul24(bin[r], (unsigned)N) + 127u) >> 7);
                rw1[r] = (nl * 4u + ((__umul24(sl, cmul) >> 16) ^ ((nl >= 40u) == (ba[r] >= 512u) ? 3u : 0u))) | ((le + r) << 9);
                asm volatile("" : "+v"(rw1[r]));
            }
        };
        unsigned pa[3];
#pragma unroll
        for (int r = 0; r < 3; ++r) { pa[r] = A_PTAB + w * 512 + (ba[r] >> 1); asm volatile("" : "+v"(pa[r])); }
        unsigned m0 = below_mask(w, 0), m1 = below_mask(w, 1);
        asm volatile("" : "+s"(m0), "+s"(m1));
        wg_barrier();
        u32x2 c[4];
#pragma unroll
        for (int i = 0; i < 4; ++i) c[i] = *(const u32x2*)(smem + L_PCNT + i * 512 + lane * 8);
        SB(); mkrw(); SB();
        unsigned t[4], run[4];
#pragma unroll
        for (int i = 0; i < 4; ++i) t[i] = bsum(c[i >> 1][i & 1], 0x01010101u);
        asm volatile("" : "+v"(t[0]), "+v"(t[1]), "+v"(t[2]), "+v"(t[3]));
#pragma unroll
        for (int i = 0; i < 4; ++i) t[i] = bsum(c[2 + (i >> 1)][i & 1], 0x01010101u, t[i]);
        asm volatile("" : "+v"(t[0]), "+v"(t[1]), "+v"(t[2]), "+v"(t[3]));
        const unsigned ts = (t[0] + t[1]) | (t[2] + t[3]) << 16;
        const unsigned ti = (unsigned)wave_incl_scan((int)ts), tx = ti - ts;
        run[0] = tx & 0xffffu; run[2] = (tx >> 16) + ((unsigned)__builtin_amdgcn_readlane((int)ti, 63) & 0xffffu);
        run[1] = run[0] + t[0]; run[3] = run[2] + t[2];
        unsigned o[4];
#pragma unroll
        for (int i = 0; i < 4; ++i) o[i] = bytesum2(c[i >> 1][i & 1], c[2 + (i >> 1)][i & 1], m0, m1, run[i]);
        *(unsigned*)(smem + A_PTAB + w * 512 + lane * 4) = o[0] | (o[1] << 16);
        *(unsigned*)(smem + A_PTAB + w * 512 + 256 + lane * 4) = o[2] | (o[3] << 16);
        unsigned pos[3];
        asm volatile("" ::: "memory");
#pragma unroll
        for (int r = 0; r < 3; ++r) pos[r] = *(const unsigned short*)(smem + pa[r]);
        SB();
#pragma unroll
        for (int i = 0; i < 4; ++i) te[i] = run[i] | (t[i] << 16);
        asm volatile("" : "+v"(ya), "+v"(yb));
        table();
#pragma unroll
        for (int r = 0; r < 3; ++r) pos[r] = A_STG + (pos[r] + ((rank[r] >> sh) & 0xffu)) * 16;
        asm volatile("" : "+v"(pos[0]), "+v"(pos[1]), "+v"(pos[2]));
#pragma unroll
        for (int r = 0; r < 3; ++r) pos[r] = inc[r] != 0u ? pos[r] : L_DUM + l16;
#pragma unroll
        for (int r = 0; r < 3; ++r) *(u32x4*)(smem + pos[r]) = (u32x4){r < 2 ? ya[2 * r] : yb.x, sv[r], r < 2 ? ya[2 * r + 1] : yb.y, rw1[r]};
        }
        wg_barrier();
        const __amdgpu_buffer_rsrc_t rsr = __builtin_amdgcn_make_buffer_rsrc((void*)(ws + W_RA + bx * (EPB_MAX * 16)), 0, (ne * 16 + 127) & ~127, 0x00020000);
        u32x4 va[3];
        const unsigned tv0 = *(const unsigned*)(smem + L_MISC);
#pragma unroll
        for (int i = 0; i < 3; ++i) va[i] = *(const u32x4*)(smem + A_STG + (tid ^ 256) * 16 + 8192 * i);
#pragma unroll
        for (int i = 0; i < 3; ++i) __builtin_amdgcn_raw_buffer_store_b128((u32x4){va[i].x, va[i].y | tv0 << 14, va[i].z, va[i].w | (tv0 & 0xfff00000u)}, rsr, (tid ^ 256) * 16, 8192 * i, SC1);
        tag = (unsigned)__builtin_amdgcn_readfirstlane((int)tv0);
        lds_arrive(lds0 + SY_CPY);
    }
    u32x4 dx = mkdesc(x, (unsigned)N * 1024u), de = mkdesc(ef, (unsigned)E * 32u);
    asm volatile("" : "+s"(dx), "+s"(de));
    if (w < 4) {
        asm volatile("" :: "v"(t0v), "v"(tlp));
        const unsigned sh = 8u * w, mb = 0x01010101u & ((1u << sh) - 1u);
        const unsigned T0 = tag << 14, T1 = tag & 0xfff00000u;
        const unsigned to = W_TAB + tid * 2048 + (b & 1) * 1024 + (b >> 1) * 16;
        __builtin_amdgcn_s_sleep(4);
        u32x4 en = __builtin_amdgcn_raw_buffer_load_b128(rsw, to, 0, SC1);
        u32x2 dec = __builtin_amdgcn_raw_buffer_load_b64(rsw, W_DEC, 0, SC1);
#pragma unroll 1
        for (unsigned spin = 0; spin < SPIN_MAX; ++spin) {
            if (__builtin_amdgcn_ballot_w64(((en.y ^ tag) | (en.w ^ tag) | (dec.y ^ tag)) != 0u) == 0ull) break;
            __builtin_amdgcn_s_sleep(2);
            asm volatile("" ::: "memory");
            en = __builtin_amdgcn_raw_buffer_load_b128(rsw, to, 0, SC1);
            dec = __builtin_amdgcn_raw_buffer_load_b64(rsw, W_DEC, 0, SC1);
        }
        half = (__builtin_amdgcn_readfirstlane((int)dec.x) >> (bx & 7)) & 1;
        if (half) en.x = en.z;
        const unsigned rc = en.x >> 16;
        const unsigned incl = (unsigned)wave_incl_scan((int)rc);
        const int twf = __builtin_amdgcn_readlane((int)incl, 63), tw = twf <= 64 * NRND ? twf : 0;
        const bool more = tw > 64 * (NRND - 1);
        unsigned ma = L_MRK + w * (256 * NRND) + (incl - rc) * 4;
        asm volatile("" : "+v"(ma));
        *(unsigned*)(smem + (((rc != 0u) & (incl <= 64u * NRND)) ? ma : L_DUM + lane * 4)) = (unsigned)lane * EPB_MAX + (en.x & 0xffffu) - (incl - rc);
        unsigned ra[NRND];
        asm volatile("" ::: "memory");
#pragma unroll
        for (int r = 0; r < NRND; ++r) ra[r] = *(const unsigned*)(smem + L_MRK + w * (256 * NRND) + r * 256 + lane * 4);
        unsigned carry = 0u;
        u32x4 A[NRND];
#pragma unroll
        for (int r = 0; r < NRND; ++r) {
            if (r == NRND - 1) { if (more) ra[r] = wave_max_scan(ra[r]); asm volatile("" : "+v"(ra[r])); }
            else if (r % 3 == 0) wave_max_scan3(ra[r], ra[r + 1], ra[r + 2]);
            const unsigned m = max(ra[r], carry);
            carry = max(carry, (unsigned)__builtin_amdgcn_readlane((int)ra[r], 63));
            ra[r] = r * 64 + lane < tw ? (m + (unsigned)(r * 64 + lane)) * 16u : OOB_OFF >> 1;
            A[r] = __builtin_amdgcn_raw_buffer_load_b128(rsw, ra[r], W_RA + 64 * w * (EPB_MAX * 16), SC1);
        }
        SB();
        lds_arrive(lds0 + SY_RI, 16u + half);
        unsigned pe[NRND], inc[NRND], inck[NRND], lim[NRND];
#pragma unroll
        for (int r = 0; r < NRND; ++r) {
            pe[r] = __umul24(__umulhi(ra[r], 209716u) + 64u * w - 2u, (unsigned)epb * 32u);
            inc[r] = ra[r] < 0x40000000u ? 1u << sh : 0u;
            inck[r] = ra[r] < 0x40000000u ? 128u : 0u;
            lim[r] = ra[r] < 0x40000000u ? 16383u : ~0u;
            asm volatile("" : "+v"(pe[r]), "+v"(inc[r]), "+v"(inck[r]), "+v"(lim[r]));
        }
        if (twf > 64 * NRND) lds_arrive(lds0 + SY_S, 256u);
        set_ta();
        SB();
        unsigned rank[NRND], nc[NRND];
        auto slot = [&](int r) {
            nc[r] = inc[r] != 0u ? L_CTAB + w * 640 + nc[r] * 2 : L_MISC + 8;
            A[r] = (u32x4){A[r].y << 10, ((A[r].w >> 4) & 0xffe0u) + pe[r], A[r].x, A[r].z};
            asm volatile("" : "+v"(nc[r]), "+v"(A[r]));
            SB();
        };
#pragma unroll
        for (int r = 0; r < NRND; ++r) {
            if (r == NRND - 1 && !more) { rank[r] = 0u; nc[r] = 0u; continue; }
            A[r].y ^= T0; A[r].w ^= T1;
            if (__builtin_expect(__builtin_amdgcn_ballot_w64((A[r].y | (A[r].w >> 6)) > lim[r]) != 0ull, 0)) {
#pragma unroll 1
                for (unsigned spin = 0; spin < SPIN_MAX; ++spin) {
                    asm volatile("" ::: "memory");
                    A[r] = __builtin_amdgcn_raw_buffer_load_b128(rsw, ra[r], W_RA + 64 * w * (EPB_MAX * 16), SC1);
                    A[r].y ^= T0; A[r].w ^= T1;
                    if (__builtin_amdgcn_ballot_w64((A[r].y | (A[r].w >> 6)) > lim[r]) == 0ull) break;
                }
            }
            nc[r] = (r < 4 || inc[r] != 0u) ? A[r].w & 0x1ffu : (unsigned)lane;
            rank[r] = atomicAdd((unsigned*)(smem + L_CCNT + nc[r] * 4), inc[r]);
            atomicAdd((unsigned*)(smem + L_KEY + (nc[r] & 0x1fcu)), inck[r]);
            SB();
            if (r < 3) slot(r);
        }
        lds_arrive(lds0 + SY_C1);
        unsigned d0 = lane * LS, d1 = (64 + c16) * LS, ca0 = L_CTAB + w * 640 + lane * 8, ca1 = L_CTAB + w * 640 + (64 + c16) * 8;
        asm volatile("" : "+v"(d0), "+v"(d1), "+v"(ca0), "+v"(ca1));
#pragma unroll
        for (int r = 3; r < NRND; ++r) slot(r);
#pragma unroll
        for (int r = 0; r < NRND; ++r) {
            rank[r] = inc[r] != 0u ? (rank[r] >> sh) & 0xffu : (L_DUM - L_PRV) / 16 + lane;
            asm volatile("" : "+v"(rank[r]));
        }
        lds_wait(lds0 + SY_PRE, 4);
        {
            u32x4 c0, c1;
            unsigned cn;
            do {
                asm volatile("ds_read_b32 %0, %3 offset:%c6\n\tds_read_b128 %1, %4 offset:%c7\n\tds_read_b128 %2, %5 offset:%c8\n\ts_waitcnt lgkmcnt(0)"
                             : "=&v"(cn), "=&v"(c0), "=&v"(c1) : "v"(lds0), "v"(lds0 + lane * 16), "v"(lds0 + c16 * 16), "i"(SY_C1), "i"(L_CCNT), "i"(L_CCNT + 1024) : "memory");
            } while (__builtin_amdgcn_readfirstlane((int)cn) < 4);
            unsigned p0[4], p1[4];
#pragma unroll
            for (int c = 0; c < 4; ++c) {
                p0[c] = bsum(c0[c], mb, d0); p1[c] = bsum(c1[c], mb, d1);
                if (c < 3) { d0 = bsum(c0[c], 0x01010101u, d0); d1 = bsum(c1[c], 0x01010101u, d1); }
            }
            *(u32x2*)(smem + ca0) = (u32x2){p0[0] | (p0[1] << 16), p0[2] | (p0[3] << 16)};
            *(u32x2*)(smem + ca1) = (u32x2){p1[0] | (p1[1] << 16), p1[2] | (p1[3] << 16)};
        }
        asm volatile("" ::: "memory");
#pragma unroll
        for (int r = 0; r < NRND; ++r) nc[r] = *(const unsigned short*)(smem + nc[r]);
        SB();
#pragma unroll
        for (int r = 0; r < NRND; ++r) {
            if (r == NRND - 1 && !more) continue;
            *(u32x4*)(smem + L_PRV + (nc[r] + rank[r]) * 16) = A[r];
        }
        load_wr();
        lds_arrive(lds0 + SY_C3);
    } else {
        const int q = w - 4, t2 = tid - 256;
        __builtin_amdgcn_s_setprio(2);
        unsigned wv[8], cv[4];
        {
            const __amdgpu_buffer_rsrc_t rsa = __builtin_amdgcn_make_buffer_rsrc((void*)w_rad, 0, DE_DIM * C_DIM * 2, 0x00020000);
            const __amdgpu_buffer_rsrc_t rsg = __builtin_amdgcn_make_buffer_rsrc((void*)cg, 0, 128, 0x00020000);
#pragma unroll
            for (int j = 0; j < 8; ++j) wv[j] = __builtin_amdgcn_raw_buffer_load_b32(rsa, (g & 1) * 2048 + 64 * q + 4 * c16 + 256 * j, 0, 0);
#pragma unroll
            for (int f = 0; f < 4; ++f) cv[f] = __builtin_amdgcn_raw_buffer_load_b32(rsg, (lane & 3) * 32 + (lane & 8u) / 2 + 8 * f, 0, 0);
            SB();
        }
        lds_wait(lds0 + SY_CPY, 8);
#pragma unroll
        for (int i = 0; i < 15; ++i) *(u32x4*)(smem + L_PRV + (t2 + 256 * i) * 16) = (u32x4){OOB_OFF, OOB_OFF, 0u, 0u};
        SB();
        asm volatile("" :: "v"(t0v), "v"(tlp));
        load_mv();
        {
            float f0[8], f1[8];
#pragma unroll
            for (int j = 0; j < 8; ++j) {
                const float a = __uint_as_float(wv[j] << 16) * -1.44269504088896340736f, c = __uint_as_float(wv[j] & 0xffff0000u) * -1.44269504088896340736f;
                const unsigned hp = g < 2 ? 0u : pack2bf(a, c);
                f0[j] = a - __uint_as_float(hp << 16);
                f1[j] = c - __uint_as_float(hp & 0xffff0000u);
            }
            *(u32x4*)(smem + L_WR + (2 * q) * 1024 + lane * 16) = (u32x4){pack2bf(f0[0], f0[1]), pack2bf(f0[2], f0[3]), pack2bf(f0[4], f0[5]), pack2bf(f0[6], f0[7])};
            *(u32x4*)(smem + L_WR + (2 * q + 1) * 1024 + lane * 16) = (u32x4){pack2bf(f1[0], f1[1]), pack2bf(f1[2], f1[3]), pack2bf(f1[4], f1[5]), pack2bf(f1[6], f1[7])};
            float a[4];
#pragma unroll
            for (int f = 0; f < 4; ++f) a[f] = __uint_as_float((lane & 4) ? cv[f] & 0xffff0000u : cv[f] << 16) * -0.69314718055994530942f;
            const unsigned h0 = pack2bf(a[0], a[1]), h1 = pack2bf(a[2], a[3]);
            *(u32x4*)(smem + L_CG + lane * 16) = (u32x4){h0, h1, pack2bf(a[0] - __uint_as_float(h0 << 16), a[1] - __uint_as_float(h0 & 0xffff0000u)),
                                                        pack2bf(a[2] - __uint_as_float(h1 << 16), a[3] - __uint_as_float(h1 & 0xffff0000u))};
        }
        lds_arrive(lds0 + SY_PRE);
        make_img();
        lds_wait(lds0 + SY_PRE, 4);
        load_wr();
        if (w == 7) {
            lds_arrive(lds0 + SY_C3);
            half = (lds_wait<1>(lds0 + SY_RI, 64) & 15u) != 0u;
#pragma unroll
            for (int i = 0; i < NT2; ++i) tv[i] = touch(tl[i]);
            SB();
            set_ta();
        } else {
            half = (lds_wait<1>(lds0 + SY_RI, 16) & 15u) != 0u;
            set_ta();
            const int n5 = min(27 * q + (lane >> 1), NPB_MAX - 1);
            const bool kp = (n5 >= 40) == (half != 0);
            u64 lpos = kp ? len_pos(true) : len_pos(false);
            unsigned an = lds0 + n5 * 4, ak = lds0 + L_KEY + (n5 >= 40 ? 160 : 0) + (lane & 1) * 80;
            unsigned aa = L_AT + (n5 >= 40 ? 160 : 0), al = L_LEN + (kp ? 4 : 0), av = (unsigned)n5 | (lds0 + L_PRV + n5 * (LS * 16)) << 8;
            asm volatile("" : "+v"(an), "+v"(ak), "+v"(aa), "+v"(al), "+v"(av), "+v"(lpos));
            lds_wait<1>(lds0 + SY_C1, 3);
            __builtin_amdgcn_s_setprio(3);
            unsigned kn, cn;
            u32x4 dk[5];
            do {
                asm volatile("ds_read_b32 %0, %7 offset:%c10\n\tds_read_b32 %1, %8 offset:%c11\n\tds_read_b128 %2, %9\n\tds_read_b128 %3, %9 offset:16\n\tds_read_b128 %4, %9 offset:32\n\t"
                             "ds_read_b128 %5, %9 offset:48\n\tds_read_b128 %6, %9 offset:64\n\ts_waitcnt lgkmcnt(0)"
                             : "=&v"(cn), "=&v"(kn), "=&v"(dk[0]), "=&v"(dk[1]), "=&v"(dk[2]), "=&v"(dk[3]), "=&v"(dk[4]) : "v"(lds0), "v"(an), "v"(ak), "i"(SY_C1), "i"(L_KEY) : "memory");
            } while (__builtin_amdgcn_readfirstlane((int)cn) < 4);
            unsigned sg[4] = {0u, 0u, 0u, 0u};
#pragma unroll
            for (int k = 0; k < 20; ++k) sg[k & 3] = __builtin_amdgcn_alignbit(sg[k & 3], kn - dk[k >> 2][k & 3], 31);
            int c = __builtin_popcount(sg[0] | sg[1] << 8 | sg[2] << 16 | sg[3] << 24);
            c += __builtin_amdgcn_update_dpp(0, c, 0xB1, 0xf, 0xf, false);
            *(unsigned*)(smem + aa + c * 4) = av;
            smem[(c & 3) == 0 ? al + ((unsigned)(lpos >> c) & 15u) : L_LEN + 20] = (unsigned char)max(kn >> 7, c < 8 ? 12u : 8u);
            lds_arrive(lds0 + SY_S, __builtin_amdgcn_ballot_w64((kn >> 7) > DEG_MAX) != 0ull ? 257u : 1u);
        }
        __builtin_amdgcn_s_setprio(3);
    }
    unsigned ax[3], lh, cn;
    u32x4 ln;
    {
        if (w == 7) lds_wait<2, 255>(lds0 + SY_S, 1);
        do {
            asm volatile("ds_read_b32 %0, %6 offset:%c10\n\tds_read_b128 %1, %6 offset:%c11\n\tds_read_b32 %2, %6 offset:%c12\n\tds_read_b32 %3, %7\n\tds_read_b32 %4, %8\n\t"
                         "ds_read_b32 %5, %9\n\ts_waitcnt lgkmcnt(0)"
                         : "=&v"(cn), "=&v"(ln), "=&v"(lh), "=&v"(ax[0]), "=&v"(ax[1]), "=&v"(ax[2])
                         : "v"(lds0), "v"(ta[0]), "v"(ta[1]), "v"(ta[2]), "i"(SY_S), "i"(L_LEN), "i"(L_LEN + 16) : "memory");
            cn = (unsigned)__builtin_amdgcn_readfirstlane((int)cn);
        } while ((cn & 255u) < 3u);
        slow = cn >= 256u;
    }
    const int p8 = __builtin_amdgcn_sdot4((int)(ln.x + ln.y), (int)cc, __builtin_amdgcn_sdot4((int)(ln.w + lh), (int)cc,
                   __builtin_amdgcn_sdot4((int)ln.z, (int)cd, k0, false), false), false);
    const int ld = (__builtin_amdgcn_readfirstlane((int)ln.z) >> ri8) & 255, p2 = min(max(__builtin_amdgcn_readfirstlane(p8) >> 3, 4), ld - 8);
    const int l2 = w < 4 ? ld - p2 : p2, od = (w < 4) == (wd >= 6) ? ld - l2 : 0;
    const unsigned aX = (ax[0] >> 8) + ((w < 4 ? od : 0) + (c16 & 3)) * 16, aF = (ax[0] >> 8) + ((w < 4 ? od : 0) + (c16 >> 2)) * 16 + 4;
    const unsigned lane_off = (unsigned)c16 * 16u;
    const unsigned aWw = L_BNC + w * 512 + lane * 8;
    const unsigned aWr = L_BNC + w * 512 + g * 128 + (c16 & 3) * 32 + (c16 >> 2) * 8;
    auto row_of = [=](unsigned at) -> unsigned {
        const unsigned nl = at & 0xffu;
        return nl < (unsigned)npb ? ((unsigned)node0 + nl) * 1024u + lane_off : OOB_OFF;
    };
#pragma unroll
    for (int i = 0; i < NT2; ++i) asm volatile("" :: "v"(tv[i]));
    if (!slow) {
        u32x32 oxa, oxb;
        u32x4 S;
        u32x2 ef0, efN;
        unsigned tmp;
        __builtin_amdgcn_s_waitcnt(0x0F70);
        asm volatile(
            "Lw2_%=:\n"
            "ds_read_b32 v220, v219\n"
            "ds_read2_b32 v[232:233], v211 offset1:16\n"
            "ds_read_b128 v[196:199], v210\n"
            "s_waitcnt lgkmcnt(1)\n"
            "v_readfirstlane_b32 %[tmp], v220\n"
            "s_cmp_lt_u32 %[tmp], 5\n"
            "s_cbranch_scc1 Lw2_%=\n"
            "v_or_b32 v232, v232, v229\n"
            "v_or_b32 v233, v233, v229\n"
            "buffer_load_dwordx2 v[238:239], v232, %[rse], 0 offen\n"
            "s_waitcnt lgkmcnt(0)\n"
            "v_add_u32_dpp v220, v196, v214 quad_perm:[0,0,0,0] row_mask:0xf bank_mask:0xf\n"
            "v_add_u32_dpp v221, v196, v214 quad_perm:[1,1,1,1] row_mask:0xf bank_mask:0xf\n"
            "v_add_u32_dpp v222, v196, v214 quad_perm:[2,2,2,2] row_mask:0xf bank_mask:0xf\n"
            "v_add_u32_dpp v223, v196, v214 quad_perm:[3,3,3,3] row_mask:0xf bank_mask:0xf\n"
            "buffer_load_dwordx4 v[32:35], v220, %[rsx], 0 offen\n"
            "buffer_load_dwordx4 v[36:39], v220, %[rsx], 0 offen offset:256\n"
            "buffer_load_dwordx4 v[48:51], v221, %[rsx], 0 offen\n"
            "buffer_load_dwordx4 v[52:55], v221, %[rsx], 0 offen offset:256\n"
            "buffer_load_dwordx2 v[232:233], v233, %[rse], 0 offen\n"
            "buffer_load_dwordx4 v[64:67], v222, %[rsx], 0 offen\n"
            "buffer_load_dwordx4 v[68:71], v222, %[rsx], 0 offen offset:256\n"
            "buffer_load_dwordx4 v[80:83], v223, %[rsx], 0 offen\n"
            "buffer_load_dwordx4 v[84:87], v223, %[rsx], 0 offen offset:256\n"
            "buffer_load_dwordx4 v[40:43], v220, %[rsx], 0 offen offset:512\n"
            "buffer_load_dwordx4 v[44:47], v220, %[rsx], 0 offen offset:768\n"
            "buffer_load_dwordx4 v[56:59], v221, %[rsx], 0 offen offset:512\n"
            "buffer_load_dwordx4 v[60:63], v221, %[rsx], 0 offen offset:768\n"
            "buffer_load_dwordx4 v[72:75], v222, %[rsx], 0 offen offset:512\n"
            "buffer_load_dwordx4 v[76:79], v222, %[rsx], 0 offen offset:768\n"
            "buffer_load_dwordx4 v[88:91], v223, %[rsx], 0 offen offset:512\n"
            "buffer_load_dwordx4 v[92:95], v223, %[rsx], 0 offen offset:768\n"
            : "={v[32:63]}"(oxa), "={v[64:95]}"(oxb), "={v[238:239]}"(ef0), "={v[196:199]}"(S), "={v[232:233]}"(efN), [tmp] "=&s"(tmp)
            : [rse] "s"(de), [rsx] "s"(dx), [ax] "{v210}"(aX), [af] "{v211}"(aF), [lo] "{v214}"(lane_off), [eo] "{v229}"((lane & 3) * 8), [sy] "{v219}"(lds0 + SY_C3)
            : "memory", "scc", "v220", "v221", "v222", "v223");
        if (bx == 0 && w == 4) flag_store(ws + W_CTL, tag);
        const int lf = (__builtin_amdgcn_readfirstlane((int)(w < 4 ? ln.w : lh)) >> (8 * (w & 3))) & 255, l1 = (__builtin_amdgcn_readfirstlane((int)(w < 4 ? ln.x : ln.y)) >> (8 * (w & 3))) & 255;
        const int e2 = lf + l1 + l2;
        int nst = (e2 + 3) >> 2;
        const unsigned r2 = row_of(ax[1]);
        u32x32 owr;
        u32x16 omi;
        u32x8 ot;
#pragma unroll
        for (int i = 0; i < 32; ++i) owr[i] = wr[i >> 2][i & 3];
        const int pw = (w + (w >> 2)) & 3;
        const unsigned trb = lds0 + L_TR + w * TR_STR;
        omi[2] = aX + 64; omi[3] = aF + 64;
        omi[4] = lds0 + aWw; omi[5] = lds0 + aWr; omi[6] = lane_off; omi[7] = W_AGG + row_of(ax[0]); omi[8] = W_AGG + r2;
        omi[9] = row_of(ax[2]); omi[10] = w >= 6 ? r2 : OOB_OFF; omi[15] = ~tag;
        ot[0] = (lane & 3) == 0 ? 0x3f80u : ((lane & 3) == 1 ? 0x3f800000u : 0u); ot[1] = (lane & 3) == 2 ? 0x3f80u : ((lane & 3) == 3 ? 0x3f800000u : 0u);
        ot[2] = trb + g * (4 * 272) + c16 * 16; ot[3] = trb + c16 * 272 + g * 16; ot[4] = lds0 + L_IMG + lane * 16; ot[5] = (lane & 3) * 8; ot[7] = lds0 + L_PK + pw * 4096 + lane * 16;
        int rem = w < 4 ? l2 : l1, lens = ((w < 4 ? l1 : l2) - 4) | 0xff00, fm = w < 4 ? 0x802 : (w < 6 ? 0x810 : 0xb0), pb = 0;
        const int j1 = (w < 4 ? -od - l2 : od - l1) * 16, j2 = (w < 4 ? -l1 : -od - l2) * 16;
        u32x2 jmp = (u32x2){(ax[1] >> 8) - (ax[0] >> 8) + j1, (ax[2] >> 8) - (ax[1] >> 8) + j2};
        unsigned bpa = c16 * 16 + (g & 1) * 8;
        unsigned evt = (unsigned)__builtin_amdgcn_readfirstlane(w < 4 ? evt_of(PTO) : (w < 6 ? evt_of(PTY) : evt_of(PTZ))) | 6u << (nst - (rem >> 2)) | 6u << (nst - ((l1 + l2) >> 2));
        u64 ra;
        asm volatile(
            "s_nop 1\n"
            "v_mfma_f32_4x4x4_16b_bf16 v[204:207], v[202:203], v[198:199], 0\n"
            "v_mov_b64 v[0:1], 0\n"
            "v_mov_b64 v[2:3], 0\n"
            "v_mov_b64 v[4:5], 0\n"
            "v_mov_b64 v[6:7], 0\n"
            "v_mfma_f32_4x4x4_16b_bf16 v[204:207], v[200:201], v[198:199], v[204:207]\n"
            "v_mov_b64 v[8:9], 0\n"
            "v_mov_b64 v[10:11], 0\n"
            "v_mov_b64 v[12:13], 0\n"
            "v_mov_b64 v[14:15], 0\n"
            "v_mov_b64 v[16:17], 0\n"
            "v_mov_b64 v[18:19], 0\n"
            "v_mov_b64 v[20:21], 0\n"
            "v_mov_b64 v[22:23], 0\n"
            "v_mov_b64 v[24:25], 0\n"
            "v_mov_b64 v[26:27], 0\n"
            "v_mov_b64 v[28:29], 0\n"
            "v_mov_b64 v[30:31], 0\n"
            "v_cvt_pk_bf16_f32 v204, v204, v205\n"
            "v_cvt_pk_bf16_f32 v205, v206, v207\n"
            "ds_write_b64 v212, v[204:205]\n"
            "ds_read_b128 v[196:199], v210\n"
            "ds_read_b64 v[208:209], v213\n"
            "s_waitcnt vmcnt(17)\n"
            "ds_bpermute_b32 v192, v234, v238\n"
            "ds_bpermute_b32 v193, v234, v239\n"
            "ds_bpermute_b32 v194, v234, v238 offset:4\n"
            "ds_bpermute_b32 v195, v234, v239 offset:4\n"
            "s_waitcnt lgkmcnt(0)\n"
            "v_mfma_f32_16x16x32_bf16 v[176:179], v[192:195], v[96:99], 0\n"
            "v_mfma_f32_16x16x32_bf16 v[180:183], v[192:195], v[100:103], 0\n"
            "v_mfma_f32_16x16x32_bf16 v[184:187], v[192:195], v[104:107], 0\n"
            "v_mfma_f32_16x16x32_bf16 v[188:191], v[192:195], v[108:111], 0\n"
            "v_mfma_f32_16x16x32_bf16 v[128:131], v[192:195], v[112:115], 0\n"
            "v_mfma_f32_16x16x32_bf16 v[132:135], v[192:195], v[116:119], 0\n"
            "s_nop 2\n"
            "v_exp_f32 v144, v176\n"
            "v_exp_f32 v145, v177\n"
            "v_exp_f32 v146, v178\n"
            "v_exp_f32 v147, v179\n"
            "v_exp_f32 v148, v180\n"
            "v_exp_f32 v149, v181\n"
            "v_exp_f32 v150, v182\n"
            "v_exp_f32 v151, v183\n"
            "v_exp_f32 v152, v184\n"
            "v_exp_f32 v153, v185\n"
            "v_exp_f32 v154, v186\n"
            "v_exp_f32 v155, v187\n"
            "v_exp_f32 v156, v188\n"
            "v_exp_f32 v157, v189\n"
            "v_exp_f32 v158, v190\n"
            "v_exp_f32 v159, v191\n"
            "v_pk_add_f32 v[144:145], v[144:145], 1.0 op_sel_hi:[1,0]\n"
            "v_pk_add_f32 v[146:147], v[146:147], 1.0 op_sel_hi:[1,0]\n"
            "v_pk_add_f32 v[148:149], v[148:149], 1.0 op_sel_hi:[1,0]\n"
            "v_pk_add_f32 v[150:151], v[150:151], 1.0 op_sel_hi:[1,0]\n"
            "v_pk_add_f32 v[152:153], v[152:153], 1.0 op_sel_hi:[1,0]\n"
            "v_pk_add_f32 v[154:155], v[154:155], 1.0 op_sel_hi:[1,0]\n"
            "v_pk_add_f32 v[156:157], v[156:157], 1.0 op_sel_hi:[1,0]\n"
            "v_pk_add_f32 v[158:159], v[158:159], 1.0 op_sel_hi:[1,0]\n"
            "v_rcp_f32 v144, v144\n"
            "v_rcp_f32 v145, v145\n"
            "v_rcp_f32 v146, v146\n"
            "v_rcp_f32 v147, v147\n"
            "v_rcp_f32 v148, v148\n"
            "v_rcp_f32 v149, v149\n"
            "v_rcp_f32 v150, v150\n"
            "v_rcp_f32 v151, v151\n"
            "v_rcp_f32 v152, v152\n"
            "v_rcp_f32 v153, v153\n"
            "v_rcp_f32 v154, v154\n"
            "v_rcp_f32 v155, v155\n"
            "v_rcp_f32 v156, v156\n"
            "v_rcp_f32 v157, v157\n"
            "v_rcp_f32 v158, v158\n"
            "v_rcp_f32 v159, v159\n"
            "v_pk_mul_f32 v[144:145], v[144:145], v[176:177]\n"
            "v_pk_mul_f32 v[146:147], v[146:147], v[178:179]\n"
            "v_pk_mul_f32 v[148:149], v[148:149], v[180:181]\n"
            "v_pk_mul_f32 v[150:151], v[150:151], v[182:183]\n"
            "v_pk_mul_f32 v[152:153], v[152:153], v[184:185]\n"
            "v_pk_mul_f32 v[154:155], v[154:155], v[186:187]\n"
            "v_pk_mul_f32 v[156:157], v[156:157], v[188:189]\n"
            "v_pk_mul_f32 v[158:159], v[158:159], v[190:191]\n"
            "s_branch Ldisp_%=\n"
            "Lfast_%=:\n"
            "v_mfma_f32_16x16x32_bf16 v[136:139], v[192:195], v[120:123], 0\n"
            "v_exp_f32 v160, v128\n"
            "v_add_u32 v211, 64, v211\n"
            "v_exp_f32 v161, v129\n"
            "s_waitcnt vmcnt(13) lgkmcnt(0)\n"
            "v_mfma_f32_4x4x4_16b_bf16 v[176:179], v[208:209], v[32:33], 0 cbsz:2\n"
            "v_exp_f32 v162, v130\n"
            "v_mfma_f32_4x4x4_16b_bf16 v[180:183], v[208:209], v[34:35], 0 cbsz:2\n"
            "v_exp_f32 v163, v131\n"
            "v_mfma_f32_4x4x4_16b_bf16 v[184:187], v[208:209], v[36:37], 0 cbsz:2\n"
            "v_exp_f32 v164, v132\n"
            "v_mfma_f32_4x4x4_16b_bf16 v[188:191], v[208:209], v[38:39], 0 cbsz:2\n"
            "v_exp_f32 v165, v133\n"
            "v_add_u32_dpp v219, v196, v214 quad_perm:[0,0,0,0] row_mask:0xf bank_mask:0xf\n"
            "v_mfma_f32_4x4x4_16b_bf16 v[204:207], v[202:203], v[198:199], 0\n"
            "v_exp_f32 v166, v134\n"
            "buffer_load_dwordx4 v[32:35], v219, %[rsx], 0 offen\n"
            "buffer_load_dwordx4 v[36:39], v219, %[rsx], 0 offen offset:256\n"
            "v_exp_f32 v167, v135\n"
            "v_pk_fma_f32 v[0:1], v[144:145], v[176:177], v[0:1] op_sel_hi:[0,1,1]\n"
            "v_pk_fma_f32 v[2:3], v[144:145], v[178:179], v[2:3] op_sel_hi:[0,1,1]\n"
            "v_pk_fma_f32 v[4:5], v[148:149], v[180:181], v[4:5] op_sel_hi:[0,1,1]\n"
            "v_pk_fma_f32 v[6:7], v[148:149], v[182:183], v[6:7] op_sel_hi:[0,1,1]\n"
            "v_pk_fma_f32 v[8:9], v[152:153], v[184:185], v[8:9] op_sel_hi:[0,1,1]\n"
            "v_pk_fma_f32 v[10:11], v[152:153], v[186:187], v[10:11] op_sel_hi:[0,1,1]\n"
            "v_pk_fma_f32 v[12:13], v[156:157], v[188:189], v[12:13] op_sel_hi:[0,1,1]\n"
            "v_pk_fma_f32 v[14:15], v[156:157], v[190:191], v[14:15] op_sel_hi:[0,1,1]\n"
            "v_pk_add_f32 v[160:161], v[160:161], 1.0 op_sel_hi:[1,0]\n"
            "v_pk_add_f32 v[162:163], v[162:163], 1.0 op_sel_hi:[1,0]\n"
            "v_pk_add_f32 v[164:165], v[164:165], 1.0 op_sel_hi:[1,0]\n"
            "v_pk_add_f32 v[166:167], v[166:167], 1.0 op_sel_hi:[1,0]\n"
            "v_mfma_f32_16x16x32_bf16 v[140:143], v[192:195], v[124:127], 0\n"
            "ds_read_b32 v230, v211\n"
            "s_waitcnt vmcnt(14)\n"
            "ds_bpermute_b32 v192, v234, v232\n"
            "ds_bpermute_b32 v193, v234, v233\n"
            "ds_bpermute_b32 v194, v234, v232 offset:4\n"
            "ds_bpermute_b32 v195, v234, v233 offset:4\n"
            "v_rcp_f32 v160, v160\n"
            "v_rcp_f32 v161, v161\n"
            "v_mfma_f32_4x4x4_16b_bf16 v[176:179], v[208:209], v[48:49], 0 cbsz:2 abid:1\n"
            "v_rcp_f32 v162, v162\n"
            "v_mfma_f32_4x4x4_16b_bf16 v[180:183], v[208:209], v[50:51], 0 cbsz:2 abid:1\n"
            "v_rcp_f32 v163, v163\n"
            "v_mfma_f32_4x4x4_16b_bf16 v[184:187], v[208:209], v[52:53], 0 cbsz:2 abid:1\n"
            "v_rcp_f32 v164, v164\n"
            "v_mfma_f32_4x4x4_16b_bf16 v[188:191], v[208:209], v[54:55], 0 cbsz:2 abid:1\n"
            "v_rcp_f32 v165, v165\n"
            "v_add_u32_dpp v220, v196, v214 quad_perm:[1,1,1,1] row_mask:0xf bank_mask:0xf\n"
            "v_mfma_f32_4x4x4_16b_bf16 v[204:207], v[200:201], v[198:199], v[204:207]\n"
            "v_rcp_f32 v166, v166\n"
            "buffer_load_dwordx4 v[48:51], v220, %[rsx], 0 offen\n"
            "buffer_load_dwordx4 v[52:55], v220, %[rsx], 0 offen offset:256\n"
            "v_rcp_f32 v167, v167\n"
            "v_pk_fma_f32 v[0:1], v[144:145], v[176:177], v[0:1] op_sel:[1,0,0]\n"
            "v_pk_fma_f32 v[2:3], v[144:145], v[178:179], v[2:3] op_sel:[1,0,0]\n"
            "v_pk_fma_f32 v[4:5], v[148:149], v[180:181], v[4:5] op_sel:[1,0,0]\n"
            "v_pk_fma_f32 v[6:7], v[148:149], v[182:183], v[6:7] op_sel:[1,0,0]\n"
            "v_pk_fma_f32 v[8:9], v[152:153], v[184:185], v[8:9] op_sel:[1,0,0]\n"
            "v_pk_fma_f32 v[10:11], v[152:153], v[186:187], v[10:11] op_sel:[1,0,0]\n"
            "v_pk_fma_f32 v[12:13], v[156:157], v[188:189], v[12:13] op_sel:[1,0,0]\n"
            "v_pk_fma_f32 v[14:15], v[156:157], v[190:191], v[14:15] op_sel:[1,0,0]\n"
            "v_pk_mul_f32 v[160:161], v[160:161], v[128:129]\n"
            "v_pk_mul_f32 v[162:163], v[162:163], v[130:131]\n"
            "v_pk_mul_f32 v[164:165], v[164:165], v[132:133]\n"
            "v_pk_mul_f32 v[166:167], v[166:167], v[134:135]\n"
            "s_waitcnt vmcnt(12) lgkmcnt(0)\n"
            "v_or_b32 v230, v230, v229\n"
            "buffer_load_dwordx2 v[232:233], v230, %[rse], 0 offen\n"
            "v_mfma_f32_16x16x32_bf16 v[128:131], v[192:195], v[96:99], 0\n"
            "v_exp_f32 v168, v136\n"
            "v_exp_f32 v169, v137\n"
            "v_mfma_f32_4x4x4_16b_bf16 v[176:179], v[208:209], v[64:65], 0 cbsz:2 abid:2\n"
            "v_exp_f32 v170, v138\n"
            "v_mfma_f32_4x4x4_16b_bf16 v[180:183], v[208:209], v[66:67], 0 cbsz:2 abid:2\n"
            "v_exp_f32 v171, v139\n"
            "v_mfma_f32_4x4x4_16b_bf16 v[184:187], v[208:209], v[68:69], 0 cbsz:2 abid:2\n"
            "v_exp_f32 v172, v140\n"
            "v_mfma_f32_4x4x4_16b_bf16 v[188:191], v[208:209], v[70:71], 0 cbsz:2 abid:2\n"
            "v_exp_f32 v173, v141\n"
            "v_add_u32_dpp v221, v196, v214 quad_perm:[2,2,2,2] row_mask:0xf bank_mask:0xf\n"
            "v_exp_f32 v174, v142\n"
            "buffer_load_dwordx4 v[64:67], v221, %[rsx], 0 offen\n"
            "buffer_load_dwordx4 v[68:71], v221, %[rsx], 0 offen offset:256\n"
            "v_exp_f32 v175, v143\n"
            "v_pk_fma_f32 v[0:1], v[146:147], v[176:177], v[0:1] op_sel_hi:[0,1,1]\n"
            "v_pk_fma_f32 v[2:3], v[146:147], v[178:179], v[2:3] op_sel_hi:[0,1,1]\n"
            "v_pk_fma_f32 v[4:5], v[150:151], v[180:181], v[4:5] op_sel_hi:[0,1,1]\n"
            "v_pk_fma_f32 v[6:7], v[150:151], v[182:183], v[6:7] op_sel_hi:[0,1,1]\n"
            "v_pk_fma_f32 v[8:9], v[154:155], v[184:185], v[8:9] op_sel_hi:[0,1,1]\n"
            "v_pk_fma_f32 v[10:11], v[154:155], v[186:187], v[10:11] op_sel_hi:[0,1,1]\n"
            "v_pk_fma_f32 v[12:13], v[158:159], v[188:189], v[12:13] op_sel_hi:[0,1,1]\n"
            "v_pk_fma_f32 v[14:15], v[158:159], v[190:191], v[14:15] op_sel_hi:[0,1,1]\n"
            "v_pk_add_f32 v[168:169], v[168:169], 1.0 op_sel_hi:[1,0]\n"
            "v_pk_add_f32 v[170:171], v[170:171], 1.0 op_sel_hi:[1,0]\n"
            "v_pk_add_f32 v[172:173], v[172:173], 1.0 op_sel_hi:[1,0]\n"
            "v_pk_add_f32 v[174:175], v[174:175], 1.0 op_sel_hi:[1,0]\n"
            "v_cvt_pk_bf16_f32 v204, v204, v205\n"
            "v_cvt_pk_bf16_f32 v205, v206, v207\n"
            "ds_write_b64 v212, v[204:205]\n"
            "v_mfma_f32_16x16x32_bf16 v[132:135], v[192:195], v[100:103], 0\n"
            "v_rcp_f32 v168, v168\n"
            "v_rcp_f32 v169, v169\n"
            "v_mfma_f32_4x4x4_16b_bf16 v[176:179], v[208:209], v[80:81], 0 cbsz:2 abid:3\n"
            "v_rcp_f32 v170, v170\n"
            "v_mfma_f32_4x4x4_16b_bf16 v[180:183], v[208:209], v[82:83], 0 cbsz:2 abid:3\n"
            "v_rcp_f32 v171, v171\n"
            "v_mfma_f32_4x4x4_16b_bf16 v[184:187], v[208:209], v[84:85], 0 cbsz:2 abid:3\n"
            "v_rcp_f32 v172, v172\n"
            "v_mfma_f32_4x4x4_16b_bf16 v[188:191], v[208:209], v[86:87], 0 cbsz:2 abid:3\n"
            "v_rcp_f32 v173, v173\n"
            "v_add_u32_dpp v222, v196, v214 quad_perm:[3,3,3,3] row_mask:0xf bank_mask:0xf\n"
            "v_rcp_f32 v174, v174\n"
            "buffer_load_dwordx4 v[80:83], v222, %[rsx], 0 offen\n"
            "buffer_load_dwordx4 v[84:87], v222, %[rsx], 0 offen offset:256\n"
            "v_rcp_f32 v175, v175\n"
            "v_pk_fma_f32 v[0:1], v[146:147], v[176:177], v[0:1] op_sel:[1,0,0]\n"
            "v_pk_fma_f32 v[2:3], v[146:147], v[178:179], v[2:3] op_sel:[1,0,0]\n"
            "v_pk_fma_f32 v[4:5], v[150:151], v[180:181], v[4:5] op_sel:[1,0,0]\n"
            "v_pk_fma_f32 v[6:7], v[150:151], v[182:183], v[6:7] op_sel:[1,0,0]\n"
            "v_pk_fma_f32 v[8:9], v[154:155], v[184:185], v[8:9] op_sel:[1,0,0]\n"
            "v_pk_fma_f32 v[10:11], v[154:155], v[186:187], v[10:11] op_sel:[1,0,0]\n"
            "v_pk_fma_f32 v[12:13], v[158:159], v[188:189], v[12:13] op_sel:[1,0,0]\n"
            "v_pk_fma_f32 v[14:15], v[158:159], v[190:191], v[14:15] op_sel:[1,0,0]\n"
            "v_pk_mul_f32 v[168:169], v[168:169], v[136:137]\n"
            "v_pk_mul_f32 v[170:171], v[170:171], v[138:139]\n"
            "v_pk_mul_f32 v[172:173], v[172:173], v[140:141]\n"
            "v_pk_mul_f32 v[174:175], v[174:175], v[142:143]\n"
            "v_add_u32 v210, 64, v210\n"
            "ds_read_b128 v[196:199], v210\n"
            "v_mfma_f32_16x16x32_bf16 v[136:139], v[192:195], v[104:107], 0\n"
            "v_exp_f32 v144, v128\n"
            "v_exp_f32 v145, v129\n"
            "s_waitcnt vmcnt(13)\n"
            "v_mfma_f32_4x4x4_16b_bf16 v[176:179], v[208:209], v[40:41], 0 cbsz:2\n"
            "v_exp_f32 v146, v130\n"
            "v_mfma_f32_4x4x4_16b_bf16 v[180:183], v[208:209], v[42:43], 0 cbsz:2\n"
            "v_exp_f32 v147, v131\n"
            "v_mfma_f32_4x4x4_16b_bf16 v[184:187], v[208:209], v[44:45], 0 cbsz:2\n"
            "v_exp_f32 v148, v132\n"
            "v_mfma_f32_4x4x4_16b_bf16 v[188:191], v[208:209], v[46:47], 0 cbsz:2\n"
            "v_exp_f32 v149, v133\n"
            "v_exp_f32 v150, v134\n"
            "buffer_load_dwordx4 v[40:43], v219, %[rsx], 0 offen offset:512\n"
            "buffer_load_dwordx4 v[44:47], v219, %[rsx], 0 offen offset:768\n"
            "v_exp_f32 v151, v135\n"
            "v_pk_fma_f32 v[16:17], v[160:161], v[176:177], v[16:17] op_sel_hi:[0,1,1]\n"
            "v_pk_fma_f32 v[18:19], v[160:161], v[178:179], v[18:19] op_sel_hi:[0,1,1]\n"
            "v_pk_fma_f32 v[20:21], v[164:165], v[180:181], v[20:21] op_sel_hi:[0,1,1]\n"
            "v_pk_fma_f32 v[22:23], v[164:165], v[182:183], v[22:23] op_sel_hi:[0,1,1]\n"
            "v_pk_fma_f32 v[24:25], v[168:169], v[184:185], v[24:25] op_sel_hi:[0,1,1]\n"
            "v_pk_fma_f32 v[26:27], v[168:169], v[186:187], v[26:27] op_sel_hi:[0,1,1]\n"
            "v_pk_fma_f32 v[28:29], v[172:173], v[188:189], v[28:29] op_sel_hi:[0,1,1]\n"
            "v_pk_fma_f32 v[30:31], v[172:173], v[190:191], v[30:31] op_sel_hi:[0,1,1]\n"
            "v_pk_add_f32 v[144:145], v[144:145], 1.0 op_sel_hi:[1,0]\n"
            "v_pk_add_f32 v[146:147], v[146:147], 1.0 op_sel_hi:[1,0]\n"
            "v_pk_add_f32 v[148:149], v[148:149], 1.0 op_sel_hi:[1,0]\n"
            "v_pk_add_f32 v[150:151], v[150:151], 1.0 op_sel_hi:[1,0]\n"
            "v_mfma_f32_16x16x32_bf16 v[140:143], v[192:195], v[108:111], 0\n"
            "v_rcp_f32 v144, v144\n"
            "v_rcp_f32 v145, v145\n"
            "v_mfma_f32_4x4x4_16b_bf16 v[176:179], v[208:209], v[56:57], 0 cbsz:2 abid:1\n"
            "v_rcp_f32 v146, v146\n"
            "v_mfma_f32_4x4x4_16b_bf16 v[180:183], v[208:209], v[58:59], 0 cbsz:2 abid:1\n"
            "v_rcp_f32 v147, v147\n"
            "v_mfma_f32_4x4x4_16b_bf16 v[184:187], v[208:209], v[60:61], 0 cbsz:2 abid:1\n"
            "v_rcp_f32 v148, v148\n"
            "v_mfma_f32_4x4x4_16b_bf16 v[188:191], v[208:209], v[62:63], 0 cbsz:2 abid:1\n"
            "v_rcp_f32 v149, v149\n"
            "v_rcp_f32 v150, v150\n"
            "buffer_load_dwordx4 v[56:59], v220, %[rsx], 0 offen offset:512\n"
            "buffer_load_dwordx4 v[60:63], v220, %[rsx], 0 offen offset:768\n"
            "v_rcp_f32 v151, v151\n"
            "v_pk_fma_f32 v[16:17], v[160:161], v[176:177], v[16:17] op_sel:[1,0,0]\n"
            "v_pk_fma_f32 v[18:19], v[160:161], v[178:179], v[18:19] op_sel:[1,0,0]\n"
            "v_pk_fma_f32 v[20:21], v[164:165], v[180:181], v[20:21] op_sel:[1,0,0]\n"
            "v_pk_fma_f32 v[22:23], v[164:165], v[182:183], v[22:23] op_sel:[1,0,0]\n"
            "v_pk_fma_f32 v[24:25], v[168:169], v[184:185], v[24:25] op_sel:[1,0,0]\n"
            "v_pk_fma_f32 v[26:27], v[168:169], v[186:187], v[26:27] op_sel:[1,0,0]\n"
            "v_pk_fma_f32 v[28:29], v[172:173], v[188:189], v[28:29] op_sel:[1,0,0]\n"
            "v_pk_fma_f32 v[30:31], v[172:173], v[190:191], v[30:31] op_sel:[1,0,0]\n"
            "v_pk_mul_f32 v[144:145], v[144:145], v[128:129]\n"
            "v_pk_mul_f32 v[146:147], v[146:147], v[130:131]\n"
            "v_pk_mul_f32 v[148:149], v[148:149], v[132:133]\n"
            "v_pk_mul_f32 v[150:151], v[150:151], v[134:135]\n"
            "v_mfma_f32_16x16x32_bf16 v[128:131], v[192:195], v[112:115], 0\n"
            "v_exp_f32 v152, v136\n"
            "v_exp_f32 v153, v137\n"
            "s_waitcnt vmcnt(13)\n"
            "v_mfma_f32_4x4x4_16b_bf16 v[176:179], v[208:209], v[72:73], 0 cbsz:2 abid:2\n"
            "v_exp_f32 v154, v138\n"
            "v_mfma_f32_4x4x4_16b_bf16 v[180:183], v[208:209], v[74:75], 0 cbsz:2 abid:2\n"
            "v_exp_f32 v155, v139\n"
            "v_mfma_f32_4x4x4_16b_bf16 v[184:187], v[208:209], v[76:77], 0 cbsz:2 abid:2\n"
            "v_exp_f32 v156, v140\n"
            "v_mfma_f32_4x4x4_16b_bf16 v[188:191], v[208:209], v[78:79], 0 cbsz:2 abid:2\n"
            "v_exp_f32 v157, v141\n"
            "v_exp_f32 v158, v142\n"
            "buffer_load_dwordx4 v[72:75], v221, %[rsx], 0 offen offset:512\n"
            "buffer_load_dwordx4 v[76:79], v221, %[rsx], 0 offen offset:768\n"
            "v_exp_f32 v159, v143\n"
            "v_pk_fma_f32 v[16:17], v[162:163], v[176:177], v[16:17] op_sel_hi:[0,1,1]\n"
            "v_pk_fma_f32 v[18:19], v[162:163], v[178:179], v[18:19] op_sel_hi:[0,1,1]\n"
            "v_pk_fma_f32 v[20:21], v[166:167], v[180:181], v[20:21] op_sel_hi:[0,1,1]\n"
            "v_pk_fma_f32 v[22:23], v[166:167], v[182:183], v[22:23] op_sel_hi:[0,1,1]\n"
            "v_pk_fma_f32 v[24:25], v[170:171], v[184:185], v[24:25] op_sel_hi:[0,1,1]\n"
            "v_pk_fma_f32 v[26:27], v[170:171], v[186:187], v[26:27] op_sel_hi:[0,1,1]\n"
            "v_pk_fma_f32 v[28:29], v[174:175], v[188:189], v[28:29] op_sel_hi:[0,1,1]\n"
            "v_pk_fma_f32 v[30:31], v[174:175], v[190:191], v[30:31] op_sel_hi:[0,1,1]\n"
            "v_pk_add_f32 v[152:153], v[152:153], 1.0 op_sel_hi:[1,0]\n"
            "v_pk_add_f32 v[154:155], v[154:155], 1.0 op_sel_hi:[1,0]\n"
            "v_pk_add_f32 v[156:157], v[156:157], 1.0 op_sel_hi:[1,0]\n"
            "v_pk_add_f32 v[158:159], v[158:159], 1.0 op_sel_hi:[1,0]\n"
            "v_mfma_f32_16x16x32_bf16 v[132:135], v[192:195], v[116:119], 0\n"
            "v_rcp_f32 v152, v152\n"
            "v_rcp_f32 v153, v153\n"
            "v_mfma_f32_4x4x4_16b_bf16 v[176:179], v[208:209], v[88:89], 0 cbsz:2 abid:3\n"
            "v_rcp_f32 v154, v154\n"
            "v_mfma_f32_4x4x4_16b_bf16 v[180:183], v[208:209], v[90:91], 0 cbsz:2 abid:3\n"
            "v_rcp_f32 v155, v155\n"
            "v_mfma_f32_4x4x4_16b_bf16 v[184:187], v[208:209], v[92:93], 0 cbsz:2 abid:3\n"
            "v_rcp_f32 v156, v156\n"
            "v_mfma_f32_4x4x4_16b_bf16 v[188:191], v[208:209], v[94:95], 0 cbsz:2 abid:3\n"
            "ds_read_b64 v[208:209], v213\n"
            "v_rcp_f32 v157, v157\n"
            "v_rcp_f32 v158, v158\n"
            "buffer_load_dwordx4 v[88:91], v222, %[rsx], 0 offen offset:512\n"
            "buffer_load_dwordx4 v[92:95], v222, %[rsx], 0 offen offset:768\n"
            "v_rcp_f32 v159, v159\n"
            "v_pk_fma_f32 v[16:17], v[162:163], v[176:177], v[16:17] op_sel:[1,0,0]\n"
            "v_pk_fma_f32 v[18:19], v[162:163], v[178:179], v[18:19] op_sel:[1,0,0]\n"
            "v_pk_fma_f32 v[20:21], v[166:167], v[180:181], v[20:21] op_sel:[1,0,0]\n"
            "v_pk_fma_f32 v[22:23], v[166:167], v[182:183], v[22:23] op_sel:[1,0,0]\n"
            "v_pk_fma_f32 v[24:25], v[170:171], v[184:185], v[24:25] op_sel:[1,0,0]\n"
            "v_pk_fma_f32 v[26:27], v[170:171], v[186:187], v[26:27] op_sel:[1,0,0]\n"
            "v_pk_fma_f32 v[28:29], v[174:175], v[188:189], v[28:29] op_sel:[1,0,0]\n"
            "v_pk_fma_f32 v[30:31], v[174:175], v[190:191], v[30:31] op_sel:[1,0,0]\n"
            "v_pk_mul_f32 v[152:153], v[152:153], v[136:137]\n"
            "v_pk_mul_f32 v[154:155], v[154:155], v[138:139]\n"
            "v_pk_mul_f32 v[156:157], v[156:157], v[140:141]\n"
            "v_pk_mul_f32 v[158:159], v[158:159], v[142:143]\n"
            "s_sub_u32 %[rem], %[rem], 4\n"
            "Lnext_%=:\n"
            "s_sub_u32 %[nst], %[nst], 1\n"
            "Ldisp_%=:\n"
            "s_bitcmp1_b32 %[evt], %[nst]\n"
            "s_cbranch_scc1 Lprio_%=\n"
            "Ld2_%=:\n"
            "s_cmp_gt_u32 %[rem], 4\n"
            "s_cbranch_scc1 Lfast_%=\n"
            "v_mfma_f32_16x16x32_bf16 v[136:139], v[192:195], v[120:123], 0\n"
            "v_exp_f32 v160, v128\n"
            "v_add_u32 v211, 64, v211\n"
            "v_exp_f32 v161, v129\n"
            "s_waitcnt vmcnt(13) lgkmcnt(0)\n"
            "v_mfma_f32_4x4x4_16b_bf16 v[176:179], v[208:209], v[32:33], 0 cbsz:2\n"
            "v_exp_f32 v162, v130\n"
            "v_mfma_f32_4x4x4_16b_bf16 v[180:183], v[208:209], v[34:35], 0 cbsz:2\n"
            "v_exp_f32 v163, v131\n"
            "v_mfma_f32_4x4x4_16b_bf16 v[184:187], v[208:209], v[36:37], 0 cbsz:2\n"
            "v_exp_f32 v164, v132\n"
            "v_mfma_f32_4x4x4_16b_bf16 v[188:191], v[208:209], v[38:39], 0 cbsz:2\n"
            "v_exp_f32 v165, v133\n"
            "v_add_u32_dpp v219, v196, v214 quad_perm:[0,0,0,0] row_mask:0xf bank_mask:0xf\n"
            "v_mfma_f32_4x4x4_16b_bf16 v[204:207], v[202:203], v[198:199], 0\n"
            "v_exp_f32 v166, v134\n"
            "buffer_load_dwordx4 v[32:35], v219, %[rsx], 0 offen\n"
            "buffer_load_dwordx4 v[36:39], v219, %[rsx], 0 offen offset:256\n"
            "v_exp_f32 v167, v135\n"
            "v_pk_fma_f32 v[0:1], v[144:145], v[176:177], v[0:1] op_sel_hi:[0,1,1]\n"
            "v_pk_fma_f32 v[2:3], v[144:145], v[178:179], v[2:3] op_sel_hi:[0,1,1]\n"
            "v_pk_fma_f32 v[4:5], v[148:149], v[180:181], v[4:5] op_sel_hi:[0,1,1]\n"
            "v_pk_fma_f32 v[6:7], v[148:149], v[182:183], v[6:7] op_sel_hi:[0,1,1]\n"
            "v_pk_fma_f32 v[8:9], v[152:153], v[184:185], v[8:9] op_sel_hi:[0,1,1]\n"
            "v_pk_fma_f32 v[10:11], v[152:153], v[186:187], v[10:11] op_sel_hi:[0,1,1]\n"
            "v_pk_fma_f32 v[12:13], v[156:157], v[188:189], v[12:13] op_sel_hi:[0,1,1]\n"
            "v_pk_fma_f32 v[14:15], v[156:157], v[190:191], v[14:15] op_sel_hi:[0,1,1]\n"
            "v_pk_add_f32 v[160:161], v[160:161], 1.0 op_sel_hi:[1,0]\n"
            "v_pk_add_f32 v[162:163], v[162:163], 1.0 op_sel_hi:[1,0]\n"
            "v_pk_add_f32 v[164:165], v[164:165], 1.0 op_sel_hi:[1,0]\n"
            "v_pk_add_f32 v[166:167], v[166:167], 1.0 op_sel_hi:[1,0]\n"
            "s_cmp_eq_u32 %[rem], 1\n"
            "s_cbranch_scc1 Lf00_%=\n"
            "Lr00_%=:\n"
            "v_mfma_f32_16x16x32_bf16 v[140:143], v[192:195], v[124:127], 0\n"
            "ds_read_b32 v230, v211\n"
            "s_waitcnt vmcnt(14)\n"
            "ds_bpermute_b32 v192, v234, v232\n"
            "ds_bpermute_b32 v193, v234, v233\n"
            "ds_bpermute_b32 v194, v234, v232 offset:4\n"
            "ds_bpermute_b32 v195, v234, v233 offset:4\n"
            "v_rcp_f32 v160, v160\n"
            "v_rcp_f32 v161, v161\n"
            "v_mfma_f32_4x4x4_16b_bf16 v[176:179], v[208:209], v[48:49], 0 cbsz:2 abid:1\n"
            "v_rcp_f32 v162, v162\n"
            "v_mfma_f32_4x4x4_16b_bf16 v[180:183], v[208:209], v[50:51], 0 cbsz:2 abid:1\n"
            "v_rcp_f32 v163, v163\n"
            "v_mfma_f32_4x4x4_16b_bf16 v[184:187], v[208:209], v[52:53], 0 cbsz:2 abid:1\n"
            "v_rcp_f32 v164, v164\n"
            "v_mfma_f32_4x4x4_16b_bf16 v[188:191], v[208:209], v[54:55], 0 cbsz:2 abid:1\n"
            "v_rcp_f32 v165, v165\n"
            "v_add_u32_dpp v220, v196, v214 quad_perm:[1,1,1,1] row_mask:0xf bank_mask:0xf\n"
            "v_mfma_f32_4x4x4_16b_bf16 v[204:207], v[200:201], v[198:199], v[204:207]\n"
            "v_rcp_f32 v166, v166\n"
            "buffer_load_dwordx4 v[48:51], v220, %[rsx], 0 offen\n"
            "buffer_load_dwordx4 v[52:55], v220, %[rsx], 0 offen offset:256\n"
            "v_rcp_f32 v167, v167\n"
            "v_pk_fma_f32 v[0:1], v[144:145], v[176:177], v[0:1] op_sel:[1,0,0]\n"
            "v_pk_fma_f32 v[2:3], v[144:145], v[178:179], v[2:3] op_sel:[1,0,0]\n"
            "v_pk_fma_f32 v[4:5], v[148:149], v[180:181], v[4:5] op_sel:[1,0,0]\n"
            "v_pk_fma_f32 v[6:7], v[148:149], v[182:183], v[6:7] op_sel:[1,0,0]\n"
            "v_pk_fma_f32 v[8:9], v[152:153], v[184:185], v[8:9] op_sel:[1,0,0]\n"
            "v_pk_fma_f32 v[10:11], v[152:153], v[186:187], v[10:11] op_sel:[1,0,0]\n"
            "v_pk_fma_f32 v[12:13], v[156:157], v[188:189], v[12:13] op_sel:[1,0,0]\n"
            "v_pk_fma_f32 v[14:15], v[156:157], v[190:191], v[14:15] op_sel:[1,0,0]\n"
            "v_pk_mul_f32 v[160:161], v[160:161], v[128:129]\n"
            "v_pk_mul_f32 v[162:163], v[162:163], v[130:131]\n"
            "v_pk_mul_f32 v[164:165], v[164:165], v[132:133]\n"
            "v_pk_mul_f32 v[166:167], v[166:167], v[134:135]\n"
            "s_cmp_eq_u32 %[rem], 2\n"
            "s_cbranch_scc1 Lf01_%=\n"
            "Lr01_%=:\n"
            "s_waitcnt vmcnt(12) lgkmcnt(0)\n"
            "v_or_b32 v230, v230, v229\n"
            "buffer_load_dwordx2 v[232:233], v230, %[rse], 0 offen\n"
            "v_mfma_f32_16x16x32_bf16 v[128:131], v[192:195], v[96:99], 0\n"
            "v_exp_f32 v168, v136\n"
            "v_exp_f32 v169, v137\n"
            "v_mfma_f32_4x4x4_16b_bf16 v[176:179], v[208:209], v[64:65], 0 cbsz:2 abid:2\n"
            "v_exp_f32 v170, v138\n"
            "v_mfma_f32_4x4x4_16b_bf16 v[180:183], v[208:209], v[66:67], 0 cbsz:2 abid:2\n"
            "v_exp_f32 v171, v139\n"
            "v_mfma_f32_4x4x4_16b_bf16 v[184:187], v[208:209], v[68:69], 0 cbsz:2 abid:2\n"
            "v_exp_f32 v172, v140\n"
            "v_mfma_f32_4x4x4_16b_bf16 v[188:191], v[208:209], v[70:71], 0 cbsz:2 abid:2\n"
            "v_exp_f32 v173, v141\n"
            "v_add_u32_dpp v221, v196, v214 quad_perm:[2,2,2,2] row_mask:0xf bank_mask:0xf\n"
            "v_exp_f32 v174, v142\n"
            "buffer_load_dwordx4 v[64:67], v221, %[rsx], 0 offen\n"
            "buffer_load_dwordx4 v[68:71], v221, %[rsx], 0 offen offset:256\n"
            "v_exp_f32 v175, v143\n"
            "v_pk_fma_f32 v[0:1], v[146:147], v[176:177], v[0:1] op_sel_hi:[0,1,1]\n"
            "v_pk_fma_f32 v[2:3], v[146:147], v[178:179], v[2:3] op_sel_hi:[0,1,1]\n"
            "v_pk_fma_f32 v[4:5], v[150:151], v[180:181], v[4:5] op_sel_hi:[0,1,1]\n"
            "v_pk_fma_f32 v[6:7], v[150:151], v[182:183], v[6:7] op_sel_hi:[0,1,1]\n"
            "v_pk_fma_f32 v[8:9], v[154:155], v[184:185], v[8:9] op_sel_hi:[0,1,1]\n"
            "v_pk_fma_f32 v[10:11], v[154:155], v[186:187], v[10:11] op_sel_hi:[0,1,1]\n"
            "v_pk_fma_f32 v[12:13], v[158:159], v[188:189], v[12:13] op_sel_hi:[0,1,1]\n"
            "v_pk_fma_f32 v[14:15], v[158:159], v[190:191], v[14:15] op_sel_hi:[0,1,1]\n"
            "v_pk_add_f32 v[168:169], v[168:169], 1.0 op_sel_hi:[1,0]\n"
            "v_pk_add_f32 v[170:171], v[170:171], 1.0 op_sel_hi:[1,0]\n"
            "v_pk_add_f32 v[172:173], v[172:173], 1.0 op_sel_hi:[1,0]\n"
            "v_pk_add_f32 v[174:175], v[174:175], 1.0 op_sel_hi:[1,0]\n"
            "v_cvt_pk_bf16_f32 v204, v204, v205\n"
            "v_cvt_pk_bf16_f32 v205, v206, v207\n"
            "ds_write_b64 v212, v[204:205]\n"
            "s_cmp_eq_u32 %[rem], 3\n"
            "s_cbranch_scc1 Lf02_%=\n"
            "Lr02_%=:\n"
            "v_mfma_f32_16x16x32_bf16 v[132:135], v[192:195], v[100:103], 0\n"
            "v_rcp_f32 v168, v168\n"
            "v_rcp_f32 v169, v169\n"
            "v_mfma_f32_4x4x4_16b_bf16 v[176:179], v[208:209], v[80:81], 0 cbsz:2 abid:3\n"
            "v_rcp_f32 v170, v170\n"
            "v_mfma_f32_4x4x4_16b_bf16 v[180:183], v[208:209], v[82:83], 0 cbsz:2 abid:3\n"
            "v_rcp_f32 v171, v171\n"
            "v_mfma_f32_4x4x4_16b_bf16 v[184:187], v[208:209], v[84:85], 0 cbsz:2 abid:3\n"
            "v_rcp_f32 v172, v172\n"
            "v_mfma_f32_4x4x4_16b_bf16 v[188:191], v[208:209], v[86:87], 0 cbsz:2 abid:3\n"
            "v_rcp_f32 v173, v173\n"
            "v_add_u32_dpp v222, v196, v214 quad_perm:[3,3,3,3] row_mask:0xf bank_mask:0xf\n"
            "v_rcp_f32 v174, v174\n"
            "buffer_load_dwordx4 v[80:83], v222, %[rsx], 0 offen\n"
            "buffer_load_dwordx4 v[84:87], v222, %[rsx], 0 offen offset:256\n"
            "v_rcp_f32 v175, v175\n"
            "v_pk_fma_f32 v[0:1], v[146:147], v[176:177], v[0:1] op_sel:[1,0,0]\n"
            "v_pk_fma_f32 v[2:3], v[146:147], v[178:179], v[2:3] op_sel:[1,0,0]\n"
            "v_pk_fma_f32 v[4:5], v[150:151], v[180:181], v[4:5] op_sel:[1,0,0]\n"
            "v_pk_fma_f32 v[6:7], v[150:151], v[182:183], v[6:7] op_sel:[1,0,0]\n"
            "v_pk_fma_f32 v[8:9], v[154:155], v[184:185], v[8:9] op_sel:[1,0,0]\n"
            "v_pk_fma_f32 v[10:11], v[154:155], v[186:187], v[10:11] op_sel:[1,0,0]\n"
            "v_pk_fma_f32 v[12:13], v[158:159], v[188:189], v[12:13] op_sel:[1,0,0]\n"
            "v_pk_fma_f32 v[14:15], v[158:159], v[190:191], v[14:15] op_sel:[1,0,0]\n"
            "v_pk_mul_f32 v[168:169], v[168:169], v[136:137]\n"
            "v_pk_mul_f32 v[170:171], v[170:171], v[138:139]\n"
            "v_pk_mul_f32 v[172:173], v[172:173], v[140:141]\n"
            "v_pk_mul_f32 v[174:175], v[174:175], v[142:143]\n"
            "s_cmp_eq_u32 %[rem], 4\n"
            "s_cbranch_scc1 Lf03_%=\n"
            "Lr03_%=:\n"
            "v_add_u32 v210, 64, v210\n"
            "ds_read_b128 v[196:199], v210\n"
            "v_mfma_f32_16x16x32_bf16 v[136:139], v[192:195], v[104:107], 0\n"
            "v_exp_f32 v144, v128\n"
            "v_exp_f32 v145, v129\n"
            "s_waitcnt vmcnt(13)\n"
            "v_mfma_f32_4x4x4_16b_bf16 v[176:179], v[208:209], v[40:41], 0 cbsz:2\n"
            "v_exp_f32 v146, v130\n"
            "v_mfma_f32_4x4x4_16b_bf16 v[180:183], v[208:209], v[42:43], 0 cbsz:2\n"
            "v_exp_f32 v147, v131\n"
            "v_mfma_f32_4x4x4_16b_bf16 v[184:187], v[208:209], v[44:45], 0 cbsz:2\n"
            "v_exp_f32 v148, v132\n"
            "v_mfma_f32_4x4x4_16b_bf16 v[188:191], v[208:209], v[46:47], 0 cbsz:2\n"
            "v_exp_f32 v149, v133\n"
            "v_exp_f32 v150, v134\n"
            "buffer_load_dwordx4 v[40:43], v219, %[rsx], 0 offen offset:512\n"
            "buffer_load_dwordx4 v[44:47], v219, %[rsx], 0 offen offset:768\n"
            "v_exp_f32 v151, v135\n"
            "v_pk_fma_f32 v[16:17], v[160:161], v[176:177], v[16:17] op_sel_hi:[0,1,1]\n"
            "v_pk_fma_f32 v[18:19], v[160:161], v[178:179], v[18:19] op_sel_hi:[0,1,1]\n"
            "v_pk_fma_f32 v[20:21], v[164:165], v[180:181], v[20:21] op_sel_hi:[0,1,1]\n"
            "v_pk_fma_f32 v[22:23], v[164:165], v[182:183], v[22:23] op_sel_hi:[0,1,1]\n"
            "v_pk_fma_f32 v[24:25], v[168:169], v[184:185], v[24:25] op_sel_hi:[0,1,1]\n"
            "v_pk_fma_f32 v[26:27], v[168:169], v[186:187], v[26:27] op_sel_hi:[0,1,1]\n"
            "v_pk_fma_f32 v[28:29], v[172:173], v[188:189], v[28:29] op_sel_hi:[0,1,1]\n"
            "v_pk_fma_f32 v[30:31], v[172:173], v[190:191], v[30:31] op_sel_hi:[0,1,1]\n"
            "v_pk_add_f32 v[144:145], v[144:145], 1.0 op_sel_hi:[1,0]\n"
            "v_pk_add_f32 v[146:147], v[146:147], 1.0 op_sel_hi:[1,0]\n"
            "v_pk_add_f32 v[148:149], v[148:149], 1.0 op_sel_hi:[1,0]\n"
            "v_pk_add_f32 v[150:151], v[150:151], 1.0 op_sel_hi:[1,0]\n"
            "s_cmp_eq_u32 %[rem], 1\n"
            "s_cbranch_scc1 Lf10_%=\n"
            "Lr10_%=:\n"
            "v_mfma_f32_16x16x32_bf16 v[140:143], v[192:195], v[108:111], 0\n"
            "v_rcp_f32 v144, v144\n"
            "v_rcp_f32 v145, v145\n"
            "v_mfma_f32_4x4x4_16b_bf16 v[176:179], v[208:209], v[56:57], 0 cbsz:2 abid:1\n"
            "v_rcp_f32 v146, v146\n"
            "v_mfma_f32_4x4x4_16b_bf16 v[180:183], v[208:209], v[58:59], 0 cbsz:2 abid:1\n"
            "v_rcp_f32 v147, v147\n"
            "v_mfma_f32_4x4x4_16b_bf16 v[184:187], v[208:209], v[60:61], 0 cbsz:2 abid:1\n"
            "v_rcp_f32 v148, v148\n"
            "v_mfma_f32_4x4x4_16b_bf16 v[188:191], v[208:209], v[62:63], 0 cbsz:2 abid:1\n"
            "v_rcp_f32 v149, v149\n"
            "v_rcp_f32 v150, v150\n"
            "buffer_load_dwordx4 v[56:59], v220, %[rsx], 0 offen offset:512\n"
            "buffer_load_dwordx4 v[60:63], v220, %[rsx], 0 offen offset:768\n"
            "v_rcp_f32 v151, v151\n"
            "v_pk_fma_f32 v[16:17], v[160:161], v[176:177], v[16:17] op_sel:[1,0,0]\n"
            "v_pk_fma_f32 v[18:19], v[160:161], v[178:179], v[18:19] op_sel:[1,0,0]\n"
            "v_pk_fma_f32 v[20:21], v[164:165], v[180:181], v[20:21] op_sel:[1,0,0]\n"
            "v_pk_fma_f32 v[22:23], v[164:165], v[182:183], v[22:23] op_sel:[1,0,0]\n"
            "v_pk_fma_f32 v[24:25], v[168:169], v[184:185], v[24:25] op_sel:[1,0,0]\n"
            "v_pk_fma_f32 v[26:27], v[168:169], v[186:187], v[26:27] op_sel:[1,0,0]\n"
            "v_pk_fma_f32 v[28:29], v[172:173], v[188:189], v[28:29] op_sel:[1,0,0]\n"
            "v_pk_fma_f32 v[30:31], v[172:173], v[190:191], v[30:31] op_sel:[1,0,0]\n"
            "v_pk_mul_f32 v[144:145], v[144:145], v[128:129]\n"
            "v_pk_mul_f32 v[146:147], v[146:147], v[130:131]\n"
            "v_pk_mul_f32 v[148:149], v[148:149], v[132:133]\n"
            "v_pk_mul_f32 v[150:151], v[150:151], v[134:135]\n"
            "s_cmp_eq_u32 %[rem], 2\n"
            "s_cbranch_scc1 Lf11_%=\n"
            "Lr11_%=:\n"
            "v_mfma_f32_16x16x32_bf16 v[128:131], v[192:195], v[112:115], 0\n"
            "v_exp_f32 v152, v136\n"
            "v_exp_f32 v153, v137\n"
            "s_waitcnt vmcnt(13)\n"
            "v_mfma_f32_4x4x4_16b_bf16 v[176:179], v[208:209], v[72:73], 0 cbsz:2 abid:2\n"
            "v_exp_f32 v154, v138\n"
            "v_mfma_f32_4x4x4_16b_bf16 v[180:183], v[208:209], v[74:75], 0 cbsz:2 abid:2\n"
            "v_exp_f32 v155, v139\n"
            "v_mfma_f32_4x4x4_16b_bf16 v[184:187], v[208:209], v[76:77], 0 cbsz:2 abid:2\n"
            "v_exp_f32 v156, v140\n"
            "v_mfma_f32_4x4x4_16b_bf16 v[188:191], v[208:209], v[78:79], 0 cbsz:2 abid:2\n"
            "v_exp_f32 v157, v141\n"
            "v_exp_f32 v158, v142\n"
            "buffer_load_dwordx4 v[72:75], v221, %[rsx], 0 offen offset:512\n"
            "buffer_load_dwordx4 v[76:79], v221, %[rsx], 0 offen offset:768\n"
            "v_exp_f32 v159, v143\n"
            "v_pk_fma_f32 v[16:17], v[162:163], v[176:177], v[16:17] op_sel_hi:[0,1,1]\n"
            "v_pk_fma_f32 v[18:19], v[162:163], v[178:179], v[18:19] op_sel_hi:[0,1,1]\n"
            "v_pk_fma_f32 v[20:21], v[166:167], v[180:181], v[20:21] op_sel_hi:[0,1,1]\n"
            "v_pk_fma_f32 v[22:23], v[166:167], v[182:183], v[22:23] op_sel_hi:[0,1,1]\n"
            "v_pk_fma_f32 v[24:25], v[170:171], v[184:185], v[24:25] op_sel_hi:[0,1,1]\n"
            "v_pk_fma_f32 v[26:27], v[170:171], v[186:187], v[26:27] op_sel_hi:[0,1,1]\n"
            "v_pk_fma_f32 v[28:29], v[174:175], v[188:189], v[28:29] op_sel_hi:[0,1,1]\n"
            "v_pk_fma_f32 v[30:31], v[174:175], v[190:191], v[30:31] op_sel_hi:[0,1,1]\n"
            "v_pk_add_f32 v[152:153], v[152:153], 1.0 op_sel_hi:[1,0]\n"
            "v_pk_add_f32 v[154:155], v[154:155], 1.0 op_sel_hi:[1,0]\n"
            "v_pk_add_f32 v[156:157], v[156:157], 1.0 op_sel_hi:[1,0]\n"
            "v_pk_add_f32 v[158:159], v[158:159], 1.0 op_sel_hi:[1,0]\n"
            "s_cmp_eq_u32 %[rem], 3\n"
            "s_cbranch_scc1 Lf12_%=\n"
            "Lr12_%=:\n"
            "v_mfma_f32_16x16x32_bf16 v[132:135], v[192:195], v[116:119], 0\n"
            "v_rcp_f32 v152, v152\n"
            "v_rcp_f32 v153, v153\n"
            "v_mfma_f32_4x4x4_16b_bf16 v[176:179], v[208:209], v[88:89], 0 cbsz:2 abid:3\n"
            "v_rcp_f32 v154, v154\n"
            "v_mfma_f32_4x4x4_16b_bf16 v[180:183], v[208:209], v[90:91], 0 cbsz:2 abid:3\n"
            "v_rcp_f32 v155, v155\n"
            "v_mfma_f32_4x4x4_16b_bf16 v[184:187], v[208:209], v[92:93], 0 cbsz:2 abid:3\n"
            "v_rcp_f32 v156, v156\n"
            "v_mfma_f32_4x4x4_16b_bf16 v[188:191], v[208:209], v[94:95], 0 cbsz:2 abid:3\n"
            "ds_read_b64 v[208:209], v213\n"
            "v_rcp_f32 v157, v157\n"
            "v_rcp_f32 v158, v158\n"
            "buffer_load_dwordx4 v[88:91], v222, %[rsx], 0 offen offset:512\n"
            "buffer_load_dwordx4 v[92:95], v222, %[rsx], 0 offen offset:768\n"
            "v_rcp_f32 v159, v159\n"
            "v_pk_fma_f32 v[16:17], v[162:163], v[176:177], v[16:17] op_sel:[1,0,0]\n"
            "v_pk_fma_f32 v[18:19], v[162:163], v[178:179], v[18:19] op_sel:[1,0,0]\n"
            "v_pk_fma_f32 v[20:21], v[166:167], v[180:181], v[20:21] op_sel:[1,0,0]\n"
            "v_pk_fma_f32 v[22:23], v[166:167], v[182:183], v[22:23] op_sel:[1,0,0]\n"
            "v_pk_fma_f32 v[24:25], v[170:171], v[184:185], v[24:25] op_sel:[1,0,0]\n"
            "v_pk_fma_f32 v[26:27], v[170:171], v[186:187], v[26:27] op_sel:[1,0,0]\n"
            "v_pk_fma_f32 v[28:29], v[174:175], v[188:189], v[28:29] op_sel:[1,0,0]\n"
            "v_pk_fma_f32 v[30:31], v[174:175], v[190:191], v[30:31] op_sel:[1,0,0]\n"
            "v_pk_mul_f32 v[152:153], v[152:153], v[136:137]\n"
            "v_pk_mul_f32 v[154:155], v[154:155], v[138:139]\n"
            "v_pk_mul_f32 v[156:157], v[156:157], v[140:141]\n"
            "v_pk_mul_f32 v[158:159], v[158:159], v[142:143]\n"
            "s_cmp_eq_u32 %[rem], 4\n"
            "s_cbranch_scc1 Lf13_%=\n"
            "Lr13_%=:\n"
            "s_and_b32 %[tmp], %[lens], 0xff\n"
            "s_add_u32 %[rem], %[rem], %[tmp]\n"
            "s_lshr_b32 %[lens], %[lens], 8\n"
            "s_lshr_b32 %[fm], %[fm], 4\n"
            "s_bitcmp1_b32 %[fm], 3\n"
            "v_mov_b32 v215, v216\n"
            "s_cbranch_scc0 Lnext_%=\n"
            "s_sub_u32 %[pb], %[nst], 2\n"
            "s_bitset1_b32 %[evt], %[pb]\n"
            "s_branch Lnext_%=\n"
            "Lpub_%=:\n"
            "s_waitcnt vmcnt(17)\n"
            "v_mov_b32 v176, %[cnt]\n"
            "v_mov_b32 v177, 1\n"
            "s_mov_b64 exec, 1\n"
            "ds_add_rtn_u32 v177, v176, v177\n"
            "s_waitcnt lgkmcnt(0)\n"
            "v_readfirstlane_b32 %[tmp], v177\n"
            "s_mov_b64 exec, 0xff\n"
            "v_mov_b32 v176, %[tag]\n"
            "v_mov_b32 v177, %[tag]\n"
            "v_mov_b32 v178, %[tag]\n"
            "v_mov_b32 v179, %[tag]\n"
            "s_cmp_lg_u32 %[tmp], 7\n"
            "s_cbranch_scc1 Lpub2_%=\n"
            "global_store_dwordx4 v214, v[176:179], %[flg] sc1\n"
            "Lpub2_%=:\n"
            "s_mov_b64 exec, -1\n"
            "s_branch Lpr2_%=\n"
            "Lprio_%=:\n"
            "s_cmp_eq_u32 %[nst], %[pb]\n"
            "s_cbranch_scc1 Lpub_%=\n"
            "Lpr2_%=:\n"
            "s_cmp_eq_u32 %[nst], 1\n"
            "s_cbranch_scc1 Llast_%=\n"
            "s_lshl2_add_u32 %[tmp], %[nst], %[c1]\n"
            "v_bfe_u32 v176, v214, 4, 2\n"
            "v_lshrrev_b32 v177, 6, v214\n"
            "v_sub_u32 v176, %[tmp], v176\n"
            "v_sub_u32 v177, %[tmp], v177\n"
            "s_min_u32 %[tmp], %[nst], 15\n"
            "v_cmp_gt_u32 vcc, 4, v176\n"
            "v_cmp_gt_u32 %[ra], 4, v177\n"
            "v_add_u32 v176, %[c2], v176\n"
            "v_add_u32 v177, %[c2], v177\n"
            "v_cndmask_b32 v178, 0, v236, vcc\n"
            "v_cndmask_b32 v179, 0, v236, %[ra]\n"
            "v_add_u32 v210, v210, v178\n"
            "v_add_u32 v211, v211, v179\n"
            "v_cmp_gt_u32 vcc, 4, v176\n"
            "v_cmp_gt_u32 %[ra], 4, v177\n"
            "s_nop 1\n"
            "v_cndmask_b32 v178, 0, v237, vcc\n"
            "v_cndmask_b32 v179, 0, v237, %[ra]\n"
            "v_add_u32 v210, v210, v178\n"
            "v_add_u32 v211, v211, v179\n"
            "s_lshl_b32 %[tmp], %[tmp], 1\n"
            "s_lshr_b32 %[tmp], %[ptab], %[tmp]\n"
            "s_bitcmp1_b32 %[tmp], 1\n"
            "s_cbranch_scc1 Lq2_%=\n"
            "s_bitcmp1_b32 %[tmp], 0\n"
            "s_setprio 0\n"
            "s_cbranch_scc0 Lq9_%=\n"
            "s_setprio 1\n"
            "s_branch Lq9_%=\n"
            "Lq2_%=:\n"
            "s_bitcmp1_b32 %[tmp], 0\n"
            "s_setprio 2\n"
            "s_cbranch_scc0 Lq9_%=\n"
            "s_setprio 3\n"
            "Lq9_%=:\n"
            "s_cmp_lg_u32 %[nst], 2\n"
            "s_cbranch_scc1 Ld2_%=\n"
            "v_mov_b32 v223, 0\n"
            "global_load_dword v223, v223, %[pfl] sc1\n"
            "s_branch Ld2_%=\n"
            "Lf00_%=:\n"
            "s_call_b64 %[ra], Lflush0_%=\n"
            "s_branch Lr00_%=\n"
            "Lf01_%=:\n"
            "s_call_b64 %[ra], Lflush0_%=\n"
            "s_branch Lr01_%=\n"
            "Lf02_%=:\n"
            "s_call_b64 %[ra], Lflush0_%=\n"
            "s_branch Lr02_%=\n"
            "Lf03_%=:\n"
            "s_call_b64 %[ra], Lflush0_%=\n"
            "s_branch Lr03_%=\n"
            "Lflush0_%=:\n"
            "v_mov_b32 v184, %[pfa]\n"
            "s_bitcmp0_b32 %[fm], 0\n"
            "s_cbranch_scc1 Lfa0_%=\n"
            "Lfw_%=:\n"
            "ds_read_b32 v185, v184\n"
            "ds_read_b128 v[176:179], v231 offset:0\n"
            "ds_read_b128 v[180:183], v231 offset:1024\n"
            "s_waitcnt lgkmcnt(0)\n"
            "v_cmp_eq_u32 vcc, 0, v185\n"
            "s_cbranch_vccnz Lfw_%=\n"
            "v_mfma_f32_4x4x4_16b_bf16 v[0:3], v[224:225], v[176:177], v[0:3]\n"
            "v_mfma_f32_4x4x4_16b_bf16 v[4:7], v[224:225], v[178:179], v[4:7]\n"
            "v_mfma_f32_4x4x4_16b_bf16 v[8:11], v[224:225], v[180:181], v[8:11]\n"
            "v_mfma_f32_4x4x4_16b_bf16 v[12:15], v[224:225], v[182:183], v[12:15]\n"
            "s_nop 1\n"
            "Lfa0_%=:\n"
            "v_cvt_pk_bf16_f32 v176, v0, v1\n"
            "v_cvt_pk_bf16_f32 v177, v2, v3\n"
            "v_cvt_pk_bf16_f32 v178, v4, v5\n"
            "v_cvt_pk_bf16_f32 v179, v6, v7\n"
            "v_cvt_pk_bf16_f32 v180, v8, v9\n"
            "v_cvt_pk_bf16_f32 v181, v10, v11\n"
            "v_cvt_pk_bf16_f32 v182, v12, v13\n"
            "v_cvt_pk_bf16_f32 v183, v14, v15\n"
            "s_bitcmp1_b32 %[fm], 1\n"
            "s_cbranch_scc1 Lpark0_%=\n"
            "buffer_store_dwordx4 v[176:179], v215, %[rsw], 0 offen offset:0 sc1\n"
            "buffer_store_dwordx4 v[180:183], v215, %[rsw], 0 offen offset:256 sc1\n"
            "s_branch Lzero0_%=\n"
            "Lpark0_%=:\n"
            "ds_write_b128 v231, v[176:179] offset:0\n"
            "ds_write_b128 v231, v[180:183] offset:1024\n"
            "Lzero0_%=:\n"
            "v_mov_b64 v[0:1], 0\n"
            "v_mov_b64 v[2:3], 0\n"
            "v_mov_b64 v[4:5], 0\n"
            "v_mov_b64 v[6:7], 0\n"
            "v_mov_b64 v[8:9], 0\n"
            "v_mov_b64 v[10:11], 0\n"
            "v_mov_b64 v[12:13], 0\n"
            "v_mov_b64 v[14:15], 0\n"
            "s_setpc_b64 %[ra]\n"
            "Lf10_%=:\n"
            "s_call_b64 %[ra], Lflush1_%=\n"
            "s_branch Lr10_%=\n"
            "Lf11_%=:\n"
            "s_call_b64 %[ra], Lflush1_%=\n"
            "s_branch Lr11_%=\n"
            "Lf12_%=:\n"
            "s_call_b64 %[ra], Lflush1_%=\n"
            "s_branch Lr12_%=\n"
            "Lf13_%=:\n"
            "s_call_b64 %[ra], Lflush1_%=\n"
            "s_branch Lr13_%=\n"
            "Lflush1_%=:\n"
            "v_mov_b32 v184, %[pfa]\n"
            "s_bitcmp0_b32 %[fm], 0\n"
            "s_cbranch_scc1 Lfa1_%=\n"
            "ds_read_b128 v[176:179], v231 offset:2048\n"
            "ds_read_b128 v[180:183], v231 offset:3072\n"
            "s_waitcnt lgkmcnt(0)\n"
            "v_mfma_f32_4x4x4_16b_bf16 v[16:19], v[224:225], v[176:177], v[16:19]\n"
            "v_mfma_f32_4x4x4_16b_bf16 v[20:23], v[224:225], v[178:179], v[20:23]\n"
            "v_mfma_f32_4x4x4_16b_bf16 v[24:27], v[224:225], v[180:181], v[24:27]\n"
            "v_mfma_f32_4x4x4_16b_bf16 v[28:31], v[224:225], v[182:183], v[28:31]\n"
            "s_nop 1\n"
            "Lfa1_%=:\n"
            "v_cvt_pk_bf16_f32 v176, v16, v17\n"
            "v_cvt_pk_bf16_f32 v177, v18, v19\n"
            "v_cvt_pk_bf16_f32 v178, v20, v21\n"
            "v_cvt_pk_bf16_f32 v179, v22, v23\n"
            "v_cvt_pk_bf16_f32 v180, v24, v25\n"
            "v_cvt_pk_bf16_f32 v181, v26, v27\n"
            "v_cvt_pk_bf16_f32 v182, v28, v29\n"
            "v_cvt_pk_bf16_f32 v183, v30, v31\n"
            "s_bitcmp1_b32 %[fm], 1\n"
            "s_cbranch_scc1 Lpark1_%=\n"
            "buffer_store_dwordx4 v[176:179], v215, %[rsw], 0 offen offset:512 sc1\n"
            "buffer_store_dwordx4 v[180:183], v215, %[rsw], 0 offen offset:768 sc1\n"
            "s_branch Lzero1_%=\n"
            "Lpark1_%=:\n"
            "ds_write_b128 v231, v[176:179] offset:2048\n"
            "ds_write_b128 v231, v[180:183] offset:3072\n"
            "ds_write_b32 v184, v184\n"
            "Lzero1_%=:\n"
            "v_mov_b64 v[16:17], 0\n"
            "v_mov_b64 v[18:19], 0\n"
            "v_mov_b64 v[20:21], 0\n"
            "v_mov_b64 v[22:23], 0\n"
            "v_mov_b64 v[24:25], 0\n"
            "v_mov_b64 v[26:27], 0\n"
            "v_mov_b64 v[28:29], 0\n"
            "v_mov_b64 v[30:31], 0\n"
            "s_setpc_b64 %[ra]\n"
            "Lspin_%=:\n"
            "s_mov_b32 %[lens], %[spin]\n"
            "Lsp1_%=:\n"
            "v_readfirstlane_b32 %[nst], v223\n"
            "s_sub_u32 %[lens], %[lens], 1\n"
            "s_cmp_eq_u32 %[lens], 0\n"
            "s_cbranch_scc1 Lsp2_%=\n"
            "s_cmp_eq_u32 %[nst], %[tag]\n"
            "s_cbranch_scc1 Lsp2_%=\n"
            "s_sleep 1\n"
            "v_mov_b32 v223, 0\n"
            "global_load_dword v223, v223, %[pfl] sc1\n"
            "s_waitcnt vmcnt(0)\n"
            "s_branch Lsp1_%=\n"
            "Lsp2_%=:\n"
            "buffer_load_dwordx4 v[32:35], v217, %[rsw], %[oagg] offen offset:0 sc1\n"
            "buffer_load_dwordx4 v[36:39], v217, %[rsw], %[oagg] offen offset:256 sc1\n"
            "buffer_load_dwordx4 v[48:51], v217, %[rsw], %[oagg] offen offset:512 sc1\n"
            "buffer_load_dwordx4 v[52:55], v217, %[rsw], %[oagg] offen offset:768 sc1\n"
            "s_branch Lgo_%=\n"
            "Llast_%=:\n"
            "s_waitcnt vmcnt(16)\n"
            "v_readfirstlane_b32 %[nst], v223\n"
            "v_mov_b32 v222, v217\n"
            "s_cmp_eq_u32 %[nst], %[tag]\n"
            "s_cbranch_scc1 Lp4_%=\n"
            "v_bfrev_b32 v222, 1\n"
            "v_mov_b32 v223, 0\n"
            "global_load_dword v223, v223, %[pfl] sc1\n"
            "Lp4_%=:\n"
            "v_mfma_f32_16x16x32_bf16 v[136:139], v[192:195], v[120:123], 0\n"
            "v_exp_f32 v160, v128\n"
            "v_exp_f32 v161, v129\n"
            "s_waitcnt vmcnt(13) lgkmcnt(0)\n"
            "v_mfma_f32_4x4x4_16b_bf16 v[176:179], v[208:209], v[32:33], 0 cbsz:2\n"
            "v_exp_f32 v162, v130\n"
            "v_mfma_f32_4x4x4_16b_bf16 v[180:183], v[208:209], v[34:35], 0 cbsz:2\n"
            "v_exp_f32 v163, v131\n"
            "v_mfma_f32_4x4x4_16b_bf16 v[184:187], v[208:209], v[36:37], 0 cbsz:2\n"
            "v_exp_f32 v164, v132\n"
            "v_mfma_f32_4x4x4_16b_bf16 v[188:191], v[208:209], v[38:39], 0 cbsz:2\n"
            "v_exp_f32 v165, v133\n"
            "v_exp_f32 v166, v134\n"
            "buffer_load_dwordx4 v[32:35], v222, %[rsw], %[oagg] offen offset:0 sc1\n"
            "buffer_load_dwordx4 v[36:39], v222, %[rsw], %[oagg] offen offset:256 sc1\n"
            "ds_read_b128 v[236:239], v228 offset:0\n"
            "ds_read_b128 v[240:243], v228 offset:1024\n"
            "ds_read_b128 v[244:247], v228 offset:2048\n"
            "ds_read_b128 v[248:251], v228 offset:3072\n"
            "ds_read_b128 v[96:99], v228 offset:4096\n"
            "v_exp_f32 v167, v135\n"
            "v_pk_fma_f32 v[0:1], v[144:145], v[176:177], v[0:1] op_sel_hi:[0,1,1]\n"
            "v_pk_fma_f32 v[2:3], v[144:145], v[178:179], v[2:3] op_sel_hi:[0,1,1]\n"
            "v_pk_fma_f32 v[4:5], v[148:149], v[180:181], v[4:5] op_sel_hi:[0,1,1]\n"
            "v_pk_fma_f32 v[6:7], v[148:149], v[182:183], v[6:7] op_sel_hi:[0,1,1]\n"
            "v_pk_fma_f32 v[8:9], v[152:153], v[184:185], v[8:9] op_sel_hi:[0,1,1]\n"
            "v_pk_fma_f32 v[10:11], v[152:153], v[186:187], v[10:11] op_sel_hi:[0,1,1]\n"
            "v_pk_fma_f32 v[12:13], v[156:157], v[188:189], v[12:13] op_sel_hi:[0,1,1]\n"
            "v_pk_fma_f32 v[14:15], v[156:157], v[190:191], v[14:15] op_sel_hi:[0,1,1]\n"
            "v_pk_add_f32 v[160:161], v[160:161], 1.0 op_sel_hi:[1,0]\n"
            "v_pk_add_f32 v[162:163], v[162:163], 1.0 op_sel_hi:[1,0]\n"
            "v_pk_add_f32 v[164:165], v[164:165], 1.0 op_sel_hi:[1,0]\n"
            "v_pk_add_f32 v[166:167], v[166:167], 1.0 op_sel_hi:[1,0]\n"
            "v_mfma_f32_16x16x32_bf16 v[140:143], v[192:195], v[124:127], 0\n"
            "v_rcp_f32 v160, v160\n"
            "v_rcp_f32 v161, v161\n"
            "v_mfma_f32_4x4x4_16b_bf16 v[176:179], v[208:209], v[48:49], 0 cbsz:2 abid:1\n"
            "v_rcp_f32 v162, v162\n"
            "v_mfma_f32_4x4x4_16b_bf16 v[180:183], v[208:209], v[50:51], 0 cbsz:2 abid:1\n"
            "v_rcp_f32 v163, v163\n"
            "v_mfma_f32_4x4x4_16b_bf16 v[184:187], v[208:209], v[52:53], 0 cbsz:2 abid:1\n"
            "v_rcp_f32 v164, v164\n"
            "v_mfma_f32_4x4x4_16b_bf16 v[188:191], v[208:209], v[54:55], 0 cbsz:2 abid:1\n"
            "v_rcp_f32 v165, v165\n"
            "v_rcp_f32 v166, v166\n"
            "buffer_load_dwordx4 v[48:51], v222, %[rsw], %[oagg] offen offset:512 sc1\n"
            "buffer_load_dwordx4 v[52:55], v222, %[rsw], %[oagg] offen offset:768 sc1\n"
            "ds_read_b128 v[100:103], v228 offset:5120\n"
            "ds_read_b128 v[104:107], v228 offset:6144\n"
            "ds_read_b128 v[108:111], v228 offset:7168\n"
            "ds_read_b128 v[112:115], v228 offset:8192\n"
            "ds_read_b128 v[116:119], v228 offset:9216\n"
            "ds_read_b128 v[196:199], v228 offset:10240\n"
            "v_rcp_f32 v167, v167\n"
            "v_pk_fma_f32 v[0:1], v[144:145], v[176:177], v[0:1] op_sel:[1,0,0]\n"
            "v_pk_fma_f32 v[2:3], v[144:145], v[178:179], v[2:3] op_sel:[1,0,0]\n"
            "v_pk_fma_f32 v[4:5], v[148:149], v[180:181], v[4:5] op_sel:[1,0,0]\n"
            "v_pk_fma_f32 v[6:7], v[148:149], v[182:183], v[6:7] op_sel:[1,0,0]\n"
            "v_pk_fma_f32 v[8:9], v[152:153], v[184:185], v[8:9] op_sel:[1,0,0]\n"
            "v_pk_fma_f32 v[10:11], v[152:153], v[186:187], v[10:11] op_sel:[1,0,0]\n"
            "v_pk_fma_f32 v[12:13], v[156:157], v[188:189], v[12:13] op_sel:[1,0,0]\n"
            "v_pk_fma_f32 v[14:15], v[156:157], v[190:191], v[14:15] op_sel:[1,0,0]\n"
            "v_pk_mul_f32 v[160:161], v[160:161], v[128:129]\n"
            "v_pk_mul_f32 v[162:163], v[162:163], v[130:131]\n"
            "v_pk_mul_f32 v[164:165], v[164:165], v[132:133]\n"
            "v_pk_mul_f32 v[166:167], v[166:167], v[134:135]\n"
            "v_exp_f32 v168, v136\n"
            "v_exp_f32 v169, v137\n"
            "s_waitcnt vmcnt(12)\n"
            "v_mfma_f32_4x4x4_16b_bf16 v[176:179], v[208:209], v[64:65], 0 cbsz:2 abid:2\n"
            "v_exp_f32 v170, v138\n"
            "v_mfma_f32_4x4x4_16b_bf16 v[180:183], v[208:209], v[66:67], 0 cbsz:2 abid:2\n"
            "v_exp_f32 v171, v139\n"
            "v_mfma_f32_4x4x4_16b_bf16 v[184:187], v[208:209], v[68:69], 0 cbsz:2 abid:2\n"
            "v_exp_f32 v172, v140\n"
            "v_mfma_f32_4x4x4_16b_bf16 v[188:191], v[208:209], v[70:71], 0 cbsz:2 abid:2\n"
            "v_exp_f32 v173, v141\n"
            "v_exp_f32 v174, v142\n"
            "ds_read_b128 v[200:203], v228 offset:11264\n"
            "ds_read_b128 v[204:207], v228 offset:12288\n"
            "ds_read_b128 v[232:235], v228 offset:13312\n"
            "v_exp_f32 v175, v143\n"
            "v_pk_fma_f32 v[0:1], v[146:147], v[176:177], v[0:1] op_sel_hi:[0,1,1]\n"
            "v_pk_fma_f32 v[2:3], v[146:147], v[178:179], v[2:3] op_sel_hi:[0,1,1]\n"
            "v_pk_fma_f32 v[4:5], v[150:151], v[180:181], v[4:5] op_sel_hi:[0,1,1]\n"
            "v_pk_fma_f32 v[6:7], v[150:151], v[182:183], v[6:7] op_sel_hi:[0,1,1]\n"
            "v_pk_fma_f32 v[8:9], v[154:155], v[184:185], v[8:9] op_sel_hi:[0,1,1]\n"
            "v_pk_fma_f32 v[10:11], v[154:155], v[186:187], v[10:11] op_sel_hi:[0,1,1]\n"
            "v_pk_fma_f32 v[12:13], v[158:159], v[188:189], v[12:13] op_sel_hi:[0,1,1]\n"
            "v_pk_fma_f32 v[14:15], v[158:159], v[190:191], v[14:15] op_sel_hi:[0,1,1]\n"
            "v_pk_add_f32 v[168:169], v[168:169], 1.0 op_sel_hi:[1,0]\n"
            "v_pk_add_f32 v[170:171], v[170:171], 1.0 op_sel_hi:[1,0]\n"
            "v_pk_add_f32 v[172:173], v[172:173], 1.0 op_sel_hi:[1,0]\n"
            "v_pk_add_f32 v[174:175], v[174:175], 1.0 op_sel_hi:[1,0]\n"
            "v_rcp_f32 v168, v168\n"
            "v_rcp_f32 v169, v169\n"
            "v_mfma_f32_4x4x4_16b_bf16 v[176:179], v[208:209], v[80:81], 0 cbsz:2 abid:3\n"
            "v_rcp_f32 v170, v170\n"
            "v_mfma_f32_4x4x4_16b_bf16 v[180:183], v[208:209], v[82:83], 0 cbsz:2 abid:3\n"
            "v_rcp_f32 v171, v171\n"
            "v_mfma_f32_4x4x4_16b_bf16 v[184:187], v[208:209], v[84:85], 0 cbsz:2 abid:3\n"
            "v_rcp_f32 v172, v172\n"
            "v_mfma_f32_4x4x4_16b_bf16 v[188:191], v[208:209], v[86:87], 0 cbsz:2 abid:3\n"
            "v_rcp_f32 v173, v173\n"
            "v_rcp_f32 v174, v174\n"
            "ds_read_b128 v[120:123], v228 offset:14336\n"
            "ds_read_b128 v[124:127], v228 offset:15360\n"
            "ds_read_b128 v[192:195], v228 offset:16384\n"
            "ds_read_b128 v[128:131], v228 offset:17408\n"
            "ds_read_b128 v[132:135], v228 offset:18432\n"
            "v_rcp_f32 v175, v175\n"
            "v_pk_fma_f32 v[0:1], v[146:147], v[176:177], v[0:1] op_sel:[1,0,0]\n"
            "v_pk_fma_f32 v[2:3], v[146:147], v[178:179], v[2:3] op_sel:[1,0,0]\n"
            "v_pk_fma_f32 v[4:5], v[150:151], v[180:181], v[4:5] op_sel:[1,0,0]\n"
            "v_pk_fma_f32 v[6:7], v[150:151], v[182:183], v[6:7] op_sel:[1,0,0]\n"
            "v_pk_fma_f32 v[8:9], v[154:155], v[184:185], v[8:9] op_sel:[1,0,0]\n"
            "v_pk_fma_f32 v[10:11], v[154:155], v[186:187], v[10:11] op_sel:[1,0,0]\n"
            "v_pk_fma_f32 v[12:13], v[158:159], v[188:189], v[12:13] op_sel:[1,0,0]\n"
            "v_pk_fma_f32 v[14:15], v[158:159], v[190:191], v[14:15] op_sel:[1,0,0]\n"
            "v_pk_mul_f32 v[168:169], v[168:169], v[136:137]\n"
            "v_pk_mul_f32 v[170:171], v[170:171], v[138:139]\n"
            "v_pk_mul_f32 v[172:173], v[172:173], v[140:141]\n"
            "v_pk_mul_f32 v[174:175], v[174:175], v[142:143]\n"
            "s_waitcnt vmcnt(8)\n"
            "v_mfma_f32_4x4x4_16b_bf16 v[176:179], v[208:209], v[40:41], 0 cbsz:2\n"
            "v_mfma_f32_4x4x4_16b_bf16 v[180:183], v[208:209], v[42:43], 0 cbsz:2\n"
            "v_mfma_f32_4x4x4_16b_bf16 v[184:187], v[208:209], v[44:45], 0 cbsz:2\n"
            "v_mfma_f32_4x4x4_16b_bf16 v[188:191], v[208:209], v[46:47], 0 cbsz:2\n"
            "ds_read_b128 v[136:139], v228 offset:19456\n"
            "ds_read_b128 v[140:143], v228 offset:20480\n"
            "ds_read_b128 v[144:147], v228 offset:21504\n"
            "ds_read_b128 v[148:151], v228 offset:22528\n"
            "ds_read_b128 v[152:155], v228 offset:23552\n"
            "ds_read_b128 v[156:159], v228 offset:24576\n"
            "s_nop 1\n"
            "v_pk_fma_f32 v[16:17], v[160:161], v[176:177], v[16:17] op_sel_hi:[0,1,1]\n"
            "v_pk_fma_f32 v[18:19], v[160:161], v[178:179], v[18:19] op_sel_hi:[0,1,1]\n"
            "v_pk_fma_f32 v[20:21], v[164:165], v[180:181], v[20:21] op_sel_hi:[0,1,1]\n"
            "v_pk_fma_f32 v[22:23], v[164:165], v[182:183], v[22:23] op_sel_hi:[0,1,1]\n"
            "v_pk_fma_f32 v[24:25], v[168:169], v[184:185], v[24:25] op_sel_hi:[0,1,1]\n"
            "v_pk_fma_f32 v[26:27], v[168:169], v[186:187], v[26:27] op_sel_hi:[0,1,1]\n"
            "v_pk_fma_f32 v[28:29], v[172:173], v[188:189], v[28:29] op_sel_hi:[0,1,1]\n"
            "v_pk_fma_f32 v[30:31], v[172:173], v[190:191], v[30:31] op_sel_hi:[0,1,1]\n"
            "s_cmp_le_u32 %[njl], 1\n"
            "s_cbranch_scc1 Ltail_%=\n"
            "v_mfma_f32_4x4x4_16b_bf16 v[176:179], v[208:209], v[56:57], 0 cbsz:2 abid:1\n"
            "v_mfma_f32_4x4x4_16b_bf16 v[180:183], v[208:209], v[58:59], 0 cbsz:2 abid:1\n"
            "v_mfma_f32_4x4x4_16b_bf16 v[184:187], v[208:209], v[60:61], 0 cbsz:2 abid:1\n"
            "v_mfma_f32_4x4x4_16b_bf16 v[188:191], v[208:209], v[62:63], 0 cbsz:2 abid:1\n"
            "s_nop 1\n"
            "v_pk_fma_f32 v[16:17], v[160:161], v[176:177], v[16:17] op_sel:[1,0,0]\n"
            "v_pk_fma_f32 v[18:19], v[160:161], v[178:179], v[18:19] op_sel:[1,0,0]\n"
            "v_pk_fma_f32 v[20:21], v[164:165], v[180:181], v[20:21] op_sel:[1,0,0]\n"
            "v_pk_fma_f32 v[22:23], v[164:165], v[182:183], v[22:23] op_sel:[1,0,0]\n"
            "v_pk_fma_f32 v[24:25], v[168:169], v[184:185], v[24:25] op_sel:[1,0,0]\n"
            "v_pk_fma_f32 v[26:27], v[168:169], v[186:187], v[26:27] op_sel:[1,0,0]\n"
            "v_pk_fma_f32 v[28:29], v[172:173], v[188:189], v[28:29] op_sel:[1,0,0]\n"
            "v_pk_fma_f32 v[30:31], v[172:173], v[190:191], v[30:31] op_sel:[1,0,0]\n"
            "s_cmp_le_u32 %[njl], 2\n"
            "s_cbranch_scc1 Ltail_%=\n"
            "s_waitcnt vmcnt(4)\n"
            "v_mfma_f32_4x4x4_16b_bf16 v[176:179], v[208:209], v[72:73], 0 cbsz:2 abid:2\n"
            "v_mfma_f32_4x4x4_16b_bf16 v[180:183], v[208:209], v[74:75], 0 cbsz:2 abid:2\n"
            "v_mfma_f32_4x4x4_16b_bf16 v[184:187], v[208:209], v[76:77], 0 cbsz:2 abid:2\n"
            "v_mfma_f32_4x4x4_16b_bf16 v[188:191], v[208:209], v[78:79], 0 cbsz:2 abid:2\n"
            "s_nop 1\n"
            "v_pk_fma_f32 v[16:17], v[162:163], v[176:177], v[16:17] op_sel_hi:[0,1,1]\n"
            "v_pk_fma_f32 v[18:19], v[162:163], v[178:179], v[18:19] op_sel_hi:[0,1,1]\n"
            "v_pk_fma_f32 v[20:21], v[166:167], v[180:181], v[20:21] op_sel_hi:[0,1,1]\n"
            "v_pk_fma_f32 v[22:23], v[166:167], v[182:183], v[22:23] op_sel_hi:[0,1,1]\n"
            "v_pk_fma_f32 v[24:25], v[170:171], v[184:185], v[24:25] op_sel_hi:[0,1,1]\n"
            "v_pk_fma_f32 v[26:27], v[170:171], v[186:187], v[26:27] op_sel_hi:[0,1,1]\n"
            "v_pk_fma_f32 v[28:29], v[174:175], v[188:189], v[28:29] op_sel_hi:[0,1,1]\n"
            "v_pk_fma_f32 v[30:31], v[174:175], v[190:191], v[30:31] op_sel_hi:[0,1,1]\n"
            "s_cmp_le_u32 %[njl], 3\n"
            "s_cbranch_scc1 Ltail_%=\n"
            "v_mfma_f32_4x4x4_16b_bf16 v[176:179], v[208:209], v[88:89], 0 cbsz:2 abid:3\n"
            "v_mfma_f32_4x4x4_16b_bf16 v[180:183], v[208:209], v[90:91], 0 cbsz:2 abid:3\n"
            "v_mfma_f32_4x4x4_16b_bf16 v[184:187], v[208:209], v[92:93], 0 cbsz:2 abid:3\n"
            "v_mfma_f32_4x4x4_16b_bf16 v[188:191], v[208:209], v[94:95], 0 cbsz:2 abid:3\n"
            "s_nop 1\n"
            "v_pk_fma_f32 v[16:17], v[162:163], v[176:177], v[16:17] op_sel:[1,0,0]\n"
            "v_pk_fma_f32 v[18:19], v[162:163], v[178:179], v[18:19] op_sel:[1,0,0]\n"
            "v_pk_fma_f32 v[20:21], v[166:167], v[180:181], v[20:21] op_sel:[1,0,0]\n"
            "v_pk_fma_f32 v[22:23], v[166:167], v[182:183], v[22:23] op_sel:[1,0,0]\n"
            "v_pk_fma_f32 v[24:25], v[170:171], v[184:185], v[24:25] op_sel:[1,0,0]\n"
            "v_pk_fma_f32 v[26:27], v[170:171], v[186:187], v[26:27] op_sel:[1,0,0]\n"
            "v_pk_fma_f32 v[28:29], v[174:175], v[188:189], v[28:29] op_sel:[1,0,0]\n"
            "v_pk_fma_f32 v[30:31], v[174:175], v[190:191], v[30:31] op_sel:[1,0,0]\n"
            "Ltail_%=:\n"
            "s_setprio 3\n"
            "s_waitcnt vmcnt(4)\n"
            "ds_read_b128 v[72:75], v228 offset:25600\n"
            "ds_read_b128 v[76:79], v228 offset:26624\n"
            "ds_read_b128 v[88:91], v228 offset:27648\n"
            "ds_read_b128 v[92:95], v228 offset:28672\n"
            "ds_read_b128 v[160:163], v228 offset:29696\n"
            "ds_read_b128 v[164:167], v228 offset:30720\n"
            "ds_read_b128 v[168:171], v228 offset:31744\n"
            "s_cmp_lg_u32 %[nst], %[tag]\n"
            "s_cbranch_scc1 Lspin_%=\n"
            "Lgo_%=:\n"
            "buffer_load_dwordx4 v[40:43], v218, %[rsw], %[oagg] offen offset:0 sc1\n"
            "buffer_load_dwordx4 v[44:47], v218, %[rsw], %[oagg] offen offset:256 sc1\n"
            "buffer_load_dwordx4 v[56:59], v218, %[rsw], %[oagg] offen offset:512 sc1\n"
            "buffer_load_dwordx4 v[60:63], v218, %[rsw], %[oagg] offen offset:768 sc1\n"
            "s_waitcnt vmcnt(4)\n"
            "v_mfma_f32_4x4x4_16b_bf16 v[0:3], v[224:225], v[32:33], v[0:3]\n"
            "v_mfma_f32_4x4x4_16b_bf16 v[4:7], v[224:225], v[34:35], v[4:7]\n"
            "v_mfma_f32_4x4x4_16b_bf16 v[8:11], v[224:225], v[36:37], v[8:11]\n"
            "v_mfma_f32_4x4x4_16b_bf16 v[12:15], v[224:225], v[38:39], v[12:15]\n"
            "v_mfma_f32_4x4x4_16b_bf16 v[16:19], v[224:225], v[48:49], v[16:19]\n"
            "v_mfma_f32_4x4x4_16b_bf16 v[20:23], v[224:225], v[50:51], v[20:23]\n"
            "v_mfma_f32_4x4x4_16b_bf16 v[24:27], v[224:225], v[52:53], v[24:27]\n"
            "v_mfma_f32_4x4x4_16b_bf16 v[28:31], v[224:225], v[54:55], v[28:31]\n"
            "s_nop 1\n"
            "v_cvt_pk_bf16_f32 v176, v0, v4\n"
            "v_cvt_pk_bf16_f32 v177, v8, v12\n"
            "v_cvt_pk_bf16_f32 v178, v16, v20\n"
            "v_cvt_pk_bf16_f32 v179, v24, v28\n"
            "ds_write_b128 v226, v[176:179] offset:0\n"
            "v_cvt_pk_bf16_f32 v180, v1, v5\n"
            "v_cvt_pk_bf16_f32 v181, v9, v13\n"
            "v_cvt_pk_bf16_f32 v182, v17, v21\n"
            "v_cvt_pk_bf16_f32 v183, v25, v29\n"
            "ds_write_b128 v226, v[180:183] offset:272\n"
            "v_cvt_pk_bf16_f32 v184, v2, v6\n"
            "v_cvt_pk_bf16_f32 v185, v10, v14\n"
            "v_cvt_pk_bf16_f32 v186, v18, v22\n"
            "v_cvt_pk_bf16_f32 v187, v26, v30\n"
            "ds_write_b128 v226, v[184:187] offset:544\n"
            "v_cvt_pk_bf16_f32 v188, v3, v7\n"
            "v_cvt_pk_bf16_f32 v189, v11, v15\n"
            "v_cvt_pk_bf16_f32 v190, v19, v23\n"
            "v_cvt_pk_bf16_f32 v191, v27, v31\n"
            "ds_write_b128 v226, v[188:191] offset:816\n"
            "ds_read_b128 v[176:179], v227 offset:0\n"
            "ds_read_b128 v[180:183], v227 offset:64\n"
            "ds_read_b128 v[184:187], v227 offset:128\n"
            "ds_read_b128 v[188:191], v227 offset:192\n"
            "s_waitcnt lgkmcnt(3)\n"
            "v_mfma_f32_16x16x32_bf16 v[0:3], v[176:179], v[236:239], 0\n"
            "v_mfma_f32_16x16x32_bf16 v[4:7], v[176:179], v[96:99], 0\n"
            "s_waitcnt lgkmcnt(2)\n"
            "v_mfma_f32_16x16x32_bf16 v[0:3], v[180:183], v[240:243], v[0:3]\n"
            "v_mfma_f32_16x16x32_bf16 v[4:7], v[180:183], v[100:103], v[4:7]\n"
            "s_waitcnt lgkmcnt(1)\n"
            "v_mfma_f32_16x16x32_bf16 v[0:3], v[184:187], v[244:247], v[0:3]\n"
            "v_mfma_f32_16x16x32_bf16 v[4:7], v[184:187], v[104:107], v[4:7]\n"
            "s_waitcnt lgkmcnt(0)\n"
            "v_mfma_f32_16x16x32_bf16 v[0:3], v[188:191], v[248:251], v[0:3]\n"
            "v_mfma_f32_16x16x32_bf16 v[4:7], v[188:191], v[108:111], v[4:7]\n"
            "v_mfma_f32_16x16x32_bf16 v[8:11], v[176:179], v[112:115], 0\n"
            "v_mfma_f32_16x16x32_bf16 v[12:15], v[176:179], v[204:207], 0\n"
            "v_mfma_f32_16x16x32_bf16 v[8:11], v[180:183], v[116:119], v[8:11]\n"
            "v_mfma_f32_16x16x32_bf16 v[12:15], v[180:183], v[232:235], v[12:15]\n"
            "v_mfma_f32_16x16x32_bf16 v[8:11], v[184:187], v[196:199], v[8:11]\n"
            "v_mfma_f32_16x16x32_bf16 v[12:15], v[184:187], v[120:123], v[12:15]\n"
            "v_mfma_f32_16x16x32_bf16 v[8:11], v[188:191], v[200:203], v[8:11]\n"
            "v_mfma_f32_16x16x32_bf16 v[12:15], v[188:191], v[124:127], v[12:15]\n"
            "v_cvt_pk_bf16_f32 v0, v0, v1\n"
            "v_cvt_pk_bf16_f32 v1, v2, v3\n"
            "v_cvt_pk_bf16_f32 v2, v4, v5\n"
            "v_cvt_pk_bf16_f32 v3, v6, v7\n"
            "buffer_store_dwordx4 v[0:3], v217, %[rso], 0 offen offset:0 sc1\n"
            "v_mfma_f32_16x16x32_bf16 v[16:19], v[176:179], v[192:195], 0\n"
            "v_mfma_f32_16x16x32_bf16 v[20:23], v[176:179], v[140:143], 0\n"
            "v_mfma_f32_16x16x32_bf16 v[16:19], v[180:183], v[128:131], v[16:19]\n"
            "v_mfma_f32_16x16x32_bf16 v[20:23], v[180:183], v[144:147], v[20:23]\n"
            "v_mfma_f32_16x16x32_bf16 v[16:19], v[184:187], v[132:135], v[16:19]\n"
            "v_mfma_f32_16x16x32_bf16 v[20:23], v[184:187], v[148:151], v[20:23]\n"
            "v_mfma_f32_16x16x32_bf16 v[16:19], v[188:191], v[136:139], v[16:19]\n"
            "v_mfma_f32_16x16x32_bf16 v[20:23], v[188:191], v[152:155], v[20:23]\n"
            "v_cvt_pk_bf16_f32 v8, v8, v9\n"
            "v_cvt_pk_bf16_f32 v9, v10, v11\n"
            "v_cvt_pk_bf16_f32 v10, v12, v13\n"
            "v_cvt_pk_bf16_f32 v11, v14, v15\n"
            "buffer_store_dwordx4 v[8:11], v217, %[rso], 0 offen offset:256 sc1\n"
            "v_mfma_f32_16x16x32_bf16 v[24:27], v[176:179], v[156:159], 0\n"
            "v_mfma_f32_16x16x32_bf16 v[28:31], v[176:179], v[92:95], 0\n"
            "v_mfma_f32_16x16x32_bf16 v[24:27], v[180:183], v[72:75], v[24:27]\n"
            "v_mfma_f32_16x16x32_bf16 v[28:31], v[180:183], v[160:163], v[28:31]\n"
            "v_mfma_f32_16x16x32_bf16 v[24:27], v[184:187], v[76:79], v[24:27]\n"
            "v_mfma_f32_16x16x32_bf16 v[28:31], v[184:187], v[164:167], v[28:31]\n"
            "v_mfma_f32_16x16x32_bf16 v[24:27], v[188:191], v[88:91], v[24:27]\n"
            "v_mfma_f32_16x16x32_bf16 v[28:31], v[188:191], v[168:171], v[28:31]\n"
            "v_cvt_pk_bf16_f32 v16, v16, v17\n"
            "v_cvt_pk_bf16_f32 v17, v18, v19\n"
            "v_cvt_pk_bf16_f32 v18, v20, v21\n"
            "v_cvt_pk_bf16_f32 v19, v22, v23\n"
            "buffer_store_dwordx4 v[16:19], v217, %[rso], 0 offen offset:512 sc1\n"
            "s_nop 2\n"
            "v_cvt_pk_bf16_f32 v24, v24, v25\n"
            "v_cvt_pk_bf16_f32 v25, v26, v27\n"
            "v_cvt_pk_bf16_f32 v26, v28, v29\n"
            "v_cvt_pk_bf16_f32 v27, v30, v31\n"
            "buffer_store_dwordx4 v[24:27], v217, %[rso], 0 offen offset:768 sc1\n"
            "s_cmp_lt_u32 %[two], 6\n"
            "s_cbranch_scc1 Lend_%=\n"
            "ds_read_b128 v[64:67], v231 offset:0\n"
            "ds_read_b128 v[68:71], v231 offset:1024\n"
            "ds_read_b128 v[80:83], v231 offset:2048\n"
            "ds_read_b128 v[84:87], v231 offset:3072\n"
            "s_waitcnt vmcnt(4) lgkmcnt(0)\n"
            "v_mfma_f32_4x4x4_16b_bf16 v[0:3], v[224:225], v[64:65], 0\n"
            "v_mfma_f32_4x4x4_16b_bf16 v[4:7], v[224:225], v[66:67], 0\n"
            "v_mfma_f32_4x4x4_16b_bf16 v[8:11], v[224:225], v[68:69], 0\n"
            "v_mfma_f32_4x4x4_16b_bf16 v[12:15], v[224:225], v[70:71], 0\n"
            "v_mfma_f32_4x4x4_16b_bf16 v[16:19], v[224:225], v[80:81], 0\n"
            "v_mfma_f32_4x4x4_16b_bf16 v[20:23], v[224:225], v[82:83], 0\n"
            "v_mfma_f32_4x4x4_16b_bf16 v[24:27], v[224:225], v[84:85], 0\n"
            "v_mfma_f32_4x4x4_16b_bf16 v[28:31], v[224:225], v[86:87], 0\n"
            "v_mfma_f32_4x4x4_16b_bf16 v[0:3], v[224:225], v[40:41], v[0:3]\n"
            "v_mfma_f32_4x4x4_16b_bf16 v[4:7], v[224:225], v[42:43], v[4:7]\n"
            "v_mfma_f32_4x4x4_16b_bf16 v[8:11], v[224:225], v[44:45], v[8:11]\n"
            "v_mfma_f32_4x4x4_16b_bf16 v[12:15], v[224:225], v[46:47], v[12:15]\n"
            "v_mfma_f32_4x4x4_16b_bf16 v[16:19], v[224:225], v[56:57], v[16:19]\n"
            "v_mfma_f32_4x4x4_16b_bf16 v[20:23], v[224:225], v[58:59], v[20:23]\n"
            "v_mfma_f32_4x4x4_16b_bf16 v[24:27], v[224:225], v[60:61], v[24:27]\n"
            "v_mfma_f32_4x4x4_16b_bf16 v[28:31], v[224:225], v[62:63], v[28:31]\n"
            "s_nop 1\n"
            "v_cvt_pk_bf16_f32 v176, v0, v4\n"
            "v_cvt_pk_bf16_f32 v177, v8, v12\n"
            "v_cvt_pk_bf16_f32 v178, v16, v20\n"
            "v_cvt_pk_bf16_f32 v179, v24, v28\n"
            "ds_write_b128 v226, v[176:179] offset:0\n"
            "v_cvt_pk_bf16_f32 v180, v1, v5\n"
            "v_cvt_pk_bf16_f32 v181, v9, v13\n"
            "v_cvt_pk_bf16_f32 v182, v17, v21\n"
            "v_cvt_pk_bf16_f32 v183, v25, v29\n"
            "ds_write_b128 v226, v[180:183] offset:272\n"
            "v_cvt_pk_bf16_f32 v184, v2, v6\n"
            "v_cvt_pk_bf16_f32 v185, v10, v14\n"
            "v_cvt_pk_bf16_f32 v186, v18, v22\n"
            "v_cvt_pk_bf16_f32 v187, v26, v30\n"
            "ds_write_b128 v226, v[184:187] offset:544\n"
            "v_cvt_pk_bf16_f32 v188, v3, v7\n"
            "v_cvt_pk_bf16_f32 v189, v11, v15\n"
            "v_cvt_pk_bf16_f32 v190, v19, v23\n"
            "v_cvt_pk_bf16_f32 v191, v27, v31\n"
            "ds_write_b128 v226, v[188:191] offset:816\n"
            "ds_read_b128 v[176:179], v227 offset:0\n"
            "ds_read_b128 v[180:183], v227 offset:64\n"
            "ds_read_b128 v[184:187], v227 offset:128\n"
            "ds_read_b128 v[188:191], v227 offset:192\n"
            "s_waitcnt lgkmcnt(3)\n"
            "v_mfma_f32_16x16x32_bf16 v[0:3], v[176:179], v[236:239], 0\n"
            "v_mfma_f32_16x16x32_bf16 v[4:7], v[176:179], v[96:99], 0\n"
            "s_waitcnt lgkmcnt(2)\n"
            "v_mfma_f32_16x16x32_bf16 v[0:3], v[180:183], v[240:243], v[0:3]\n"
            "v_mfma_f32_16x16x32_bf16 v[4:7], v[180:183], v[100:103], v[4:7]\n"
            "s_waitcnt lgkmcnt(1)\n"
            "v_mfma_f32_16x16x32_bf16 v[0:3], v[184:187], v[244:247], v[0:3]\n"
            "v_mfma_f32_16x16x32_bf16 v[4:7], v[184:187], v[104:107], v[4:7]\n"
            "s_waitcnt lgkmcnt(0)\n"
            "v_mfma_f32_16x16x32_bf16 v[0:3], v[188:191], v[248:251], v[0:3]\n"
            "v_mfma_f32_16x16x32_bf16 v[4:7], v[188:191], v[108:111], v[4:7]\n"
            "v_mfma_f32_16x16x32_bf16 v[8:11], v[176:179], v[112:115], 0\n"
            "v_mfma_f32_16x16x32_bf16 v[12:15], v[176:179], v[204:207], 0\n"
            "v_mfma_f32_16x16x32_bf16 v[8:11], v[180:183], v[116:119], v[8:11]\n"
            "v_mfma_f32_16x16x32_bf16 v[12:15], v[180:183], v[232:235], v[12:15]\n"
            "v_mfma_f32_16x16x32_bf16 v[8:11], v[184:187], v[196:199], v[8:11]\n"
            "v_mfma_f32_16x16x32_bf16 v[12:15], v[184:187], v[120:123], v[12:15]\n"
            "v_mfma_f32_16x16x32_bf16 v[8:11], v[188:191], v[200:203], v[8:11]\n"
            "v_mfma_f32_16x16x32_bf16 v[12:15], v[188:191], v[124:127], v[12:15]\n"
            "v_cvt_pk_bf16_f32 v0, v0, v1\n"
            "v_cvt_pk_bf16_f32 v1, v2, v3\n"
            "v_cvt_pk_bf16_f32 v2, v4, v5\n"
            "v_cvt_pk_bf16_f32 v3, v6, v7\n"
            "buffer_store_dwordx4 v[0:3], v218, %[rso], 0 offen offset:0 sc1\n"
            "v_mfma_f32_16x16x32_bf16 v[16:19], v[176:179], v[192:195], 0\n"
            "v_mfma_f32_16x16x32_bf16 v[20:23], v[176:179], v[140:143], 0\n"
            "v_mfma_f32_16x16x32_bf16 v[16:19], v[180:183], v[128:131], v[16:19]\n"
            "v_mfma_f32_16x16x32_bf16 v[20:23], v[180:183], v[144:147], v[20:23]\n"
            "v_mfma_f32_16x16x32_bf16 v[16:19], v[184:187], v[132:135], v[16:19]\n"
            "v_mfma_f32_16x16x32_bf16 v[20:23], v[184:187], v[148:151], v[20:23]\n"
            "v_mfma_f32_16x16x32_bf16 v[16:19], v[188:191], v[136:139], v[16:19]\n"
            "v_mfma_f32_16x16x32_bf16 v[20:23], v[188:191], v[152:155], v[20:23]\n"
            "v_cvt_pk_bf16_f32 v8, v8, v9\n"
            "v_cvt_pk_bf16_f32 v9, v10, v11\n"
            "v_cvt_pk_bf16_f32 v10, v12, v13\n"
            "v_cvt_pk_bf16_f32 v11, v14, v15\n"
            "buffer_store_dwordx4 v[8:11], v218, %[rso], 0 offen offset:256 sc1\n"
            "v_mfma_f32_16x16x32_bf16 v[24:27], v[176:179], v[156:159], 0\n"
            "v_mfma_f32_16x16x32_bf16 v[28:31], v[176:179], v[92:95], 0\n"
            "v_mfma_f32_16x16x32_bf16 v[24:27], v[180:183], v[72:75], v[24:27]\n"
            "v_mfma_f32_16x16x32_bf16 v[28:31], v[180:183], v[160:163], v[28:31]\n"
            "v_mfma_f32_16x16x32_bf16 v[24:27], v[184:187], v[76:79], v[24:27]\n"
            "v_mfma_f32_16x16x32_bf16 v[28:31], v[184:187], v[164:167], v[28:31]\n"
            "v_mfma_f32_16x16x32_bf16 v[24:27], v[188:191], v[88:91], v[24:27]\n"
            "v_mfma_f32_16x16x32_bf16 v[28:31], v[188:191], v[168:171], v[28:31]\n"
            "v_cvt_pk_bf16_f32 v16, v16, v17\n"
            "v_cvt_pk_bf16_f32 v17, v18, v19\n"
            "v_cvt_pk_bf16_f32 v18, v20, v21\n"
            "v_cvt_pk_bf16_f32 v19, v22, v23\n"
            "buffer_store_dwordx4 v[16:19], v218, %[rso], 0 offen offset:512 sc1\n"
            "s_nop 2\n"
            "v_cvt_pk_bf16_f32 v24, v24, v25\n"
            "v_cvt_pk_bf16_f32 v25, v26, v27\n"
            "v_cvt_pk_bf16_f32 v26, v28, v29\n"
            "v_cvt_pk_bf16_f32 v27, v30, v31\n"
            "buffer_store_dwordx4 v[24:27], v218, %[rso], 0 offen offset:768 sc1\n"
            "Lend_%=:\n"
            "s_waitcnt vmcnt(4)\n"
            : "+{v[32:63]}"(oxa), "+{v[64:95]}"(oxb), "+{v[96:127]}"(owr), "+{v[196:199]}"(S), "+{v[200:203]}"(cgA), "+{v[208:223]}"(omi), "+{v[224:231]}"(ot),
              "+{v[232:233]}"(efN), "+{v234}"(bpa), "+{v[236:237]}"(jmp), "+{v[238:239]}"(ef0),
              [rem] "+s"(rem), [lens] "+s"(lens), [fm] "+s"(fm), [nst] "+s"(nst), [pb] "+s"(pb), [evt] "+s"(evt), [ra] "=&s"(ra), [tmp] "=&s"(tmp)
            : [rse] "s"(de), [rsx] "s"(dx), [rso] "s"(mkdesc(out, (unsigned)N * 1024u)), [rsw] "s"(mkdesc(ws, W_END)),
              [cnt] "s"(lds0 + L_MISC + 4), [pfa] "s"(lds0 + L_MISC + 16 + pw * 4), [tag] "s"(tag), [flg] "s"(ws + W_FLG + bx * 128), [pfl] "s"(ws + W_FLG + (bx ^ 4) * 128),
              [njl] "s"(((e2 - 1) & 3) + 1), [c1] "s"((w < 4 ? l2 : l1) - 4 * ((e2 + 3) >> 2) - 5), [c2] "s"(w < 4 ? l1 : l2), [two] "s"(w), [ptab] "s"((unsigned)__builtin_amdgcn_readfirstlane(w < 4 ? PTO : (w < 6 ? PTY : PTZ))), [oagg] "s"(W_AGG), [spin] "s"(SPIN_MAX)
            : "memory", "vcc", "scc", "v0", "v1", "v2", "v3", "v4", "v5", "v6", "v7", "v8", "v9", "v10", "v11", "v12", "v13", "v14", "v15", "v16", "v17", "v18", "v19", "v20", "v21", "v22", "v23", "v24", "v25", "v26", "v27", "v28", "v29", "v30", "v31", "v128", "v129", "v130", "v131", "v132", "v133", "v134", "v135", "v136", "v137", "v138", "v139", "v140", "v141", "v142", "v143", "v144", "v145", "v146", "v147", "v148", "v149", "v150", "v151", "v152", "v153", "v154", "v155", "v156", "v157", "v158", "v159", "v160", "v161", "v162", "v163", "v164", "v165", "v166", "v167", "v168", "v169", "v170", "v171", "v172", "v173", "v174", "v175", "v176", "v177", "v178", "v179", "v180", "v181", "v182", "v183", "v184", "v185", "v186", "v187", "v188", "v189", "v190", "v191", "v192", "v193", "v194", "v195", "v235", "v204", "v205", "v206", "v207", "v240", "v241", "v242", "v243", "v244", "v245", "v246", "v247", "v248", "v249", "v250", "v251");
    } else {
        if (bx == 0 && w == 4) flag_store(ws + W_CTL, tag);
        lds_wait(lds0 + SY_C3, 5);
        const __amdgpu_buffer_rsrc_t rso = __builtin_amdgcn_make_buffer_rsrc((void*)out, 0, N * 1024, 0x00020000);
#pragma unroll 1
        for (int it = 0; it < 10; ++it) {
            const int nl = (it < 5 ? 40 - 40 * half + 8 * it : 40 * half + 8 * (it - 5)) + w, node = node0 + nl;
            const bool nv = nl < npb;
            const unsigned ro = nv ? (unsigned)node * 1024u + (unsigned)lane * 16u : OOB_OFF;
            float a8[8] = {0.f, 0.f, 0.f, 0.f, 0.f, 0.f, 0.f, 0.f};
#pragma unroll 1
            for (int e0 = 0; e0 < (nv ? E : 0); e0 += 64) {
                const int ec = min(e0 + lane, E - 1);
                u64 mm = __ballot(e0 + lane < E && dst[ec] == node && (int)(((src[ec] >> 3) ^ (smask >> ((src[ec] >> 9) & 31))) & 1) == half);
#pragma unroll 1
                while (mm) {
                    const int e = e0 + __builtin_ctzll(mm);
                    mm &= mm - 1;
                    const unsigned short* fe = (const unsigned short*)ef + (size_t)e * DE_DIM;
                    const unsigned short* ye = (const unsigned short*)Y + (size_t)e * M_DIM;
                    const unsigned short* xp = x + (size_t)src[e] * (C_DIM * M_DIM) + lane * 8;
                    float z0 = 0.f, z1 = 0.f;
#pragma unroll 1
                    for (int f = 0; f < DE_DIM; ++f) { z0 += bf2f(fe[f]) * bf2f(w_rad[f * C_DIM + 2 * lane]); z1 += bf2f(fe[f]) * bf2f(w_rad[f * C_DIM + 2 * lane + 1]); }
                    const float r0 = z0 / (1.f + __expf(-z0)), r1 = z1 / (1.f + __expf(-z1));
#pragma unroll 1
                    for (int i = 0; i < 4; ++i) {
                        const float x0 = bf2f(xp[i]) * r0, x1 = bf2f(xp[4 + i]) * r1;
#pragma unroll
                        for (int o = 0; o < 4; ++o) {
                            float wv4 = 0.f;
#pragma unroll
                            for (int f = 0; f < 4; ++f) wv4 += bf2f(cg[(i * 4 + f) * 4 + o]) * bf2f(ye[f]);
                            a8[o] += x0 * wv4; a8[4 + o] += x1 * wv4;
                        }
                    }
                }
            }
            if (it >= 5) {
                const u32x4 pv = __builtin_amdgcn_raw_buffer_load_b128(rsw, ro, W_AGG, SC1);
#pragma unroll
                for (int i = 0; i < 4; ++i) { a8[2 * i] += __uint_as_float(pv[i] << 16); a8[2 * i + 1] += __uint_as_float(pv[i] & 0xffff0000u); }
            }
            const u32x4 rv = (u32x4){pack2bf(a8[0], a8[1]), pack2bf(a8[2], a8[3]), pack2bf(a8[4], a8[5]), pack2bf(a8[6], a8[7])};
            if (it < 5) {
                __builtin_amdgcn_raw_buffer_store_b128(rv, rsw, ro, W_AGG, SC1);
                if (it == 4) {
                    asm volatile("s_waitcnt vmcnt(0)" ::: "memory");
                    if (lds_arrive_rtn(lds0 + L_MISC + 4) == 7u) flag_store(ws + W_FLG + bx * 128, tag);
                    poll_tag(ws + W_FLG + (bx ^ 4) * 128, tag);
                }
            } else {
                *(u32x4*)(smem + L_PRV + w * 1024 + lane * 16) = rv;
#pragma unroll
                for (int i = 0; i < 8; ++i) a8[i] = 0.f;
#pragma unroll 1
                for (int c = 0; c < C_DIM; ++c) {
                    const unsigned short* ap = (const unsigned short*)(smem + L_PRV + w * 1024 + c * 8);
                    const float w0 = bf2f(w_mix[c * C_DIM + 2 * lane]), w1 = bf2f(w_mix[c * C_DIM + 2 * lane + 1]);
#pragma unroll
                    for (int m = 0; m < 4; ++m) { a8[m] += bf2f(ap[m]) * w0; a8[4 + m] += bf2f(ap[m]) * w1; }
                }
                __builtin_amdgcn_raw_buffer_store_b128((u32x4){pack2bf(a8[0], a8[1]), pack2bf(a8[2], a8[3]), pack2bf(a8[4], a8[5]), pack2bf(a8[6], a8[7])}, rso, ro, 0, 0);
            }
        }
    }
}

__global__ void kf_zero(float* __restrict__ agg, size_t n) {
    for (size_t i = (size_t)blockIdx.x * blockDim.x + threadIdx.x; i < n; i += (size_t)gridDim.x * blockDim.x) agg[i] = 0.f;
}
__global__ void kf_edges(const unsigned short* __restrict__ x, const int* __restrict__ src, const int* __restrict__ dst,
                         const unsigned short* __restrict__ Y, const unsigned short* __restrict__ ef,
                         const unsigned short* __restrict__ w_rad, const unsigned short* __restrict__ cg,
                         float* __restrict__ agg, int E) {
    const int ch = threadIdx.x;
    for (int e = blockIdx.x; e < E; e += gridDim.x) {
        float z = 0.f;
        for (int f = 0; f < DE_DIM; ++f) z += bf2f(ef[(size_t)e * DE_DIM + f]) * bf2f(w_rad[f * C_DIM + ch]);
        const float r = z / (1.f + __expf(-z));
        float a4[4] = {0.f, 0.f, 0.f, 0.f};
        for (int ii = 0; ii < 4; ++ii) {
            const float xv = bf2f(x[((size_t)src[e] * C_DIM + ch) * M_DIM + ii]) * r;
            for (int o = 0; o < 4; ++o) {
                float wv = 0.f;
                for (int f = 0; f < 4; ++f) wv += bf2f(cg[(ii * 4 + f) * 4 + o]) * bf2f(Y[(size_t)e * 4 + f]);
                a4[o] += xv * wv;
            }
        }
        for (int o = 0; o < 4; ++o) atomicAdd(&agg[((size_t)dst[e] * C_DIM + ch) * M_DIM + o], a4[o]);
    }
}
__global__ void kf_mix(const float* __restrict__ agg, const unsigned short* __restrict__ w_mix, unsigned short* __restrict__ out, int N) {
    const int d = threadIdx.x;
    for (int n = blockIdx.x; n < N; n += gridDim.x) {
        float a4[4] = {0.f, 0.f, 0.f, 0.f};
        for (int c = 0; c < C_DIM; ++c) {
            const float wv = bf2f(w_mix[c * C_DIM + d]);
            for (int m = 0; m < 4; ++m) a4[m] += bf2f(f2bf(agg[((size_t)n * C_DIM + c) * M_DIM + m])) * wv;
        }
        for (int m = 0; m < 4; ++m) out[((size_t)n * C_DIM + d) * M_DIM + m] = f2bf(a4[m]);
    }
}

extern "C" void kernel_launch(void* const* d_in, const int* in_sizes, int n_in,
                              void* d_out, int out_size, void* d_ws, size_t ws_size,
                              hipStream_t stream) {
    const unsigned short* x = (const unsigned short*)d_in[0];
    const int* src = (const int*)d_in[1];
    const int* dst = (const int*)d_in[2];
    const unsigned short* Y = (const unsigned short*)d_in[3];
    const unsigned short* ef = (const unsigned short*)d_in[4];
    const unsigned short* w_rad = (const unsigned short*)d_in[5];
    const unsigned short* cg = (const unsigned short*)d_in[6];
    const unsigned short* w_mix = (const unsigned short*)d_in[7];
    unsigned short* out = (unsigned short*)d_out;
    const int N = in_sizes[0] / (C_DIM * M_DIM);
    const int E = in_sizes[1];
    const int epb = (E + 253) / 254;
    const bool fast = N >= 256 && N <= NB_MAX * NPB_MAX && E > 0 && epb <= EPB_MAX && ws_size >= (size_t)W_END;
    if (!fast) {
        float* agg = (float*)d_ws;
        if (N <= 0) return;
        kf_zero<<<1024, 256, 0, stream>>>(agg, (size_t)N * C_DIM * M_DIM);
        if (E > 0) kf_edges<<<4096, C_DIM, 0, stream>>>(x, src, dst, Y, ef, w_rad, cg, agg, E);
        kf_mix<<<2048, C_DIM, 0, stream>>>(agg, w_mix, out, N);
        return;
    }
    const unsigned nb_magic = (unsigned)(((1ull << 39) + N - 1) / N);
    unsigned smask = 0;
    const int ng = (N + 511) / 512;
    int pw = 1;
    while (2 * pw * 4 < ng + 4) pw *= 2;
    for (int i = 0; i < ng; ++i) smask |= (unsigned)(i < 4 * pw ? ((i >> 2) ^ (i >= WLO * ng / 20 && i < WHI * ng / 20)) & 1 : i & 1) << i;
    const int r0 = ((N < 2048 * pw ? N : 2048 * pw) - 128) & ~63, r1 = r0 - (r0 >= 2048 ? 2048 : 128);
    k_fused<<<256, 512, 0, stream>>>(dst, (unsigned)E * 4u, 0x20000u, src, (unsigned)E * 4u, 0x20000u, Y, d_ws, x, ef, w_rad, cg, w_mix, out,
                                     N, E, epb * 4, nb_magic, smask, (unsigned)(4 * 65536 / N), (unsigned)(r0 + (r0 >> 8 & 8)) * 1024u, (unsigned)(r1 + (r1 >> 8 & 8)) * 1024u);
}
```
